```python
import torch

def _softmax(x, axis=-1):
    return torch.softmax(x, dim=axis)
def _gelu(x):
    return torch.nn.functional.gelu(x, approximate="tanh")
def _silu(x):
    return torch.nn.functional.silu(x)
def _sigmoid(x):
    return torch.sigmoid(x)
def _layernorm(x, axis=-1, eps=1e-5):
    m = x.mean(dim=axis, keepdim=True)
    v = x.var(dim=axis, keepdim=True, unbiased=False)
    return (x - m) / torch.sqrt(v + eps)
def _scatter_add(tgt, idx, src, axis=0):
    return tgt.clone().index_add_(axis, idx, src)
def _segment_sum(src, seg, n):
    out = torch.zeros((n,) + src.shape[1:], dtype=src.dtype)
    return out.index_add_(0, seg, src)
def _pad(x, pw):
    flat = tuple(v for pair in reversed(pw) for v in pair)
    return torch.nn.functional.pad(x, flat)

kernel_name = 'parallel_sum__sliding_block__glu_gated_bilinear__prompt'

def setup_inputs():

    _w = (lambda a: (lambda t: t.to(torch.bfloat16) if t.is_floating_point() else t)(torch.as_tensor(a)))
    def _A_setup():
        B, S, D = 1, 2048, 512
        return {
            "x": _w((lambda *s: torch.randn(*s, dtype=torch.bfloat16) * 0.05)(B, S, D)),
            "wqkv": _w((lambda *s: torch.randn(*s, dtype=torch.bfloat16) * 0.05)(D, 3 * D)),
            "wo": _w((lambda *s: torch.randn(*s, dtype=torch.bfloat16) * 0.05)(D, D)),
        }
    _A_in = _A_setup()

    def _B_setup():
        B, S, D, F = 1, 2048, 512, 1792
        return {
            "x": _w((lambda *s: torch.randn(*s, dtype=torch.bfloat16) * 0.05)(B, S, D)),
            "w_gate": _w((lambda *s: torch.randn(*s, dtype=torch.bfloat16) * 0.8)(D, F) * (D ** -0.5)),
            "w_up": _w((lambda *s: torch.randn(*s, dtype=torch.bfloat16) * 0.8)(D, F) * (D ** -0.5)),
            "w_down": _w((lambda *s: torch.randn(*s, dtype=torch.bfloat16) * 0.8)(F, D) * (F ** -0.5)),
        }
    _B_in = _B_setup()

    return {
        "A_x": _A_in["x"],
        "A_wqkv": _A_in["wqkv"],
        "A_wo": _A_in["wo"],
        "B_x": _B_in["x"],
        "B_w_gate": _B_in["w_gate"],
        "B_w_up": _B_in["w_up"],
        "B_w_down": _B_in["w_down"],
    }

def reference(A_x, A_wqkv, A_wo, B_x, B_w_gate, B_w_up, B_w_down):
    def _A_ref(x, wqkv, wo):
        B, S, D = x.shape
        H = 32; Dh = D // H; W = 256
        qkv = (lambda x, *d: x.permute(*d))((x @ wqkv).reshape(B, S, 3, H, Dh), 2, 0, 3, 1, 4)
        q, k, v = qkv[0], qkv[1], qkv[2]
        scores = torch.einsum("bhqd,bhkd->bhqk", q, k) * (Dh ** -0.5)
        idx = torch.arange(S)
        mask = (idx[:, None] - idx[None, :] >= 0) & (idx[:, None] - idx[None, :] < W)
        scores = torch.where(mask[None, None], scores, -1e9)
        p = _softmax(scores, axis=-1)
        o = torch.einsum("bhqk,bhkd->bhqd", p, v)
        return (lambda x, *d: x.permute(*d))(o, 0, 2, 1, 3).reshape(B, S, D) @ wo

    def _B_ref(x, w_gate, w_up, w_down):
        g = torch.einsum("bsd,df->bsf", x, w_gate)
        u = torch.einsum("bsd,df->bsf", x, w_up)
        h = g * u
        return torch.einsum("bsf,fd->bsd", h, w_down)

    x = A_x
    xn = _layernorm(x, axis=-1)
    return x + _A_ref(xn, A_wqkv, A_wo) + _B_ref(xn, B_w_gate, B_w_up, B_w_down)
```

```cpp
#include <hip/hip_runtime.h>
#include <stdint.h>
#include <type_traits>

typedef unsigned short u16;
typedef unsigned long long u64;
typedef short bf16x8 __attribute__((ext_vector_type(8)));
typedef short s16x4 __attribute__((ext_vector_type(4)));
typedef float f32x2 __attribute__((ext_vector_type(2)));
typedef float f32x4 __attribute__((ext_vector_type(4)));
typedef float f32x16 __attribute__((ext_vector_type(16)));
typedef unsigned u32x2 __attribute__((ext_vector_type(2)));
typedef unsigned u32x4 __attribute__((ext_vector_type(4)));
typedef int i32x4 __attribute__((ext_vector_type(4)));
#define LDSP __attribute__((address_space(3)))
#define DI __device__ __forceinline__
template <class T> DI LDSP T* lp(unsigned a) { return (LDSP T*)(size_t)a; }
template <int I, int N, class F> DI void static_for(F&& f) { if constexpr (I < N) { f(std::integral_constant<int, I>{}); static_for<I + 1, N>(f); } }

constexpr size_t WS_EPOCH = 0;
constexpr size_t WS_ERR   = 64;
constexpr size_t WS_FLAGS = 4096;
constexpr size_t WS_QKV   = 1u << 20;
constexpr size_t WS_A2    = 8u << 20;
constexpr size_t WS_DBG   = 24u << 20;

constexpr unsigned R0 = 0, R1 = 32768, R2 = 65536, R3 = 98304;
constexpr unsigned L_K = 98304, L_V = 122880;
constexpr unsigned L_ONES = 147456;
constexpr unsigned L_CSP = 148480;
constexpr unsigned L_CSW = 150528;
constexpr unsigned L_STAT = 152576;
constexpr unsigned L_JUNK = 154624;
constexpr unsigned LDS_BYTES = 163840;
constexpr unsigned CODE_BYTES = 36864;
constexpr unsigned POISON = 0xffffffffu;
constexpr unsigned SPIN_CAP = 1u << 13;

constexpr float QSCALE = 0.25f * 1.4426950408889634f;

DI i32x4 mk_srd(const void* p, unsigned bytes) { u64 a = (u64)p; i32x4 r; r[0] = (int)(unsigned)a; r[1] = (int)((unsigned)(a >> 32) & 0xffffu); r[2] = (int)bytes; r[3] = 0x00020000; return r; }
DI void dma16(i32x4 srd, unsigned voff, unsigned soff, unsigned lds) {
  unsigned keep;
  asm volatile("s_mov_b32 %0, m0\n\ts_mov_b32 m0, %4\n\ts_nop 0\n\tbuffer_load_dwordx4 %1, %2, %3 offen lds\n\ts_mov_b32 m0, %0"
               : "=&s"(keep) : "v"(voff), "s"(srd), "s"(soff), "s"(lds) : "memory");
}
DI void dma4(i32x4 srd, unsigned voff, unsigned lds) {
  unsigned keep;
  asm volatile("s_mov_b32 %0, m0\n\ts_mov_b32 m0, %3\n\ts_nop 0\n\tbuffer_load_dword %1, %2, 0 offen lds\n\ts_mov_b32 m0, %0"
               : "=&s"(keep) : "v"(voff), "s"(srd), "s"(lds) : "memory");
}
DI void ld16_sc1(u32x4& d, i32x4 srd, unsigned voff, unsigned soff) {
  asm volatile("buffer_load_dwordx4 %0, %1, %2, %3 offen sc1" : "=v"(d) : "v"(voff), "s"(srd), "s"(soff) : "memory");
}
DI void ld16(u32x4& d, i32x4 srd, unsigned voff, unsigned soff) {
  asm volatile("buffer_load_dwordx4 %0, %1, %2, %3 offen" : "=v"(d) : "v"(voff), "s"(srd), "s"(soff) : "memory");
}
DI void ld8_sc1(u32x2& d, i32x4 srd, unsigned voff) {
  asm volatile("buffer_load_dwordx2 %0, %1, %2, 0 offen sc1" : "=v"(d) : "v"(voff), "s"(srd) : "memory");
}
DI void st16_sc1(u32x4 d, i32x4 srd, unsigned voff, unsigned soff) {
  asm volatile("buffer_store_dwordx4 %0, %1, %2, %3 offen sc1\n\ts_nop 1" :: "v"(d), "v"(voff), "s"(srd), "s"(soff) : "memory");
}
DI void st16(u32x4 d, i32x4 srd, unsigned voff, unsigned soff) {
  asm volatile("buffer_store_dwordx4 %0, %1, %2, %3 offen\n\ts_nop 1" :: "v"(d), "v"(voff), "s"(srd), "s"(soff) : "memory");
}
template <int N> DI void bar_vm() { static_assert(N >= 0 && N < 64, "vmcnt range"); asm volatile("s_waitcnt vmcnt(%0) lgkmcnt(0)\n\ts_barrier" :: "n"(N) : "memory"); }
template <int N> DI void wait_vm() { static_assert(N >= 0 && N < 64, "vmcnt range"); asm volatile("s_waitcnt vmcnt(%0)" :: "n"(N) : "memory"); }
DI s16x4 ldtr(unsigned a) { return __builtin_amdgcn_ds_read_tr16_b64_v4i16(lp<s16x4>(a)); }
DI bf16x8 ldfrag_tr(unsigned a, unsigned second) {
  s16x4 lo = ldtr(a), hi = ldtr(a + second);
  return __builtin_shufflevector(lo, hi, 0, 1, 2, 3, 4, 5, 6, 7);
}
DI bf16x8 ld128(unsigned a) { return *lp<bf16x8>(a); }
DI unsigned cvtpk(float lo, float hi) { typedef __bf16 b2 __attribute__((ext_vector_type(2))); f32x2 v = {lo, hi}; return __builtin_bit_cast(unsigned, __builtin_convertvector(v, b2)); }
DI float bflo(unsigned v) { return __uint_as_float(v << 16); }
DI float bfhi(unsigned v) { return __uint_as_float(v & 0xffff0000u); }
DI float rbf(float f) { return bflo(cvtpk(f, f) & 0xffffu); }
DI int fresh(int v) { asm volatile("" : "+v"(v)); return v; }
template <class T> DI void pin(T& v) { asm volatile("" : "+v"(v)); }
DI unsigned pinv(unsigned v) { asm volatile("" : "+v"(v)); return v; }
#define MFMA32(a, b, c) __builtin_amdgcn_mfma_f32_32x32x16_bf16((a), (b), (c), 0, 0, 0)
#define MFMA16(a, b, c) __builtin_amdgcn_mfma_f32_16x16x32_bf16((a), (b), (c), 0, 0, 0)
#define MFMA4(a, b, c) __builtin_amdgcn_mfma_f32_4x4x4bf16_1k((a), (b), (c), 0, 0, 0)
#define SB() __builtin_amdgcn_sched_barrier(0)
DI float swap_add(float v) { auto r = __builtin_amdgcn_permlane32_swap(__float_as_uint(v), __float_as_uint(v), false, false); return __uint_as_float(r[0]) + __uint_as_float(r[1]); }
DI float swap_max(float v) { auto r = __builtin_amdgcn_permlane32_swap(__float_as_uint(v), __float_as_uint(v), false, false); return __builtin_fmaxf(__uint_as_float(r[0]), __uint_as_float(r[1])); }

#define STAMP(i) do { } while (0)

struct Ctx {
  char* ws; u64 tag;
  i32x4 sX, sQKVw, sG, sU, sWO, sWD, sWS;
  int lane, wid, mg, ng, rb, cs, c, cg; unsigned stampreg;
};

DI unsigned flag_off(int ev, int rb, int cs) { return (unsigned)(WS_FLAGS + (((ev * 16 + rb) * 16 + cs) << 7)); }
DI void publish(const Ctx& C, int ev) { if (C.wid == 0 && C.lane == 0) __hip_atomic_store((u64*)(C.ws + flag_off(ev, C.rb, C.cs)), C.tag, __ATOMIC_RELAXED, __HIP_MEMORY_SCOPE_AGENT); }
DI void give_up(char* ws) { __hip_atomic_store((unsigned*)(ws + WS_ERR), 1u, __ATOMIC_RELAXED, __HIP_MEMORY_SCOPE_AGENT); }
DI void spin_flags(char* ws, unsigned off, bool need, u64 tag) {
#pragma nounroll
  for (unsigned it = 0; it < SPIN_CAP; ++it) {
    u64 v = __hip_atomic_load((u64*)(ws + off), __ATOMIC_RELAXED, __HIP_MEMORY_SCOPE_AGENT);
    if (__all(!need || v == tag)) return;
    __builtin_amdgcn_s_sleep(2);
  }
  give_up(ws);
}
DI void check_flags(char* ws, unsigned off, bool need, u64 tag, u32x2& got) {
  asm volatile("" : "+v"(got));
  bool ok = !need || (got[0] == (unsigned)tag && got[1] == (unsigned)(tag >> 32));
  if (!__all(ok)) spin_flags(ws, off, need, tag);
}
template <int K0, int K1> DI void check_frags(const Ctx& C, u32x4 (&a2)[36], unsigned base) {
  auto dirty = [&]() { unsigned mx = 0;
    static_for<K0, K1>([&](auto kc) { constexpr int k = decltype(kc)::value;
      mx = __builtin_elementwise_max(mx, __builtin_elementwise_max(__builtin_elementwise_max(a2[k][0], a2[k][1]), __builtin_elementwise_max(a2[k][2], a2[k][3]))); });
    return __any(mx == POISON); };
  if (dirty()) {
#pragma nounroll
    for (unsigned it = 0; it < SPIN_CAP; ++it) {
      __builtin_amdgcn_s_sleep(2);
      static_for<K0, K1>([&](auto kc) { constexpr int k = decltype(kc)::value; ld16_sc1(a2[k], C.sWS, C.lane * 16, base + k * 1024); });
      wait_vm<0>();
      static_for<K0, K1>([&](auto kc) { constexpr int k = decltype(kc)::value; pin(a2[k]); });
      if (!dirty()) return;
    }
    give_up(C.ws);
  }
}

enum { T_X2, T_X3, T_S0, T_E0 = T_S0 + 12, T_G0 = T_E0 + 3, T_POISON = T_G0 + 3, T_POLL, T_KL, T_VL, T_POLL2, T_NONE };
template <int TYPE> struct G1 {
  static constexpr int kind(int p) { return p == 1 ? 2 : TYPE == 0 ? (p == 0 ? 0 : 2) : (p == 0 ? 1 : 3); }
  static constexpr int nt(int p) { return (kind(p) == 0 || kind(p) == 3) ? 1 : 2; }
  static constexpr int nld(int s) { return nt(s >> 2) == 1 ? 2 : 4; }
  static constexpr int nst(int p) { return kind(p) == 1 ? 4 : kind(p) == 3 ? 1 : 2; }
  static constexpr int npoison() { return (TYPE == 0 ? 2 : 1) + 1; }
  static constexpr unsigned slot(int s) {
    return s == 0 ? R1 : s == 1 ? R3 : s == 2 ? R1 : s == 3 ? R3 : s == 4 ? R0 : s == 5 ? R1 : s == 6 ? R2 : s == 7 ? R3 : s == 8 ? R0 : s == 9 ? R1 : s == 10 ? R2 : R0; }
  static constexpr int grp(int b, int g) {
    return b == 0 ? (g == 0 ? T_X2 : T_S0 + 2) : b == 1 ? (g == 0 ? T_X3 : T_S0 + 3) : b == 2 ? T_S0 + 4 + g : b == 3 ? T_S0 + 6 + g
         : g == 1 ? T_NONE : b == 4 ? T_S0 + 8 : b == 5 ? T_S0 + 9 : b == 6 ? T_S0 + 10 : b == 7 ? T_NONE : b == 8 ? T_S0 + 11 : T_G0 + (b - 9); }
  static constexpr int gsize(int t) { return t == T_NONE ? 0 : (t >= T_S0 && t < T_S0 + 12) ? nld(t - T_S0) : 4; }
  static constexpr int rel(int b, int k) { const int n = gsize(grp(b, 0)) + gsize(grp(b, 1)); return b < 3 ? 0 : b == 11 ? (k >> 1) : n > 4 ? k : 2 * k; }
  static constexpr int after(int t, int b) {
    int cnt = -1;
    auto see = [&](int tag, int n) { for (int i = 0; i < n; ++i) { if (tag == t) cnt = 0; else if (cnt >= 0) ++cnt; } };
    see(-1, 4); see(T_S0, nld(0)); see(-2, 4); see(T_S0 + 1, nld(1));
    for (int pos = 0; pos < 96; ++pos) {
      const int s = pos >> 3, i = pos & 7;
      if (i == 6) {
        if (s == b) return cnt;
        if (s == 8) see(T_POLL, 1);
        if (s == 9) see(T_KL, 3);
        if (s == 10) see(T_VL, 4);
        if (s == 11) see(T_POLL2, 1);
      }
      if (pos >= 6) { const int bb = (pos - 6) >> 3, r = (pos - 6) & 7; int k = 0;
        for (int g = 0; g < 2; ++g) for (int j = 0; j < gsize(grp(bb, g)); ++j, ++k) if (rel(bb, k) == r) see(grp(bb, g), 1);
        if (bb == 2 && r == 0) see(T_POISON, npoison()); }
      if ((pos & 31) == 31) see(T_E0 + (pos >> 5), nst(pos >> 5));
    }
    return cnt;
  }
  static constexpr int mn(int a, int b) { return a < b ? a : b; }
};

template <int TYPE> DI unsigned tile_addr(const Ctx& C, int p, int t) {
  const int lane = fresh(C.lane), kind = G1<TYPE>::kind(p);
  const int g = (lane >> 4) & 1, h = lane >> 5, q = (lane & 15) >> 2, pp = lane & 3;
  const unsigned rdrow = (8 * h + q) * 128 + pp * 8;
  if (kind == 3) return g * 8192 + (8 * h + q) * 64 + pp * 8 + ((C.ng ^ g) << 5);
  if (kind == 2) return g * 16384 + rdrow + (((2 * C.ng + t) ^ (2 * (q >> 1)) ^ g) << 5);
  if (kind == 0) return rdrow + ((C.ng ^ (q >> 1)) << 6) + g * 32;
  return C.ng * 16384 + rdrow + ((t ^ (q >> 1)) << 6) + g * 32;
}
template <int TYPE> DI unsigned stage_voff(const Ctx& C, int p) {
  const int kind = G1<TYPE>::kind(p), lane = fresh(C.lane);
  if (kind == 3) return (16 * C.wid + (lane >> 2)) * 3584 + (24 + ((C.cs - 8) >> 1)) * 128 + ((C.cs - 8) & 1) * 64 + ((lane & 3) << 4);
  const unsigned rowl = 16 * C.wid + (lane >> 3), sw = ((lane & 7) ^ (((lane >> 4) & 1) << 2)) << 4;
  if (kind == 2) return rowl * 3584 + (p == 1 ? C.cs : 16 + C.cs) * 128 + sw;
  return rowl * 3072 + (TYPE == 0 ? C.cs : 8 + 2 * (C.cs - 8)) * 128 + sw;
}
template <int TYPE> DI void issue_stage(const Ctx& C, int s, int k, unsigned v0) {
  const int p = s >> 2, kind = G1<TYPE>::kind(p);
  const unsigned slot = G1<TYPE>::slot(s), r0 = 128 * (s & 3);
  if (kind == 3) dma16(k ? C.sU : C.sG, k ? v0 ^ 32 : v0, r0 * 3584, slot + k * 8192 + C.wid * 1024);
  else if (kind == 2) dma16(k >> 1 ? C.sU : C.sG, k >> 1 ? v0 ^ 32 : v0, (r0 + 8 * (k & 1)) * 3584, slot + (k >> 1) * 16384 + C.wid * 2048 + (k & 1) * 1024);
  else dma16(C.sQKVw, v0 + (k >> 1) * 128, (r0 + 8 * (k & 1)) * 3072, slot + (k >> 1) * 16384 + C.wid * 2048 + (k & 1) * 1024);
}
DI void issue_x(const Ctx& C, int kq, unsigned lds) {
  const int lane = fresh(C.lane), r4 = lane >> 4;
#pragma unroll
  for (int i = 0; i < 4; ++i) {
    const unsigned r15 = 4 * i + r4;
    const unsigned voff = (16 * C.wid + r15) * 1024 + (((lane & 15) ^ r15) << 4);
    dma16(C.sX, voff, kq * 256, lds + (16 * C.wid + 4 * i) * 256);
  }
}
DI unsigned g_voff(const Ctx& C) { const int lane = fresh(C.lane); return (32 * (C.wid & 1) + (lane >> 3)) * 1024 + C.cg * 128 + (((lane & 7) ^ (((lane >> 4) & 1) << 2)) << 4); }
constexpr unsigned gslot(int j) { return j == 0 ? R1 : j == 1 ? R2 : j == 2 ? R0 : j == 3 ? R3 : j == 4 ? R1 : j == 5 ? R2 : j == 6 ? R0 : j == 7 ? R3 : R1; }
DI void issue_g(const Ctx& C, int j, int i, unsigned v) {
  const int kq = C.wid >> 1;
  const int row0 = j < 4 ? (4 * kq + j) * 64 : j < 7 ? (16 + 3 * kq + (j - 4)) * 64 : 128 * kq + 64 * (j - 7);
  dma16(j < 7 ? C.sWD : C.sWO, v, (unsigned)(row0 + 8 * i) * 1024, gslot(j) + kq * 8192 + (C.wid & 1) * 4096 + i * 1024);
}

template <int TYPE> DI void epi_qkv(const Ctx& C, const f32x16& acc, const f32x4 (&csr)[4], float m, float rinv, int qu, int h4) {
  const int h = C.lane >> 5;
  const float sc = TYPE == 0 ? rinv * QSCALE : rinv;
  float y[16];
#pragma unroll
  for (int i4 = 0; i4 < 4; ++i4)
#pragma unroll
    for (int e = 0; e < 4; ++e) y[4 * i4 + e] = (acc[4 * i4 + e] - m * csr[i4][e]) * sc;
  const unsigned base = (unsigned)WS_QKV + (((C.rb * 24 + qu) * 4 + C.mg) * 4 + h4) * 1024;
#pragma unroll
  for (int hh = 0; hh < 2; ++hh) {
    u32x4 f; f[0] = cvtpk(y[8 * hh], y[8 * hh + 1]); f[1] = cvtpk(y[8 * hh + 2], y[8 * hh + 3]); f[2] = cvtpk(y[8 * hh + 4], y[8 * hh + 5]); f[3] = cvtpk(y[8 * hh + 6], y[8 * hh + 7]);
    unsigned voff = C.lane * 16;
    if (TYPE == 1 && qu >= 16) {
      auto a = __builtin_amdgcn_permlane32_swap(f[0], f[2], false, false); auto b = __builtin_amdgcn_permlane32_swap(f[1], f[3], false, false);
      f[0] = a[0]; f[2] = a[1]; f[1] = b[0]; f[3] = b[1];
      voff = (C.lane & 31) * 32 + h * 16;
    }
    st16_sc1(f, C.sWS, voff, base + hh * 1024);
  }
}
DI void epi_gu(const Ctx& C, const f32x16& acc, const f32x4 (&csr)[4], float m, float r2, unsigned a2off) {
  float y[8];
#pragma unroll
  for (int i4 = 0; i4 < 2; ++i4)
#pragma unroll
    for (int e = 0; e < 4; ++e) y[4 * i4 + e] = ((acc[4 * i4 + e] - m * csr[i4][e]) * r2) * (acc[8 + 4 * i4 + e] - m * csr[2 + i4][e]);
  u32x4 f; f[0] = cvtpk(y[0], y[1]); f[1] = cvtpk(y[2], y[3]); f[2] = cvtpk(y[4], y[5]); f[3] = cvtpk(y[6], y[7]);
  st16_sc1(f, C.sWS, C.lane * 16, a2off);
}
template <int TYPE> DI unsigned a2_pass(const Ctx& C, int p) {
  if (p == 1) return (unsigned)WS_A2 + (((4 * C.rb + C.mg) * 4 + (C.cs >> 2)) * 36 + 4 * (C.cs & 3) + 2 * C.ng) * 1024;
  if (TYPE == 0) return (unsigned)WS_A2 + (((4 * C.rb + C.mg) * 4 + C.cs / 3) * 36 + 4 * (4 + C.cs % 3) + 2 * C.ng) * 1024;
  const int u = 8 + ((C.cs - 8) >> 1), half = (C.cs - 8) & 1;
  return (unsigned)WS_A2 + (((4 * C.rb + C.mg) * 4 + u / 3) * 36 + 4 * (4 + u % 3) + 2 * half + C.ng) * 1024;
}
DI unsigned a2_o(const Ctx& C) { return (unsigned)WS_A2 + (((4 * C.rb + C.mg) * 4 + (C.cs >> 2)) * 36 + 28 + 2 * (C.cs & 3) + C.ng) * 1024; }

template <int TYPE> DI void gemm1(Ctx& C, u32x4& qfrag, u32x2& poll2, unsigned poll2off) {
  using G = G1<TYPE>;
  const int lane = C.lane;
  char* ws = C.ws;
  bf16x8 xa[32];
  bf16x8 wf[3][2];
  f32x16 acc[2];
  unsigned ta[2], ab[2], sv = 0;
  ta[0] = tile_addr<TYPE>(C, 0, 0); ta[1] = tile_addr<TYPE>(C, 0, 1);
  const unsigned r31 = lane & 31, hh = lane >> 5;
  const unsigned xlane = (32 * C.mg + r31) * 256 + ((hh ^ (r31 & 15)) << 4);
  f32x4 ssum = {0, 0, 0, 0}, ssq = {0, 0, 0, 0};
  f32x4 csacc[2];
  bf16x8 sel;
  { const short one = ((lane & 15) == ((lane >> 4) & 1)) ? (short)0x3f80 : (short)0;
#pragma unroll
    for (int e = 0; e < 8; ++e) sel[e] = one; }
  float csn[2];
  f32x4 csr[2][4];
  float m = 0.f, rinv = 0.f;
  u32x2 pollv; u32x4 gk[4];
  unsigned polloff = 0; bool pollneed = false;

  STAMP(1);
  bar_vm<G::after(T_S0, 0) >= 0 ? 4 + G::nld(1) : 0>();
  STAMP(2);
  u64 ev = __hip_atomic_load((u64*)(ws + WS_EPOCH), __ATOMIC_RELAXED, __HIP_MEMORY_SCOPE_AGENT);
  auto read_x = [&](int st) {
    const unsigned base = ((st & 1) ? R2 : R0) + xlane;
#pragma unroll
    for (int i = 0; i < 8; ++i) xa[8 * st + i] = ld128(base ^ (((i + 2 * C.c) & 7) << 5));
  };
  auto read_w = [&](int pos) {
    const int p = pos >> 5, s = pos >> 3, i = pos & 7;
    const bool half = G::kind(p) == 3;
    if ((pos & 31) == 0 && pos > 0) { ta[0] = tile_addr<TYPE>(C, p, 0); ta[1] = tile_addr<TYPE>(C, p, 1); }
    if ((i & 1) == 0) { const unsigned so = G::slot(s) + (((i >> 1) + C.c) & 3) * (half ? 2048 : 4096);
      ab[0] = pinv(ta[0] + so); if (G::nt(p) == 2) ab[1] = pinv(ta[1] + so); }
    const unsigned o = (i & 1) * (half ? 1024 : 2048), second = half ? 256 : 512;
    wf[pos % 3][0] = ldfrag_tr(ab[0] + o, second);
    if (G::nt(p) == 2) wf[pos % 3][1] = ldfrag_tr(ab[1] + o, second);
  };
  read_x(0); read_w(0); read_w(1);
  static_for<0, 96>([&](auto ic) {
    constexpr int pos = decltype(ic)::value;
    constexpr int p = pos >> 5, s = pos >> 3, st = s & 3, i = pos & 7, NT = G::nt(p);
    if constexpr (i == 6) {
      STAMP(10 + 2 * s);
      if constexpr (s == 0) { bar_vm<0>();
        pin(ev);
        { const u64 t = ev * 6364136223846793005ull + 1442695040888963407ull;
          C.tag = ((u64)__builtin_amdgcn_readfirstlane((unsigned)(t >> 32)) << 32) | __builtin_amdgcn_readfirstlane((unsigned)t); } }
      else if constexpr (s <= 2) bar_vm<0>();
      else if constexpr (s == 5) { bar_vm<G::mn(G::after(T_S0 + 6, 5), G::after(T_E0, 5))>(); publish(C, 0); }
      else if constexpr (s == 9) { bar_vm<G::mn(G::mn(G::after(T_S0 + 10, 9), G::after(T_E0 + 1, 9)), G::after(T_POLL, 9))>(); }
      else if constexpr (s == 10) bar_vm<G::mn(G::after(T_S0 + 11, 10), G::after(T_KL, 10))>();
      else if constexpr (s == 11) bar_vm<G::after(T_VL, 11)>();
      else bar_vm<G::after(T_S0 + s + 1, s)>();
      if constexpr (s == 8) {
        const int u = C.cs >> 1, l = lane & 7;
        const int ty = l == 0 ? 0 : l < 4 ? 1 : 2, rbp = l == 0 ? C.rb : C.rb - 2 + (l - 1) % 3;
        const int qu = 8 * ty + u;
        const int pcs = qu < 8 ? qu : 8 + ((qu - 8) >> 1);
        pollneed = l < 7 && rbp >= 0;
        polloff = flag_off(0, pollneed ? rbp : C.rb, pcs);
        ld8_sc1(pollv, C.sWS, polloff);
      }
      if constexpr (s == 9) {
        check_flags(ws, polloff, pollneed, C.tag, pollv);
        const int u = C.cs >> 1, h4 = 2 * (C.cs & 1) + C.ng;
#pragma unroll
        for (int b = 0; b < 3; ++b) { const int rbp = C.rb - 2 + b < 0 ? 0 : C.rb - 2 + b;
          ld16_sc1(gk[b], C.sWS, lane * 16, (unsigned)WS_QKV + (((rbp * 24 + 8 + u) * 4 + C.mg) * 4 + h4) * 1024); }
        publish(C, 1);
      }
      if constexpr (s == 10) {
        pin(gk[0]); pin(gk[1]); pin(gk[2]);
#pragma unroll
        for (int b = 0; b < 3; ++b) *lp<u32x4>(L_K + ((4 * b + C.mg) * 2 + C.ng) * 1024 + lane * 16) = gk[b];
        const int u = C.cs >> 1, h4 = 2 * (C.cs & 1) + C.ng;
#pragma unroll
        for (int b = 0; b < 3; ++b) { const int rbp = C.rb - 2 + b < 0 ? 0 : C.rb - 2 + b;
          ld16_sc1(gk[b], C.sWS, lane * 16, (unsigned)WS_QKV + (((rbp * 24 + 16 + u) * 4 + C.mg) * 4 + h4) * 1024); }
        ld16_sc1(qfrag, C.sWS, lane * 16, (unsigned)WS_QKV + (((C.rb * 24 + u) * 4 + C.mg) * 4 + h4) * 1024);
      }
      if constexpr (s == 11) {
        pin(qfrag); pin(gk[0]); pin(gk[1]); pin(gk[2]);
#pragma unroll
        for (int b = 0; b < 3; ++b) *lp<u32x4>(L_V + C.ng * 12288 + (4 * b + C.mg) * 1024 + lane * 16) = gk[b];
        ld8_sc1(poll2, C.sWS, poll2off);
      }
      if constexpr (s < 3) read_x(s + 1);
      if constexpr (st == 3) {
#pragma unroll
        for (int t = 0; t < NT; ++t) { const unsigned a = L_CSP + (C.ng * 2 + t) * 512 + r31 * 4;
          csn[t] = (*lp<float>(a) + *lp<float>(a + 128)) + (*lp<float>(a + 256) + *lp<float>(a + 384)); }
      }
      STAMP(11 + 2 * s);
    }
    if constexpr (pos >= 6) {
      constexpr int bb = (pos - 6) >> 3, r = (pos - 6) & 7;
      static_for<0, 8>([&](auto kc) {
        constexpr int k = decltype(kc)::value, n0 = G::gsize(G::grp(bb, 0)), n1 = G::gsize(G::grp(bb, 1));
        if constexpr (k < n0 + n1 && G::rel(bb, k) == r) {
          constexpr int tg = k < n0 ? G::grp(bb, 0) : G::grp(bb, 1), j = k < n0 ? k : k - n0;
          if constexpr (tg == T_X2) { if constexpr (j == 0) issue_x(C, 2, R0); }
          else if constexpr (tg == T_X3) { if constexpr (j == 0) issue_x(C, 3, R2); }
          else if constexpr (tg >= T_G0) { if constexpr (j == 0) sv = g_voff(C); issue_g(C, tg - T_G0, j, sv); }
          else { if constexpr (j == 0) sv = stage_voff<TYPE>(C, (tg - T_S0) >> 2); issue_stage<TYPE>(C, tg - T_S0, j, sv); }
        }
      });
      if constexpr (bb == 2 && r == 0) {
        const u32x4 pz = {POISON, POISON, POISON, POISON};
        const unsigned a = a2_pass<TYPE>(C, 2);
        st16_sc1(pz, C.sWS, lane * 16, a);
        if (TYPE == 0) st16_sc1(pz, C.sWS, lane * 16, a + 1024);
        st16_sc1(pz, C.sWS, lane * 16, a2_o(C));
      }
    }
    if constexpr (pos + 2 < 96) read_w(pos + 2);
    if constexpr ((pos & 31) == 0) { acc[0] = MFMA32(wf[pos % 3][0], xa[pos & 31], f32x16{}); if (NT == 2) acc[1] = MFMA32(wf[pos % 3][1], xa[pos & 31], f32x16{}); }
    else { acc[0] = MFMA32(wf[pos % 3][0], xa[pos & 31], acc[0]); if (NT == 2) acc[1] = MFMA32(wf[pos % 3][1], xa[pos & 31], acc[1]); }
    if constexpr (i < 2) {
      if constexpr ((pos & 31) == 0) { csacc[0] = MFMA16(wf[pos % 3][0], sel, (f32x4{0, 0, 0, 0})); if (NT == 2) csacc[1] = MFMA16(wf[pos % 3][1], sel, (f32x4{0, 0, 0, 0})); }
      else { csacc[0] = MFMA16(wf[pos % 3][0], sel, csacc[0]); if (NT == 2) csacc[1] = MFMA16(wf[pos % 3][1], sel, csacc[1]); }
    }
    if constexpr (p == 0 && i < 4) {
      const bf16x8 xv = xa[pos & 31]; const s16x4 lo = __builtin_shufflevector(xv, xv, 0, 1, 2, 3), hi = __builtin_shufflevector(xv, xv, 4, 5, 6, 7);
      const s16x4 ones = {0x3f80, 0x3f80, 0x3f80, 0x3f80};
      ssum = MFMA4(ones, lo, ssum); ssq = MFMA4(lo, lo, ssq); ssum = MFMA4(ones, hi, ssum); ssq = MFMA4(hi, hi, ssq);
    }
    if constexpr (st == 3 && i == 4) {
      if ((lane & 15) < 2) {
#pragma unroll
        for (int t = 0; t < NT; ++t) *lp<f32x4>(L_CSP + ((C.ng * 2 + t) * 4 + C.mg) * 128 + (16 * (lane & 15) + 4 * (lane >> 4)) * 4) = csacc[t];
      }
      if constexpr (p == 0) {
        const float sq = (lane & 3) == 0 ? ssq[0] : (lane & 3) == 1 ? ssq[1] : (lane & 3) == 2 ? ssq[2] : ssq[3];
        const f32x2 v = {swap_add(ssum[0]), swap_add(sq)};
        if (lane < 32) *lp<f32x2>(L_STAT + (C.ng * 128 + 32 * C.mg + lane) * 8) = v;
      }
    }
    if constexpr (st == 3 && i == 6) {
#pragma unroll
      for (int t = 0; t < NT; ++t) *lp<float>(L_CSW + (C.wid * 2 + t) * 128 + r31 * 4) = csn[t];
    }
    if constexpr (st == 3 && i == 7) {
#pragma unroll
      for (int t = 0; t < NT; ++t)
#pragma unroll
        for (int i4 = 0; i4 < 4; ++i4) csr[t][i4] = *lp<f32x4>(L_CSW + (C.wid * 2 + t) * 128 + (8 * i4 + 4 * hh) * 4);
    }
    SB();
    if constexpr ((pos & 31) == 31) {
      if constexpr (p == 0) {
        const f32x2 a = *lp<f32x2>(L_STAT + (32 * C.mg + r31) * 8), b = *lp<f32x2>(L_STAT + (128 + 32 * C.mg + r31) * 8);
        const float mean = (a[0] + b[0]) * (1.f / 512.f);
        const float var = __builtin_fmaxf((a[1] + b[1]) * (1.f / 512.f) - mean * mean, 0.f);
        m = rbf(mean);
        rinv = __builtin_amdgcn_rcpf(rbf(__builtin_amdgcn_sqrtf(rbf(rbf(var) + 1e-5f))));
      }
      if constexpr (G::kind(p) == 2) {
        const unsigned a2 = a2_pass<TYPE>(C, p);
        epi_gu(C, acc[0], csr[0], m, rinv * rinv, a2); epi_gu(C, acc[1], csr[1], m, rinv * rinv, a2 + 1024);
      } else if constexpr (G::kind(p) == 3) {
        epi_gu(C, acc[0], csr[0], m, rinv * rinv, a2_pass<TYPE>(C, p));
      } else if constexpr (G::kind(p) == 0) {
        epi_qkv<0>(C, acc[0], csr[0], m, rinv, C.cs, 2 * C.ng);
      } else {
        const int qu = 8 + 2 * (C.cs - 8) + C.ng;
        epi_qkv<1>(C, acc[0], csr[0], m, rinv, qu, 0); epi_qkv<1>(C, acc[1], csr[1], m, rinv, qu, 2);
      }
      SB();
    }
  });
  static_assert(G::after(T_POLL2, 12) >= 0, "");
  wait_vm<G::after(T_POLL2, 12)>();
}

template <bool EDGE, class SIDE> DI u32x4 attention(Ctx& C, const u32x4& qraw, SIDE&& side) {
  const int lane = C.lane, h = lane >> 5, g = (lane >> 4) & 1, q = (lane & 15) >> 2, pp = lane & 3;
  const bf16x8 qf = __builtin_bit_cast(bf16x8, qraw);
  const unsigned kaddr = pinv(L_K + (C.mg * 2 + C.ng) * 1024 + lane * 16);
  const int t0 = EDGE ? (C.rb == 0 ? 8 : 4) - C.mg : 1;
  const int dq = (lane & 31) - 4 * h;
  const float NEG = -1e30f;
  auto older = [&](int r) { return ((r & 3) + 8 * (r >> 2)) > dq; };
  auto max16 = [&](float& a, float& b, const f32x16& s) {
#pragma unroll
    for (int r = 0; r < 16; r += 4) { a = __builtin_fmaxf(__builtin_fmaxf(a, s[r]), s[r + 1]); b = __builtin_fmaxf(__builtin_fmaxf(b, s[r + 2]), s[r + 3]); } };
  float mx;
  {
    f32x16 S[3];
    f32x16 sl = MFMA32(ld128(kaddr + 8 * 2048), qf, f32x16{});
    f32x16 sf;
    if (!EDGE) sf = MFMA32(ld128(kaddr), qf, f32x16{});
    S[1] = MFMA32(ld128(kaddr + 1 * 2048), qf, f32x16{});
    S[2] = MFMA32(ld128(kaddr + 2 * 2048), qf, f32x16{});
    float a = NEG, b = NEG;
    side(std::integral_constant<int, 0>{});
    SB();
#pragma unroll
    for (int r = 0; r < 16; ++r) sl[r] = older(r) ? (EDGE ? NEG : sf[r]) : sl[r];
    max16(a, b, sl);
    static_for<1, 8>([&](auto tc) { constexpr int t = decltype(tc)::value;
      if constexpr (t + 2 < 8) S[(t + 2) % 3] = MFMA32(ld128(kaddr + (t + 2) * 2048), qf, f32x16{});
      side(std::integral_constant<int, t>{});
      SB();
      if (!EDGE || t >= t0) max16(a, b, S[t % 3]);
      SB(); });
    mx = swap_max(__builtin_fmaxf(a, b));
  }
  f32x16 negm;
#pragma unroll
  for (int r = 0; r < 16; ++r) negm[r] = -mx;
  const unsigned vbase = pinv(g == 0 ? L_V + C.ng * 12288 + C.mg * 1024 + (4 * h + q) * 32 + pp * 8 : L_ONES + 128 * (1 - h) + q * 32 + pp * 8);
  const unsigned vstr = g == 0 ? 1024u : 0u;
  f32x16 o;
  auto expack = [&](const f32x16& s, u32x4& pa, u32x4& pb) {
#pragma unroll
    for (int i = 0; i < 4; ++i) pa[i] = cvtpk(__builtin_amdgcn_exp2f(s[2 * i]), __builtin_amdgcn_exp2f(s[2 * i + 1]));
#pragma unroll
    for (int i = 0; i < 4; ++i) pb[i] = cvtpk(__builtin_amdgcn_exp2f(s[8 + 2 * i]), __builtin_amdgcn_exp2f(s[9 + 2 * i])); };
  f32x16 S[2];
  {
    f32x16 sl = MFMA32(ld128(kaddr + 8 * 2048), qf, negm);
    f32x16 sf;
    if (!EDGE) sf = MFMA32(ld128(kaddr), qf, negm);
    S[1] = MFMA32(ld128(kaddr + 1 * 2048), qf, negm);
    const unsigned v8 = vbase + 8 * vstr;
    const bf16x8 va = ldfrag_tr(v8, 256), vb = ldfrag_tr(v8 + 512, 256);
    bf16x8 vc, vd;
    if (!EDGE) { vc = ldfrag_tr(vbase, 256); vd = ldfrag_tr(vbase + 512, 256); }
    side(std::integral_constant<int, 8>{});
    SB();
#pragma unroll
    for (int r = 0; r < 16; ++r) sl[r] = older(r) ? (EDGE ? NEG : sf[r]) : sl[r];
    u32x4 pa, pb, mk0, mk1;
    expack(sl, pa, pb);
#pragma unroll
    for (int i = 0; i < 4; ++i) {
      mk0[i] = (older(2 * i) ? 0xffffu : 0u) | (older(2 * i + 1) ? 0xffff0000u : 0u);
      mk1[i] = (older(8 + 2 * i) ? 0xffffu : 0u) | (older(9 + 2 * i) ? 0xffff0000u : 0u); }
    o = MFMA32(va, __builtin_bit_cast(bf16x8, pa & ~mk0), f32x16{});
    o = MFMA32(vb, __builtin_bit_cast(bf16x8, pb & ~mk1), o);
    if (!EDGE) {
      o = MFMA32(vc, __builtin_bit_cast(bf16x8, pa & mk0), o);
      o = MFMA32(vd, __builtin_bit_cast(bf16x8, pb & mk1), o);
    }
    SB();
  }
  static_for<1, 8>([&](auto tc) { constexpr int t = decltype(tc)::value;
    const unsigned vt = vbase + t * vstr;
    const bf16x8 va = ldfrag_tr(vt, 256), vb = ldfrag_tr(vt + 512, 256);
    if constexpr (t + 1 < 8) S[(t + 1) & 1] = MFMA32(ld128(kaddr + (t + 1) * 2048), qf, negm);
    side(std::integral_constant<int, 8 + t>{});
    SB();
    if (!EDGE || t >= t0) {
      u32x4 pa, pb;
      expack(S[t & 1], pa, pb);
      o = MFMA32(va, __builtin_bit_cast(bf16x8, pa), o);
      o = MFMA32(vb, __builtin_bit_cast(bf16x8, pb), o);
    }
    SB(); });
  const float li = __builtin_amdgcn_rcpf(o[8]);
  u32x4 f; f[0] = cvtpk(o[0] * li, o[1] * li); f[1] = cvtpk(o[2] * li, o[3] * li); f[2] = cvtpk(o[4] * li, o[5] * li); f[3] = cvtpk(o[6] * li, o[7] * li);
  return f;
}

__global__ void __launch_bounds__(512, 2)
fused_kernel(const u16* __restrict__ x, const u16* __restrict__ wqkv, const u16* __restrict__ wo, const u16* __restrict__ wg, const u16* __restrict__ wu,
             const u16* __restrict__ wd, u16* __restrict__ out, char* __restrict__ ws) {
  Ctx C;
  const int tid = threadIdx.x, lane = tid & 63, wid = __builtin_amdgcn_readfirstlane(tid >> 6);
  const int b = blockIdx.x, xj = b & 7, xi = b >> 3;
  C.stampreg = 0;
  STAMP(0);
  C.ws = ws; C.lane = lane; C.wid = wid; C.mg = wid & 3; C.ng = wid >> 2;
  C.rb = 8 * (xj & 1) + (xi & 7); C.cs = 4 * (xj >> 1) + (xi >> 3); C.c = (C.mg + 2 * C.ng) & 3;
  if (wid == 7) { u64 pc; asm volatile("s_getpc_b64 %0" : "=s"(pc));
    dma4(mk_srd((const void*)(pc & ~255ull), CODE_BYTES), (xi * 9 + (lane < 9 ? lane : 0)) * 128, L_JUNK); }
  C.sX = mk_srd(x + (size_t)C.rb * 128 * 512, 128 * 1024);
  issue_x(C, 0, R0);
  C.sQKVw = mk_srd(wqkv, 512 * 3072);
  { const unsigned qu = C.cs < 8 ? C.cs : 8 + 2 * (C.cs - 8);
    const unsigned v = (16 * wid + (lane >> 3)) * 3072 + qu * 128 + (((lane & 7) ^ (((lane >> 4) & 1) << 2)) << 4);
    dma16(C.sQKVw, v, 0, R1 + wid * 2048); dma16(C.sQKVw, v, 8 * 3072, R1 + wid * 2048 + 1024);
    if (C.cs >= 8) { dma16(C.sQKVw, v + 128, 0, R1 + 16384 + wid * 2048); dma16(C.sQKVw, v + 128, 8 * 3072, R1 + 16384 + wid * 2048 + 1024); }
    issue_x(C, 1, R2);
    dma16(C.sQKVw, v, 128 * 3072, R3 + wid * 2048); dma16(C.sQKVw, v, 136 * 3072, R3 + wid * 2048 + 1024);
    if (C.cs >= 8) { dma16(C.sQKVw, v + 128, 128 * 3072, R3 + 16384 + wid * 2048); dma16(C.sQKVw, v + 128, 136 * 3072, R3 + 16384 + wid * 2048 + 1024); } }
  C.sWS = mk_srd(ws, 32u << 20);
  C.sG = mk_srd(wg, 512 * 3584); C.sU = mk_srd(wu, 512 * 3584); C.sWO = mk_srd(wo, 512 * 1024); C.sWD = mk_srd(wd, 1792 * 1024);
  if (tid < 256) lp<unsigned>(L_ONES)[tid] = 0x3f803f80u;
  const int rt64 = 4 * xj + (xi & 3), cg = xi >> 2;
  C.cg = cg;
  const int rt = wid & 1, kq = wid >> 1, RT = 2 * rt64 + rt, rb2 = RT >> 2;
  const bool p2need = lane < 10;
  unsigned p2off;
  { const int l = lane < 10 ? lane : 0, u = 3 * kq + ((l - 4) >> 1);
    p2off = l < 4 ? flag_off(1, rb2, 4 * kq + l) : flag_off(0, rb2, u < 8 ? u : 8 + 2 * (u - 8) + (l & 1)); }
  u32x4 qfrag; u32x2 poll2;
  if (C.cs < 8) gemm1<0>(C, qfrag, poll2, p2off); else gemm1<1>(C, qfrag, poll2, p2off);
  STAMP(6);

  const unsigned a2base = (unsigned)WS_A2 + (RT * 4 + kq) * 36 * 1024;
  u32x4 a2[36];
  check_flags(ws, p2off, p2need, C.tag, poll2);
  asm volatile("s_waitcnt lgkmcnt(0)\n\ts_barrier" ::: "memory");
  auto side = [&](auto kc) { constexpr int k = decltype(kc)::value; ld16_sc1(a2[k], C.sWS, lane * 16, a2base + k * 1024); };
  u32x4 of;
  if (C.rb >= 2) of = attention<false>(C, qfrag, side); else of = attention<true>(C, qfrag, side);
  st16_sc1(of, C.sWS, lane * 16, a2_o(C));
  STAMP(7);

  const int g = (lane >> 4) & 1, h = lane >> 5, q = (lane & 15) >> 2, pp = lane & 3;
  const unsigned w2l0 = kq * 8192 + (4 * h + q) * 128 + ((0 ^ (q >> 1)) << 6) + g * 32 + pp * 8;
  const unsigned w2l1 = kq * 8192 + (4 * h + q) * 128 + ((1 ^ (q >> 1)) << 6) + g * 32 + pp * 8;
  bf16x8 wf[3][2];
  f32x16 acc[2];
  u32x4 xres;
  unsigned wb[2], gv;
  auto read_w2 = [&](int pos) {
    if ((pos & 3) == 0) { wb[0] = pinv(w2l0 + gslot(pos >> 2)); wb[1] = pinv(w2l1 + gslot(pos >> 2)); }
    wf[pos % 3][0] = ldfrag_tr(wb[0] + (pos & 3) * 2048, 1024); wf[pos % 3][1] = ldfrag_tr(wb[1] + (pos & 3) * 2048, 1024); };
  bar_vm<1>();
  static_for<0, 16>([&](auto kc) { constexpr int k = decltype(kc)::value; pin(a2[k]); });
  read_w2(0); read_w2(1);
  gv = g_voff(C);
  STAMP(8);
  u32x4 ef[4];
  static_for<0, 36>([&](auto ic) {
    constexpr int pos = decltype(ic)::value;
    constexpr int j = pos >> 2, i = pos & 3;
    if constexpr (i == 2) {
      STAMP(40 + 2 * j);
      if constexpr (j <= 1) bar_vm<63>();
      if constexpr (j == 2) bar_vm<12>();
      if constexpr (j == 3) bar_vm<6>();
      if constexpr (j == 4) bar_vm<7>();
      if constexpr (j == 5) bar_vm<15>();
      if constexpr (j == 6) bar_vm<4>();
      if constexpr (j == 7) bar_vm<0>();
      STAMP(41 + 2 * j);
    }
    if constexpr (pos == 14) { static_for<16, 28>([&](auto kc) { pin(a2[decltype(kc)::value]); }); check_frags<16, 28>(C, a2, a2base); }
    if constexpr (pos == 26) { static_for<28, 36>([&](auto kc) { pin(a2[decltype(kc)::value]); }); pin(xres); check_frags<28, 36>(C, a2, a2base); }
    if constexpr (pos >= 2 && pos <= 5) static_for<0, 3>([&](auto kc) { constexpr int k = 16 + 3 * (pos - 2) + decltype(kc)::value; ld16_sc1(a2[k], C.sWS, lane * 16, a2base + k * 1024); });
    if constexpr (pos == 16) { const i32x4 sXall = mk_srd(x, 2048 * 1024); ld16(xres, sXall, (RT * 32 + (lane & 31)) * 1024 + cg * 128 + kq * 32 + h * 16, 0); }
    if constexpr (pos == 18 || pos == 19) static_for<0, 4>([&](auto kc) { constexpr int k = 28 + 4 * (pos - 18) + decltype(kc)::value; ld16_sc1(a2[k], C.sWS, lane * 16, a2base + k * 1024); });
    if constexpr (pos < 24) issue_g(C, 3 + (pos >> 2), pos & 3, gv);
    if constexpr (pos == 30) { if (b == 0 && wid == 7) {
#pragma unroll
        for (int k = 0; k < 4; ++k) { u32x2 t; ld8_sc1(t, C.sWS, flag_off(0, 0, 0) + (lane * 4 + k) * 128); ef[k][0] = t[0]; ef[k][1] = t[1]; } } }
    if constexpr (pos + 2 < 36) read_w2(pos + 2);
    const bf16x8 af = __builtin_bit_cast(bf16x8, a2[pos]);
    if constexpr (pos == 0) { acc[0] = MFMA32(wf[0][0], af, f32x16{}); acc[1] = MFMA32(wf[0][1], af, f32x16{}); }
    else { acc[0] = MFMA32(wf[pos % 3][0], af, acc[0]); acc[1] = MFMA32(wf[pos % 3][1], af, acc[1]); }
    SB();
  });
  STAMP(9);
  asm volatile("s_waitcnt lgkmcnt(0)\n\ts_barrier" ::: "memory");
#pragma unroll
  for (int d = 0; d < 4; ++d) if (d != kq) {
    const int ct = d >> 1, r0 = (d & 1) * 8;
    const unsigned a = ((rt * 4 + d) * 4 + kq) * 2048 + lane * 16;
    *lp<f32x4>(a) = f32x4{acc[ct][r0], acc[ct][r0 + 1], acc[ct][r0 + 2], acc[ct][r0 + 3]};
    *lp<f32x4>(a + 1024) = f32x4{acc[ct][r0 + 4], acc[ct][r0 + 5], acc[ct][r0 + 6], acc[ct][r0 + 7]};
  }
  float s[8];
#pragma unroll
  for (int e = 0; e < 8; ++e) { const float lo = (kq & 1) ? acc[0][8 + e] : acc[0][e], hi = (kq & 1) ? acc[1][8 + e] : acc[1][e]; s[e] = (kq & 2) ? hi : lo; }
  asm volatile("s_waitcnt lgkmcnt(0)\n\ts_barrier" ::: "memory");
#pragma unroll
  for (int d = 0; d < 4; ++d) if (d != kq) {
    const unsigned a = ((rt * 4 + kq) * 4 + d) * 2048 + lane * 16;
    const f32x4 u = *lp<f32x4>(a), v = *lp<f32x4>(a + 1024);
#pragma unroll
    for (int e = 0; e < 4; ++e) { s[e] += u[e]; s[4 + e] += v[e]; }
  }
  {
    unsigned u0 = __float_as_uint(s[0]), u1 = __float_as_uint(s[1]), u2 = __float_as_uint(s[2]), u3 = __float_as_uint(s[3]);
    unsigned v0 = __float_as_uint(s[4]), v1 = __float_as_uint(s[5]), v2 = __float_as_uint(s[6]), v3 = __float_as_uint(s[7]);
    auto r0 = __builtin_amdgcn_permlane32_swap(u0, v0, false, false); auto r1 = __builtin_amdgcn_permlane32_swap(u1, v1, false, false);
    auto r2 = __builtin_amdgcn_permlane32_swap(u2, v2, false, false); auto r3 = __builtin_amdgcn_permlane32_swap(u3, v3, false, false);
    const float c0 = __uint_as_float(r0[0]), c1 = __uint_as_float(r1[0]), c2 = __uint_as_float(r2[0]), c3 = __uint_as_float(r3[0]);
    const float c4 = __uint_as_float(r0[1]), c5 = __uint_as_float(r1[1]), c6 = __uint_as_float(r2[1]), c7 = __uint_as_float(r3[1]);
    u32x4 f;
    f[0] = cvtpk(c0 + bflo(xres[0]), c1 + bfhi(xres[0])); f[1] = cvtpk(c2 + bflo(xres[1]), c3 + bfhi(xres[1]));
    f[2] = cvtpk(c4 + bflo(xres[2]), c5 + bfhi(xres[2])); f[3] = cvtpk(c6 + bflo(xres[3]), c7 + bfhi(xres[3]));
    const i32x4 sO = mk_srd(out, 2048 * 1024);
    st16(f, sO, (RT * 32 + (lane & 31)) * 1024 + cg * 128 + kq * 32 + h * 16, 0);
  }
  if (b == 0 && wid == 7) {
    wait_vm<0>();
    asm volatile("" : "+v"(ef[0]), "+v"(ef[1]), "+v"(ef[2]), "+v"(ef[3]));
    bool ok = true;
#pragma unroll
    for (int k = 0; k < 4; ++k) ok = ok && ef[k][0] == (unsigned)C.tag && ef[k][1] == (unsigned)(C.tag >> 32);
    if (!__all(ok)) {
#pragma unroll 1
      for (int k = 0; k < 4; ++k) spin_flags(ws, flag_off(0, 0, 0) + (lane * 4 + k) * 128, true, C.tag);
    }
    if (lane == 0) __hip_atomic_store((u64*)(ws + WS_EPOCH), C.tag, __ATOMIC_RELAXED, __HIP_MEMORY_SCOPE_AGENT);
  }
  STAMP(60);
}

extern "C" void kernel_launch(void* const* d_in, const int* in_sizes, int n_in, void* d_out, int out_size, void* d_ws, size_t ws_size, hipStream_t stream) {
  (void)in_sizes; (void)n_in; (void)out_size; (void)ws_size;
  (void)hipFuncSetAttribute((const void*)fused_kernel, hipFuncAttributeMaxDynamicSharedMemorySize, LDS_BYTES);
  fused_kernel<<<256, 512, LDS_BYTES, stream>>>((const u16*)d_in[0], (const u16*)d_in[1], (const u16*)d_in[2], (const u16*)d_in[4], (const u16*)d_in[5], (const u16*)d_in[6],
                                                 (u16*)d_out, (char*)d_ws);
}
```

```cpp
#include <hip/hip_runtime.h>
#include <stdint.h>
#include <type_traits>

typedef unsigned short u16;
typedef unsigned long long u64;
typedef short bf16x8 __attribute__((ext_vector_type(8)));
typedef short s16x4 __attribute__((ext_vector_type(4)));
typedef float f32x2 __attribute__((ext_vector_type(2)));
typedef float f32x4 __attribute__((ext_vector_type(4)));
typedef float f32x16 __attribute__((ext_vector_type(16)));
typedef unsigned u32x2 __attribute__((ext_vector_type(2)));
typedef unsigned u32x4 __attribute__((ext_vector_type(4)));
typedef int i32x4 __attribute__((ext_vector_type(4)));
#define LDSP __attribute__((address_space(3)))
#define DI __device__ __forceinline__
template <class T> DI LDSP T* lp(unsigned a) { return (LDSP T*)(size_t)a; }
template <int I, int N, class F> DI void static_for(F&& f) { if constexpr (I < N) { f(std::integral_constant<int, I>{}); static_for<I + 1, N>(f); } }

constexpr size_t WS_EPOCH = 0;
constexpr size_t WS_ERR   = 64;
constexpr size_t WS_FLAGS = 4096;
constexpr size_t WS_QKV   = 1u << 20;
constexpr size_t WS_A2    = 8u << 20;
constexpr size_t WS_DBG   = 24u << 20;

constexpr unsigned R0 = 0, R1 = 32768, R2 = 65536, R3 = 98304;
constexpr unsigned L_K = 98304, L_V = 122880;
constexpr unsigned L_ONES = 147456;
constexpr unsigned L_CSP = 148480;
constexpr unsigned L_CSW = 150528;
constexpr unsigned L_STAT = 152576;
constexpr unsigned L_JUNK = 154624;
constexpr unsigned LDS_BYTES = 163840;
constexpr unsigned CODE_BYTES = 128;
constexpr unsigned POISON = 0xffffffffu;
constexpr unsigned SPIN_CAP = 1u << 13;

#define NO_WARM 0
#define NO_CS 0
#define NO_DMA 0
constexpr float QSCALE = 0.25f * 1.4426950408889634f;

DI i32x4 mk_srd(const void* p, unsigned bytes) { u64 a = (u64)p; i32x4 r; r[0] = (int)(unsigned)a; r[1] = (int)((unsigned)(a >> 32) & 0xffffu); r[2] = (int)bytes; r[3] = 0x00020000; return r; }
DI void dma16(i32x4 srd, unsigned voff, unsigned soff, unsigned lds) {
  if (NO_DMA) return;
  asm volatile("s_mov_b32 m0, %3\n\ts_nop 0\n\tbuffer_load_dwordx4 %0, %1, %2 offen lds" :: "v"(voff), "s"(srd), "s"(soff), "s"(lds) : "memory");
}
DI void dma4(i32x4 srd, unsigned voff, unsigned lds) {
  asm volatile("s_mov_b32 m0, %2\n\ts_nop 0\n\tbuffer_load_dword %0, %1, 0 offen lds" :: "v"(voff), "s"(srd), "s"(lds) : "memory");
}
DI void ld16_sc1(u32x4& d, i32x4 srd, unsigned voff, unsigned soff) {
  asm volatile("buffer_load_dwordx4 %0, %1, %2, %3 offen sc1" : "=v"(d) : "v"(voff), "s"(srd), "s"(soff) : "memory");
}
DI void ld16(u32x4& d, i32x4 srd, unsigned voff, unsigned soff) {
  asm volatile("buffer_load_dwordx4 %0, %1, %2, %3 offen" : "=v"(d) : "v"(voff), "s"(srd), "s"(soff) : "memory");
}
DI void ld8_sc1(u32x2& d, i32x4 srd, unsigned voff) {
  asm volatile("buffer_load_dwordx2 %0, %1, %2, 0 offen sc1" : "=v"(d) : "v"(voff), "s"(srd) : "memory");
}
DI void st16_sc1(u32x4 d, i32x4 srd, unsigned voff, unsigned soff) {
  asm volatile("buffer_store_dwordx4 %0, %1, %2, %3 offen sc1\n\ts_nop 1" :: "v"(d), "v"(voff), "s"(srd), "s"(soff) : "memory");
}
DI void st16(u32x4 d, i32x4 srd, unsigned voff, unsigned soff) {
  asm volatile("buffer_store_dwordx4 %0, %1, %2, %3 offen\n\ts_nop 1" :: "v"(d), "v"(voff), "s"(srd), "s"(soff) : "memory");
}
template <int N> DI void bar_vm() { static_assert(N >= 0 && N < 64, "vmcnt range"); asm volatile("s_waitcnt vmcnt(%0) lgkmcnt(0)\n\ts_barrier" :: "n"(N) : "memory"); }
template <int N> DI void wait_vm() { static_assert(N >= 0 && N < 64, "vmcnt range"); asm volatile("s_waitcnt vmcnt(%0)" :: "n"(N) : "memory"); }
DI s16x4 ldtr(unsigned a) { return __builtin_amdgcn_ds_read_tr16_b64_v4i16(lp<s16x4>(a)); }
DI bf16x8 ldfrag_tr(unsigned a, unsigned second) {
  s16x4 lo = ldtr(a), hi = ldtr(a + second);
  return __builtin_shufflevector(lo, hi, 0, 1, 2, 3, 4, 5, 6, 7);
}
DI bf16x8 ld128(unsigned a) { return *lp<bf16x8>(a); }
DI unsigned cvtpk(float lo, float hi) { typedef __bf16 b2 __attribute__((ext_vector_type(2))); f32x2 v = {lo, hi}; return __builtin_bit_cast(unsigned, __builtin_convertvector(v, b2)); }
DI float bflo(unsigned v) { return __uint_as_float(v << 16); }
DI float bfhi(unsigned v) { return __uint_as_float(v & 0xffff0000u); }
DI float rbf(float f) { return bflo(cvtpk(f, f) & 0xffffu); }
DI int fresh(int v) { asm volatile("" : "+v"(v)); return v; }
template <class T> DI void pin(T& v) { asm volatile("" : "+v"(v)); }
DI unsigned pinv(unsigned v) { asm volatile("" : "+v"(v)); return v; }
#define MFMA32(a, b, c) __builtin_amdgcn_mfma_f32_32x32x16_bf16((a), (b), (c), 0, 0, 0)
#define MFMA16(a, b, c) __builtin_amdgcn_mfma_f32_16x16x32_bf16((a), (b), (c), 0, 0, 0)
#define MFMA4(a, b, c) __builtin_amdgcn_mfma_f32_4x4x4bf16_1k((a), (b), (c), 0, 0, 0)
#define SB() __builtin_amdgcn_sched_barrier(0)
DI float swap_add(float v) { auto r = __builtin_amdgcn_permlane32_swap(__float_as_uint(v), __float_as_uint(v), false, false); return __uint_as_float(r[0]) + __uint_as_float(r[1]); }
DI float swap_max(float v) { auto r = __builtin_amdgcn_permlane32_swap(__float_as_uint(v), __float_as_uint(v), false, false); return __builtin_fmaxf(__uint_as_float(r[0]), __uint_as_float(r[1])); }

#define STAMP(i) do { } while (0)

struct Ctx {
  char* ws; u64 tag;
  i32x4 sX, sQKVw, sG, sU, sWO, sWD, sWS;
  int lane, wid, mg, ng, rb, cs, c, cg, xi; unsigned stampreg;
};

DI unsigned flag_off(int ev, int rb, int cs) { return (unsigned)(WS_FLAGS + (((ev * 16 + rb) * 16 + cs) << 7)); }
DI void publish(const Ctx& C, int ev) { if (C.wid == 0 && C.lane == 0) __hip_atomic_store((u64*)(C.ws + flag_off(ev, C.rb, C.cs)), C.tag, __ATOMIC_RELAXED, __HIP_MEMORY_SCOPE_AGENT); }
DI void give_up(char* ws) { __hip_atomic_store((unsigned*)(ws + WS_ERR), 1u, __ATOMIC_RELAXED, __HIP_MEMORY_SCOPE_AGENT); }
DI void spin_flags(char* ws, unsigned off, bool need, u64 tag) {
#pragma nounroll
  for (unsigned it = 0; it < SPIN_CAP; ++it) {
    u64 v = __hip_atomic_load((u64*)(ws + off), __ATOMIC_RELAXED, __HIP_MEMORY_SCOPE_AGENT);
    if (__all(!need || v == tag)) return;
    __builtin_amdgcn_s_sleep(2);
  }
  give_up(ws);
}
DI void check_flags(char* ws, unsigned off, bool need, u64 tag, u32x2& got) {
  asm volatile("" : "+v"(got));
  bool ok = !need || (got[0] == (unsigned)tag && got[1] == (unsigned)(tag >> 32));
  if (!__all(ok)) spin_flags(ws, off, need, tag);
}
template <int K0, int K1> DI void check_frags(const Ctx& C, u32x4 (&a2)[36], unsigned base) {
  auto dirty = [&]() { unsigned mx = 0;
    static_for<K0, K1>([&](auto kc) { constexpr int k = decltype(kc)::value;
      mx = __builtin_elementwise_max(mx, __builtin_elementwise_max(__builtin_elementwise_max(a2[k][0], a2[k][1]), __builtin_elementwise_max(a2[k][2], a2[k][3]))); });
    return __any(mx == POISON); };
  if (dirty()) {
#pragma nounroll
    for (unsigned it = 0; it < SPIN_CAP; ++it) {
      __builtin_amdgcn_s_sleep(2);
      static_for<K0, K1>([&](auto kc) { constexpr int k = decltype(kc)::value; ld16_sc1(a2[k], C.sWS, C.lane * 16, base + k * 1024); });
      wait_vm<0>();
      static_for<K0, K1>([&](auto kc) { constexpr int k = decltype(kc)::value; pin(a2[k]); });
      if (!dirty()) return;
    }
    give_up(C.ws);
  }
}

enum { T_H0 = 0, T_S4 = 8, T_E0 = 16, T_G0 = 19, T_POISON = 22, T_POLL, T_KL, T_VL, T_POLL2, T_EPOCH, T_NONE };
template <int TYPE> struct G1 {
  static constexpr int kind(int p) { return p == 1 ? 2 : TYPE == 0 ? (p == 0 ? 0 : 2) : (p == 0 ? 1 : 3); }
  static constexpr int nt(int p) { return (kind(p) == 0 || kind(p) == 3) ? 1 : 2; }
  static constexpr int nst(int p) { return kind(p) == 1 ? 4 : kind(p) == 3 ? 1 : 2; }
  static constexpr int npoison() { return (TYPE == 0 ? 2 : 1) + 1; }
  static constexpr unsigned slot(int s) {
    return s == 4 ? R0 : s == 5 ? R1 : s == 6 ? R2 : s == 7 ? R3 : s == 8 ? R0 : s == 9 ? R1 : s == 10 ? R2 : R0; }
  static constexpr int barpos(int bi) { return bi < 8 ? 4 * bi + 2 : 8 * (bi - 4) + 6; }
  static constexpr int barat(int pos) { return pos < 32 ? ((pos & 3) == 2 ? pos >> 2 : -1) : ((pos & 7) == 6 ? 4 + (pos >> 3) : -1); }
  static constexpr int grp(int bi) { return bi < 4 ? T_H0 + bi + 4 : bi < 11 ? T_S4 + (bi - 4) : bi == 11 ? T_NONE : bi == 12 ? T_S4 + 7 : T_G0 + (bi - 13); }
  static constexpr int gsize(int t) { return t == T_NONE ? 0 : t < T_S4 ? 2 + nt(0) : t < T_E0 ? (nt((t - T_S4 + 4) >> 2) == 1 ? 2 : 4) : 4; }
  static constexpr int rel(int bi, int k) { return bi < 8 ? k : bi == 15 ? (k >> 1) : 2 * k; }
  static constexpr int after(int t, int bi) {
    int cnt = -1;
    auto see = [&](int tag, int n) { for (int i = 0; i < n; ++i) { if (tag == t) cnt = 0; else if (cnt >= 0) ++cnt; } };
    for (int h = 0; h < 2; ++h) see(T_H0 + h, gsize(T_H0 + h));
    see(T_EPOCH, 1);
    for (int pos = 0; pos < 96; ++pos) {
      if (pos < 2) see(T_H0 + 2 + pos, gsize(T_H0 + 2 + pos));
      const int b = barat(pos);
      if (b >= 0) {
        if (b == bi) return cnt;
        if (b == 12) see(T_POLL, 1);
        if (b == 13) see(T_KL, 3);
        if (b == 14) see(T_VL, 4);
        if (b == 15) see(T_POLL2, 1);
      }
      for (int bb = 0; bb < 16; ++bb) for (int k = 0; k < gsize(grp(bb)); ++k) if (barpos(bb) + rel(bb, k) == pos) see(grp(bb), 1);
      if (pos == 16) see(T_POISON, npoison());
      if ((pos & 31) == 31) see(T_E0 + (pos >> 5), nst(pos >> 5));
    }
    return cnt;
  }
  static constexpr int mn(int a, int b) { return a < b ? a : b; }
};

template <int TYPE> DI unsigned tile_addr(const Ctx& C, int p, int t) {
  const int lane = fresh(C.lane), kind = G1<TYPE>::kind(p);
  const int g = (lane >> 4) & 1, h = lane >> 5, q = (lane & 15) >> 2, pp = lane & 3;
  const unsigned rdrow = (8 * h + q) * 128 + pp * 8;
  if (kind == 3) return g * 8192 + (8 * h + q) * 64 + pp * 8 + ((C.ng ^ g) << 5);
  if (kind == 2) return g * 16384 + rdrow + (((2 * C.ng + t) ^ (2 * (q >> 1)) ^ g) << 5);
  if (kind == 0) return 16384 + rdrow + ((C.ng ^ (q >> 1)) << 6) + g * 32;
  return 16384 + C.ng * 8192 + rdrow + ((t ^ (q >> 1)) << 6) + g * 32;
}
template <int TYPE> DI unsigned stage_voff(const Ctx& C, int p) {
  const int kind = G1<TYPE>::kind(p), lane = fresh(C.lane);
  if (kind == 3) return (16 * C.wid + (lane >> 2)) * 3584 + (24 + ((C.cs - 8) >> 1)) * 128 + ((C.cs - 8) & 1) * 64 + ((lane & 3) << 4);
  return (16 * C.wid + (lane >> 3)) * 3584 + (p == 1 ? C.cs : 16 + C.cs) * 128 + (((lane & 7) ^ (((lane >> 4) & 1) << 2)) << 4);
}
template <int TYPE> DI void issue_stage(const Ctx& C, int s, int k, unsigned v0) {
  const int kind = G1<TYPE>::kind(s >> 2);
  const unsigned slot = G1<TYPE>::slot(s), r0 = 128 * (s & 3);
  if (kind == 3) dma16(k ? C.sU : C.sG, k ? v0 ^ 32 : v0, r0 * 3584, slot + k * 8192 + C.wid * 1024);
  else dma16(k >> 1 ? C.sU : C.sG, k >> 1 ? v0 ^ 32 : v0, (r0 + 8 * (k & 1)) * 3584, slot + (k >> 1) * 16384 + C.wid * 2048 + (k & 1) * 1024);
}
DI unsigned hx_voff(const Ctx& C) { const int lane = fresh(C.lane); return (16 * C.wid + (lane >> 3)) * 1024 + (((lane & 7) ^ ((lane >> 4) & 3)) << 4); }
DI unsigned hw_voff(const Ctx& C) { const int lane = fresh(C.lane);
  return (8 * C.wid + (lane >> 3)) * 3072 + (C.cs < 8 ? C.cs : 8 + 2 * (C.cs - 8)) * 128 + (((lane & 7) ^ (((lane >> 4) & 1) << 2)) << 4); }
DI void issue_half(const Ctx& C, int h, int k, unsigned vx, unsigned vw) {
  const unsigned slot = (h & 3) * 32768;
  if (k < 2) dma16(C.sX, k ? (vx ^ 64) + 8 * 1024 : vx, h * 128, slot + C.wid * 2048 + k * 1024);
  else dma16(C.sQKVw, vw + (k - 2) * 128, h * 64 * 3072, slot + 16384 + (k - 2) * 8192 + C.wid * 1024);
}
DI void warm(i32x4 srd, unsigned line0, unsigned lines_per_row_log2, unsigned pitch, unsigned colbyte, int lane) {
  const unsigned l = line0 + lane;
  dma4(srd, (l >> lines_per_row_log2) * pitch + colbyte + (l & ((1u << lines_per_row_log2) - 1)) * 128, L_JUNK);
}
DI unsigned g_voff(const Ctx& C) { const int lane = fresh(C.lane); return (32 * (C.wid & 1) + (lane >> 3)) * 1024 + C.cg * 128 + (((lane & 7) ^ (((lane >> 4) & 1) << 2)) << 4); }
constexpr unsigned gslot(int j) { return j == 0 ? R1 : j == 1 ? R2 : j == 2 ? R0 : j == 3 ? R3 : j == 4 ? R1 : j == 5 ? R2 : j == 6 ? R0 : j == 7 ? R3 : R1; }
DI void issue_g(const Ctx& C, int j, int i, unsigned v) {
  const int kq = C.wid >> 1;
  const int row0 = j < 4 ? (4 * kq + j) * 64 : j < 7 ? (16 + 3 * kq + (j - 4)) * 64 : 128 * kq + 64 * (j - 7);
  dma16(j < 7 ? C.sWD : C.sWO, v, (unsigned)(row0 + 8 * i) * 1024, gslot(j) + kq * 8192 + (C.wid & 1) * 4096 + i * 1024);
}

template <int TYPE> DI void epi_qkv(const Ctx& C, const f32x16& acc, const f32x4 (&csr)[4], float m, float rinv, int qu, int h4) {
  const int h = C.lane >> 5;
  const float sc = TYPE == 0 ? rinv * QSCALE : rinv;
  float y[16];
#pragma unroll
  for (int i4 = 0; i4 < 4; ++i4)
#pragma unroll
    for (int e = 0; e < 4; ++e) y[4 * i4 + e] = (acc[4 * i4 + e] - m * csr[i4][e]) * sc;
  const unsigned base = (unsigned)WS_QKV + (((C.rb * 24 + qu) * 4 + C.mg) * 4 + h4) * 1024;
#pragma unroll
  for (int hh = 0; hh < 2; ++hh) {
    u32x4 f; f[0] = cvtpk(y[8 * hh], y[8 * hh + 1]); f[1] = cvtpk(y[8 * hh + 2], y[8 * hh + 3]); f[2] = cvtpk(y[8 * hh + 4], y[8 * hh + 5]); f[3] = cvtpk(y[8 * hh + 6], y[8 * hh + 7]);
    unsigned voff = C.lane * 16;
    if (TYPE == 1 && qu >= 16) {
      auto a = __builtin_amdgcn_permlane32_swap(f[0], f[2], false, false); auto b = __builtin_amdgcn_permlane32_swap(f[1], f[3], false, false);
      f[0] = a[0]; f[2] = a[1]; f[1] = b[0]; f[3] = b[1];
      voff = (C.lane & 31) * 32 + h * 16;
    }
    st16_sc1(f, C.sWS, voff, base + hh * 1024);
  }
}
DI void epi_gu(const Ctx& C, const f32x16& acc, const f32x4 (&csr)[4], float m, float r2, unsigned a2off) {
  float y[8];
#pragma unroll
  for (int i4 = 0; i4 < 2; ++i4)
#pragma unroll
    for (int e = 0; e < 4; ++e) y[4 * i4 + e] = ((acc[4 * i4 + e] - m * csr[i4][e]) * r2) * (acc[8 + 4 * i4 + e] - m * csr[2 + i4][e]);
  u32x4 f; f[0] = cvtpk(y[0], y[1]); f[1] = cvtpk(y[2], y[3]); f[2] = cvtpk(y[4], y[5]); f[3] = cvtpk(y[6], y[7]);
  st16_sc1(f, C.sWS, C.lane * 16, a2off);
}
template <int TYPE> DI unsigned a2_pass(const Ctx& C, int p) {
  if (p == 1) return (unsigned)WS_A2 + (((4 * C.rb + C.mg) * 4 + (C.cs >> 2)) * 36 + 4 * (C.cs & 3) + 2 * C.ng) * 1024;
  if (TYPE == 0) return (unsigned)WS_A2 + (((4 * C.rb + C.mg) * 4 + C.cs / 3) * 36 + 4 * (4 + C.cs % 3) + 2 * C.ng) * 1024;
  const int u = 8 + ((C.cs - 8) >> 1), half = (C.cs - 8) & 1;
  return (unsigned)WS_A2 + (((4 * C.rb + C.mg) * 4 + u / 3) * 36 + 4 * (4 + u % 3) + 2 * half + C.ng) * 1024;
}
DI unsigned a2_o(const Ctx& C) { return (unsigned)WS_A2 + (((4 * C.rb + C.mg) * 4 + (C.cs >> 2)) * 36 + 28 + 2 * (C.cs & 3) + C.ng) * 1024; }

template <int TYPE> DI void gemm1(Ctx& C, unsigned vx, unsigned vw, u32x4& qfrag, u32x2& poll2, unsigned poll2off) {
  using G = G1<TYPE>;
  const int lane = C.lane;
  char* ws = C.ws;
  bf16x8 xa[32];
  bf16x8 wf[3][2];
  f32x16 acc[2];
  unsigned ta[2], sv = 0;
  ta[0] = tile_addr<TYPE>(C, 0, 0); ta[1] = tile_addr<TYPE>(C, 0, 1);
  const unsigned r31 = lane & 31, hh = lane >> 5;
  const unsigned xlane = (32 * C.mg + r31) * 128 + ((hh ^ ((r31 >> 1) & 7)) << 4);
  f32x4 ssum = {0, 0, 0, 0}, ssq = {0, 0, 0, 0};
  f32x4 csacc[2] = {{0,0,0,0},{0,0,0,0}};
  bf16x8 sel;
  { const short one = ((lane & 15) == ((lane >> 4) & 1)) ? (short)0x3f80 : (short)0;
#pragma unroll
    for (int e = 0; e < 8; ++e) sel[e] = one; }
  float csn[2];
  f32x4 csr[2][4];
  float m = 0.f, rinv = 0.f;
  u32x2 pollv; u32x4 gk[3];
  unsigned polloff = 0; bool pollneed = false;

  STAMP(1);
  bar_vm<G::gsize(T_H0)>();
  STAMP(2);
  u32x2 ev; ld8_sc1(ev, C.sWS, (unsigned)WS_EPOCH);
  auto read_x = [&](int h) {
    const unsigned base = (h & 3) * 32768 + xlane;
#pragma unroll
    for (int i = 0; i < 4; ++i) xa[4 * h + i] = ld128(base ^ (((i + C.c) & 3) << 5));
  };
  auto read_w = [&](int pos) {
    const int p = pos >> 5, s = pos >> 3, i = pos & 7;
    const bool half = G::kind(p) == 3;
    if ((pos & 31) == 0 && pos > 0) { ta[0] = tile_addr<TYPE>(C, p, 0); ta[1] = tile_addr<TYPE>(C, p, 1); }
    const unsigned stride = half ? 1024 : 2048, second = half ? 256 : 512;
    const unsigned so = (p == 0 ? ((pos >> 2) & 3) * 32768 : G::slot(s) + (i & 4) * stride) + ((i + C.c) & 3) * stride;
    wf[pos % 3][0] = ldfrag_tr(pinv(ta[0] + so), second);
    if (G::nt(p) == 2) wf[pos % 3][1] = ldfrag_tr(pinv(ta[1] + so), second);
  };
  read_x(0); read_w(0); read_w(1);
  static_for<0, 96>([&](auto ic) {
    constexpr int pos = decltype(ic)::value;
    constexpr int p = pos >> 5, s = pos >> 3, st = s & 3, i = pos & 7, NT = G::nt(p), bi = G::barat(pos);
    if constexpr (pos < 2) static_for<0, G::gsize(T_H0)>([&](auto kc) { issue_half(C, 2 + pos, decltype(kc)::value, vx, vw); });
    if constexpr (bi >= 0) {
      STAMP(10 + 2 * bi);
      if constexpr (bi == 0) { bar_vm<G::after(T_H0 + 1, 0)>(); }
      else if constexpr (bi < 7) bar_vm<G::after(T_H0 + bi + 1, bi)>();
      else if constexpr (bi == 9) { bar_vm<G::mn(G::after(T_S4 + 2, 9), G::mn(G::after(T_E0, 9), G::after(T_POISON, 9)))>(); publish(C, 0); }
      else if constexpr (bi == 13) { bar_vm<G::mn(G::mn(G::after(T_S4 + 6, 13), G::after(T_E0 + 1, 13)), G::after(T_POLL, 13))>(); }
      else if constexpr (bi == 14) bar_vm<G::mn(G::after(T_S4 + 7, 14), G::after(T_KL, 14))>();
      else if constexpr (bi == 15) bar_vm<G::after(T_VL, 15)>();
      else bar_vm<G::after(T_S4 + bi - 7, bi)>();
      if constexpr (bi == 4) {
        static_assert(G::after(T_H0 + 5, 4) <= G::after(T_EPOCH, 4), "");
        pin(ev);
        const u64 t = (((u64)ev[1] << 32) | ev[0]) * 6364136223846793005ull + 1442695040888963407ull;
        C.tag = ((u64)__builtin_amdgcn_readfirstlane((unsigned)(t >> 32)) << 32) | __builtin_amdgcn_readfirstlane((unsigned)t);
      }
      if constexpr (bi == 12) {
        const int u = C.cs >> 1, l = lane & 7;
        const int ty = l == 0 ? 0 : l < 4 ? 1 : 2, rbp = l == 0 ? C.rb : C.rb - 2 + (l - 1) % 3;
        const int qu = 8 * ty + u;
        const int pcs = qu < 8 ? qu : 8 + ((qu - 8) >> 1);
        pollneed = l < 7 && rbp >= 0;
        polloff = flag_off(0, pollneed ? rbp : C.rb, pcs);
        ld8_sc1(pollv, C.sWS, polloff);
      }
      if constexpr (bi == 13) {
        check_flags(ws, polloff, pollneed, C.tag, pollv);
        const int u = C.cs >> 1, h4 = 2 * (C.cs & 1) + C.ng;
#pragma unroll
        for (int b = 0; b < 3; ++b) { const int rbp = C.rb - 2 + b < 0 ? 0 : C.rb - 2 + b;
          ld16_sc1(gk[b], C.sWS, lane * 16, (unsigned)WS_QKV + (((rbp * 24 + 8 + u) * 4 + C.mg) * 4 + h4) * 1024); }
        publish(C, 1);
      }
      if constexpr (bi == 14) {
        pin(gk[0]); pin(gk[1]); pin(gk[2]);
#pragma unroll
        for (int b = 0; b < 3; ++b) *lp<u32x4>(L_K + ((4 * b + C.mg) * 2 + C.ng) * 1024 + lane * 16) = gk[b];
        const int u = C.cs >> 1, h4 = 2 * (C.cs & 1) + C.ng;
#pragma unroll
        for (int b = 0; b < 3; ++b) { const int rbp = C.rb - 2 + b < 0 ? 0 : C.rb - 2 + b;
          ld16_sc1(gk[b], C.sWS, lane * 16, (unsigned)WS_QKV + (((rbp * 24 + 16 + u) * 4 + C.mg) * 4 + h4) * 1024); }
        ld16_sc1(qfrag, C.sWS, lane * 16, (unsigned)WS_QKV + (((C.rb * 24 + u) * 4 + C.mg) * 4 + h4) * 1024);
      }
      if constexpr (bi == 15) {
        pin(qfrag); pin(gk[0]); pin(gk[1]); pin(gk[2]);
#pragma unroll
        for (int b = 0; b < 3; ++b) *lp<u32x4>(L_V + C.ng * 12288 + (4 * b + C.mg) * 1024 + lane * 16) = gk[b];
        ld8_sc1(poll2, C.sWS, poll2off);
      }
      if constexpr (bi < 7) read_x(bi + 1);
      if constexpr (st == 3 && i == 6) {
#pragma unroll
        for (int t = 0; t < NT; ++t) { const unsigned a = L_CSP + (C.ng * 2 + t) * 512 + r31 * 4;
          csn[t] = (*lp<float>(a) + *lp<float>(a + 128)) + (*lp<float>(a + 256) + *lp<float>(a + 384)); }
      }
      STAMP(11 + 2 * bi);
    }
    static_for<0, 16>([&](auto bc) {
      constexpr int bb = decltype(bc)::value, tg = G::grp(bb), k0 = pos - G::barpos(bb);
      static_for<0, 4>([&](auto kc) {
        constexpr int k = decltype(kc)::value;
        if constexpr (k < G::gsize(tg) && G::rel(bb, k) == k0) {
          if constexpr (tg < T_S4) issue_half(C, tg, k, vx, vw);
          else if constexpr (tg >= T_G0) { if constexpr (k == 0) sv = g_voff(C); issue_g(C, tg - T_G0, k, sv); }
          else { if constexpr (k == 0) sv = stage_voff<TYPE>(C, (tg - T_S4 + 4) >> 2); issue_stage<TYPE>(C, tg - T_S4 + 4, k, sv); }
        }
      });
    });
    if constexpr (pos == 12 && !NO_WARM) {
      const int gu = TYPE == 0 ? 16 + C.cs : 24 + ((C.cs - 8) >> 1);
      const unsigned l0 = (C.rb & 7) * 64;
      if (C.wid == 5) { warm(C.sG, l0, 0, 3584, gu * 128, fresh(lane)); warm(C.sU, l0, 0, 3584, gu * 128, fresh(lane)); }
    }
    if constexpr (pos >= 34 && pos < 52 && (pos & 1) == 0 && !NO_WARM) {
      constexpr int k = (pos - 34) >> 1;
      const unsigned l = ((C.xi * 9 + k) * 64);
      if (C.wid == (k & 7)) { if (l < 4096) warm(C.sWO, l, 3, 1024, 0, fresh(lane)); else warm(C.sWD, l - 4096, 3, 1024, 0, fresh(lane)); }
    }
    if constexpr (pos == 16) {
      const u32x4 pz = {POISON, POISON, POISON, POISON};
      const unsigned a = a2_pass<TYPE>(C, 2);
      st16_sc1(pz, C.sWS, lane * 16, a);
      if (TYPE == 0) st16_sc1(pz, C.sWS, lane * 16, a + 1024);
      st16_sc1(pz, C.sWS, lane * 16, a2_o(C));
    }
    if constexpr (pos + 2 < 96) read_w(pos + 2);
    if constexpr ((pos & 31) == 0) { acc[0] = MFMA32(wf[pos % 3][0], xa[pos & 31], f32x16{}); if (NT == 2) acc[1] = MFMA32(wf[pos % 3][1], xa[pos & 31], f32x16{}); }
    else { acc[0] = MFMA32(wf[pos % 3][0], xa[pos & 31], acc[0]); if (NT == 2) acc[1] = MFMA32(wf[pos % 3][1], xa[pos & 31], acc[1]); }
    if constexpr ((i & 3) == 0 && !NO_CS) {
      if constexpr ((pos & 31) == 0) { csacc[0] = MFMA16(wf[pos % 3][0], sel, (f32x4{0, 0, 0, 0})); if (NT == 2) csacc[1] = MFMA16(wf[pos % 3][1], sel, (f32x4{0, 0, 0, 0})); }
      else { csacc[0] = MFMA16(wf[pos % 3][0], sel, csacc[0]); if (NT == 2) csacc[1] = MFMA16(wf[pos % 3][1], sel, csacc[1]); }
    }
    if constexpr (p == 0 && (i & 3) < 2) {
      const bf16x8 xv = xa[pos & 31]; const s16x4 lo = __builtin_shufflevector(xv, xv, 0, 1, 2, 3), hi = __builtin_shufflevector(xv, xv, 4, 5, 6, 7);
      const s16x4 ones = {0x3f80, 0x3f80, 0x3f80, 0x3f80};
      ssum = MFMA4(ones, lo, ssum); ssq = MFMA4(lo, lo, ssq); ssum = MFMA4(ones, hi, ssum); ssq = MFMA4(hi, hi, ssq);
    }
    if constexpr (st == 3 && i == 5) {
      if ((lane & 15) < 2) {
#pragma unroll
        for (int t = 0; t < NT; ++t) *lp<f32x4>(L_CSP + ((C.ng * 2 + t) * 4 + C.mg) * 128 + (16 * (lane & 15) + 4 * (lane >> 4)) * 4) = csacc[t];
      }
      if constexpr (p == 0) {
        const float sq = (lane & 3) == 0 ? ssq[0] : (lane & 3) == 1 ? ssq[1] : (lane & 3) == 2 ? ssq[2] : ssq[3];
        const f32x2 v = {swap_add(ssum[0]), swap_add(sq)};
        if (lane < 32) *lp<f32x2>(L_STAT + (C.ng * 128 + 32 * C.mg + lane) * 8) = v;
      }
    }
    if constexpr (st == 3 && i == 6) {
#pragma unroll
      for (int t = 0; t < NT; ++t) *lp<float>(L_CSW + (C.wid * 2 + t) * 128 + r31 * 4) = csn[t];
    }
    if constexpr (st == 3 && i == 7) {
#pragma unroll
      for (int t = 0; t < NT; ++t)
#pragma unroll
        for (int i4 = 0; i4 < 4; ++i4) csr[t][i4] = *lp<f32x4>(L_CSW + (C.wid * 2 + t) * 128 + (8 * i4 + 4 * hh) * 4);
    }
    SB();
    if constexpr ((pos & 31) == 31) {
      if constexpr (p == 0) {
        const f32x2 a = *lp<f32x2>(L_STAT + (32 * C.mg + r31) * 8), b = *lp<f32x2>(L_STAT + (128 + 32 * C.mg + r31) * 8);
        const float mean = (a[0] + b[0]) * (1.f / 512.f);
        const float var = __builtin_fmaxf((a[1] + b[1]) * (1.f / 512.f) - mean * mean, 0.f);
        m = rbf(mean);
        rinv = __builtin_amdgcn_rcpf(rbf(__builtin_amdgcn_sqrtf(rbf(rbf(var) + 1e-5f))));
      }
      if constexpr (G::kind(p) == 2) {
        const unsigned a2 = a2_pass<TYPE>(C, p);
        epi_gu(C, acc[0], csr[0], m, rinv * rinv, a2); epi_gu(C, acc[1], csr[1], m, rinv * rinv, a2 + 1024);
      } else if constexpr (G::kind(p) == 3) {
        epi_gu(C, acc[0], csr[0], m, rinv * rinv, a2_pass<TYPE>(C, p));
      } else if constexpr (G::kind(p) == 0) {
        epi_qkv<0>(C, acc[0], csr[0], m, rinv, C.cs, 2 * C.ng);
      } else {
        const int qu = 8 + 2 * (C.cs - 8) + C.ng;
        epi_qkv<1>(C, acc[0], csr[0], m, rinv, qu, 0); epi_qkv<1>(C, acc[1], csr[1], m, rinv, qu, 2);
      }
      SB();
    }
  });
  wait_vm<G::after(T_POLL2, 16)>();
}

template <bool EDGE, class SIDE> DI u32x4 attention(Ctx& C, const u32x4& qraw, SIDE&& side) {
  const int lane = C.lane, h = lane >> 5, g = (lane >> 4) & 1, q = (lane & 15) >> 2, pp = lane & 3;
  const bf16x8 qf = __builtin_bit_cast(bf16x8, qraw);
  const unsigned kaddr = pinv(L_K + (C.mg * 2 + C.ng) * 1024 + lane * 16);
  const int t0 = EDGE ? (C.rb == 0 ? 8 : 4) - C.mg : 1;
  const int dq = (lane & 31) - 4 * h;
  const float NEG = -1e30f;
  auto older = [&](int r) { return ((r & 3) + 8 * (r >> 2)) > dq; };
  auto max16 = [&](float& a, float& b, const f32x16& s) {
#pragma unroll
    for (int r = 0; r < 16; r += 4) { a = __builtin_fmaxf(__builtin_fmaxf(a, s[r]), s[r + 1]); b = __builtin_fmaxf(__builtin_fmaxf(b, s[r + 2]), s[r + 3]); } };
  bf16x8 kf[9];
#pragma unroll
  for (int t = 0; t < 9; ++t) if (!EDGE || t > 0) kf[t] = ld128(kaddr + t * 2048);
  float mx;
  {
    f32x16 S[3];
    f32x16 sl = MFMA32(kf[8], qf, f32x16{});
    f32x16 sf;
    if (!EDGE) sf = MFMA32(kf[0], qf, f32x16{});
    S[1] = MFMA32(kf[1], qf, f32x16{});
    S[2] = MFMA32(kf[2], qf, f32x16{});
    float a = NEG, b = NEG;
    side(std::integral_constant<int, 0>{});
    SB();
#pragma unroll
    for (int r = 0; r < 16; ++r) sl[r] = older(r) ? (EDGE ? NEG : sf[r]) : sl[r];
    max16(a, b, sl);
    static_for<1, 8>([&](auto tc) { constexpr int t = decltype(tc)::value;
      if constexpr (t + 2 < 8) S[(t + 2) % 3] = MFMA32(kf[t + 2], qf, f32x16{});
      side(std::integral_constant<int, t>{});
      SB();
      if (!EDGE || t >= t0) max16(a, b, S[t % 3]);
      SB(); });
    mx = swap_max(__builtin_fmaxf(a, b));
  }
  f32x16 negm;
#pragma unroll
  for (int r = 0; r < 16; ++r) negm[r] = -mx;
  const unsigned vbase = pinv(g == 0 ? L_V + C.ng * 12288 + C.mg * 1024 + (4 * h + q) * 32 + pp * 8 : L_ONES + 128 * (1 - h) + q * 32 + pp * 8);
  const unsigned vstr = g == 0 ? 1024u : 0u;
  f32x16 o;
  auto expack = [&](const f32x16& s, u32x4& pa, u32x4& pb) {
#pragma unroll
    for (int i = 0; i < 4; ++i) pa[i] = cvtpk(__builtin_amdgcn_exp2f(s[2 * i]), __builtin_amdgcn_exp2f(s[2 * i + 1]));
#pragma unroll
    for (int i = 0; i < 4; ++i) pb[i] = cvtpk(__builtin_amdgcn_exp2f(s[8 + 2 * i]), __builtin_amdgcn_exp2f(s[9 + 2 * i])); };
  f32x16 S[2];
  bf16x8 vf[2][2];
  {
    f32x16 sl = MFMA32(kf[8], qf, negm);
    f32x16 sf;
    if (!EDGE) sf = MFMA32(kf[0], qf, negm);
    S[1] = MFMA32(kf[1], qf, negm);
    const unsigned v8 = vbase + 8 * vstr;
    const bf16x8 va = ldfrag_tr(v8, 256), vb = ldfrag_tr(v8 + 512, 256);
    bf16x8 vc, vd;
    if (!EDGE) { vc = ldfrag_tr(vbase, 256); vd = ldfrag_tr(vbase + 512, 256); }
    vf[1][0] = ldfrag_tr(vbase + vstr, 256); vf[1][1] = ldfrag_tr(vbase + vstr + 512, 256);
    side(std::integral_constant<int, 8>{});
    SB();
#pragma unroll
    for (int r = 0; r < 16; ++r) sl[r] = older(r) ? (EDGE ? NEG : sf[r]) : sl[r];
    u32x4 pa, pb, mk0, mk1;
    expack(sl, pa, pb);
#pragma unroll
    for (int i = 0; i < 4; ++i) {
      mk0[i] = (older(2 * i) ? 0xffffu : 0u) | (older(2 * i + 1) ? 0xffff0000u : 0u);
      mk1[i] = (older(8 + 2 * i) ? 0xffffu : 0u) | (older(9 + 2 * i) ? 0xffff0000u : 0u); }
    o = MFMA32(va, __builtin_bit_cast(bf16x8, pa & ~mk0), f32x16{});
    o = MFMA32(vb, __builtin_bit_cast(bf16x8, pb & ~mk1), o);
    if (!EDGE) {
      o = MFMA32(vc, __builtin_bit_cast(bf16x8, pa & mk0), o);
      o = MFMA32(vd, __builtin_bit_cast(bf16x8, pb & mk1), o);
    }
    SB();
  }
  static_for<1, 8>([&](auto tc) { constexpr int t = decltype(tc)::value;
    if constexpr (t + 1 < 8) {
      const unsigned vt = vbase + (t + 1) * vstr;
      vf[(t + 1) & 1][0] = ldfrag_tr(vt, 256); vf[(t + 1) & 1][1] = ldfrag_tr(vt + 512, 256);
      S[(t + 1) & 1] = MFMA32(kf[t + 1], qf, negm); }
    side(std::integral_constant<int, 8 + t>{});
    SB();
    if (!EDGE || t >= t0) {
      u32x4 pa, pb;
      expack(S[t & 1], pa, pb);
      o = MFMA32(vf[t & 1][0], __builtin_bit_cast(bf16x8, pa), o);
      o = MFMA32(vf[t & 1][1], __builtin_bit_cast(bf16x8, pb), o);
    }
    SB(); });
  const float li = __builtin_amdgcn_rcpf(o[8]);
  u32x4 f; f[0] = cvtpk(o[0] * li, o[1] * li); f[1] = cvtpk(o[2] * li, o[3] * li); f[2] = cvtpk(o[4] * li, o[5] * li); f[3] = cvtpk(o[6] * li, o[7] * li);
  return f;
}

__global__ void __launch_bounds__(512, 2)
fused_kernel(const u16* __restrict__ x, const u16* __restrict__ wqkv, const u16* __restrict__ wo, const u16* __restrict__ wg, const u16* __restrict__ wu,
             const u16* __restrict__ wd, u16* __restrict__ out, char* __restrict__ ws) {
  Ctx C;
  const int tid = threadIdx.x, lane = tid & 63, wid = __builtin_amdgcn_readfirstlane(tid >> 6);
  const int b = blockIdx.x, xj = b & 7, xi = b >> 3;
  C.stampreg = 0;
  STAMP(0);
  asm volatile("" :: "s"(x), "s"(wqkv), "s"(wo), "s"(wg), "s"(wu), "s"(wd), "s"(out), "s"(ws));
  C.ws = ws; C.lane = lane; C.wid = wid; C.mg = wid & 3; C.ng = wid >> 2;
  C.rb = 8 * (xj & 1) + (xi & 7); C.cs = 4 * (xj >> 1) + (xi >> 3); C.c = (C.mg + 2 * C.ng) & 3;
  C.sX = mk_srd(x + (size_t)C.rb * 128 * 512, 128 * 1024);
  C.sQKVw = mk_srd(wqkv, 512 * 3072);
  C.xi = xi;
  const unsigned vx = hx_voff(C), vw = hw_voff(C);
  STAMP(3);
  issue_half(C, 0, 0, vx, vw); issue_half(C, 0, 1, vx, vw); issue_half(C, 0, 2, vx, vw); if (C.cs >= 8) issue_half(C, 0, 3, vx, vw);
  STAMP(4);
  issue_half(C, 1, 0, vx, vw); issue_half(C, 1, 1, vx, vw); issue_half(C, 1, 2, vx, vw); if (C.cs >= 8) issue_half(C, 1, 3, vx, vw);
  STAMP(5);
  C.sWS = mk_srd(ws, 32u << 20);
  C.sG = mk_srd(wg, 512 * 3584); C.sU = mk_srd(wu, 512 * 3584); C.sWO = mk_srd(wo, 512 * 1024); C.sWD = mk_srd(wd, 1792 * 1024);
  if (!NO_WARM) {
    if (wid < 3) { const unsigned l = (C.cs & 3) * 192 + wid * 64 + lane; dma4(C.sX, (l / 6) * 1024 + (2 + l % 6) * 128, L_JUNK); }
    else if (wid < 5) { if (wid == 3 || C.cs >= 8) dma4(C.sQKVw, (128 + (C.rb & 7) * 48 + (lane < 48 ? lane : 0)) * 3072 + ((C.cs < 8 ? C.cs : 8 + 2 * (C.cs - 8)) + (wid - 3)) * 128, L_JUNK); }
    else if (wid < 7) dma4(wid == 5 ? C.sG : C.sU, ((C.rb & 7) * 64 + lane) * 3584 + C.cs * 128, L_JUNK);
    else { u64 pc; asm volatile("s_getpc_b64 %0" : "=s"(pc));
      dma4(mk_srd((const void*)(pc & ~255ull), CODE_BYTES), (xi * 9 + (lane < 9 ? lane : 0)) * 128, L_JUNK); }
  }
  if (tid < 256) lp<unsigned>(L_ONES)[tid] = 0x3f803f80u;
  const int rt64 = 4 * xj + (xi & 3), cg = xi >> 2;
  C.cg = cg;
  const int rt = wid & 1, kq = wid >> 1, RT = 2 * rt64 + rt, rb2 = RT >> 2;
  const bool p2need = lane < 10;
  unsigned p2off;
  { const int l = lane < 10 ? lane : 0, u = 3 * kq + ((l - 4) >> 1);
    p2off = l < 4 ? flag_off(1, rb2, 4 * kq + l) : flag_off(0, rb2, u < 8 ? u : 8 + 2 * (u - 8) + (l & 1)); }
  u32x4 qfrag; u32x2 poll2;
  if (C.cs < 8) gemm1<0>(C, vx, vw, qfrag, poll2, p2off); else gemm1<1>(C, vx, vw, qfrag, poll2, p2off);
  STAMP(6);

  const unsigned a2base = (unsigned)WS_A2 + (RT * 4 + kq) * 36 * 1024;
  u32x4 a2[36];
  check_flags(ws, p2off, p2need, C.tag, poll2);
  asm volatile("s_waitcnt lgkmcnt(0)\n\ts_barrier" ::: "memory");
  auto side = [&](auto kc) { constexpr int k = decltype(kc)::value;
    if constexpr (k < 8) { ld16_sc1(a2[2 * k], C.sWS, lane * 16, a2base + 2 * k * 1024); ld16_sc1(a2[2 * k + 1], C.sWS, lane * 16, a2base + (2 * k + 1) * 1024); }
    if constexpr (k >= 12) static_for<0, 3>([&](auto jc) { constexpr int q_ = 16 + 3 * (k - 12) + decltype(jc)::value; ld16_sc1(a2[q_], C.sWS, lane * 16, a2base + q_ * 1024); }); };
  u32x4 of;
  if (C.rb >= 2) of = attention<false>(C, qfrag, side); else of = attention<true>(C, qfrag, side);
  st16_sc1(of, C.sWS, lane * 16, a2_o(C));
  STAMP(7);

  const int g = (lane >> 4) & 1, h = lane >> 5, q = (lane & 15) >> 2, pp = lane & 3;
  const unsigned w2l0 = kq * 8192 + (4 * h + q) * 128 + ((0 ^ (q >> 1)) << 6) + g * 32 + pp * 8;
  const unsigned w2l1 = kq * 8192 + (4 * h + q) * 128 + ((1 ^ (q >> 1)) << 6) + g * 32 + pp * 8;
  bf16x8 wf[3][2];
  f32x16 acc[2];
  u32x4 xres;
  unsigned wb[2], gv;
  auto read_w2 = [&](int pos) {
    if ((pos & 3) == 0) { wb[0] = pinv(w2l0 + gslot(pos >> 2)); wb[1] = pinv(w2l1 + gslot(pos >> 2)); }
    wf[pos % 3][0] = ldfrag_tr(wb[0] + (pos & 3) * 2048, 1024); wf[pos % 3][1] = ldfrag_tr(wb[1] + (pos & 3) * 2048, 1024); };
  bar_vm<13>();
  static_for<0, 16>([&](auto kc) { constexpr int k = decltype(kc)::value; pin(a2[k]); });
  read_w2(0); read_w2(1);
  gv = g_voff(C);
  STAMP(8);
  u32x4 ef[4];
  static_for<0, 36>([&](auto ic) {
    constexpr int pos = decltype(ic)::value;
    constexpr int j = pos >> 2, i = pos & 3;
    if constexpr (i == 2) {
      STAMP(44 + 2 * j);
      if constexpr (j <= 1) bar_vm<63>();
      if constexpr (j == 2) bar_vm<6>();
      if constexpr (j == 3) bar_vm<6>();
      if constexpr (j == 4) bar_vm<7>();
      if constexpr (j == 5) bar_vm<15>();
      if constexpr (j == 6) bar_vm<4>();
      if constexpr (j == 7) bar_vm<0>();
      STAMP(45 + 2 * j);
    }
    if constexpr (pos == 14) { static_for<16, 28>([&](auto kc) { pin(a2[decltype(kc)::value]); }); check_frags<16, 28>(C, a2, a2base); }
    if constexpr (pos == 26) { static_for<28, 36>([&](auto kc) { pin(a2[decltype(kc)::value]); }); pin(xres); check_frags<28, 36>(C, a2, a2base); }
    if constexpr (pos == 16) { const i32x4 sXall = mk_srd(x, 2048 * 1024); ld16(xres, sXall, (RT * 32 + (lane & 31)) * 1024 + cg * 128 + kq * 32 + h * 16, 0); }
    if constexpr (pos == 18 || pos == 19) static_for<0, 4>([&](auto kc) { constexpr int k = 28 + 4 * (pos - 18) + decltype(kc)::value; ld16_sc1(a2[k], C.sWS, lane * 16, a2base + k * 1024); });
    if constexpr (pos < 24) issue_g(C, 3 + (pos >> 2), pos & 3, gv);
    if constexpr (pos == 30) { if (b == 0 && wid == 7) {
#pragma unroll
        for (int k = 0; k < 4; ++k) { u32x2 t; ld8_sc1(t, C.sWS, flag_off(0, 0, 0) + (lane * 4 + k) * 128); ef[k][0] = t[0]; ef[k][1] = t[1]; } } }
    if constexpr (pos + 2 < 36) read_w2(pos + 2);
    const bf16x8 af = __builtin_bit_cast(bf16x8, a2[pos]);
    if constexpr (pos == 0) { acc[0] = MFMA32(wf[0][0], af, f32x16{}); acc[1] = MFMA32(wf[0][1], af, f32x16{}); }
    else { acc[0] = MFMA32(wf[pos % 3][0], af, acc[0]); acc[1] = MFMA32(wf[pos % 3][1], af, acc[1]); }
    SB();
  });
  STAMP(9);
#pragma unroll
  for (int d = 0; d < 4; ++d) if (d != kq) {
    const int ct = d >> 1, r0 = (d & 1) * 8;
    const unsigned a = R2 + ((rt * 4 + d) * 4 + kq) * 2048 + lane * 16;
    *lp<f32x4>(a) = f32x4{acc[ct][r0], acc[ct][r0 + 1], acc[ct][r0 + 2], acc[ct][r0 + 3]};
    *lp<f32x4>(a + 1024) = f32x4{acc[ct][r0 + 4], acc[ct][r0 + 5], acc[ct][r0 + 6], acc[ct][r0 + 7]};
  }
  float s[8];
#pragma unroll
  for (int e = 0; e < 8; ++e) { const float lo = (kq & 1) ? acc[0][8 + e] : acc[0][e], hi = (kq & 1) ? acc[1][8 + e] : acc[1][e]; s[e] = (kq & 2) ? hi : lo; }
  asm volatile("s_waitcnt lgkmcnt(0)\n\ts_barrier" ::: "memory");
#pragma unroll
  for (int d = 0; d < 4; ++d) if (d != kq) {
    const unsigned a = R2 + ((rt * 4 + kq) * 4 + d) * 2048 + lane * 16;
    const f32x4 u = *lp<f32x4>(a), v = *lp<f32x4>(a + 1024);
#pragma unroll
    for (int e = 0; e < 4; ++e) { s[e] += u[e]; s[4 + e] += v[e]; }
  }
  {
    unsigned u0 = __float_as_uint(s[0]), u1 = __float_as_uint(s[1]), u2 = __float_as_uint(s[2]), u3 = __float_as_uint(s[3]);
    unsigned v0 = __float_as_uint(s[4]), v1 = __float_as_uint(s[5]), v2 = __float_as_uint(s[6]), v3 = __float_as_uint(s[7]);
    auto r0 = __builtin_amdgcn_permlane32_swap(u0, v0, false, false); auto r1 = __builtin_amdgcn_permlane32_swap(u1, v1, false, false);
    auto r2 = __builtin_amdgcn_permlane32_swap(u2, v2, false, false); auto r3 = __builtin_amdgcn_permlane32_swap(u3, v3, false, false);
    const float c0 = __uint_as_float(r0[0]), c1 = __uint_as_float(r1[0]), c2 = __uint_as_float(r2[0]), c3 = __uint_as_float(r3[0]);
    const float c4 = __uint_as_float(r0[1]), c5 = __uint_as_float(r1[1]), c6 = __uint_as_float(r2[1]), c7 = __uint_as_float(r3[1]);
    u32x4 f;
    f[0] = cvtpk(c0 + bflo(xres[0]), c1 + bfhi(xres[0])); f[1] = cvtpk(c2 + bflo(xres[1]), c3 + bfhi(xres[1]));
    f[2] = cvtpk(c4 + bflo(xres[2]), c5 + bfhi(xres[2])); f[3] = cvtpk(c6 + bflo(xres[3]), c7 + bfhi(xres[3]));
    const i32x4 sO = mk_srd(out, 2048 * 1024);
    st16(f, sO, (RT * 32 + (lane & 31)) * 1024 + cg * 128 + kq * 32 + h * 16, 0);
  }
  if (b == 0 && wid == 7) {
    wait_vm<0>();
    asm volatile("" : "+v"(ef[0]), "+v"(ef[1]), "+v"(ef[2]), "+v"(ef[3]));
    bool ok = true;
#pragma unroll
    for (int k = 0; k < 4; ++k) ok = ok && ef[k][0] == (unsigned)C.tag && ef[k][1] == (unsigned)(C.tag >> 32);
    if (!__all(ok)) {
#pragma unroll 1
      for (int k = 0; k < 4; ++k) spin_flags(ws, flag_off(0, 0, 0) + (lane * 4 + k) * 128, true, C.tag);
    }
    if (lane == 0) __hip_atomic_store((u64*)(ws + WS_EPOCH), C.tag, __ATOMIC_RELAXED, __HIP_MEMORY_SCOPE_AGENT);
  }
  STAMP(60);
}

extern "C" void kernel_launch(void* const* d_in, const int* in_sizes, int n_in, void* d_out, int out_size, void* d_ws, size_t ws_size, hipStream_t stream) {
  (void)in_sizes; (void)n_in; (void)out_size; (void)ws_size;
  (void)hipFuncSetAttribute((const void*)fused_kernel, hipFuncAttributeMaxDynamicSharedMemorySize, LDS_BYTES);
  fused_kernel<<<256, 512, LDS_BYTES, stream>>>((const u16*)d_in[0], (const u16*)d_in[1], (const u16*)d_in[2], (const u16*)d_in[4], (const u16*)d_in[5], (const u16*)d_in[6],
                                                 (u16*)d_out, (char*)d_ws);
}
```

```cpp
#include <hip/hip_runtime.h>
#include <stdint.h>
#include <type_traits>

typedef unsigned short u16;
typedef unsigned long long u64;
typedef short bf16x8 __attribute__((ext_vector_type(8)));
typedef short s16x4 __attribute__((ext_vector_type(4)));
typedef float f32x2 __attribute__((ext_vector_type(2)));
typedef float f32x4 __attribute__((ext_vector_type(4)));
typedef float f32x16 __attribute__((ext_vector_type(16)));
typedef unsigned u32x2 __attribute__((ext_vector_type(2)));
typedef unsigned u32x4 __attribute__((ext_vector_type(4)));
typedef int i32x4 __attribute__((ext_vector_type(4)));
#define LDSP __attribute__((address_space(3)))
#define DI __device__ __forceinline__
template <class T> DI LDSP T* lp(unsigned a) { return (LDSP T*)(size_t)a; }
template <int I, int N, class F> DI void static_for(F&& f) { if constexpr (I < N) { f(std::integral_constant<int, I>{}); static_for<I + 1, N>(f); } }

constexpr size_t WS_EPOCH = 0;
constexpr size_t WS_ERR   = 64;
constexpr size_t WS_FLAGS = 4096;
constexpr size_t WS_QKV   = 1u << 20;
constexpr size_t WS_A2    = 8u << 20;
constexpr size_t WS_DBG   = 24u << 20;

constexpr unsigned R0 = 0, R1 = 32768, R2 = 65536, R3 = 98304;
constexpr unsigned L_K = 98304, L_V = 122880;
constexpr unsigned L_ONES = 147456;
constexpr unsigned L_CSP = 148480;
constexpr unsigned L_CSW = 150528;
constexpr unsigned L_STAT = 152576;
constexpr unsigned L_JUNK = 154624;
constexpr unsigned LDS_BYTES = 163840;
constexpr unsigned CODE_BYTES = 36864;
constexpr unsigned POISON = 0xffffffffu;
constexpr unsigned SPIN_CAP = 1u << 13;

#define NO_WARM 0
#define NO_CS 0
#define NO_DMA 0
constexpr float QSCALE = 0.25f * 1.4426950408889634f;

DI i32x4 mk_srd(const void* p, unsigned bytes) { u64 a = (u64)p; i32x4 r; r[0] = (int)(unsigned)a; r[1] = (int)((unsigned)(a >> 32) & 0xffffu); r[2] = (int)bytes; r[3] = 0x00020000; return r; }
DI void dma16(i32x4 srd, unsigned voff, unsigned soff, unsigned lds) {
  if (NO_DMA) return;
  asm volatile("s_mov_b32 m0, %3\n\ts_nop 0\n\tbuffer_load_dwordx4 %0, %1, %2 offen lds" :: "v"(voff), "s"(srd), "s"(soff), "s"(lds) : "memory");
}
DI void dma4(i32x4 srd, unsigned voff, unsigned lds) {
  asm volatile("s_mov_b32 m0, %2\n\ts_nop 0\n\tbuffer_load_dword %0, %1, 0 offen lds" :: "v"(voff), "s"(srd), "s"(lds) : "memory");
}
DI void ld16_sc1(u32x4& d, i32x4 srd, unsigned voff, unsigned soff) {
  asm volatile("buffer_load_dwordx4 %0, %1, %2, %3 offen sc1" : "=v"(d) : "v"(voff), "s"(srd), "s"(soff) : "memory");
}
DI void ld16(u32x4& d, i32x4 srd, unsigned voff, unsigned soff) {
  asm volatile("buffer_load_dwordx4 %0, %1, %2, %3 offen" : "=v"(d) : "v"(voff), "s"(srd), "s"(soff) : "memory");
}
DI void ld8_sc1(u32x2& d, i32x4 srd, unsigned voff) {
  asm volatile("buffer_load_dwordx2 %0, %1, %2, 0 offen sc1" : "=v"(d) : "v"(voff), "s"(srd) : "memory");
}
DI void st16_sc1(u32x4 d, i32x4 srd, unsigned voff, unsigned soff) {
  asm volatile("buffer_store_dwordx4 %0, %1, %2, %3 offen sc1\n\ts_nop 1" :: "v"(d), "v"(voff), "s"(srd), "s"(soff) : "memory");
}
DI void st16(u32x4 d, i32x4 srd, unsigned voff, unsigned soff) {
  asm volatile("buffer_store_dwordx4 %0, %1, %2, %3 offen\n\ts_nop 1" :: "v"(d), "v"(voff), "s"(srd), "s"(soff) : "memory");
}
template <int N> DI void bar_vm() { static_assert(N >= 0 && N < 64, "vmcnt range"); asm volatile("s_waitcnt vmcnt(%0) lgkmcnt(0)\n\ts_barrier" :: "n"(N) : "memory"); }
template <int N> DI void wait_vm() { static_assert(N >= 0 && N < 64, "vmcnt range"); asm volatile("s_waitcnt vmcnt(%0)" :: "n"(N) : "memory"); }
DI s16x4 ldtr(unsigned a) { return __builtin_amdgcn_ds_read_tr16_b64_v4i16(lp<s16x4>(a)); }
DI bf16x8 ldfrag_tr(unsigned a, unsigned second) {
  s16x4 lo = ldtr(a), hi = ldtr(a + second);
  return __builtin_shufflevector(lo, hi, 0, 1, 2, 3, 4, 5, 6, 7);
}
DI bf16x8 ld128(unsigned a) { return *lp<bf16x8>(a); }
DI unsigned cvtpk(float lo, float hi) { typedef __bf16 b2 __attribute__((ext_vector_type(2))); f32x2 v = {lo, hi}; return __builtin_bit_cast(unsigned, __builtin_convertvector(v, b2)); }
DI float bflo(unsigned v) { return __uint_as_float(v << 16); }
DI float bfhi(unsigned v) { return __uint_as_float(v & 0xffff0000u); }
DI float rbf(float f) { return bflo(cvtpk(f, f) & 0xffffu); }
DI int fresh(int v) { asm volatile("" : "+v"(v)); return v; }
template <class T> DI void pin(T& v) { asm volatile("" : "+v"(v)); }
DI unsigned pinv(unsigned v) { asm volatile("" : "+v"(v)); return v; }
#define MFMA32(a, b, c) __builtin_amdgcn_mfma_f32_32x32x16_bf16((a), (b), (c), 0, 0, 0)
#define MFMA16(a, b, c) __builtin_amdgcn_mfma_f32_16x16x32_bf16((a), (b), (c), 0, 0, 0)
#define MFMA4(a, b, c) __builtin_amdgcn_mfma_f32_4x4x4bf16_1k((a), (b), (c), 0, 0, 0)
#define SB() __builtin_amdgcn_sched_barrier(0)
DI float swap_add(float v) { auto r = __builtin_amdgcn_permlane32_swap(__float_as_uint(v), __float_as_uint(v), false, false); return __uint_as_float(r[0]) + __uint_as_float(r[1]); }
DI float swap_max(float v) { auto r = __builtin_amdgcn_permlane32_swap(__float_as_uint(v), __float_as_uint(v), false, false); return __builtin_fmaxf(__uint_as_float(r[0]), __uint_as_float(r[1])); }

#define STAMP(i) do { } while (0)

struct Ctx {
  char* ws; u64 tag;
  i32x4 sX, sQKVw, sG, sU, sWO, sWD, sWS;
  int lane, wid, mg, ng, rb, cs, c, cg, xi; unsigned stampreg; u64 prt1;
};

DI unsigned flag_off(int ev, int rb, int cs) { return (unsigned)(WS_FLAGS + (((ev * 16 + rb) * 16 + cs) << 7)); }
DI void publish(const Ctx& C, int ev) { if (C.wid == 0 && C.lane == 0) __hip_atomic_store((u64*)(C.ws + flag_off(ev, C.rb, C.cs)), C.tag, __ATOMIC_RELAXED, __HIP_MEMORY_SCOPE_AGENT); }
DI void give_up(char* ws) { __hip_atomic_store((unsigned*)(ws + WS_ERR), 1u, __ATOMIC_RELAXED, __HIP_MEMORY_SCOPE_AGENT); }
DI void spin_flags(char* ws, unsigned off, bool need, u64 tag) {
#pragma nounroll
  for (unsigned it = 0; it < SPIN_CAP; ++it) {
    u64 v = __hip_atomic_load((u64*)(ws + off), __ATOMIC_RELAXED, __HIP_MEMORY_SCOPE_AGENT);
    if (__all(!need || v == tag)) return;
    __builtin_amdgcn_s_sleep(2);
  }
  give_up(ws);
}
DI void check_flags(char* ws, unsigned off, bool need, u64 tag, u32x2& got) {
  asm volatile("" : "+v"(got));
  bool ok = !need || (got[0] == (unsigned)tag && got[1] == (unsigned)(tag >> 32));
  if (!__all(ok)) spin_flags(ws, off, need, tag);
}
template <int K0, int K1> DI void check_frags(const Ctx& C, u32x4 (&a2)[36], unsigned base) {
  auto dirty = [&]() { unsigned mx = 0;
    static_for<K0, K1>([&](auto kc) { constexpr int k = decltype(kc)::value;
      mx = __builtin_elementwise_max(mx, __builtin_elementwise_max(__builtin_elementwise_max(a2[k][0], a2[k][1]), __builtin_elementwise_max(a2[k][2], a2[k][3]))); });
    return __any(mx == POISON); };
  if (dirty()) {
#pragma nounroll
    for (unsigned it = 0; it < SPIN_CAP; ++it) {
      __builtin_amdgcn_s_sleep(2);
      static_for<K0, K1>([&](auto kc) { constexpr int k = decltype(kc)::value; ld16_sc1(a2[k], C.sWS, C.lane * 16, base + k * 1024); });
      wait_vm<0>();
      static_for<K0, K1>([&](auto kc) { constexpr int k = decltype(kc)::value; pin(a2[k]); });
      if (!dirty()) return;
    }
    give_up(C.ws);
  }
}

enum { T_H0 = 0, T_S4 = 8, T_E0 = 16, T_G0 = 19, T_POISON = 22, T_POLL, T_KL, T_VL, T_POLL2, T_EPOCH, T_NONE };
template <int TYPE> struct G1 {
  static constexpr int kind(int p) { return p == 1 ? 2 : TYPE == 0 ? (p == 0 ? 0 : 2) : (p == 0 ? 1 : 3); }
  static constexpr int nt(int p) { return (kind(p) == 0 || kind(p) == 3) ? 1 : 2; }
  static constexpr int nst(int p) { return kind(p) == 1 ? 4 : kind(p) == 3 ? 1 : 2; }
  static constexpr int npoison() { return (TYPE == 0 ? 2 : 1) + 1; }
  static constexpr unsigned slot(int s) {
    return s == 4 ? R0 : s == 5 ? R1 : s == 6 ? R2 : s == 7 ? R3 : s == 8 ? R0 : s == 9 ? R1 : s == 10 ? R2 : R0; }
  static constexpr int barpos(int bi) { return bi < 8 ? 4 * bi + 2 : 8 * (bi - 4) + 6; }
  static constexpr int barat(int pos) { return pos < 32 ? ((pos & 3) == 2 ? pos >> 2 : -1) : ((pos & 7) == 6 ? 4 + (pos >> 3) : -1); }
  static constexpr int grp(int bi) { return bi < 4 ? T_H0 + bi + 4 : bi < 11 ? T_S4 + (bi - 4) : bi == 11 ? T_NONE : bi == 12 ? T_S4 + 7 : T_G0 + (bi - 13); }
  static constexpr int gsize(int t) { return t == T_NONE ? 0 : t < T_S4 ? 2 + nt(0) : t < T_E0 ? (nt((t - T_S4 + 4) >> 2) == 1 ? 2 : 4) : 4; }
  static constexpr int rel(int bi, int k) { return bi < 8 ? k : bi == 15 ? (k >> 1) : 2 * k; }
  static constexpr int after(int t, int bi) {
    int cnt = -1;
    auto see = [&](int tag, int n) { for (int i = 0; i < n; ++i) { if (tag == t) cnt = 0; else if (cnt >= 0) ++cnt; } };
    for (int h = 0; h < 2; ++h) see(T_H0 + h, gsize(T_H0 + h));
    see(T_EPOCH, 1);
    for (int pos = 0; pos < 96; ++pos) {
      if (pos < 2) see(T_H0 + 2 + pos, gsize(T_H0 + 2 + pos));
      const int b = barat(pos);
      if (b >= 0) {
        if (b == bi) return cnt;
        if (b == 12) see(T_POLL, 1);
        if (b == 13) see(T_KL, 3);
        if (b == 14) see(T_VL, 4);
        if (b == 15) see(T_POLL2, 1);
      }
      for (int bb = 0; bb < 16; ++bb) for (int k = 0; k < gsize(grp(bb)); ++k) if (barpos(bb) + rel(bb, k) == pos) see(grp(bb), 1);
      if (pos == 16) see(T_POISON, npoison());
      if ((pos & 31) == 31) see(T_E0 + (pos >> 5), nst(pos >> 5));
    }
    return cnt;
  }
  static constexpr int mn(int a, int b) { return a < b ? a : b; }
};

template <int TYPE> DI unsigned tile_addr(const Ctx& C, int p, int t) {
  const int lane = fresh(C.lane), kind = G1<TYPE>::kind(p);
  const int g = (lane >> 4) & 1, h = lane >> 5, q = (lane & 15) >> 2, pp = lane & 3;
  const unsigned rdrow = (8 * h + q) * 128 + pp * 8;
  if (kind == 3) return g * 8192 + (8 * h + q) * 64 + pp * 8 + ((C.ng ^ g) << 5);
  if (kind == 2) return g * 16384 + rdrow + (((2 * C.ng + t) ^ (2 * (q >> 1)) ^ g) << 5);
  if (kind == 0) return 16384 + rdrow + ((C.ng ^ (q >> 1)) << 6) + g * 32;
  return 16384 + C.ng * 8192 + rdrow + ((t ^ (q >> 1)) << 6) + g * 32;
}
template <int TYPE> DI unsigned stage_voff(const Ctx& C, int p) {
  const int kind = G1<TYPE>::kind(p), lane = fresh(C.lane);
  if (kind == 3) return (16 * C.wid + (lane >> 2)) * 3584 + (24 + ((C.cs - 8) >> 1)) * 128 + ((C.cs - 8) & 1) * 64 + ((lane & 3) << 4);
  return (16 * C.wid + (lane >> 3)) * 3584 + (p == 1 ? C.cs : 16 + C.cs) * 128 + (((lane & 7) ^ (((lane >> 4) & 1) << 2)) << 4);
}
template <int TYPE> DI void issue_stage(const Ctx& C, int s, int k, unsigned v0) {
  const int kind = G1<TYPE>::kind(s >> 2);
  const unsigned slot = G1<TYPE>::slot(s), r0 = 128 * (s & 3);
  if (kind == 3) dma16(k ? C.sU : C.sG, k ? v0 ^ 32 : v0, r0 * 3584, slot + k * 8192 + C.wid * 1024);
  else dma16(k >> 1 ? C.sU : C.sG, k >> 1 ? v0 ^ 32 : v0, (r0 + 8 * (k & 1)) * 3584, slot + (k >> 1) * 16384 + C.wid * 2048 + (k & 1) * 1024);
}
DI unsigned hx_voff(const Ctx& C) { const int lane = fresh(C.lane); return (16 * C.wid + (lane >> 3)) * 1024 + (((lane & 7) ^ ((lane >> 4) & 3)) << 4); }
DI unsigned hw_voff(const Ctx& C) { const int lane = fresh(C.lane);
  return (8 * C.wid + (lane >> 3)) * 3072 + (C.cs < 8 ? C.cs : 8 + 2 * (C.cs - 8)) * 128 + (((lane & 7) ^ (((lane >> 4) & 1) << 2)) << 4); }
DI void issue_half(const Ctx& C, int h, int k, unsigned vx, unsigned vw) {
  const unsigned slot = (h & 3) * 32768;
  if (k < 2) dma16(C.sX, k ? (vx ^ 64) + 8 * 1024 : vx, h * 128, slot + C.wid * 2048 + k * 1024);
  else dma16(C.sQKVw, vw + (k - 2) * 128, h * 64 * 3072, slot + 16384 + (k - 2) * 8192 + C.wid * 1024);
}
DI void warm(i32x4 srd, unsigned line0, unsigned lines_per_row_log2, unsigned pitch, unsigned colbyte, int lane) {
  const unsigned l = line0 + lane;
  dma4(srd, (l >> lines_per_row_log2) * pitch + colbyte + (l & ((1u << lines_per_row_log2) - 1)) * 128, L_JUNK);
}
DI unsigned g_voff(const Ctx& C) { const int lane = fresh(C.lane); return (32 * (C.wid & 1) + (lane >> 3)) * 1024 + C.cg * 128 + (((lane & 7) ^ (((lane >> 4) & 1) << 2)) << 4); }
constexpr unsigned gslot(int j) { return j == 0 ? R1 : j == 1 ? R2 : j == 2 ? R0 : j == 3 ? R3 : j == 4 ? R1 : j == 5 ? R2 : j == 6 ? R0 : j == 7 ? R3 : R1; }
DI void issue_g(const Ctx& C, int j, int i, unsigned v) {
  const int kq = C.wid >> 1;
  const int row0 = j < 4 ? (4 * kq + j) * 64 : j < 7 ? (16 + 3 * kq + (j - 4)) * 64 : 128 * kq + 64 * (j - 7);
  dma16(j < 7 ? C.sWD : C.sWO, v, (unsigned)(row0 + 8 * i) * 1024, gslot(j) + kq * 8192 + (C.wid & 1) * 4096 + i * 1024);
}

template <int TYPE> DI void epi_qkv(const Ctx& C, const f32x16& acc, const f32x4 (&csr)[4], float m, float rinv, int qu, int h4) {
  const int h = C.lane >> 5;
  const float sc = TYPE == 0 ? rinv * QSCALE : rinv;
  float y[16];
#pragma unroll
  for (int i4 = 0; i4 < 4; ++i4)
#pragma unroll
    for (int e = 0; e < 4; ++e) y[4 * i4 + e] = (acc[4 * i4 + e] - m * csr[i4][e]) * sc;
  const unsigned base = (unsigned)WS_QKV + (((C.rb * 24 + qu) * 4 + C.mg) * 4 + h4) * 1024;
#pragma unroll
  for (int hh = 0; hh < 2; ++hh) {
    u32x4 f; f[0] = cvtpk(y[8 * hh], y[8 * hh + 1]); f[1] = cvtpk(y[8 * hh + 2], y[8 * hh + 3]); f[2] = cvtpk(y[8 * hh + 4], y[8 * hh + 5]); f[3] = cvtpk(y[8 * hh + 6], y[8 * hh + 7]);
    unsigned voff = C.lane * 16;
    if (TYPE == 1 && qu >= 16) {
      auto a = __builtin_amdgcn_permlane32_swap(f[0], f[2], false, false); auto b = __builtin_amdgcn_permlane32_swap(f[1], f[3], false, false);
      f[0] = a[0]; f[2] = a[1]; f[1] = b[0]; f[3] = b[1];
      voff = (C.lane & 31) * 32 + h * 16;
    }
    st16_sc1(f, C.sWS, voff, base + hh * 1024);
  }
}
DI void epi_gu(const Ctx& C, const f32x16& acc, const f32x4 (&csr)[4], float m, float r2, unsigned a2off) {
  float y[8];
#pragma unroll
  for (int i4 = 0; i4 < 2; ++i4)
#pragma unroll
    for (int e = 0; e < 4; ++e) y[4 * i4 + e] = ((acc[4 * i4 + e] - m * csr[i4][e]) * r2) * (acc[8 + 4 * i4 + e] - m * csr[2 + i4][e]);
  u32x4 f; f[0] = cvtpk(y[0], y[1]); f[1] = cvtpk(y[2], y[3]); f[2] = cvtpk(y[4], y[5]); f[3] = cvtpk(y[6], y[7]);
  st16_sc1(f, C.sWS, C.lane * 16, a2off);
}
template <int TYPE> DI unsigned a2_pass(const Ctx& C, int p) {
  if (p == 1) return (unsigned)WS_A2 + (((4 * C.rb + C.mg) * 4 + (C.cs >> 2)) * 36 + 4 * (C.cs & 3) + 2 * C.ng) * 1024;
  if (TYPE == 0) return (unsigned)WS_A2 + (((4 * C.rb + C.mg) * 4 + C.cs / 3) * 36 + 4 * (4 + C.cs % 3) + 2 * C.ng) * 1024;
  const int u = 8 + ((C.cs - 8) >> 1), half = (C.cs - 8) & 1;
  return (unsigned)WS_A2 + (((4 * C.rb + C.mg) * 4 + u / 3) * 36 + 4 * (4 + u % 3) + 2 * half + C.ng) * 1024;
}
DI unsigned a2_o(const Ctx& C) { return (unsigned)WS_A2 + (((4 * C.rb + C.mg) * 4 + (C.cs >> 2)) * 36 + 28 + 2 * (C.cs & 3) + C.ng) * 1024; }

template <int TYPE> DI void gemm1(Ctx& C, unsigned vx, unsigned vw, u32x4& qfrag, u32x2& poll2, unsigned poll2off) {
  using G = G1<TYPE>;
  const int lane = C.lane;
  char* ws = C.ws;
  bf16x8 xa[32];
  bf16x8 wf[3][2];
  f32x16 acc[2];
  unsigned ta[2], sv = 0;
  ta[0] = tile_addr<TYPE>(C, 0, 0); ta[1] = tile_addr<TYPE>(C, 0, 1);
  const unsigned r31 = lane & 31, hh = lane >> 5;
  const unsigned xlane = (32 * C.mg + r31) * 128 + ((hh ^ ((r31 >> 1) & 7)) << 4);
  f32x4 ssum = {0, 0, 0, 0}, ssq = {0, 0, 0, 0};
  f32x4 csacc[2] = {{0,0,0,0},{0,0,0,0}};
  bf16x8 sel;
  { const short one = ((lane & 15) == ((lane >> 4) & 1)) ? (short)0x3f80 : (short)0;
#pragma unroll
    for (int e = 0; e < 8; ++e) sel[e] = one; }
  float csn[2];
  f32x4 csr[2][4];
  float m = 0.f, rinv = 0.f;
  u32x2 pollv; u32x4 gk[3];
  unsigned polloff = 0; bool pollneed = false;

  STAMP(1);
  bar_vm<G::gsize(T_H0)>();
  STAMP(2);
  C.prt1 = __builtin_amdgcn_s_memrealtime();
  u32x2 ev; ld8_sc1(ev, C.sWS, (unsigned)WS_EPOCH);
  auto read_x = [&](int h) {
    const unsigned base = (h & 3) * 32768 + xlane;
#pragma unroll
    for (int i = 0; i < 4; ++i) xa[4 * h + i] = ld128(base ^ (((i + C.c) & 3) << 5));
  };
  auto read_w = [&](int pos) {
    const int p = pos >> 5, s = pos >> 3, i = pos & 7;
    const bool half = G::kind(p) == 3;
    if ((pos & 31) == 0 && pos > 0) { ta[0] = tile_addr<TYPE>(C, p, 0); ta[1] = tile_addr<TYPE>(C, p, 1); }
    const unsigned stride = half ? 1024 : 2048, second = half ? 256 : 512;
    const unsigned so = (p == 0 ? ((pos >> 2) & 3) * 32768 : G::slot(s) + (i & 4) * stride) + ((i + C.c) & 3) * stride;
    wf[pos % 3][0] = ldfrag_tr(pinv(ta[0] + so), second);
    if (G::nt(p) == 2) wf[pos % 3][1] = ldfrag_tr(pinv(ta[1] + so), second);
  };
  read_x(0); read_w(0); read_w(1);
  static_for<0, 96>([&](auto ic) {
    constexpr int pos = decltype(ic)::value;
    constexpr int p = pos >> 5, s = pos >> 3, st = s & 3, i = pos & 7, NT = G::nt(p), bi = G::barat(pos);
    if constexpr (pos < 2) static_for<0, G::gsize(T_H0)>([&](auto kc) { issue_half(C, 2 + pos, decltype(kc)::value, vx, vw); });
    if constexpr (bi >= 0) {
      STAMP(10 + 2 * bi);
      if constexpr (bi == 0) { bar_vm<G::after(T_H0 + 1, 0)>(); }
      else if constexpr (bi < 7) bar_vm<G::after(T_H0 + bi + 1, bi)>();
      else if constexpr (bi == 9) { bar_vm<G::mn(G::after(T_S4 + 2, 9), G::mn(G::after(T_E0, 9), G::after(T_POISON, 9)))>(); publish(C, 0); }
      else if constexpr (bi == 13) { bar_vm<G::mn(G::mn(G::after(T_S4 + 6, 13), G::after(T_E0 + 1, 13)), G::after(T_POLL, 13))>(); }
      else if constexpr (bi == 14) bar_vm<G::mn(G::after(T_S4 + 7, 14), G::after(T_KL, 14))>();
      else if constexpr (bi == 15) bar_vm<G::after(T_VL, 15)>();
      else bar_vm<G::after(T_S4 + bi - 7, bi)>();
      if constexpr (bi == 4) {
        static_assert(G::after(T_H0 + 5, 4) <= G::after(T_EPOCH, 4), "");
        pin(ev);
        const u64 t = (((u64)ev[1] << 32) | ev[0]) * 6364136223846793005ull + 1442695040888963407ull;
        C.tag = ((u64)__builtin_amdgcn_readfirstlane((unsigned)(t >> 32)) << 32) | __builtin_amdgcn_readfirstlane((unsigned)t);
      }
      if constexpr (bi == 12) {
        const int u = C.cs >> 1, l = lane & 7;
        const int ty = l == 0 ? 0 : l < 4 ? 1 : 2, rbp = l == 0 ? C.rb : C.rb - 2 + (l - 1) % 3;
        const int qu = 8 * ty + u;
        const int pcs = qu < 8 ? qu : 8 + ((qu - 8) >> 1);
        pollneed = l < 7 && rbp >= 0;
        polloff = flag_off(0, pollneed ? rbp : C.rb, pcs);
        ld8_sc1(pollv, C.sWS, polloff);
      }
      if constexpr (bi == 13) {
        check_flags(ws, polloff, pollneed, C.tag, pollv);
        const int u = C.cs >> 1, h4 = 2 * (C.cs & 1) + C.ng;
#pragma unroll
        for (int b = 0; b < 3; ++b) { const int rbp = C.rb - 2 + b < 0 ? 0 : C.rb - 2 + b;
          ld16_sc1(gk[b], C.sWS, lane * 16, (unsigned)WS_QKV + (((rbp * 24 + 8 + u) * 4 + C.mg) * 4 + h4) * 1024); }
        publish(C, 1);
      }
      if constexpr (bi == 14) {
        pin(gk[0]); pin(gk[1]); pin(gk[2]);
#pragma unroll
        for (int b = 0; b < 3; ++b) *lp<u32x4>(L_K + ((4 * b + C.mg) * 2 + C.ng) * 1024 + lane * 16) = gk[b];
        const int u = C.cs >> 1, h4 = 2 * (C.cs & 1) + C.ng;
#pragma unroll
        for (int b = 0; b < 3; ++b) { const int rbp = C.rb - 2 + b < 0 ? 0 : C.rb - 2 + b;
          ld16_sc1(gk[b], C.sWS, lane * 16, (unsigned)WS_QKV + (((rbp * 24 + 16 + u) * 4 + C.mg) * 4 + h4) * 1024); }
        ld16_sc1(qfrag, C.sWS, lane * 16, (unsigned)WS_QKV + (((C.rb * 24 + u) * 4 + C.mg) * 4 + h4) * 1024);
      }
      if constexpr (bi == 15) {
        pin(qfrag); pin(gk[0]); pin(gk[1]); pin(gk[2]);
#pragma unroll
        for (int b = 0; b < 3; ++b) *lp<u32x4>(L_V + C.ng * 12288 + (4 * b + C.mg) * 1024 + lane * 16) = gk[b];
        ld8_sc1(poll2, C.sWS, poll2off);
      }
      if constexpr (bi < 7) read_x(bi + 1);
      if constexpr (st == 3 && i == 6) {
#pragma unroll
        for (int t = 0; t < NT; ++t) { const unsigned a = L_CSP + (C.ng * 2 + t) * 512 + r31 * 4;
          csn[t] = (*lp<float>(a) + *lp<float>(a + 128)) + (*lp<float>(a + 256) + *lp<float>(a + 384)); }
      }
      STAMP(11 + 2 * bi);
    }
    static_for<0, 16>([&](auto bc) {
      constexpr int bb = decltype(bc)::value, tg = G::grp(bb), k0 = pos - G::barpos(bb);
      static_for<0, 4>([&](auto kc) {
        constexpr int k = decltype(kc)::value;
        if constexpr (k < G::gsize(tg) && G::rel(bb, k) == k0) {
          if constexpr (tg < T_S4) issue_half(C, tg, k, vx, vw);
          else if constexpr (tg >= T_G0) { if constexpr (k == 0) sv = g_voff(C); issue_g(C, tg - T_G0, k, sv); }
          else { if constexpr (k == 0) sv = stage_voff<TYPE>(C, (tg - T_S4 + 4) >> 2); issue_stage<TYPE>(C, tg - T_S4 + 4, k, sv); }
        }
      });
    });
    if constexpr (pos == 12 && !NO_WARM) {
      const int gu = TYPE == 0 ? 16 + C.cs : 24 + ((C.cs - 8) >> 1);
      const unsigned l0 = (C.rb & 7) * 64;
      if (C.wid == 5) { warm(C.sG, l0, 0, 3584, gu * 128, fresh(lane)); warm(C.sU, l0, 0, 3584, gu * 128, fresh(lane)); }
    }
    if constexpr (pos >= 34 && pos < 52 && (pos & 1) == 0 && !NO_WARM) {
      constexpr int k = (pos - 34) >> 1;
      const unsigned l = ((C.xi * 9 + k) * 64);
      if (C.wid == (k & 7)) { if (l < 4096) warm(C.sWO, l, 3, 1024, 0, fresh(lane)); else warm(C.sWD, l - 4096, 3, 1024, 0, fresh(lane)); }
    }
    if constexpr (pos == 16) {
      const u32x4 pz = {POISON, POISON, POISON, POISON};
      const unsigned a = a2_pass<TYPE>(C, 2);
      st16_sc1(pz, C.sWS, lane * 16, a);
      if (TYPE == 0) st16_sc1(pz, C.sWS, lane * 16, a + 1024);
      st16_sc1(pz, C.sWS, lane * 16, a2_o(C));
    }
    if constexpr (pos + 2 < 96) read_w(pos + 2);
    if constexpr ((pos & 31) == 0) { acc[0] = MFMA32(wf[pos % 3][0], xa[pos & 31], f32x16{}); if (NT == 2) acc[1] = MFMA32(wf[pos % 3][1], xa[pos & 31], f32x16{}); }
    else { acc[0] = MFMA32(wf[pos % 3][0], xa[pos & 31], acc[0]); if (NT == 2) acc[1] = MFMA32(wf[pos % 3][1], xa[pos & 31], acc[1]); }
    if constexpr ((i & 3) == 0 && !NO_CS) {
      if constexpr ((pos & 31) == 0) { csacc[0] = MFMA16(wf[pos % 3][0], sel, (f32x4{0, 0, 0, 0})); if (NT == 2) csacc[1] = MFMA16(wf[pos % 3][1], sel, (f32x4{0, 0, 0, 0})); }
      else { csacc[0] = MFMA16(wf[pos % 3][0], sel, csacc[0]); if (NT == 2) csacc[1] = MFMA16(wf[pos % 3][1], sel, csacc[1]); }
    }
    if constexpr (p == 0 && (i & 3) < 2) {
      const bf16x8 xv = xa[pos & 31]; const s16x4 lo = __builtin_shufflevector(xv, xv, 0, 1, 2, 3), hi = __builtin_shufflevector(xv, xv, 4, 5, 6, 7);
      const s16x4 ones = {0x3f80, 0x3f80, 0x3f80, 0x3f80};
      ssum = MFMA4(ones, lo, ssum); ssq = MFMA4(lo, lo, ssq); ssum = MFMA4(ones, hi, ssum); ssq = MFMA4(hi, hi, ssq);
    }
    if constexpr (st == 3 && i == 5) {
      if ((lane & 15) < 2) {
#pragma unroll
        for (int t = 0; t < NT; ++t) *lp<f32x4>(L_CSP + ((C.ng * 2 + t) * 4 + C.mg) * 128 + (16 * (lane & 15) + 4 * (lane >> 4)) * 4) = csacc[t];
      }
      if constexpr (p == 0) {
        const float sq = (lane & 3) == 0 ? ssq[0] : (lane & 3) == 1 ? ssq[1] : (lane & 3) == 2 ? ssq[2] : ssq[3];
        const f32x2 v = {swap_add(ssum[0]), swap_add(sq)};
        if (lane < 32) *lp<f32x2>(L_STAT + (C.ng * 128 + 32 * C.mg + lane) * 8) = v;
      }
    }
    if constexpr (st == 3 && i == 6) {
#pragma unroll
      for (int t = 0; t < NT; ++t) *lp<float>(L_CSW + (C.wid * 2 + t) * 128 + r31 * 4) = csn[t];
    }
    if constexpr (st == 3 && i == 7) {
#pragma unroll
      for (int t = 0; t < NT; ++t)
#pragma unroll
        for (int i4 = 0; i4 < 4; ++i4) csr[t][i4] = *lp<f32x4>(L_CSW + (C.wid * 2 + t) * 128 + (8 * i4 + 4 * hh) * 4);
    }
    SB();
    if constexpr ((pos & 31) == 31) {
      if constexpr (p == 0) {
        const f32x2 a = *lp<f32x2>(L_STAT + (32 * C.mg + r31) * 8), b = *lp<f32x2>(L_STAT + (128 + 32 * C.mg + r31) * 8);
        const float mean = (a[0] + b[0]) * (1.f / 512.f);
        const float var = __builtin_fmaxf((a[1] + b[1]) * (1.f / 512.f) - mean * mean, 0.f);
        m = rbf(mean);
        rinv = __builtin_amdgcn_rcpf(rbf(__builtin_amdgcn_sqrtf(rbf(rbf(var) + 1e-5f))));
      }
      if constexpr (G::kind(p) == 2) {
        const unsigned a2 = a2_pass<TYPE>(C, p);
        epi_gu(C, acc[0], csr[0], m, rinv * rinv, a2); epi_gu(C, acc[1], csr[1], m, rinv * rinv, a2 + 1024);
      } else if constexpr (G::kind(p) == 3) {
        epi_gu(C, acc[0], csr[0], m, rinv * rinv, a2_pass<TYPE>(C, p));
      } else if constexpr (G::kind(p) == 0) {
        epi_qkv<0>(C, acc[0], csr[0], m, rinv, C.cs, 2 * C.ng);
      } else {
        const int qu = 8 + 2 * (C.cs - 8) + C.ng;
        epi_qkv<1>(C, acc[0], csr[0], m, rinv, qu, 0); epi_qkv<1>(C, acc[1], csr[1], m, rinv, qu, 2);
      }
      SB();
    }
  });
  wait_vm<G::after(T_POLL2, 16)>();
}

template <bool EDGE, class SIDE> DI u32x4 attention(Ctx& C, const u32x4& qraw, SIDE&& side) {
  const int lane = C.lane, h = lane >> 5, g = (lane >> 4) & 1, q = (lane & 15) >> 2, pp = lane & 3;
  const bf16x8 qf = __builtin_bit_cast(bf16x8, qraw);
  const unsigned kaddr = pinv(L_K + (C.mg * 2 + C.ng) * 1024 + lane * 16);
  const int t0 = EDGE ? (C.rb == 0 ? 8 : 4) - C.mg : 1;
  const int dq = (lane & 31) - 4 * h;
  const float NEG = -1e30f;
  auto older = [&](int r) { return ((r & 3) + 8 * (r >> 2)) > dq; };
  auto max16 = [&](float& a, float& b, const f32x16& s) {
#pragma unroll
    for (int r = 0; r < 16; r += 4) { a = __builtin_fmaxf(__builtin_fmaxf(a, s[r]), s[r + 1]); b = __builtin_fmaxf(__builtin_fmaxf(b, s[r + 2]), s[r + 3]); } };
  bf16x8 kf[9];
#pragma unroll
  for (int t = 0; t < 9; ++t) if (!EDGE || t > 0) kf[t] = ld128(kaddr + t * 2048);
  float mx;
  {
    f32x16 S[3];
    f32x16 sl = MFMA32(kf[8], qf, f32x16{});
    f32x16 sf;
    if (!EDGE) sf = MFMA32(kf[0], qf, f32x16{});
    S[1] = MFMA32(kf[1], qf, f32x16{});
    S[2] = MFMA32(kf[2], qf, f32x16{});
    float a = NEG, b = NEG;
    side(std::integral_constant<int, 0>{});
    SB();
#pragma unroll
    for (int r = 0; r < 16; ++r) sl[r] = older(r) ? (EDGE ? NEG : sf[r]) : sl[r];
    max16(a, b, sl);
    static_for<1, 8>([&](auto tc) { constexpr int t = decltype(tc)::value;
      if constexpr (t + 2 < 8) S[(t + 2) % 3] = MFMA32(kf[t + 2], qf, f32x16{});
      side(std::integral_constant<int, t>{});
      SB();
      if (!EDGE || t >= t0) max16(a, b, S[t % 3]);
      SB(); });
    mx = swap_max(__builtin_fmaxf(a, b));
  }
  f32x16 negm;
#pragma unroll
  for (int r = 0; r < 16; ++r) negm[r] = -mx;
  const unsigned vbase = pinv(g == 0 ? L_V + C.ng * 12288 + C.mg * 1024 + (4 * h + q) * 32 + pp * 8 : L_ONES + 128 * (1 - h) + q * 32 + pp * 8);
  const unsigned vstr = g == 0 ? 1024u : 0u;
  f32x16 o;
  auto expack = [&](const f32x16& s, u32x4& pa, u32x4& pb) {
#pragma unroll
    for (int i = 0; i < 4; ++i) pa[i] = cvtpk(__builtin_amdgcn_exp2f(s[2 * i]), __builtin_amdgcn_exp2f(s[2 * i + 1]));
#pragma unroll
    for (int i = 0; i < 4; ++i) pb[i] = cvtpk(__builtin_amdgcn_exp2f(s[8 + 2 * i]), __builtin_amdgcn_exp2f(s[9 + 2 * i])); };
  f32x16 S[2];
  bf16x8 vf[2][2];
  {
    f32x16 sl = MFMA32(kf[8], qf, negm);
    f32x16 sf;
    if (!EDGE) sf = MFMA32(kf[0], qf, negm);
    S[1] = MFMA32(kf[1], qf, negm);
    const unsigned v8 = vbase + 8 * vstr;
    const bf16x8 va = ldfrag_tr(v8, 256), vb = ldfrag_tr(v8 + 512, 256);
    bf16x8 vc, vd;
    if (!EDGE) { vc = ldfrag_tr(vbase, 256); vd = ldfrag_tr(vbase + 512, 256); }
    vf[1][0] = ldfrag_tr(vbase + vstr, 256); vf[1][1] = ldfrag_tr(vbase + vstr + 512, 256);
    side(std::integral_constant<int, 8>{});
    SB();
#pragma unroll
    for (int r = 0; r < 16; ++r) sl[r] = older(r) ? (EDGE ? NEG : sf[r]) : sl[r];
    u32x4 pa, pb, mk0, mk1;
    expack(sl, pa, pb);
#pragma unroll
    for (int i = 0; i < 4; ++i) {
      mk0[i] = (older(2 * i) ? 0xffffu : 0u) | (older(2 * i + 1) ? 0xffff0000u : 0u);
      mk1[i] = (older(8 + 2 * i) ? 0xffffu : 0u) | (older(9 + 2 * i) ? 0xffff0000u : 0u); }
    o = MFMA32(va, __builtin_bit_cast(bf16x8, pa & ~mk0), f32x16{});
    o = MFMA32(vb, __builtin_bit_cast(bf16x8, pb & ~mk1), o);
    if (!EDGE) {
      o = MFMA32(vc, __builtin_bit_cast(bf16x8, pa & mk0), o);
      o = MFMA32(vd, __builtin_bit_cast(bf16x8, pb & mk1), o);
    }
    SB();
  }
  static_for<1, 8>([&](auto tc) { constexpr int t = decltype(tc)::value;
    if constexpr (t + 1 < 8) {
      const unsigned vt = vbase + (t + 1) * vstr;
      vf[(t + 1) & 1][0] = ldfrag_tr(vt, 256); vf[(t + 1) & 1][1] = ldfrag_tr(vt + 512, 256);
      S[(t + 1) & 1] = MFMA32(kf[t + 1], qf, negm); }
    side(std::integral_constant<int, 8 + t>{});
    SB();
    if (!EDGE || t >= t0) {
      u32x4 pa, pb;
      expack(S[t & 1], pa, pb);
      o = MFMA32(vf[t & 1][0], __builtin_bit_cast(bf16x8, pa), o);
      o = MFMA32(vf[t & 1][1], __builtin_bit_cast(bf16x8, pb), o);
    }
    SB(); });
  const float li = __builtin_amdgcn_rcpf(o[8]);
  u32x4 f; f[0] = cvtpk(o[0] * li, o[1] * li); f[1] = cvtpk(o[2] * li, o[3] * li); f[2] = cvtpk(o[4] * li, o[5] * li); f[3] = cvtpk(o[6] * li, o[7] * li);
  return f;
}

__global__ void __launch_bounds__(512, 2)
fused_kernel(const u16* __restrict__ x, const u16* __restrict__ wqkv, const u16* __restrict__ wo, const u16* __restrict__ wg, const u16* __restrict__ wu,
             const u16* __restrict__ wd, u16* __restrict__ out, char* __restrict__ ws) {
  Ctx C;
  const int tid = threadIdx.x, lane = tid & 63, wid = __builtin_amdgcn_readfirstlane(tid >> 6);
  const int b = blockIdx.x, xj = b & 7, xi = b >> 3;
  C.stampreg = 0;
  STAMP(0);
  const u64 prt0 = __builtin_amdgcn_s_memrealtime();
  asm volatile("" :: "s"(x), "s"(wqkv), "s"(wo), "s"(wg), "s"(wu), "s"(wd), "s"(out), "s"(ws));
  C.ws = ws; C.lane = lane; C.wid = wid; C.mg = wid & 3; C.ng = wid >> 2;
  C.rb = 8 * (xj & 1) + (xi & 7); C.cs = 4 * (xj >> 1) + (xi >> 3); C.c = (C.mg + 2 * C.ng) & 3;
  C.sX = mk_srd(x + (size_t)C.rb * 128 * 512, 128 * 1024);
  C.sQKVw = mk_srd(wqkv, 512 * 3072);
  C.xi = xi;
  const unsigned vx = hx_voff(C), vw = hw_voff(C);
  STAMP(3);
  issue_half(C, 0, 0, vx, vw); issue_half(C, 0, 1, vx, vw); issue_half(C, 0, 2, vx, vw); if (C.cs >= 8) issue_half(C, 0, 3, vx, vw);
  STAMP(4);
  issue_half(C, 1, 0, vx, vw); issue_half(C, 1, 1, vx, vw); issue_half(C, 1, 2, vx, vw); if (C.cs >= 8) issue_half(C, 1, 3, vx, vw);
  STAMP(5);
  C.sWS = mk_srd(ws, 32u << 20);
  C.sG = mk_srd(wg, 512 * 3584); C.sU = mk_srd(wu, 512 * 3584); C.sWO = mk_srd(wo, 512 * 1024); C.sWD = mk_srd(wd, 1792 * 1024);
  if (!NO_WARM) {
    if (wid < 3) { const unsigned l = (C.cs & 3) * 192 + wid * 64 + lane; dma4(C.sX, (l / 6) * 1024 + (2 + l % 6) * 128, L_JUNK); }
    else if (wid < 5) { if (wid == 3 || C.cs >= 8) dma4(C.sQKVw, (128 + (C.rb & 7) * 48 + (lane < 48 ? lane : 0)) * 3072 + ((C.cs < 8 ? C.cs : 8 + 2 * (C.cs - 8)) + (wid - 3)) * 128, L_JUNK); }
    else if (wid < 7) dma4(wid == 5 ? C.sG : C.sU, ((C.rb & 7) * 64 + lane) * 3584 + C.cs * 128, L_JUNK);
    else { u64 pc; asm volatile("s_getpc_b64 %0" : "=s"(pc));
      dma4(mk_srd((const void*)(pc & ~255ull), CODE_BYTES), (xi * 9 + (lane < 9 ? lane : 0)) * 128, L_JUNK); }
  }
  if (tid < 256) lp<unsigned>(L_ONES)[tid] = 0x3f803f80u;
  const int rt64 = 4 * xj + (xi & 3), cg = xi >> 2;
  C.cg = cg;
  const int rt = wid & 1, kq = wid >> 1, RT = 2 * rt64 + rt, rb2 = RT >> 2;
  const bool p2need = lane < 10;
  unsigned p2off;
  { const int l = lane < 10 ? lane : 0, u = 3 * kq + ((l - 4) >> 1);
    p2off = l < 4 ? flag_off(1, rb2, 4 * kq + l) : flag_off(0, rb2, u < 8 ? u : 8 + 2 * (u - 8) + (l & 1)); }
  u32x4 qfrag; u32x2 poll2;
  if (C.cs < 8) gemm1<0>(C, vx, vw, qfrag, poll2, p2off); else gemm1<1>(C, vx, vw, qfrag, poll2, p2off);
  STAMP(6);

  const unsigned a2base = (unsigned)WS_A2 + (RT * 4 + kq) * 36 * 1024;
  u32x4 a2[36];
  check_flags(ws, p2off, p2need, C.tag, poll2);
  asm volatile("s_waitcnt lgkmcnt(0)\n\ts_barrier" ::: "memory");
  auto side = [&](auto kc) { constexpr int k = decltype(kc)::value;
    if constexpr (k < 8) { ld16_sc1(a2[2 * k], C.sWS, lane * 16, a2base + 2 * k * 1024); ld16_sc1(a2[2 * k + 1], C.sWS, lane * 16, a2base + (2 * k + 1) * 1024); }
    if constexpr (k >= 12) static_for<0, 3>([&](auto jc) { constexpr int q_ = 16 + 3 * (k - 12) + decltype(jc)::value; ld16_sc1(a2[q_], C.sWS, lane * 16, a2base + q_ * 1024); }); };
  u32x4 of;
  if (C.rb >= 2) of = attention<false>(C, qfrag, side); else of = attention<true>(C, qfrag, side);
  st16_sc1(of, C.sWS, lane * 16, a2_o(C));
  STAMP(7);

  const int g = (lane >> 4) & 1, h = lane >> 5, q = (lane & 15) >> 2, pp = lane & 3;
  const unsigned w2l0 = kq * 8192 + (4 * h + q) * 128 + ((0 ^ (q >> 1)) << 6) + g * 32 + pp * 8;
  const unsigned w2l1 = kq * 8192 + (4 * h + q) * 128 + ((1 ^ (q >> 1)) << 6) + g * 32 + pp * 8;
  bf16x8 wf[3][2];
  f32x16 acc[2];
  u32x4 xres;
  unsigned wb[2], gv;
  auto read_w2 = [&](int pos) {
    if ((pos & 3) == 0) { wb[0] = pinv(w2l0 + gslot(pos >> 2)); wb[1] = pinv(w2l1 + gslot(pos >> 2)); }
    wf[pos % 3][0] = ldfrag_tr(wb[0] + (pos & 3) * 2048, 1024); wf[pos % 3][1] = ldfrag_tr(wb[1] + (pos & 3) * 2048, 1024); };
  bar_vm<13>();
  static_for<0, 16>([&](auto kc) { constexpr int k = decltype(kc)::value; pin(a2[k]); });
  read_w2(0); read_w2(1);
  gv = g_voff(C);
  STAMP(8);
  u32x4 ef[4];
  static_for<0, 36>([&](auto ic) {
    constexpr int pos = decltype(ic)::value;
    constexpr int j = pos >> 2, i = pos & 3;
    if constexpr (i == 2) {
      STAMP(44 + 2 * j);
      if constexpr (j <= 1) bar_vm<63>();
      if constexpr (j == 2) bar_vm<6>();
      if constexpr (j == 3) bar_vm<6>();
      if constexpr (j == 4) bar_vm<7>();
      if constexpr (j == 5) bar_vm<15>();
      if constexpr (j == 6) bar_vm<4>();
      if constexpr (j == 7) bar_vm<0>();
      STAMP(45 + 2 * j);
    }
    if constexpr (pos == 14) { static_for<16, 28>([&](auto kc) { pin(a2[decltype(kc)::value]); }); check_frags<16, 28>(C, a2, a2base); }
    if constexpr (pos == 26) { static_for<28, 36>([&](auto kc) { pin(a2[decltype(kc)::value]); }); pin(xres); check_frags<28, 36>(C, a2, a2base); }
    if constexpr (pos == 16) { const i32x4 sXall = mk_srd(x, 2048 * 1024); ld16(xres, sXall, (RT * 32 + (lane & 31)) * 1024 + cg * 128 + kq * 32 + h * 16, 0); }
    if constexpr (pos == 18 || pos == 19) static_for<0, 4>([&](auto kc) { constexpr int k = 28 + 4 * (pos - 18) + decltype(kc)::value; ld16_sc1(a2[k], C.sWS, lane * 16, a2base + k * 1024); });
    if constexpr (pos < 24) issue_g(C, 3 + (pos >> 2), pos & 3, gv);
    if constexpr (pos == 30) { if (b == 0 && wid == 7) {
#pragma unroll
        for (int k = 0; k < 4; ++k) { u32x2 t; ld8_sc1(t, C.sWS, flag_off(0, 0, 0) + (lane * 4 + k) * 128); ef[k][0] = t[0]; ef[k][1] = t[1]; } } }
    if constexpr (pos + 2 < 36) read_w2(pos + 2);
    const bf16x8 af = __builtin_bit_cast(bf16x8, a2[pos]);
    if constexpr (pos == 0) { acc[0] = MFMA32(wf[0][0], af, f32x16{}); acc[1] = MFMA32(wf[0][1], af, f32x16{}); }
    else { acc[0] = MFMA32(wf[pos % 3][0], af, acc[0]); acc[1] = MFMA32(wf[pos % 3][1], af, acc[1]); }
    SB();
  });
  STAMP(9);
#pragma unroll
  for (int d = 0; d < 4; ++d) if (d != kq) {
    const int ct = d >> 1, r0 = (d & 1) * 8;
    const unsigned a = R2 + ((rt * 4 + d) * 4 + kq) * 2048 + lane * 16;
    *lp<f32x4>(a) = f32x4{acc[ct][r0], acc[ct][r0 + 1], acc[ct][r0 + 2], acc[ct][r0 + 3]};
    *lp<f32x4>(a + 1024) = f32x4{acc[ct][r0 + 4], acc[ct][r0 + 5], acc[ct][r0 + 6], acc[ct][r0 + 7]};
  }
  float s[8];
#pragma unroll
  for (int e = 0; e < 8; ++e) { const float lo = (kq & 1) ? acc[0][8 + e] : acc[0][e], hi = (kq & 1) ? acc[1][8 + e] : acc[1][e]; s[e] = (kq & 2) ? hi : lo; }
  asm volatile("s_waitcnt lgkmcnt(0)\n\ts_barrier" ::: "memory");
#pragma unroll
  for (int d = 0; d < 4; ++d) if (d != kq) {
    const unsigned a = R2 + ((rt * 4 + kq) * 4 + d) * 2048 + lane * 16;
    const f32x4 u = *lp<f32x4>(a), v = *lp<f32x4>(a + 1024);
#pragma unroll
    for (int e = 0; e < 4; ++e) { s[e] += u[e]; s[4 + e] += v[e]; }
  }
  {
    unsigned u0 = __float_as_uint(s[0]), u1 = __float_as_uint(s[1]), u2 = __float_as_uint(s[2]), u3 = __float_as_uint(s[3]);
    unsigned v0 = __float_as_uint(s[4]), v1 = __float_as_uint(s[5]), v2 = __float_as_uint(s[6]), v3 = __float_as_uint(s[7]);
    auto r0 = __builtin_amdgcn_permlane32_swap(u0, v0, false, false); auto r1 = __builtin_amdgcn_permlane32_swap(u1, v1, false, false);
    auto r2 = __builtin_amdgcn_permlane32_swap(u2, v2, false, false); auto r3 = __builtin_amdgcn_permlane32_swap(u3, v3, false, false);
    const float c0 = __uint_as_float(r0[0]), c1 = __uint_as_float(r1[0]), c2 = __uint_as_float(r2[0]), c3 = __uint_as_float(r3[0]);
    const float c4 = __uint_as_float(r0[1]), c5 = __uint_as_float(r1[1]), c6 = __uint_as_float(r2[1]), c7 = __uint_as_float(r3[1]);
    u32x4 f;
    f[0] = cvtpk(c0 + bflo(xres[0]), c1 + bfhi(xres[0])); f[1] = cvtpk(c2 + bflo(xres[1]), c3 + bfhi(xres[1]));
    f[2] = cvtpk(c4 + bflo(xres[2]), c5 + bfhi(xres[2])); f[3] = cvtpk(c6 + bflo(xres[3]), c7 + bfhi(xres[3]));
    const i32x4 sO = mk_srd(out, 2048 * 1024);
    st16(f, sO, (RT * 32 + (lane & 31)) * 1024 + cg * 128 + kq * 32 + h * 16, 0);
  }
  if (b == 0 && wid == 7) {
    wait_vm<0>();
    asm volatile("" : "+v"(ef[0]), "+v"(ef[1]), "+v"(ef[2]), "+v"(ef[3]));
    bool ok = true;
#pragma unroll
    for (int k = 0; k < 4; ++k) ok = ok && ef[k][0] == (unsigned)C.tag && ef[k][1] == (unsigned)(C.tag >> 32);
    if (!__all(ok)) {
#pragma unroll 1
      for (int k = 0; k < 4; ++k) spin_flags(ws, flag_off(0, 0, 0) + (lane * 4 + k) * 128, true, C.tag);
    }
    if (lane == 0) __hip_atomic_store((u64*)(ws + WS_EPOCH), C.tag, __ATOMIC_RELAXED, __HIP_MEMORY_SCOPE_AGENT);
  }
  STAMP(60);
  if (b == 0 && wid == 0) { const u64 e = __builtin_amdgcn_s_memrealtime(); const u64 until = e + 4 * (C.prt1 - prt0); while (__builtin_amdgcn_s_memrealtime() < until) __builtin_amdgcn_s_sleep(1); }
}

extern "C" void kernel_launch(void* const* d_in, const int* in_sizes, int n_in, void* d_out, int out_size, void* d_ws, size_t ws_size, hipStream_t stream) {
  (void)in_sizes; (void)n_in; (void)out_size; (void)ws_size;
  (void)hipFuncSetAttribute((const void*)fused_kernel, hipFuncAttributeMaxDynamicSharedMemorySize, LDS_BYTES);
  fused_kernel<<<256, 512, LDS_BYTES, stream>>>((const u16*)d_in[0], (const u16*)d_in[1], (const u16*)d_in[2], (const u16*)d_in[4], (const u16*)d_in[5], (const u16*)d_in[6],
                                                 (u16*)d_out, (char*)d_ws);
}
```

```cpp
#include <hip/hip_runtime.h>
#include <stdint.h>
#include <type_traits>

typedef unsigned short u16;
typedef unsigned long long u64;
typedef short bf16x8 __attribute__((ext_vector_type(8)));
typedef short s16x4 __attribute__((ext_vector_type(4)));
typedef float f32x2 __attribute__((ext_vector_type(2)));
typedef float f32x4 __attribute__((ext_vector_type(4)));
typedef float f32x16 __attribute__((ext_vector_type(16)));
typedef unsigned u32x2 __attribute__((ext_vector_type(2)));
typedef unsigned u32x4 __attribute__((ext_vector_type(4)));
typedef int i32x4 __attribute__((ext_vector_type(4)));
#define LDSP __attribute__((address_space(3)))
#define DI __device__ __forceinline__
template <class T> DI LDSP T* lp(unsigned a) { return (LDSP T*)(size_t)a; }
template <int I, int N, class F> DI void static_for(F&& f) { if constexpr (I < N) { f(std::integral_constant<int, I>{}); static_for<I + 1, N>(f); } }

constexpr size_t WS_EPOCH = 0;
constexpr size_t WS_ERR   = 64;
constexpr size_t WS_FLAGS = 4096;
constexpr size_t WS_QKV   = 1u << 20;
constexpr size_t WS_A2    = 8u << 20;
constexpr size_t WS_DBG   = 24u << 20;

constexpr unsigned R0 = 0, R1 = 32768, R2 = 65536, R3 = 98304;
constexpr unsigned L_K = 98304, L_V = 122880;
constexpr unsigned L_ONES = 147456;
constexpr unsigned L_CSP = 148480;
constexpr unsigned L_CSW = 150528;
constexpr unsigned L_STAT = 152576;
constexpr unsigned L_JUNK = 154624;
constexpr unsigned LDS_BYTES = 163840;
constexpr unsigned CODE_BYTES = 36864;
constexpr unsigned POISON = 0xffffffffu;
constexpr unsigned SPIN_CAP = 1u << 13;

#define NO_WARM 0
#define NO_CS 0
#define NO_DMA 0
constexpr float QSCALE = 0.25f * 1.4426950408889634f;

DI i32x4 mk_srd(const void* p, unsigned bytes) { u64 a = (u64)p; i32x4 r; r[0] = (int)(unsigned)a; r[1] = (int)((unsigned)(a >> 32) & 0xffffu); r[2] = (int)bytes; r[3] = 0x00020000; return r; }
DI void dma16(i32x4 srd, unsigned voff, unsigned soff, unsigned lds) {
  if (NO_DMA) return;
  asm volatile("s_mov_b32 m0, %3\n\ts_nop 0\n\tbuffer_load_dwordx4 %0, %1, %2 offen lds" :: "v"(voff), "s"(srd), "s"(soff), "s"(lds) : "memory");
}
DI void dma4(i32x4 srd, unsigned voff, unsigned lds) {
  asm volatile("s_mov_b32 m0, %2\n\ts_nop 0\n\tbuffer_load_dword %0, %1, 0 offen lds" :: "v"(voff), "s"(srd), "s"(lds) : "memory");
}
DI void ld16_sc1(u32x4& d, i32x4 srd, unsigned voff, unsigned soff) {
  asm volatile("buffer_load_dwordx4 %0, %1, %2, %3 offen sc1" : "=v"(d) : "v"(voff), "s"(srd), "s"(soff) : "memory");
}
DI void ld16(u32x4& d, i32x4 srd, unsigned voff, unsigned soff) {
  asm volatile("buffer_load_dwordx4 %0, %1, %2, %3 offen" : "=v"(d) : "v"(voff), "s"(srd), "s"(soff) : "memory");
}
DI void ld8_sc1(u32x2& d, i32x4 srd, unsigned voff) {
  asm volatile("buffer_load_dwordx2 %0, %1, %2, 0 offen sc1" : "=v"(d) : "v"(voff), "s"(srd) : "memory");
}
DI void st16_sc1(u32x4 d, i32x4 srd, unsigned voff, unsigned soff) {
  asm volatile("buffer_store_dwordx4 %0, %1, %2, %3 offen sc1\n\ts_nop 1" :: "v"(d), "v"(voff), "s"(srd), "s"(soff) : "memory");
}
DI void st16(u32x4 d, i32x4 srd, unsigned voff, unsigned soff) {
  asm volatile("buffer_store_dwordx4 %0, %1, %2, %3 offen\n\ts_nop 1" :: "v"(d), "v"(voff), "s"(srd), "s"(soff) : "memory");
}
template <int N> DI void bar_vm() { static_assert(N >= 0 && N < 64, "vmcnt range"); asm volatile("s_waitcnt vmcnt(%0) lgkmcnt(0)\n\ts_barrier" :: "n"(N) : "memory"); }
template <int N> DI void wait_vm() { static_assert(N >= 0 && N < 64, "vmcnt range"); asm volatile("s_waitcnt vmcnt(%0)" :: "n"(N) : "memory"); }
DI s16x4 ldtr(unsigned a) { return __builtin_amdgcn_ds_read_tr16_b64_v4i16(lp<s16x4>(a)); }
DI bf16x8 ldfrag_tr(unsigned a, unsigned second) {
  s16x4 lo = ldtr(a), hi = ldtr(a + second);
  return __builtin_shufflevector(lo, hi, 0, 1, 2, 3, 4, 5, 6, 7);
}
DI bf16x8 ld128(unsigned a) { return *lp<bf16x8>(a); }
DI unsigned cvtpk(float lo, float hi) { typedef __bf16 b2 __attribute__((ext_vector_type(2))); f32x2 v = {lo, hi}; return __builtin_bit_cast(unsigned, __builtin_convertvector(v, b2)); }
DI float bflo(unsigned v) { return __uint_as_float(v << 16); }
DI float bfhi(unsigned v) { return __uint_as_float(v & 0xffff0000u); }
DI float rbf(float f) { return bflo(cvtpk(f, f) & 0xffffu); }
DI int fresh(int v) { asm volatile("" : "+v"(v)); return v; }
template <class T> DI void pin(T& v) { asm volatile("" : "+v"(v)); }
DI unsigned pinv(unsigned v) { asm volatile("" : "+v"(v)); return v; }
#define MFMA32(a, b, c) __builtin_amdgcn_mfma_f32_32x32x16_bf16((a), (b), (c), 0, 0, 0)
#define MFMA16(a, b, c) __builtin_amdgcn_mfma_f32_16x16x32_bf16((a), (b), (c), 0, 0, 0)
#define MFMA4(a, b, c) __builtin_amdgcn_mfma_f32_4x4x4bf16_1k((a), (b), (c), 0, 0, 0)
#define SB() __builtin_amdgcn_sched_barrier(0)
DI float swap_add(float v) { auto r = __builtin_amdgcn_permlane32_swap(__float_as_uint(v), __float_as_uint(v), false, false); return __uint_as_float(r[0]) + __uint_as_float(r[1]); }
DI float swap_max(float v) { auto r = __builtin_amdgcn_permlane32_swap(__float_as_uint(v), __float_as_uint(v), false, false); return __builtin_fmaxf(__uint_as_float(r[0]), __uint_as_float(r[1])); }

#define STAMP(i) do { } while (0)

struct Ctx {
  char* ws; u64 tag;
  i32x4 sX, sQKVw, sG, sU, sWO, sWD, sWS;
  int lane, wid, mg, ng, rb, cs, c, cg, xi; unsigned stampreg;
};

DI unsigned flag_off(int ev, int rb, int cs) { return (unsigned)(WS_FLAGS + (((ev * 16 + rb) * 16 + cs) << 7)); }
DI void publish(const Ctx& C, int ev) { if (C.wid == 0 && C.lane == 0) __hip_atomic_store((u64*)(C.ws + flag_off(ev, C.rb, C.cs)), C.tag, __ATOMIC_RELAXED, __HIP_MEMORY_SCOPE_AGENT); }
DI void give_up(char* ws) { __hip_atomic_store((unsigned*)(ws + WS_ERR), 1u, __ATOMIC_RELAXED, __HIP_MEMORY_SCOPE_AGENT); }
DI void spin_flags(char* ws, unsigned off, bool need, u64 tag) {
#pragma nounroll
  for (unsigned it = 0; it < SPIN_CAP; ++it) {
    u64 v = __hip_atomic_load((u64*)(ws + off), __ATOMIC_RELAXED, __HIP_MEMORY_SCOPE_AGENT);
    if (__all(!need || v == tag)) return;
    __builtin_amdgcn_s_sleep(2);
  }
  give_up(ws);
}
DI void check_flags(char* ws, unsigned off, bool need, u64 tag, u32x2& got) {
  asm volatile("" : "+v"(got));
  bool ok = !need || (got[0] == (unsigned)tag && got[1] == (unsigned)(tag >> 32));
  if (!__all(ok)) spin_flags(ws, off, need, tag);
}
template <int K0, int K1> DI void check_frags(const Ctx& C, u32x4 (&a2)[36], unsigned base) {
  auto dirty = [&]() { unsigned mx = 0;
    static_for<K0, K1>([&](auto kc) { constexpr int k = decltype(kc)::value;
      mx = __builtin_elementwise_max(mx, __builtin_elementwise_max(__builtin_elementwise_max(a2[k][0], a2[k][1]), __builtin_elementwise_max(a2[k][2], a2[k][3]))); });
    return __any(mx == POISON); };
  if (dirty()) {
#pragma nounroll
    for (unsigned it = 0; it < SPIN_CAP; ++it) {
      __builtin_amdgcn_s_sleep(2);
      static_for<K0, K1>([&](auto kc) { constexpr int k = decltype(kc)::value; ld16_sc1(a2[k], C.sWS, C.lane * 16, base + k * 1024); });
      wait_vm<0>();
      static_for<K0, K1>([&](auto kc) { constexpr int k = decltype(kc)::value; pin(a2[k]); });
      if (!dirty()) return;
    }
    give_up(C.ws);
  }
}

enum { T_H0 = 0, T_S4 = 8, T_E0 = 16, T_G0 = 19, T_POISON = 22, T_POLL, T_KL, T_VL, T_POLL2, T_EPOCH, T_NONE };
template <int TYPE> struct G1 {
  static constexpr int kind(int p) { return p == 1 ? 2 : TYPE == 0 ? (p == 0 ? 0 : 2) : (p == 0 ? 1 : 3); }
  static constexpr int nt(int p) { return (kind(p) == 0 || kind(p) == 3) ? 1 : 2; }
  static constexpr int nst(int p) { return kind(p) == 1 ? 4 : kind(p) == 3 ? 1 : 2; }
  static constexpr int npoison() { return (TYPE == 0 ? 2 : 1) + 1; }
  static constexpr unsigned slot(int s) {
    return s == 4 ? R0 : s == 5 ? R1 : s == 6 ? R2 : s == 7 ? R3 : s == 8 ? R0 : s == 9 ? R1 : s == 10 ? R2 : R0; }
  static constexpr int barpos(int bi) { return bi < 8 ? 4 * bi + 2 : 8 * (bi - 4) + 6; }
  static constexpr int barat(int pos) { return pos < 32 ? ((pos & 3) == 2 ? pos >> 2 : -1) : ((pos & 7) == 6 ? 4 + (pos >> 3) : -1); }
  static constexpr int grp(int bi) { return bi < 4 ? T_H0 + bi + 4 : bi < 11 ? T_S4 + (bi - 4) : bi == 11 ? T_NONE : bi == 12 ? T_S4 + 7 : T_G0 + (bi - 13); }
  static constexpr int gsize(int t) { return t == T_NONE ? 0 : t < T_S4 ? 2 + nt(0) : t < T_E0 ? (nt((t - T_S4 + 4) >> 2) == 1 ? 2 : 4) : 4; }
  static constexpr int rel(int bi, int k) { return bi < 8 ? k : bi == 15 ? (k >> 1) : 2 * k; }
  static constexpr int after(int t, int bi) {
    int cnt = -1;
    auto see = [&](int tag, int n) { for (int i = 0; i < n; ++i) { if (tag == t) cnt = 0; else if (cnt >= 0) ++cnt; } };
    for (int h = 0; h < 2; ++h) see(T_H0 + h, gsize(T_H0 + h));
    see(T_EPOCH, 1);
    for (int pos = 0; pos < 96; ++pos) {
      if (pos < 2) see(T_H0 + 2 + pos, gsize(T_H0 + 2 + pos));
      const int b = barat(pos);
      if (b >= 0) {
        if (b == bi) return cnt;
        if (b == 12) see(T_POLL, 1);
        if (b == 13) see(T_KL, 3);
        if (b == 14) see(T_VL, 4);
        if (b == 15) see(T_POLL2, 1);
      }
      for (int bb = 0; bb < 16; ++bb) for (int k = 0; k < gsize(grp(bb)); ++k) if (barpos(bb) + rel(bb, k) == pos) see(grp(bb), 1);
      if (pos == 16) see(T_POISON, npoison());
      if ((pos & 31) == 31) see(T_E0 + (pos >> 5), nst(pos >> 5));
    }
    return cnt;
  }
  static constexpr int mn(int a, int b) { return a < b ? a : b; }
};

template <int TYPE> DI unsigned tile_addr(const Ctx& C, int p, int t) {
  const int lane = fresh(C.lane), kind = G1<TYPE>::kind(p);
  const int g = (lane >> 4) & 1, h = lane >> 5, q = (lane & 15) >> 2, pp = lane & 3;
  const unsigned rdrow = (8 * h + q) * 128 + pp * 8;
  if (kind == 3) return g * 8192 + (8 * h + q) * 64 + pp * 8 + ((C.ng ^ g) << 5);
  if (kind == 2) return g * 16384 + rdrow + (((2 * C.ng + t) ^ (2 * (q >> 1)) ^ g) << 5);
  if (kind == 0) return 16384 + rdrow + ((C.ng ^ (q >> 1)) << 6) + g * 32;
  return 16384 + C.ng * 8192 + rdrow + ((t ^ (q >> 1)) << 6) + g * 32;
}
template <int TYPE> DI unsigned stage_voff(const Ctx& C, int p) {
  const int kind = G1<TYPE>::kind(p), lane = fresh(C.lane);
  if (kind == 3) return (16 * C.wid + (lane >> 2)) * 3584 + (24 + ((C.cs - 8) >> 1)) * 128 + ((C.cs - 8) & 1) * 64 + ((lane & 3) << 4);
  return (16 * C.wid + (lane >> 3)) * 3584 + (p == 1 ? C.cs : 16 + C.cs) * 128 + (((lane & 7) ^ (((lane >> 4) & 1) << 2)) << 4);
}
template <int TYPE> DI void issue_stage(const Ctx& C, int s, int k, unsigned v0) {
  const int kind = G1<TYPE>::kind(s >> 2);
  const unsigned slot = G1<TYPE>::slot(s), r0 = 128 * (s & 3);
  if (kind == 3) dma16(k ? C.sU : C.sG, k ? v0 ^ 32 : v0, r0 * 3584, slot + k * 8192 + C.wid * 1024);
  else dma16(k >> 1 ? C.sU : C.sG, k >> 1 ? v0 ^ 32 : v0, (r0 + 8 * (k & 1)) * 3584, slot + (k >> 1) * 16384 + C.wid * 2048 + (k & 1) * 1024);
}
DI unsigned hx_voff(const Ctx& C) { const int lane = fresh(C.lane); return (16 * C.wid + (lane >> 3)) * 1024 + (((lane & 7) ^ ((lane >> 4) & 3)) << 4); }
DI unsigned hw_voff(const Ctx& C) { const int lane = fresh(C.lane);
  return (8 * C.wid + (lane >> 3)) * 3072 + (C.cs < 8 ? C.cs : 8 + 2 * (C.cs - 8)) * 128 + (((lane & 7) ^ (((lane >> 4) & 1) << 2)) << 4); }
DI void issue_half(const Ctx& C, int h, int k, unsigned vx, unsigned vw) {
  const unsigned slot = (h & 3) * 32768;
  if (k < 2) dma16(C.sX, k ? (vx ^ 64) + 8 * 1024 : vx, h * 128, slot + C.wid * 2048 + k * 1024);
  else dma16(C.sQKVw, vw + (k - 2) * 128, h * 64 * 3072, slot + 16384 + (k - 2) * 8192 + C.wid * 1024);
}
DI void warm(i32x4 srd, unsigned line0, unsigned lines_per_row_log2, unsigned pitch, unsigned colbyte, int lane) {
  const unsigned l = line0 + lane;
  dma4(srd, (l >> lines_per_row_log2) * pitch + colbyte + (l & ((1u << lines_per_row_log2) - 1)) * 128, L_JUNK);
}
DI unsigned g_voff(const Ctx& C) { const int lane = fresh(C.lane); return (32 * (C.wid & 1) + (lane >> 3)) * 1024 + C.cg * 128 + (((lane & 7) ^ (((lane >> 4) & 1) << 2)) << 4); }
constexpr unsigned gslot(int j) { return j == 0 ? R1 : j == 1 ? R2 : j == 2 ? R0 : j == 3 ? R3 : j == 4 ? R1 : j == 5 ? R2 : j == 6 ? R0 : j == 7 ? R3 : R1; }
DI void issue_g(const Ctx& C, int j, int i, unsigned v) {
  const int kq = C.wid >> 1;
  const int row0 = j < 4 ? (4 * kq + j) * 64 : j < 7 ? (16 + 3 * kq + (j - 4)) * 64 : 128 * kq + 64 * (j - 7);
  dma16(j < 7 ? C.sWD : C.sWO, v, (unsigned)(row0 + 8 * i) * 1024, gslot(j) + kq * 8192 + (C.wid & 1) * 4096 + i * 1024);
}

template <int TYPE> DI void epi_qkv(const Ctx& C, const f32x16& acc, const f32x4 (&csr)[4], float m, float rinv, int qu, int h4) {
  const int h = C.lane >> 5;
  const float sc = TYPE == 0 ? rinv * QSCALE : rinv;
  float y[16];
#pragma unroll
  for (int i4 = 0; i4 < 4; ++i4)
#pragma unroll
    for (int e = 0; e < 4; ++e) y[4 * i4 + e] = (acc[4 * i4 + e] - m * csr[i4][e]) * sc;
  const unsigned base = (unsigned)WS_QKV + (((C.rb * 24 + qu) * 4 + C.mg) * 4 + h4) * 1024;
#pragma unroll
  for (int hh = 0; hh < 2; ++hh) {
    u32x4 f; f[0] = cvtpk(y[8 * hh], y[8 * hh + 1]); f[1] = cvtpk(y[8 * hh + 2], y[8 * hh + 3]); f[2] = cvtpk(y[8 * hh + 4], y[8 * hh + 5]); f[3] = cvtpk(y[8 * hh + 6], y[8 * hh + 7]);
    unsigned voff = C.lane * 16;
    if (TYPE == 1 && qu >= 16) {
      auto a = __builtin_amdgcn_permlane32_swap(f[0], f[2], false, false); auto b = __builtin_amdgcn_permlane32_swap(f[1], f[3], false, false);
      f[0] = a[0]; f[2] = a[1]; f[1] = b[0]; f[3] = b[1];
      voff = (C.lane & 31) * 32 + h * 16;
    }
    st16_sc1(f, C.sWS, voff, base + hh * 1024);
  }
}
DI void epi_gu(const Ctx& C, const f32x16& acc, const f32x4 (&csr)[4], float m, float r2, unsigned a2off) {
  float y[8];
#pragma unroll
  for (int i4 = 0; i4 < 2; ++i4)
#pragma unroll
    for (int e = 0; e < 4; ++e) y[4 * i4 + e] = ((acc[4 * i4 + e] - m * csr[i4][e]) * r2) * (acc[8 + 4 * i4 + e] - m * csr[2 + i4][e]);
  u32x4 f; f[0] = cvtpk(y[0], y[1]); f[1] = cvtpk(y[2], y[3]); f[2] = cvtpk(y[4], y[5]); f[3] = cvtpk(y[6], y[7]);
  st16_sc1(f, C.sWS, C.lane * 16, a2off);
}
template <int TYPE> DI unsigned a2_pass(const Ctx& C, int p) {
  if (p == 1) return (unsigned)WS_A2 + (((4 * C.rb + C.mg) * 4 + (C.cs >> 2)) * 36 + 4 * (C.cs & 3) + 2 * C.ng) * 1024;
  if (TYPE == 0) return (unsigned)WS_A2 + (((4 * C.rb + C.mg) * 4 + C.cs / 3) * 36 + 4 * (4 + C.cs % 3) + 2 * C.ng) * 1024;
  const int u = 8 + ((C.cs - 8) >> 1), half = (C.cs - 8) & 1;
  return (unsigned)WS_A2 + (((4 * C.rb + C.mg) * 4 + u / 3) * 36 + 4 * (4 + u % 3) + 2 * half + C.ng) * 1024;
}
DI unsigned a2_o(const Ctx& C) { return (unsigned)WS_A2 + (((4 * C.rb + C.mg) * 4 + (C.cs >> 2)) * 36 + 28 + 2 * (C.cs & 3) + C.ng) * 1024; }

template <int TYPE> DI void gemm1(Ctx& C, unsigned vx, unsigned vw, u32x4& qfrag, u32x2& poll2, unsigned poll2off) {
  using G = G1<TYPE>;
  const int lane = C.lane;
  char* ws = C.ws;
  bf16x8 xa[32];
  bf16x8 wf[3][2];
  f32x16 acc[2];
  unsigned ta[2], sv = 0;
  ta[0] = tile_addr<TYPE>(C, 0, 0); ta[1] = tile_addr<TYPE>(C, 0, 1);
  const unsigned r31 = lane & 31, hh = lane >> 5;
  const unsigned xlane = (32 * C.mg + r31) * 128 + ((hh ^ ((r31 >> 1) & 7)) << 4);
  f32x4 ssum = {0, 0, 0, 0}, ssq = {0, 0, 0, 0};
  f32x4 csacc[2] = {{0,0,0,0},{0,0,0,0}};
  bf16x8 sel;
  { const short one = ((lane & 15) == ((lane >> 4) & 1)) ? (short)0x3f80 : (short)0;
#pragma unroll
    for (int e = 0; e < 8; ++e) sel[e] = one; }
  float csn[2];
  f32x4 csr[2][4];
  float m = 0.f, rinv = 0.f;
  u32x2 pollv; u32x4 gk[3];
  unsigned polloff = 0; bool pollneed = false;

  STAMP(1);
  bar_vm<G::gsize(T_H0)>();
  STAMP(2);
  u32x2 ev; ld8_sc1(ev, C.sWS, (unsigned)WS_EPOCH);
  auto read_x = [&](int h) {
    const unsigned base = (h & 3) * 32768 + xlane;
#pragma unroll
    for (int i = 0; i < 4; ++i) xa[4 * h + i] = ld128(base ^ (((i + C.c) & 3) << 5));
  };
  auto read_w = [&](int pos) {
    const int p = pos >> 5, s = pos >> 3, i = pos & 7;
    const bool half = G::kind(p) == 3;
    if ((pos & 31) == 0 && pos > 0) { ta[0] = tile_addr<TYPE>(C, p, 0); ta[1] = tile_addr<TYPE>(C, p, 1); }
    const unsigned stride = half ? 1024 : 2048, second = half ? 256 : 512;
    const unsigned so = (p == 0 ? ((pos >> 2) & 3) * 32768 : G::slot(s) + (i & 4) * stride) + ((i + C.c) & 3) * stride;
    wf[pos % 3][0] = ldfrag_tr(pinv(ta[0] + so), second);
    if (G::nt(p) == 2) wf[pos % 3][1] = ldfrag_tr(pinv(ta[1] + so), second);
  };
  read_x(0); read_w(0); read_w(1);
  static_for<0, 96>([&](auto ic) {
    constexpr int pos = decltype(ic)::value;
    constexpr int p = pos >> 5, s = pos >> 3, st = s & 3, i = pos & 7, NT = G::nt(p), bi = G::barat(pos);
    if constexpr (pos < 2) static_for<0, G::gsize(T_H0)>([&](auto kc) { issue_half(C, 2 + pos, decltype(kc)::value, vx, vw); });
    if constexpr (bi >= 0) {
      STAMP(10 + 2 * bi);
      if constexpr (bi == 0) { bar_vm<G::after(T_H0 + 1, 0)>(); }
      else if constexpr (bi < 7) bar_vm<G::after(T_H0 + bi + 1, bi)>();
      else if constexpr (bi == 9) { bar_vm<G::mn(G::after(T_S4 + 2, 9), G::mn(G::after(T_E0, 9), G::after(T_POISON, 9)))>(); publish(C, 0); }
      else if constexpr (bi == 13) { bar_vm<G::mn(G::mn(G::after(T_S4 + 6, 13), G::after(T_E0 + 1, 13)), G::after(T_POLL, 13))>(); }
      else if constexpr (bi == 14) bar_vm<G::mn(G::after(T_S4 + 7, 14), G::after(T_KL, 14))>();
      else if constexpr (bi == 15) bar_vm<G::after(T_VL, 15)>();
      else bar_vm<G::after(T_S4 + bi - 7, bi)>();
      if constexpr (bi == 4) {
        static_assert(G::after(T_H0 + 5, 4) <= G::after(T_EPOCH, 4), "");
        pin(ev);
        const u64 t = (((u64)ev[1] << 32) | ev[0]) * 6364136223846793005ull + 1442695040888963407ull;
        C.tag = ((u64)__builtin_amdgcn_readfirstlane((unsigned)(t >> 32)) << 32) | __builtin_amdgcn_readfirstlane((unsigned)t);
      }
      if constexpr (bi == 12) {
        const int u = C.cs >> 1, l = lane & 7;
        const int ty = l == 0 ? 0 : l < 4 ? 1 : 2, rbp = l == 0 ? C.rb : C.rb - 2 + (l - 1) % 3;
        const int qu = 8 * ty + u;
        const int pcs = qu < 8 ? qu : 8 + ((qu - 8) >> 1);
        pollneed = l < 7 && rbp >= 0;
        polloff = flag_off(0, pollneed ? rbp : C.rb, pcs);
        ld8_sc1(pollv, C.sWS, polloff);
      }
      if constexpr (bi == 13) {
        check_flags(ws, polloff, pollneed, C.tag, pollv);
        const int u = C.cs >> 1, h4 = 2 * (C.cs & 1) + C.ng;
#pragma unroll
        for (int b = 0; b < 3; ++b) { const int rbp = C.rb - 2 + b < 0 ? 0 : C.rb - 2 + b;
          ld16_sc1(gk[b], C.sWS, lane * 16, (unsigned)WS_QKV + (((rbp * 24 + 8 + u) * 4 + C.mg) * 4 + h4) * 1024); }
        publish(C, 1);
      }
      if constexpr (bi == 14) {
        pin(gk[0]); pin(gk[1]); pin(gk[2]);
#pragma unroll
        for (int b = 0; b < 3; ++b) *lp<u32x4>(L_K + ((4 * b + C.mg) * 2 + C.ng) * 1024 + lane * 16) = gk[b];
        const int u = C.cs >> 1, h4 = 2 * (C.cs & 1) + C.ng;
#pragma unroll
        for (int b = 0; b < 3; ++b) { const int rbp = C.rb - 2 + b < 0 ? 0 : C.rb - 2 + b;
          ld16_sc1(gk[b], C.sWS, lane * 16, (unsigned)WS_QKV + (((rbp * 24 + 16 + u) * 4 + C.mg) * 4 + h4) * 1024); }
        ld16_sc1(qfrag, C.sWS, lane * 16, (unsigned)WS_QKV + (((C.rb * 24 + u) * 4 + C.mg) * 4 + h4) * 1024);
      }
      if constexpr (bi == 15) {
        pin(qfrag); pin(gk[0]); pin(gk[1]); pin(gk[2]);
#pragma unroll
        for (int b = 0; b < 3; ++b) *lp<u32x4>(L_V + C.ng * 12288 + (4 * b + C.mg) * 1024 + lane * 16) = gk[b];
        ld8_sc1(poll2, C.sWS, poll2off);
      }
      if constexpr (bi < 7) read_x(bi + 1);
      if constexpr (st == 3 && i == 6) {
#pragma unroll
        for (int t = 0; t < NT; ++t) { const unsigned a = L_CSP + (C.ng * 2 + t) * 512 + r31 * 4;
          csn[t] = (*lp<float>(a) + *lp<float>(a + 128)) + (*lp<float>(a + 256) + *lp<float>(a + 384)); }
      }
      STAMP(11 + 2 * bi);
    }
    static_for<0, 16>([&](auto bc) {
      constexpr int bb = decltype(bc)::value, tg = G::grp(bb), k0 = pos - G::barpos(bb);
      static_for<0, 4>([&](auto kc) {
        constexpr int k = decltype(kc)::value;
        if constexpr (k < G::gsize(tg) && G::rel(bb, k) == k0) {
          if constexpr (tg < T_S4) issue_half(C, tg, k, vx, vw);
          else if constexpr (tg >= T_G0) { if constexpr (k == 0) sv = g_voff(C); issue_g(C, tg - T_G0, k, sv); }
          else { if constexpr (k == 0) sv = stage_voff<TYPE>(C, (tg - T_S4 + 4) >> 2); issue_stage<TYPE>(C, tg - T_S4 + 4, k, sv); }
        }
      });
    });
    if constexpr (pos == 12 && !NO_WARM) {
      const int gu = TYPE == 0 ? 16 + C.cs : 24 + ((C.cs - 8) >> 1);
      const unsigned l0 = (C.rb & 7) * 64;
      if (C.wid == 5) { warm(C.sG, l0, 0, 3584, gu * 128, fresh(lane)); warm(C.sU, l0, 0, 3584, gu * 128, fresh(lane)); }
    }
    if constexpr (pos >= 34 && pos < 52 && (pos & 1) == 0 && !NO_WARM) {
      constexpr int k = (pos - 34) >> 1;
      const unsigned l = ((C.xi * 9 + k) * 64);
      if (C.wid == (k & 7)) { if (l < 4096) warm(C.sWO, l, 3, 1024, 0, fresh(lane)); else warm(C.sWD, l - 4096, 3, 1024, 0, fresh(lane)); }
    }
    if constexpr (pos == 16) {
      const u32x4 pz = {POISON, POISON, POISON, POISON};
      const unsigned a = a2_pass<TYPE>(C, 2);
      st16_sc1(pz, C.sWS, lane * 16, a);
      if (TYPE == 0) st16_sc1(pz, C.sWS, lane * 16, a + 1024);
      st16_sc1(pz, C.sWS, lane * 16, a2_o(C));
    }
    if constexpr (pos + 2 < 96) read_w(pos + 2);
    if constexpr ((pos & 31) == 0) { acc[0] = MFMA32(wf[pos % 3][0], xa[pos & 31], f32x16{}); if (NT == 2) acc[1] = MFMA32(wf[pos % 3][1], xa[pos & 31], f32x16{}); }
    else { acc[0] = MFMA32(wf[pos % 3][0], xa[pos & 31], acc[0]); if (NT == 2) acc[1] = MFMA32(wf[pos % 3][1], xa[pos & 31], acc[1]); }
    if constexpr ((i & 3) == 0 && !NO_CS) {
      if constexpr ((pos & 31) == 0) { csacc[0] = MFMA16(wf[pos % 3][0], sel, (f32x4{0, 0, 0, 0})); if (NT == 2) csacc[1] = MFMA16(wf[pos % 3][1], sel, (f32x4{0, 0, 0, 0})); }
      else { csacc[0] = MFMA16(wf[pos % 3][0], sel, csacc[0]); if (NT == 2) csacc[1] = MFMA16(wf[pos % 3][1], sel, csacc[1]); }
    }
    if constexpr (p == 0 && (i & 3) < 2) {
      const bf16x8 xv = xa[pos & 31]; const s16x4 lo = __builtin_shufflevector(xv, xv, 0, 1, 2, 3), hi = __builtin_shufflevector(xv, xv, 4, 5, 6, 7);
      const s16x4 ones = {0x3f80, 0x3f80, 0x3f80, 0x3f80};
      ssum = MFMA4(ones, lo, ssum); ssq = MFMA4(lo, lo, ssq); ssum = MFMA4(ones, hi, ssum); ssq = MFMA4(hi, hi, ssq);
    }
    if constexpr (st == 3 && i == 5) {
      if ((lane & 15) < 2) {
#pragma unroll
        for (int t = 0; t < NT; ++t) *lp<f32x4>(L_CSP + ((C.ng * 2 + t) * 4 + C.mg) * 128 + (16 * (lane & 15) + 4 * (lane >> 4)) * 4) = csacc[t];
      }
      if constexpr (p == 0) {
        const float sq = (lane & 3) == 0 ? ssq[0] : (lane & 3) == 1 ? ssq[1] : (lane & 3) == 2 ? ssq[2] : ssq[3];
        const f32x2 v = {swap_add(ssum[0]), swap_add(sq)};
        if (lane < 32) *lp<f32x2>(L_STAT + (C.ng * 128 + 32 * C.mg + lane) * 8) = v;
      }
    }
    if constexpr (st == 3 && i == 6) {
#pragma unroll
      for (int t = 0; t < NT; ++t) *lp<float>(L_CSW + (C.wid * 2 + t) * 128 + r31 * 4) = csn[t];
    }
    if constexpr (st == 3 && i == 7) {
#pragma unroll
      for (int t = 0; t < NT; ++t)
#pragma unroll
        for (int i4 = 0; i4 < 4; ++i4) csr[t][i4] = *lp<f32x4>(L_CSW + (C.wid * 2 + t) * 128 + (8 * i4 + 4 * hh) * 4);
    }
    SB();
    if constexpr ((pos & 31) == 31) {
      if constexpr (p == 0) {
        const f32x2 a = *lp<f32x2>(L_STAT + (32 * C.mg + r31) * 8), b = *lp<f32x2>(L_STAT + (128 + 32 * C.mg + r31) * 8);
        const float mean = (a[0] + b[0]) * (1.f / 512.f);
        const float var = __builtin_fmaxf((a[1] + b[1]) * (1.f / 512.f) - mean * mean, 0.f);
        m = rbf(mean);
        rinv = __builtin_amdgcn_rcpf(rbf(__builtin_amdgcn_sqrtf(rbf(rbf(var) + 1e-5f))));
      }
      if constexpr (G::kind(p) == 2) {
        const unsigned a2 = a2_pass<TYPE>(C, p);
        epi_gu(C, acc[0], csr[0], m, rinv * rinv, a2); epi_gu(C, acc[1], csr[1], m, rinv * rinv, a2 + 1024);
      } else if constexpr (G::kind(p) == 3) {
        epi_gu(C, acc[0], csr[0], m, rinv * rinv, a2_pass<TYPE>(C, p));
      } else if constexpr (G::kind(p) == 0) {
        epi_qkv<0>(C, acc[0], csr[0], m, rinv, C.cs, 2 * C.ng);
      } else {
        const int qu = 8 + 2 * (C.cs - 8) + C.ng;
        epi_qkv<1>(C, acc[0], csr[0], m, rinv, qu, 0); epi_qkv<1>(C, acc[1], csr[1], m, rinv, qu, 2);
      }
      SB();
    }
  });
  wait_vm<G::after(T_POLL2, 16)>();
}

template <bool EDGE, class SIDE> DI u32x4 attention(Ctx& C, const u32x4& qraw, SIDE&& side) {
  const int lane = C.lane, h = lane >> 5, g = (lane >> 4) & 1, q = (lane & 15) >> 2, pp = lane & 3;
  const bf16x8 qf = __builtin_bit_cast(bf16x8, qraw);
  const unsigned kaddr = pinv(L_K + (C.mg * 2 + C.ng) * 1024 + lane * 16);
  const int t0 = EDGE ? (C.rb == 0 ? 8 : 4) - C.mg : 1;
  const int dq = (lane & 31) - 4 * h;
  const float NEG = -1e30f;
  auto older = [&](int r) { return ((r & 3) + 8 * (r >> 2)) > dq; };
  auto max16 = [&](float& a, float& b, const f32x16& s) {
#pragma unroll
    for (int r = 0; r < 16; r += 4) { a = __builtin_fmaxf(__builtin_fmaxf(a, s[r]), s[r + 1]); b = __builtin_fmaxf(__builtin_fmaxf(b, s[r + 2]), s[r + 3]); } };
  bf16x8 kf[9];
#pragma unroll
  for (int t = 0; t < 9; ++t) if (!EDGE || t > 0) kf[t] = ld128(kaddr + t * 2048);
  float mx;
  {
    f32x16 S[3];
    f32x16 sl = MFMA32(kf[8], qf, f32x16{});
    f32x16 sf;
    if (!EDGE) sf = MFMA32(kf[0], qf, f32x16{});
    S[1] = MFMA32(kf[1], qf, f32x16{});
    S[2] = MFMA32(kf[2], qf, f32x16{});
    float a = NEG, b = NEG;
    side(std::integral_constant<int, 0>{});
    SB();
#pragma unroll
    for (int r = 0; r < 16; ++r) sl[r] = older(r) ? (EDGE ? NEG : sf[r]) : sl[r];
    max16(a, b, sl);
    static_for<1, 8>([&](auto tc) { constexpr int t = decltype(tc)::value;
      if constexpr (t + 2 < 8) S[(t + 2) % 3] = MFMA32(kf[t + 2], qf, f32x16{});
      side(std::integral_constant<int, t>{});
      SB();
      if (!EDGE || t >= t0) max16(a, b, S[t % 3]);
      SB(); });
    mx = swap_max(__builtin_fmaxf(a, b));
  }
  f32x16 negm;
#pragma unroll
  for (int r = 0; r < 16; ++r) negm[r] = -mx;
  const unsigned vbase = pinv(g == 0 ? L_V + C.ng * 12288 + C.mg * 1024 + (4 * h + q) * 32 + pp * 8 : L_ONES + 128 * (1 - h) + q * 32 + pp * 8);
  const unsigned vstr = g == 0 ? 1024u : 0u;
  f32x16 o;
  auto expack = [&](const f32x16& s, u32x4& pa, u32x4& pb) {
#pragma unroll
    for (int i = 0; i < 4; ++i) pa[i] = cvtpk(__builtin_amdgcn_exp2f(s[2 * i]), __builtin_amdgcn_exp2f(s[2 * i + 1]));
#pragma unroll
    for (int i = 0; i < 4; ++i) pb[i] = cvtpk(__builtin_amdgcn_exp2f(s[8 + 2 * i]), __builtin_amdgcn_exp2f(s[9 + 2 * i])); };
  f32x16 S[2];
  bf16x8 vf[2][2];
  {
    f32x16 sl = MFMA32(kf[8], qf, negm);
    f32x16 sf;
    if (!EDGE) sf = MFMA32(kf[0], qf, negm);
    S[1] = MFMA32(kf[1], qf, negm);
    const unsigned v8 = vbase + 8 * vstr;
    const bf16x8 va = ldfrag_tr(v8, 256), vb = ldfrag_tr(v8 + 512, 256);
    bf16x8 vc, vd;
    if (!EDGE) { vc = ldfrag_tr(vbase, 256); vd = ldfrag_tr(vbase + 512, 256); }
    vf[1][0] = ldfrag_tr(vbase + vstr, 256); vf[1][1] = ldfrag_tr(vbase + vstr + 512, 256);
    side(std::integral_constant<int, 8>{});
    SB();
#pragma unroll
    for (int r = 0; r < 16; ++r) sl[r] = older(r) ? (EDGE ? NEG : sf[r]) : sl[r];
    u32x4 pa, pb, mk0, mk1;
    expack(sl, pa, pb);
#pragma unroll
    for (int i = 0; i < 4; ++i) {
      mk0[i] = (older(2 * i) ? 0xffffu : 0u) | (older(2 * i + 1) ? 0xffff0000u : 0u);
      mk1[i] = (older(8 + 2 * i) ? 0xffffu : 0u) | (older(9 + 2 * i) ? 0xffff0000u : 0u); }
    o = MFMA32(va, __builtin_bit_cast(bf16x8, pa & ~mk0), f32x16{});
    o = MFMA32(vb, __builtin_bit_cast(bf16x8, pb & ~mk1), o);
    if (!EDGE) {
      o = MFMA32(vc, __builtin_bit_cast(bf16x8, pa & mk0), o);
      o = MFMA32(vd, __builtin_bit_cast(bf16x8, pb & mk1), o);
    }
    SB();
  }
  static_for<1, 8>([&](auto tc) { constexpr int t = decltype(tc)::value;
    if constexpr (t + 1 < 8) {
      const unsigned vt = vbase + (t + 1) * vstr;
      vf[(t + 1) & 1][0] = ldfrag_tr(vt, 256); vf[(t + 1) & 1][1] = ldfrag_tr(vt + 512, 256);
      S[(t + 1) & 1] = MFMA32(kf[t + 1], qf, negm); }
    side(std::integral_constant<int, 8 + t>{});
    SB();
    if (!EDGE || t >= t0) {
      u32x4 pa, pb;
      expack(S[t & 1], pa, pb);
      o = MFMA32(vf[t & 1][0], __builtin_bit_cast(bf16x8, pa), o);
      o = MFMA32(vf[t & 1][1], __builtin_bit_cast(bf16x8, pb), o);
    }
    SB(); });
  const float li = __builtin_amdgcn_rcpf(o[8]);
  u32x4 f; f[0] = cvtpk(o[0] * li, o[1] * li); f[1] = cvtpk(o[2] * li, o[3] * li); f[2] = cvtpk(o[4] * li, o[5] * li); f[3] = cvtpk(o[6] * li, o[7] * li);
  return f;
}

__global__ void __launch_bounds__(512, 2)
fused_kernel(const u16* __restrict__ x, const u16* __restrict__ wqkv, const u16* __restrict__ wo, const u16* __restrict__ wg, const u16* __restrict__ wu,
             const u16* __restrict__ wd, u16* __restrict__ out, char* __restrict__ ws) {
  Ctx C;
  const int tid = threadIdx.x, lane = tid & 63, wid = __builtin_amdgcn_readfirstlane(tid >> 6);
  const int b = blockIdx.x, xj = b & 7, xi = b >> 3;
  C.stampreg = 0;
  STAMP(0);
  const u64 prt0 = __builtin_amdgcn_s_memrealtime();
  asm volatile("" :: "s"(x), "s"(wqkv), "s"(wo), "s"(wg), "s"(wu), "s"(wd), "s"(out), "s"(ws));
  C.ws = ws; C.lane = lane; C.wid = wid; C.mg = wid & 3; C.ng = wid >> 2;
  C.rb = 8 * (xj & 1) + (xi & 7); C.cs = 4 * (xj >> 1) + (xi >> 3); C.c = (C.mg + 2 * C.ng) & 3;
  C.sX = mk_srd(x + (size_t)C.rb * 128 * 512, 128 * 1024);
  C.sQKVw = mk_srd(wqkv, 512 * 3072);
  C.xi = xi;
  const unsigned vx = hx_voff(C), vw = hw_voff(C);
  STAMP(3);
  issue_half(C, 0, 0, vx, vw); issue_half(C, 0, 1, vx, vw); issue_half(C, 0, 2, vx, vw); if (C.cs >= 8) issue_half(C, 0, 3, vx, vw);
  STAMP(4);
  issue_half(C, 1, 0, vx, vw); issue_half(C, 1, 1, vx, vw); issue_half(C, 1, 2, vx, vw); if (C.cs >= 8) issue_half(C, 1, 3, vx, vw);
  STAMP(5);
  C.sWS = mk_srd(ws, 32u << 20);
  C.sG = mk_srd(wg, 512 * 3584); C.sU = mk_srd(wu, 512 * 3584); C.sWO = mk_srd(wo, 512 * 1024); C.sWD = mk_srd(wd, 1792 * 1024);
  if (!NO_WARM) {
    if (wid < 3) { const unsigned l = (C.cs & 3) * 192 + wid * 64 + lane; dma4(C.sX, (l / 6) * 1024 + (2 + l % 6) * 128, L_JUNK); }
    else if (wid < 5) { if (wid == 3 || C.cs >= 8) dma4(C.sQKVw, (128 + (C.rb & 7) * 48 + (lane < 48 ? lane : 0)) * 3072 + ((C.cs < 8 ? C.cs : 8 + 2 * (C.cs - 8)) + (wid - 3)) * 128, L_JUNK); }
    else if (wid < 7) dma4(wid == 5 ? C.sG : C.sU, ((C.rb & 7) * 64 + lane) * 3584 + C.cs * 128, L_JUNK);
    else { u64 pc; asm volatile("s_getpc_b64 %0" : "=s"(pc));
      dma4(mk_srd((const void*)(pc & ~255ull), CODE_BYTES), (xi * 9 + (lane < 9 ? lane : 0)) * 128, L_JUNK); }
  }
  if (tid < 256) lp<unsigned>(L_ONES)[tid] = 0x3f803f80u;
  const int rt64 = 4 * xj + (xi & 3), cg = xi >> 2;
  C.cg = cg;
  const int rt = wid & 1, kq = wid >> 1, RT = 2 * rt64 + rt, rb2 = RT >> 2;
  const bool p2need = lane < 10;
  unsigned p2off;
  { const int l = lane < 10 ? lane : 0, u = 3 * kq + ((l - 4) >> 1);
    p2off = l < 4 ? flag_off(1, rb2, 4 * kq + l) : flag_off(0, rb2, u < 8 ? u : 8 + 2 * (u - 8) + (l & 1)); }
  u32x4 qfrag; u32x2 poll2;
  if (C.cs < 8) gemm1<0>(C, vx, vw, qfrag, poll2, p2off); else gemm1<1>(C, vx, vw, qfrag, poll2, p2off);
  STAMP(6);

  const unsigned a2base = (unsigned)WS_A2 + (RT * 4 + kq) * 36 * 1024;
  u32x4 a2[36];
  check_flags(ws, p2off, p2need, C.tag, poll2);
  asm volatile("s_waitcnt lgkmcnt(0)\n\ts_barrier" ::: "memory");
  auto side = [&](auto kc) { constexpr int k = decltype(kc)::value;
    if constexpr (k < 8) { ld16_sc1(a2[2 * k], C.sWS, lane * 16, a2base + 2 * k * 1024); ld16_sc1(a2[2 * k + 1], C.sWS, lane * 16, a2base + (2 * k + 1) * 1024); }
    if constexpr (k >= 12) static_for<0, 3>([&](auto jc) { constexpr int q_ = 16 + 3 * (k - 12) + decltype(jc)::value; ld16_sc1(a2[q_], C.sWS, lane * 16, a2base + q_ * 1024); }); };
  u32x4 of;
  if (C.rb >= 2) of = attention<false>(C, qfrag, side); else of = attention<true>(C, qfrag, side);
  st16_sc1(of, C.sWS, lane * 16, a2_o(C));
  STAMP(7);

  const int g = (lane >> 4) & 1, h = lane >> 5, q = (lane & 15) >> 2, pp = lane & 3;
  const unsigned w2l0 = kq * 8192 + (4 * h + q) * 128 + ((0 ^ (q >> 1)) << 6) + g * 32 + pp * 8;
  const unsigned w2l1 = kq * 8192 + (4 * h + q) * 128 + ((1 ^ (q >> 1)) << 6) + g * 32 + pp * 8;
  bf16x8 wf[3][2];
  f32x16 acc[2];
  u32x4 xres;
  unsigned wb[2], gv;
  auto read_w2 = [&](int pos) {
    if ((pos & 3) == 0) { wb[0] = pinv(w2l0 + gslot(pos >> 2)); wb[1] = pinv(w2l1 + gslot(pos >> 2)); }
    wf[pos % 3][0] = ldfrag_tr(wb[0] + (pos & 3) * 2048, 1024); wf[pos % 3][1] = ldfrag_tr(wb[1] + (pos & 3) * 2048, 1024); };
  bar_vm<13>();
  static_for<0, 16>([&](auto kc) { constexpr int k = decltype(kc)::value; pin(a2[k]); });
  read_w2(0); read_w2(1);
  gv = g_voff(C);
  STAMP(8);
  u32x4 ef[4];
  static_for<0, 36>([&](auto ic) {
    constexpr int pos = decltype(ic)::value;
    constexpr int j = pos >> 2, i = pos & 3;
    if constexpr (i == 2) {
      STAMP(44 + 2 * j);
      if constexpr (j <= 1) bar_vm<63>();
      if constexpr (j == 2) bar_vm<6>();
      if constexpr (j == 3) bar_vm<6>();
      if constexpr (j == 4) bar_vm<7>();
      if constexpr (j == 5) bar_vm<15>();
      if constexpr (j == 6) bar_vm<4>();
      if constexpr (j == 7) bar_vm<0>();
      STAMP(45 + 2 * j);
    }
    if constexpr (pos == 14) { static_for<16, 28>([&](auto kc) { pin(a2[decltype(kc)::value]); }); check_frags<16, 28>(C, a2, a2base); }
    if constexpr (pos == 26) { static_for<28, 36>([&](auto kc) { pin(a2[decltype(kc)::value]); }); pin(xres); check_frags<28, 36>(C, a2, a2base); }
    if constexpr (pos == 16) { const i32x4 sXall = mk_srd(x, 2048 * 1024); ld16(xres, sXall, (RT * 32 + (lane & 31)) * 1024 + cg * 128 + kq * 32 + h * 16, 0); }
    if constexpr (pos == 18 || pos == 19) static_for<0, 4>([&](auto kc) { constexpr int k = 28 + 4 * (pos - 18) + decltype(kc)::value; ld16_sc1(a2[k], C.sWS, lane * 16, a2base + k * 1024); });
    if constexpr (pos < 24) issue_g(C, 3 + (pos >> 2), pos & 3, gv);
    if constexpr (pos == 30) { if (b == 0 && wid == 7) {
#pragma unroll
        for (int k = 0; k < 4; ++k) { u32x2 t; ld8_sc1(t, C.sWS, flag_off(0, 0, 0) + (lane * 4 + k) * 128); ef[k][0] = t[0]; ef[k][1] = t[1]; } } }
    if constexpr (pos + 2 < 36) read_w2(pos + 2);
    const bf16x8 af = __builtin_bit_cast(bf16x8, a2[pos]);
    if constexpr (pos == 0) { acc[0] = MFMA32(wf[0][0], af, f32x16{}); acc[1] = MFMA32(wf[0][1], af, f32x16{}); }
    else { acc[0] = MFMA32(wf[pos % 3][0], af, acc[0]); acc[1] = MFMA32(wf[pos % 3][1], af, acc[1]); }
    SB();
  });
  STAMP(9);
#pragma unroll
  for (int d = 0; d < 4; ++d) if (d != kq) {
    const int ct = d >> 1, r0 = (d & 1) * 8;
    const unsigned a = R2 + ((rt * 4 + d) * 4 + kq) * 2048 + lane * 16;
    *lp<f32x4>(a) = f32x4{acc[ct][r0], acc[ct][r0 + 1], acc[ct][r0 + 2], acc[ct][r0 + 3]};
    *lp<f32x4>(a + 1024) = f32x4{acc[ct][r0 + 4], acc[ct][r0 + 5], acc[ct][r0 + 6], acc[ct][r0 + 7]};
  }
  float s[8];
#pragma unroll
  for (int e = 0; e < 8; ++e) { const float lo = (kq & 1) ? acc[0][8 + e] : acc[0][e], hi = (kq & 1) ? acc[1][8 + e] : acc[1][e]; s[e] = (kq & 2) ? hi : lo; }
  asm volatile("s_waitcnt lgkmcnt(0)\n\ts_barrier" ::: "memory");
#pragma unroll
  for (int d = 0; d < 4; ++d) if (d != kq) {
    const unsigned a = R2 + ((rt * 4 + kq) * 4 + d) * 2048 + lane * 16;
    const f32x4 u = *lp<f32x4>(a), v = *lp<f32x4>(a + 1024);
#pragma unroll
    for (int e = 0; e < 4; ++e) { s[e] += u[e]; s[4 + e] += v[e]; }
  }
  {
    unsigned u0 = __float_as_uint(s[0]), u1 = __float_as_uint(s[1]), u2 = __float_as_uint(s[2]), u3 = __float_as_uint(s[3]);
    unsigned v0 = __float_as_uint(s[4]), v1 = __float_as_uint(s[5]), v2 = __float_as_uint(s[6]), v3 = __float_as_uint(s[7]);
    auto r0 = __builtin_amdgcn_permlane32_swap(u0, v0, false, false); auto r1 = __builtin_amdgcn_permlane32_swap(u1, v1, false, false);
    auto r2 = __builtin_amdgcn_permlane32_swap(u2, v2, false, false); auto r3 = __builtin_amdgcn_permlane32_swap(u3, v3, false, false);
    const float c0 = __uint_as_float(r0[0]), c1 = __uint_as_float(r1[0]), c2 = __uint_as_float(r2[0]), c3 = __uint_as_float(r3[0]);
    const float c4 = __uint_as_float(r0[1]), c5 = __uint_as_float(r1[1]), c6 = __uint_as_float(r2[1]), c7 = __uint_as_float(r3[1]);
    u32x4 f;
    f[0] = cvtpk(c0 + bflo(xres[0]), c1 + bfhi(xres[0])); f[1] = cvtpk(c2 + bflo(xres[1]), c3 + bfhi(xres[1]));
    f[2] = cvtpk(c4 + bflo(xres[2]), c5 + bfhi(xres[2])); f[3] = cvtpk(c6 + bflo(xres[3]), c7 + bfhi(xres[3]));
    const i32x4 sO = mk_srd(out, 2048 * 1024);
    st16(f, sO, (RT * 32 + (lane & 31)) * 1024 + cg * 128 + kq * 32 + h * 16, 0);
  }
  if (b == 0 && wid == 7) {
    wait_vm<0>();
    asm volatile("" : "+v"(ef[0]), "+v"(ef[1]), "+v"(ef[2]), "+v"(ef[3]));
    bool ok = true;
#pragma unroll
    for (int k = 0; k < 4; ++k) ok = ok && ef[k][0] == (unsigned)C.tag && ef[k][1] == (unsigned)(C.tag >> 32);
    if (!__all(ok)) {
#pragma unroll 1
      for (int k = 0; k < 4; ++k) spin_flags(ws, flag_off(0, 0, 0) + (lane * 4 + k) * 128, true, C.tag);
    }
    if (lane == 0) __hip_atomic_store((u64*)(ws + WS_EPOCH), C.tag, __ATOMIC_RELAXED, __HIP_MEMORY_SCOPE_AGENT);
  }
  STAMP(60);
  { const u64 e = __builtin_amdgcn_s_memrealtime();
    if (wid == 0 && lane == 0) { __hip_atomic_store((u64*)(ws + WS_DBG + b * 16), ((u64)(unsigned)C.tag << 32) | (unsigned)prt0, __ATOMIC_RELAXED, __HIP_MEMORY_SCOPE_AGENT);
                                 __hip_atomic_store((u64*)(ws + WS_DBG + b * 16 + 8), ((u64)(unsigned)C.tag << 32) | (unsigned)e, __ATOMIC_RELAXED, __HIP_MEMORY_SCOPE_AGENT); }
    if (b == 0 && wid == 0) {
      unsigned mn = 0xffffffffu, mx = 0, mxs = 0;
      for (int k = 0; k < 4; ++k) { const int w = lane * 4 + k; u64 s0, s1;
        for (int it = 0; it < 100000; ++it) { s0 = __hip_atomic_load((u64*)(ws + WS_DBG + w * 16), __ATOMIC_RELAXED, __HIP_MEMORY_SCOPE_AGENT); s1 = __hip_atomic_load((u64*)(ws + WS_DBG + w * 16 + 8), __ATOMIC_RELAXED, __HIP_MEMORY_SCOPE_AGENT);
          if ((unsigned)(s0 >> 32) == (unsigned)C.tag && (unsigned)(s1 >> 32) == (unsigned)C.tag) break; }
        mn = min(mn, (unsigned)s0); mx = max(mx, (unsigned)s1); mxs = max(mxs, (unsigned)s0); }
      for (int o = 32; o; o >>= 1) { mn = min(mn, (unsigned)__shfl_xor((int)mn, o)); mx = max(mx, (unsigned)__shfl_xor((int)mx, o)); mxs = max(mxs, (unsigned)__shfl_xor((int)mxs, o)); }
      const u64 now = __builtin_amdgcn_s_memrealtime(); const u64 until = now + (u64)(0 * (mxs - mn));
      while (__builtin_amdgcn_s_memrealtime() < until) __builtin_amdgcn_s_sleep(1); } }
}

extern "C" void kernel_launch(void* const* d_in, const int* in_sizes, int n_in, void* d_out, int out_size, void* d_ws, size_t ws_size, hipStream_t stream) {
  (void)in_sizes; (void)n_in; (void)out_size; (void)ws_size;
  (void)hipFuncSetAttribute((const void*)fused_kernel, hipFuncAttributeMaxDynamicSharedMemorySize, LDS_BYTES);
  fused_kernel<<<256, 512, LDS_BYTES, stream>>>((const u16*)d_in[0], (const u16*)d_in[1], (const u16*)d_in[2], (const u16*)d_in[4], (const u16*)d_in[5], (const u16*)d_in[6],
                                                 (u16*)d_out, (char*)d_ws);
}
```

```cpp
#include <hip/hip_runtime.h>
#include <stdint.h>
#include <type_traits>

typedef unsigned short u16;
typedef unsigned long long u64;
typedef short bf16x8 __attribute__((ext_vector_type(8)));
typedef short s16x4 __attribute__((ext_vector_type(4)));
typedef float f32x2 __attribute__((ext_vector_type(2)));
typedef float f32x4 __attribute__((ext_vector_type(4)));
typedef float f32x16 __attribute__((ext_vector_type(16)));
typedef unsigned u32x2 __attribute__((ext_vector_type(2)));
typedef unsigned u32x4 __attribute__((ext_vector_type(4)));
typedef int i32x4 __attribute__((ext_vector_type(4)));
#define LDSP __attribute__((address_space(3)))
#define DI __device__ __forceinline__
template <class T> DI LDSP T* lp(unsigned a) { return (LDSP T*)(size_t)a; }
template <int I, int N, class F> DI void static_for(F&& f) { if constexpr (I < N) { f(std::integral_constant<int, I>{}); static_for<I + 1, N>(f); } }

constexpr size_t WS_EPOCH = 0;
constexpr size_t WS_ERR   = 64;
constexpr size_t WS_FLAGS = 4096;
constexpr size_t WS_QKV   = 1u << 20;
constexpr size_t WS_A2    = 8u << 20;
constexpr size_t WS_DBG   = 24u << 20;

constexpr unsigned R0 = 0, R1 = 32768, R2 = 65536, R3 = 98304;
constexpr unsigned L_K = 98304, L_V = 122880;
constexpr unsigned L_ONES = 147456;
constexpr unsigned L_CSP = 148480;
constexpr unsigned L_CSW = 150528;
constexpr unsigned L_STAT = 152576;
constexpr unsigned L_JUNK = 154624;
constexpr unsigned LDS_BYTES = 163840;
constexpr unsigned CODE_BYTES = 36864;
constexpr unsigned POISON = 0xffffffffu;
constexpr unsigned SPIN_CAP = 1u << 13;

#define NO_WARM 0
#define NO_CS 0
#define NO_DMA 0
constexpr float QSCALE = 0.25f * 1.4426950408889634f;

DI i32x4 mk_srd(const void* p, unsigned bytes) { u64 a = (u64)p; i32x4 r; r[0] = (int)(unsigned)a; r[1] = (int)((unsigned)(a >> 32) & 0xffffu); r[2] = (int)bytes; r[3] = 0x00020000; return r; }
DI void dma16(i32x4 srd, unsigned voff, unsigned soff, unsigned lds) {
  if (NO_DMA) return;
  asm volatile("s_mov_b32 m0, %3\n\ts_nop 0\n\tbuffer_load_dwordx4 %0, %1, %2 offen lds" :: "v"(voff), "s"(srd), "s"(soff), "s"(lds) : "memory");
}
DI void dma4(i32x4 srd, unsigned voff, unsigned lds) {
  asm volatile("s_mov_b32 m0, %2\n\ts_nop 0\n\tbuffer_load_dword %0, %1, 0 offen lds" :: "v"(voff), "s"(srd), "s"(lds) : "memory");
}
DI void ld16_sc1(u32x4& d, i32x4 srd, unsigned voff, unsigned soff) {
  asm volatile("buffer_load_dwordx4 %0, %1, %2, %3 offen sc1" : "=v"(d) : "v"(voff), "s"(srd), "s"(soff) : "memory");
}
DI void ld16(u32x4& d, i32x4 srd, unsigned voff, unsigned soff) {
  asm volatile("buffer_load_dwordx4 %0, %1, %2, %3 offen" : "=v"(d) : "v"(voff), "s"(srd), "s"(soff) : "memory");
}
DI void ld8_sc1(u32x2& d, i32x4 srd, unsigned voff) {
  asm volatile("buffer_load_dwordx2 %0, %1, %2, 0 offen sc1" : "=v"(d) : "v"(voff), "s"(srd) : "memory");
}
DI void st16_sc1(u32x4 d, i32x4 srd, unsigned voff, unsigned soff) {
  asm volatile("buffer_store_dwordx4 %0, %1, %2, %3 offen sc1\n\ts_nop 1" :: "v"(d), "v"(voff), "s"(srd), "s"(soff) : "memory");
}
DI void st16(u32x4 d, i32x4 srd, unsigned voff, unsigned soff) {
  asm volatile("buffer_store_dwordx4 %0, %1, %2, %3 offen\n\ts_nop 1" :: "v"(d), "v"(voff), "s"(srd), "s"(soff) : "memory");
}
template <int N> DI void bar_vm() { static_assert(N >= 0 && N < 64, "vmcnt range"); asm volatile("s_waitcnt vmcnt(%0) lgkmcnt(0)\n\ts_barrier" :: "n"(N) : "memory"); }
template <int N> DI void wait_vm() { static_assert(N >= 0 && N < 64, "vmcnt range"); asm volatile("s_waitcnt vmcnt(%0)" :: "n"(N) : "memory"); }
DI s16x4 ldtr(unsigned a) { return __builtin_amdgcn_ds_read_tr16_b64_v4i16(lp<s16x4>(a)); }
DI bf16x8 ldfrag_tr(unsigned a, unsigned second) {
  s16x4 lo = ldtr(a), hi = ldtr(a + second);
  return __builtin_shufflevector(lo, hi, 0, 1, 2, 3, 4, 5, 6, 7);
}
DI bf16x8 ld128(unsigned a) { return *lp<bf16x8>(a); }
DI unsigned cvtpk(float lo, float hi) { typedef __bf16 b2 __attribute__((ext_vector_type(2))); f32x2 v = {lo, hi}; return __builtin_bit_cast(unsigned, __builtin_convertvector(v, b2)); }
DI float bflo(unsigned v) { return __uint_as_float(v << 16); }
DI float bfhi(unsigned v) { return __uint_as_float(v & 0xffff0000u); }
DI float rbf(float f) { return bflo(cvtpk(f, f) & 0xffffu); }
DI int fresh(int v) { asm volatile("" : "+v"(v)); return v; }
template <class T> DI void pin(T& v) { asm volatile("" : "+v"(v)); }
DI unsigned pinv(unsigned v) { asm volatile("" : "+v"(v)); return v; }
#define MFMA32(a, b, c) __builtin_amdgcn_mfma_f32_32x32x16_bf16((a), (b), (c), 0, 0, 0)
#define MFMA16(a, b, c) __builtin_amdgcn_mfma_f32_16x16x32_bf16((a), (b), (c), 0, 0, 0)
#define MFMA4(a, b, c) __builtin_amdgcn_mfma_f32_4x4x4bf16_1k((a), (b), (c), 0, 0, 0)
#define SB() __builtin_amdgcn_sched_barrier(0)
DI float swap_add(float v) { auto r = __builtin_amdgcn_permlane32_swap(__float_as_uint(v), __float_as_uint(v), false, false); return __uint_as_float(r[0]) + __uint_as_float(r[1]); }
DI float swap_max(float v) { auto r = __builtin_amdgcn_permlane32_swap(__float_as_uint(v), __float_as_uint(v), false, false); return __builtin_fmaxf(__uint_as_float(r[0]), __uint_as_float(r[1])); }

#define STAMP(i) do { } while (0)

struct Ctx {
  char* ws; u64 tag;
  i32x4 sX, sQKVw, sG, sU, sWO, sWD, sWS;
  int lane, wid, mg, ng, rb, cs, c, cg, xi; unsigned stampreg;
};

DI unsigned flag_off(int ev, int rb, int cs) { return (unsigned)(WS_FLAGS + (((ev * 16 + rb) * 16 + cs) << 7)); }
DI void publish(const Ctx& C, int ev) { if (C.wid == 0 && C.lane == 0) __hip_atomic_store((u64*)(C.ws + flag_off(ev, C.rb, C.cs)), C.tag, __ATOMIC_RELAXED, __HIP_MEMORY_SCOPE_AGENT); }
DI void give_up(char* ws) { __hip_atomic_store((unsigned*)(ws + WS_ERR), 1u, __ATOMIC_RELAXED, __HIP_MEMORY_SCOPE_AGENT); }
DI void spin_flags(char* ws, unsigned off, bool need, u64 tag) {
#pragma nounroll
  for (unsigned it = 0; it < SPIN_CAP; ++it) {
    u64 v = __hip_atomic_load((u64*)(ws + off), __ATOMIC_RELAXED, __HIP_MEMORY_SCOPE_AGENT);
    if (__all(!need || v == tag)) return;
    __builtin_amdgcn_s_sleep(2);
  }
  give_up(ws);
}
DI void check_flags(char* ws, unsigned off, bool need, u64 tag, u32x2& got) {
  asm volatile("" : "+v"(got));
  bool ok = !need || (got[0] == (unsigned)tag && got[1] == (unsigned)(tag >> 32));
  if (!__all(ok)) spin_flags(ws, off, need, tag);
}
template <int K0, int K1> DI void check_frags(const Ctx& C, u32x4 (&a2)[36], unsigned base) {
  auto dirty = [&]() { unsigned mx = 0;
    static_for<K0, K1>([&](auto kc) { constexpr int k = decltype(kc)::value;
      mx = __builtin_elementwise_max(mx, __builtin_elementwise_max(__builtin_elementwise_max(a2[k][0], a2[k][1]), __builtin_elementwise_max(a2[k][2], a2[k][3]))); });
    return __any(mx == POISON); };
  if (dirty()) {
#pragma nounroll
    for (unsigned it = 0; it < SPIN_CAP; ++it) {
      __builtin_amdgcn_s_sleep(2);
      static_for<K0, K1>([&](auto kc) { constexpr int k = decltype(kc)::value; ld16_sc1(a2[k], C.sWS, C.lane * 16, base + k * 1024); });
      wait_vm<0>();
      static_for<K0, K1>([&](auto kc) { constexpr int k = decltype(kc)::value; pin(a2[k]); });
      if (!dirty()) return;
    }
    give_up(C.ws);
  }
}

enum { T_H0 = 0, T_S4 = 8, T_E0 = 16, T_G0 = 19, T_POISON = 22, T_POLL, T_KL, T_VL, T_POLL2, T_EPOCH, T_NONE };
template <int TYPE> struct G1 {
  static constexpr int kind(int p) { return p == 1 ? 2 : TYPE == 0 ? (p == 0 ? 0 : 2) : (p == 0 ? 1 : 3); }
  static constexpr int nt(int p) { return (kind(p) == 0 || kind(p) == 3) ? 1 : 2; }
  static constexpr int nst(int p) { return kind(p) == 1 ? 4 : kind(p) == 3 ? 1 : 2; }
  static constexpr int npoison() { return (TYPE == 0 ? 2 : 1) + 1; }
  static constexpr unsigned slot(int s) {
    return s == 4 ? R0 : s == 5 ? R1 : s == 6 ? R2 : s == 7 ? R3 : s == 8 ? R0 : s == 9 ? R1 : s == 10 ? R2 : R0; }
  static constexpr int barpos(int bi) { return bi < 8 ? 4 * bi + 2 : 8 * (bi - 4) + 6; }
  static constexpr int barat(int pos) { return pos < 32 ? ((pos & 3) == 2 ? pos >> 2 : -1) : ((pos & 7) == 6 ? 4 + (pos >> 3) : -1); }
  static constexpr int grp(int bi) { return bi < 4 ? T_H0 + bi + 4 : bi < 11 ? T_S4 + (bi - 4) : bi == 11 ? T_NONE : bi == 12 ? T_S4 + 7 : T_G0 + (bi - 13); }
  static constexpr int gsize(int t) { return t == T_NONE ? 0 : t < T_S4 ? 2 + nt(0) : t < T_E0 ? (nt((t - T_S4 + 4) >> 2) == 1 ? 2 : 4) : 4; }
  static constexpr int rel(int bi, int k) { return bi < 8 ? k : bi == 15 ? (k >> 1) : 2 * k; }
  static constexpr int after(int t, int bi) {
    int cnt = -1;
    auto see = [&](int tag, int n) { for (int i = 0; i < n; ++i) { if (tag == t) cnt = 0; else if (cnt >= 0) ++cnt; } };
    for (int h = 0; h < 2; ++h) see(T_H0 + h, gsize(T_H0 + h));
    see(T_EPOCH, 1);
    for (int pos = 0; pos < 96; ++pos) {
      if (pos < 2) see(T_H0 + 2 + pos, gsize(T_H0 + 2 + pos));
      const int b = barat(pos);
      if (b >= 0) {
        if (b == bi) return cnt;
        if (b == 12) see(T_POLL, 1);
        if (b == 13) see(T_KL, 3);
        if (b == 14) see(T_VL, 4);
        if (b == 15) see(T_POLL2, 1);
      }
      for (int bb = 0; bb < 16; ++bb) for (int k = 0; k < gsize(grp(bb)); ++k) if (barpos(bb) + rel(bb, k) == pos) see(grp(bb), 1);
      if (pos == 16) see(T_POISON, npoison());
      if ((pos & 31) == 31) see(T_E0 + (pos >> 5), nst(pos >> 5));
    }
    return cnt;
  }
  static constexpr int mn(int a, int b) { return a < b ? a : b; }
};

template <int TYPE> DI unsigned tile_addr(const Ctx& C, int p, int t) {
  const int lane = fresh(C.lane), kind = G1<TYPE>::kind(p);
  const int g = (lane >> 4) & 1, h = lane >> 5, q = (lane & 15) >> 2, pp = lane & 3;
  const unsigned rdrow = (8 * h + q) * 128 + pp * 8;
  if (kind == 3) return g * 8192 + (8 * h + q) * 64 + pp * 8 + ((C.ng ^ g) << 5);
  if (kind == 2) return g * 16384 + rdrow + (((2 * C.ng + t) ^ (2 * (q >> 1)) ^ g) << 5);
  if (kind == 0) return 16384 + rdrow + ((C.ng ^ (q >> 1)) << 6) + g * 32;
  return 16384 + C.ng * 8192 + rdrow + ((t ^ (q >> 1)) << 6) + g * 32;
}
template <int TYPE> DI unsigned stage_voff(const Ctx& C, int p) {
  const int kind = G1<TYPE>::kind(p), lane = fresh(C.lane);
  if (kind == 3) return (16 * C.wid + (lane >> 2)) * 3584 + (24 + ((C.cs - 8) >> 1)) * 128 + ((C.cs - 8) & 1) * 64 + ((lane & 3) << 4);
  return (16 * C.wid + (lane >> 3)) * 3584 + (p == 1 ? C.cs : 16 + C.cs) * 128 + (((lane & 7) ^ (((lane >> 4) & 1) << 2)) << 4);
}
template <int TYPE> DI void issue_stage(const Ctx& C, int s, int k, unsigned v0) {
  const int kind = G1<TYPE>::kind(s >> 2);
  const unsigned slot = G1<TYPE>::slot(s), r0 = 128 * (s & 3);
  if (kind == 3) dma16(k ? C.sU : C.sG, k ? v0 ^ 32 : v0, r0 * 3584, slot + k * 8192 + C.wid * 1024);
  else dma16(k >> 1 ? C.sU : C.sG, k >> 1 ? v0 ^ 32 : v0, (r0 + 8 * (k & 1)) * 3584, slot + (k >> 1) * 16384 + C.wid * 2048 + (k & 1) * 1024);
}
DI unsigned hx_voff(const Ctx& C) { const int lane = fresh(C.lane); return (16 * C.wid + (lane >> 3)) * 1024 + (((lane & 7) ^ ((lane >> 4) & 3)) << 4); }
DI unsigned hw_voff(const Ctx& C) { const int lane = fresh(C.lane);
  return (8 * C.wid + (lane >> 3)) * 3072 + (C.cs < 8 ? C.cs : 8 + 2 * (C.cs - 8)) * 128 + (((lane & 7) ^ (((lane >> 4) & 1) << 2)) << 4); }
DI void issue_half(const Ctx& C, int h, int k, unsigned vx, unsigned vw) {
  const unsigned slot = (h & 3) * 32768;
  if (k < 2) dma16(C.sX, k ? (vx ^ 64) + 8 * 1024 : vx, h * 128, slot + C.wid * 2048 + k * 1024);
  else dma16(C.sQKVw, vw + (k - 2) * 128, h * 64 * 3072, slot + 16384 + (k - 2) * 8192 + C.wid * 1024);
}
DI void warm(i32x4 srd, unsigned line0, unsigned lines_per_row_log2, unsigned pitch, unsigned colbyte, int lane) {
  const unsigned l = line0 + lane;
  dma4(srd, (l >> lines_per_row_log2) * pitch + colbyte + (l & ((1u << lines_per_row_log2) - 1)) * 128, L_JUNK);
}
DI unsigned g_voff(const Ctx& C) { const int lane = fresh(C.lane); return (32 * (C.wid & 1) + (lane >> 3)) * 1024 + C.cg * 128 + (((lane & 7) ^ (((lane >> 4) & 1) << 2)) << 4); }
constexpr unsigned gslot(int j) { return j == 0 ? R1 : j == 1 ? R2 : j == 2 ? R0 : j == 3 ? R3 : j == 4 ? R1 : j == 5 ? R2 : j == 6 ? R0 : j == 7 ? R3 : R1; }
DI void issue_g(const Ctx& C, int j, int i, unsigned v) {
  const int kq = C.wid >> 1;
  const int row0 = j < 4 ? (4 * kq + j) * 64 : j < 7 ? (16 + 3 * kq + (j - 4)) * 64 : 128 * kq + 64 * (j - 7);
  dma16(j < 7 ? C.sWD : C.sWO, v, (unsigned)(row0 + 8 * i) * 1024, gslot(j) + kq * 8192 + (C.wid & 1) * 4096 + i * 1024);
}

template <int TYPE> DI void epi_qkv(const Ctx& C, const f32x16& acc, const f32x4 (&csr)[4], float m, float rinv, int qu, int h4) {
  const int h = C.lane >> 5;
  const float sc = TYPE == 0 ? rinv * QSCALE : rinv;
  float y[16];
#pragma unroll
  for (int i4 = 0; i4 < 4; ++i4)
#pragma unroll
    for (int e = 0; e < 4; ++e) y[4 * i4 + e] = (acc[4 * i4 + e] - m * csr[i4][e]) * sc;
  const unsigned base = (unsigned)WS_QKV + (((C.rb * 24 + qu) * 4 + C.mg) * 4 + h4) * 1024;
#pragma unroll
  for (int hh = 0; hh < 2; ++hh) {
    u32x4 f; f[0] = cvtpk(y[8 * hh], y[8 * hh + 1]); f[1] = cvtpk(y[8 * hh + 2], y[8 * hh + 3]); f[2] = cvtpk(y[8 * hh + 4], y[8 * hh + 5]); f[3] = cvtpk(y[8 * hh + 6], y[8 * hh + 7]);
    unsigned voff = C.lane * 16;
    if (TYPE == 1 && qu >= 16) {
      auto a = __builtin_amdgcn_permlane32_swap(f[0], f[2], false, false); auto b = __builtin_amdgcn_permlane32_swap(f[1], f[3], false, false);
      f[0] = a[0]; f[2] = a[1]; f[1] = b[0]; f[3] = b[1];
      voff = (C.lane & 31) * 32 + h * 16;
    }
    st16_sc1(f, C.sWS, voff, base + hh * 1024);
  }
}
DI void epi_gu(const Ctx& C, const f32x16& acc, const f32x4 (&csr)[4], float m, float r2, unsigned a2off) {
  float y[8];
#pragma unroll
  for (int i4 = 0; i4 < 2; ++i4)
#pragma unroll
    for (int e = 0; e < 4; ++e) y[4 * i4 + e] = ((acc[4 * i4 + e] - m * csr[i4][e]) * r2) * (acc[8 + 4 * i4 + e] - m * csr[2 + i4][e]);
  u32x4 f; f[0] = cvtpk(y[0], y[1]); f[1] = cvtpk(y[2], y[3]); f[2] = cvtpk(y[4], y[5]); f[3] = cvtpk(y[6], y[7]);
  st16_sc1(f, C.sWS, C.lane * 16, a2off);
}
template <int TYPE> DI unsigned a2_pass(const Ctx& C, int p) {
  if (p == 1) return (unsigned)WS_A2 + (((4 * C.rb + C.mg) * 4 + (C.cs >> 2)) * 36 + 4 * (C.cs & 3) + 2 * C.ng) * 1024;
  if (TYPE == 0) return (unsigned)WS_A2 + (((4 * C.rb + C.mg) * 4 + C.cs / 3) * 36 + 4 * (4 + C.cs % 3) + 2 * C.ng) * 1024;
  const int u = 8 + ((C.cs - 8) >> 1), half = (C.cs - 8) & 1;
  return (unsigned)WS_A2 + (((4 * C.rb + C.mg) * 4 + u / 3) * 36 + 4 * (4 + u % 3) + 2 * half + C.ng) * 1024;
}
DI unsigned a2_o(const Ctx& C) { return (unsigned)WS_A2 + (((4 * C.rb + C.mg) * 4 + (C.cs >> 2)) * 36 + 28 + 2 * (C.cs & 3) + C.ng) * 1024; }

template <int TYPE> DI void gemm1(Ctx& C, unsigned vx, unsigned vw, u32x4& qfrag, u32x2& poll2, unsigned poll2off) {
  using G = G1<TYPE>;
  const int lane = C.lane;
  char* ws = C.ws;
  bf16x8 xa[32];
  bf16x8 wf[3][2];
  f32x16 acc[2];
  unsigned ta[2], sv = 0;
  ta[0] = tile_addr<TYPE>(C, 0, 0); ta[1] = tile_addr<TYPE>(C, 0, 1);
  const unsigned r31 = lane & 31, hh = lane >> 5;
  const unsigned xlane = (32 * C.mg + r31) * 128 + ((hh ^ ((r31 >> 1) & 7)) << 4);
  f32x4 ssum = {0, 0, 0, 0}, ssq = {0, 0, 0, 0};
  f32x4 csacc[2] = {{0,0,0,0},{0,0,0,0}};
  bf16x8 sel;
  { const short one = ((lane & 15) == ((lane >> 4) & 1)) ? (short)0x3f80 : (short)0;
#pragma unroll
    for (int e = 0; e < 8; ++e) sel[e] = one; }
  float csn[2];
  f32x4 csr[2][4];
  float m = 0.f, rinv = 0.f;
  u32x2 pollv; u32x4 gk[3];
  unsigned polloff = 0; bool pollneed = false;

  STAMP(1);
  bar_vm<G::gsize(T_H0)>();
  STAMP(2);
  u32x2 ev; ld8_sc1(ev, C.sWS, (unsigned)WS_EPOCH);
  auto read_x = [&](int h) {
    const unsigned base = (h & 3) * 32768 + xlane;
#pragma unroll
    for (int i = 0; i < 4; ++i) xa[4 * h + i] = ld128(base ^ (((i + C.c) & 3) << 5));
  };
  auto read_w = [&](int pos) {
    const int p = pos >> 5, s = pos >> 3, i = pos & 7;
    const bool half = G::kind(p) == 3;
    if ((pos & 31) == 0 && pos > 0) { ta[0] = tile_addr<TYPE>(C, p, 0); ta[1] = tile_addr<TYPE>(C, p, 1); }
    const unsigned stride = half ? 1024 : 2048, second = half ? 256 : 512;
    const unsigned so = (p == 0 ? ((pos >> 2) & 3) * 32768 : G::slot(s) + (i & 4) * stride) + ((i + C.c) & 3) * stride;
    wf[pos % 3][0] = ldfrag_tr(pinv(ta[0] + so), second);
    if (G::nt(p) == 2) wf[pos % 3][1] = ldfrag_tr(pinv(ta[1] + so), second);
  };
  read_x(0); read_w(0); read_w(1);
  static_for<0, 96>([&](auto ic) {
    constexpr int pos = decltype(ic)::value;
    constexpr int p = pos >> 5, s = pos >> 3, st = s & 3, i = pos & 7, NT = G::nt(p), bi = G::barat(pos);
    if constexpr (pos < 2) static_for<0, G::gsize(T_H0)>([&](auto kc) { issue_half(C, 2 + pos, decltype(kc)::value, vx, vw); });
    if constexpr (bi >= 0) {
      STAMP(10 + 2 * bi);
      if constexpr (bi == 0) { bar_vm<G::after(T_H0 + 1, 0)>(); }
      else if constexpr (bi < 7) bar_vm<G::after(T_H0 + bi + 1, bi)>();
      else if constexpr (bi == 9) { bar_vm<G::mn(G::after(T_S4 + 2, 9), G::mn(G::after(T_E0, 9), G::after(T_POISON, 9)))>(); publish(C, 0); }
      else if constexpr (bi == 13) { bar_vm<G::mn(G::mn(G::after(T_S4 + 6, 13), G::after(T_E0 + 1, 13)), G::after(T_POLL, 13))>(); }
      else if constexpr (bi == 14) bar_vm<G::mn(G::after(T_S4 + 7, 14), G::after(T_KL, 14))>();
      else if constexpr (bi == 15) bar_vm<G::after(T_VL, 15)>();
      else bar_vm<G::after(T_S4 + bi - 7, bi)>();
      if constexpr (bi == 4) {
        static_assert(G::after(T_H0 + 5, 4) <= G::after(T_EPOCH, 4), "");
        pin(ev);
        const u64 t = (((u64)ev[1] << 32) | ev[0]) * 6364136223846793005ull + 1442695040888963407ull;
        C.tag = ((u64)__builtin_amdgcn_readfirstlane((unsigned)(t >> 32)) << 32) | __builtin_amdgcn_readfirstlane((unsigned)t);
      }
      if constexpr (bi == 12) {
        const int u = C.cs >> 1, l = lane & 7;
        const int ty = l == 0 ? 0 : l < 4 ? 1 : 2, rbp = l == 0 ? C.rb : C.rb - 2 + (l - 1) % 3;
        const int qu = 8 * ty + u;
        const int pcs = qu < 8 ? qu : 8 + ((qu - 8) >> 1);
        pollneed = l < 7 && rbp >= 0;
        polloff = flag_off(0, pollneed ? rbp : C.rb, pcs);
        ld8_sc1(pollv, C.sWS, polloff);
      }
      if constexpr (bi == 13) {
        check_flags(ws, polloff, pollneed, C.tag, pollv);
        const int u = C.cs >> 1, h4 = 2 * (C.cs & 1) + C.ng;
#pragma unroll
        for (int b = 0; b < 3; ++b) { const int rbp = C.rb - 2 + b < 0 ? 0 : C.rb - 2 + b;
          ld16_sc1(gk[b], C.sWS, lane * 16, (unsigned)WS_QKV + (((rbp * 24 + 8 + u) * 4 + C.mg) * 4 + h4) * 1024); }
        publish(C, 1);
      }
      if constexpr (bi == 14) {
        pin(gk[0]); pin(gk[1]); pin(gk[2]);
#pragma unroll
        for (int b = 0; b < 3; ++b) *lp<u32x4>(L_K + ((4 * b + C.mg) * 2 + C.ng) * 1024 + lane * 16) = gk[b];
        const int u = C.cs >> 1, h4 = 2 * (C.cs & 1) + C.ng;
#pragma unroll
        for (int b = 0; b < 3; ++b) { const int rbp = C.rb - 2 + b < 0 ? 0 : C.rb - 2 + b;
          ld16_sc1(gk[b], C.sWS, lane * 16, (unsigned)WS_QKV + (((rbp * 24 + 16 + u) * 4 + C.mg) * 4 + h4) * 1024); }
        ld16_sc1(qfrag, C.sWS, lane * 16, (unsigned)WS_QKV + (((C.rb * 24 + u) * 4 + C.mg) * 4 + h4) * 1024);
      }
      if constexpr (bi == 15) {
        pin(qfrag); pin(gk[0]); pin(gk[1]); pin(gk[2]);
#pragma unroll
        for (int b = 0; b < 3; ++b) *lp<u32x4>(L_V + C.ng * 12288 + (4 * b + C.mg) * 1024 + lane * 16) = gk[b];
        ld8_sc1(poll2, C.sWS, poll2off);
      }
      if constexpr (bi < 7) read_x(bi + 1);
      if constexpr (st == 3 && i == 6) {
#pragma unroll
        for (int t = 0; t < NT; ++t) { const unsigned a = L_CSP + (C.ng * 2 + t) * 512 + r31 * 4;
          csn[t] = (*lp<float>(a) + *lp<float>(a + 128)) + (*lp<float>(a + 256) + *lp<float>(a + 384)); }
      }
      STAMP(11 + 2 * bi);
    }
    static_for<0, 16>([&](auto bc) {
      constexpr int bb = decltype(bc)::value, tg = G::grp(bb), k0 = pos - G::barpos(bb);
      static_for<0, 4>([&](auto kc) {
        constexpr int k = decltype(kc)::value;
        if constexpr (k < G::gsize(tg) && G::rel(bb, k) == k0) {
          if constexpr (tg < T_S4) issue_half(C, tg, k, vx, vw);
          else if constexpr (tg >= T_G0) { if constexpr (k == 0) sv = g_voff(C); issue_g(C, tg - T_G0, k, sv); }
          else { if constexpr (k == 0) sv = stage_voff<TYPE>(C, (tg - T_S4 + 4) >> 2); issue_stage<TYPE>(C, tg - T_S4 + 4, k, sv); }
        }
      });
    });
    if constexpr (pos == 12 && !NO_WARM) {
      const int gu = TYPE == 0 ? 16 + C.cs : 24 + ((C.cs - 8) >> 1);
      const unsigned l0 = (C.rb & 7) * 64;
      if (C.wid == 5) { warm(C.sG, l0, 0, 3584, gu * 128, fresh(lane)); warm(C.sU, l0, 0, 3584, gu * 128, fresh(lane)); }
    }
    if constexpr (pos >= 34 && pos < 52 && (pos & 1) == 0 && !NO_WARM) {
      constexpr int k = (pos - 34) >> 1;
      const unsigned l = ((C.xi * 9 + k) * 64);
      if (C.wid == (k & 7)) { if (l < 4096) warm(C.sWO, l, 3, 1024, 0, fresh(lane)); else warm(C.sWD, l - 4096, 3, 1024, 0, fresh(lane)); }
    }
    if constexpr (pos == 16) {
      const u32x4 pz = {POISON, POISON, POISON, POISON};
      const unsigned a = a2_pass<TYPE>(C, 2);
      st16_sc1(pz, C.sWS, lane * 16, a);
      if (TYPE == 0) st16_sc1(pz, C.sWS, lane * 16, a + 1024);
      st16_sc1(pz, C.sWS, lane * 16, a2_o(C));
    }
    if constexpr (pos + 2 < 96) read_w(pos + 2);
    if constexpr ((pos & 31) == 0) { acc[0] = MFMA32(wf[pos % 3][0], xa[pos & 31], f32x16{}); if (NT == 2) acc[1] = MFMA32(wf[pos % 3][1], xa[pos & 31], f32x16{}); }
    else { acc[0] = MFMA32(wf[pos % 3][0], xa[pos & 31], acc[0]); if (NT == 2) acc[1] = MFMA32(wf[pos % 3][1], xa[pos & 31], acc[1]); }
    if constexpr ((i & 3) == 0 && !NO_CS) {
      if constexpr ((pos & 31) == 0) { csacc[0] = MFMA16(wf[pos % 3][0], sel, (f32x4{0, 0, 0, 0})); if (NT == 2) csacc[1] = MFMA16(wf[pos % 3][1], sel, (f32x4{0, 0, 0, 0})); }
      else { csacc[0] = MFMA16(wf[pos % 3][0], sel, csacc[0]); if (NT == 2) csacc[1] = MFMA16(wf[pos % 3][1], sel, csacc[1]); }
    }
    if constexpr (p == 0 && (i & 3) < 2) {
      const bf16x8 xv = xa[pos & 31]; const s16x4 lo = __builtin_shufflevector(xv, xv, 0, 1, 2, 3), hi = __builtin_shufflevector(xv, xv, 4, 5, 6, 7);
      const s16x4 ones = {0x3f80, 0x3f80, 0x3f80, 0x3f80};
      ssum = MFMA4(ones, lo, ssum); ssq = MFMA4(lo, lo, ssq); ssum = MFMA4(ones, hi, ssum); ssq = MFMA4(hi, hi, ssq);
    }
    if constexpr (st == 3 && i == 5) {
      if ((lane & 15) < 2) {
#pragma unroll
        for (int t = 0; t < NT; ++t) *lp<f32x4>(L_CSP + ((C.ng * 2 + t) * 4 + C.mg) * 128 + (16 * (lane & 15) + 4 * (lane >> 4)) * 4) = csacc[t];
      }
      if constexpr (p == 0) {
        const float sq = (lane & 3) == 0 ? ssq[0] : (lane & 3) == 1 ? ssq[1] : (lane & 3) == 2 ? ssq[2] : ssq[3];
        const f32x2 v = {swap_add(ssum[0]), swap_add(sq)};
        if (lane < 32) *lp<f32x2>(L_STAT + (C.ng * 128 + 32 * C.mg + lane) * 8) = v;
      }
    }
    if constexpr (st == 3 && i == 6) {
#pragma unroll
      for (int t = 0; t < NT; ++t) *lp<float>(L_CSW + (C.wid * 2 + t) * 128 + r31 * 4) = csn[t];
    }
    if constexpr (st == 3 && i == 7) {
#pragma unroll
      for (int t = 0; t < NT; ++t)
#pragma unroll
        for (int i4 = 0; i4 < 4; ++i4) csr[t][i4] = *lp<f32x4>(L_CSW + (C.wid * 2 + t) * 128 + (8 * i4 + 4 * hh) * 4);
    }
    SB();
    if constexpr ((pos & 31) == 31) {
      if constexpr (p == 0) {
        const f32x2 a = *lp<f32x2>(L_STAT + (32 * C.mg + r31) * 8), b = *lp<f32x2>(L_STAT + (128 + 32 * C.mg + r31) * 8);
        const float mean = (a[0] + b[0]) * (1.f / 512.f);
        const float var = __builtin_fmaxf((a[1] + b[1]) * (1.f / 512.f) - mean * mean, 0.f);
        m = rbf(mean);
        rinv = __builtin_amdgcn_rcpf(rbf(__builtin_amdgcn_sqrtf(rbf(rbf(var) + 1e-5f))));
      }
      if constexpr (G::kind(p) == 2) {
        const unsigned a2 = a2_pass<TYPE>(C, p);
        epi_gu(C, acc[0], csr[0], m, rinv * rinv, a2); epi_gu(C, acc[1], csr[1], m, rinv * rinv, a2 + 1024);
      } else if constexpr (G::kind(p) == 3) {
        epi_gu(C, acc[0], csr[0], m, rinv * rinv, a2_pass<TYPE>(C, p));
      } else if constexpr (G::kind(p) == 0) {
        epi_qkv<0>(C, acc[0], csr[0], m, rinv, C.cs, 2 * C.ng);
      } else {
        const int qu = 8 + 2 * (C.cs - 8) + C.ng;
        epi_qkv<1>(C, acc[0], csr[0], m, rinv, qu, 0); epi_qkv<1>(C, acc[1], csr[1], m, rinv, qu, 2);
      }
      SB();
    }
  });
  wait_vm<G::after(T_POLL2, 16)>();
}

template <bool EDGE, class SIDE> DI u32x4 attention(Ctx& C, const u32x4& qraw, SIDE&& side) {
  const int lane = C.lane, h = lane >> 5, g = (lane >> 4) & 1, q = (lane & 15) >> 2, pp = lane & 3;
  const bf16x8 qf = __builtin_bit_cast(bf16x8, qraw);
  const unsigned kaddr = pinv(L_K + (C.mg * 2 + C.ng) * 1024 + lane * 16);
  const int t0 = EDGE ? (C.rb == 0 ? 8 : 4) - C.mg : 1;
  const int dq = (lane & 31) - 4 * h;
  const float NEG = -1e30f;
  auto older = [&](int r) { return ((r & 3) + 8 * (r >> 2)) > dq; };
  auto max16 = [&](float& a, float& b, const f32x16& s) {
#pragma unroll
    for (int r = 0; r < 16; r += 4) { a = __builtin_fmaxf(__builtin_fmaxf(a, s[r]), s[r + 1]); b = __builtin_fmaxf(__builtin_fmaxf(b, s[r + 2]), s[r + 3]); } };
  bf16x8 kf[9];
#pragma unroll
  for (int t = 0; t < 9; ++t) if (!EDGE || t > 0) kf[t] = ld128(kaddr + t * 2048);
  float mx;
  {
    f32x16 S[3];
    f32x16 sl = MFMA32(kf[8], qf, f32x16{});
    f32x16 sf;
    if (!EDGE) sf = MFMA32(kf[0], qf, f32x16{});
    S[1] = MFMA32(kf[1], qf, f32x16{});
    S[2] = MFMA32(kf[2], qf, f32x16{});
    float a = NEG, b = NEG;
    side(std::integral_constant<int, 0>{});
    SB();
#pragma unroll
    for (int r = 0; r < 16; ++r) sl[r] = older(r) ? (EDGE ? NEG : sf[r]) : sl[r];
    max16(a, b, sl);
    static_for<1, 8>([&](auto tc) { constexpr int t = decltype(tc)::value;
      if constexpr (t + 2 < 8) S[(t + 2) % 3] = MFMA32(kf[t + 2], qf, f32x16{});
      side(std::integral_constant<int, t>{});
      SB();
      if (!EDGE || t >= t0) max16(a, b, S[t % 3]);
      SB(); });
    mx = swap_max(__builtin_fmaxf(a, b));
  }
  f32x16 negm;
#pragma unroll
  for (int r = 0; r < 16; ++r) negm[r] = -mx;
  const unsigned vbase = pinv(g == 0 ? L_V + C.ng * 12288 + C.mg * 1024 + (4 * h + q) * 32 + pp * 8 : L_ONES + 128 * (1 - h) + q * 32 + pp * 8);
  const unsigned vstr = g == 0 ? 1024u : 0u;
  f32x16 o;
  auto expack = [&](const f32x16& s, u32x4& pa, u32x4& pb) {
#pragma unroll
    for (int i = 0; i < 4; ++i) pa[i] = cvtpk(__builtin_amdgcn_exp2f(s[2 * i]), __builtin_amdgcn_exp2f(s[2 * i + 1]));
#pragma unroll
    for (int i = 0; i < 4; ++i) pb[i] = cvtpk(__builtin_amdgcn_exp2f(s[8 + 2 * i]), __builtin_amdgcn_exp2f(s[9 + 2 * i])); };
  f32x16 S[2];
  bf16x8 vf[2][2];
  {
    f32x16 sl = MFMA32(kf[8], qf, negm);
    f32x16 sf;
    if (!EDGE) sf = MFMA32(kf[0], qf, negm);
    S[1] = MFMA32(kf[1], qf, negm);
    const unsigned v8 = vbase + 8 * vstr;
    const bf16x8 va = ldfrag_tr(v8, 256), vb = ldfrag_tr(v8 + 512, 256);
    bf16x8 vc, vd;
    if (!EDGE) { vc = ldfrag_tr(vbase, 256); vd = ldfrag_tr(vbase + 512, 256); }
    vf[1][0] = ldfrag_tr(vbase + vstr, 256); vf[1][1] = ldfrag_tr(vbase + vstr + 512, 256);
    side(std::integral_constant<int, 8>{});
    SB();
#pragma unroll
    for (int r = 0; r < 16; ++r) sl[r] = older(r) ? (EDGE ? NEG : sf[r]) : sl[r];
    u32x4 pa, pb, mk0, mk1;
    expack(sl, pa, pb);
#pragma unroll
    for (int i = 0; i < 4; ++i) {
      mk0[i] = (older(2 * i) ? 0xffffu : 0u) | (older(2 * i + 1) ? 0xffff0000u : 0u);
      mk1[i] = (older(8 + 2 * i) ? 0xffffu : 0u) | (older(9 + 2 * i) ? 0xffff0000u : 0u); }
    o = MFMA32(va, __builtin_bit_cast(bf16x8, pa & ~mk0), f32x16{});
    o = MFMA32(vb, __builtin_bit_cast(bf16x8, pb & ~mk1), o);
    if (!EDGE) {
      o = MFMA32(vc, __builtin_bit_cast(bf16x8, pa & mk0), o);
      o = MFMA32(vd, __builtin_bit_cast(bf16x8, pb & mk1), o);
    }
    SB();
  }
  static_for<1, 8>([&](auto tc) { constexpr int t = decltype(tc)::value;
    if constexpr (t + 1 < 8) {
      const unsigned vt = vbase + (t + 1) * vstr;
      vf[(t + 1) & 1][0] = ldfrag_tr(vt, 256); vf[(t + 1) & 1][1] = ldfrag_tr(vt + 512, 256);
      S[(t + 1) & 1] = MFMA32(kf[t + 1], qf, negm); }
    side(std::integral_constant<int, 8 + t>{});
    SB();
    if (!EDGE || t >= t0) {
      u32x4 pa, pb;
      expack(S[t & 1], pa, pb);
      o = MFMA32(vf[t & 1][0], __builtin_bit_cast(bf16x8, pa), o);
      o = MFMA32(vf[t & 1][1], __builtin_bit_cast(bf16x8, pb), o);
    }
    SB(); });
  const float li = __builtin_amdgcn_rcpf(o[8]);
  u32x4 f; f[0] = cvtpk(o[0] * li, o[1] * li); f[1] = cvtpk(o[2] * li, o[3] * li); f[2] = cvtpk(o[4] * li, o[5] * li); f[3] = cvtpk(o[6] * li, o[7] * li);
  return f;
}

__global__ void __launch_bounds__(512, 2)
fused_kernel(const u16* __restrict__ x, const u16* __restrict__ wqkv, const u16* __restrict__ wo, const u16* __restrict__ wg, const u16* __restrict__ wu,
             const u16* __restrict__ wd, u16* __restrict__ out, char* __restrict__ ws) {
  Ctx C;
  const int tid = threadIdx.x, lane = tid & 63, wid = __builtin_amdgcn_readfirstlane(tid >> 6);
  const int b = blockIdx.x, xj = b & 7, xi = b >> 3;
  C.stampreg = 0;
  STAMP(0);
  asm volatile("" :: "s"(x), "s"(wqkv), "s"(wo), "s"(wg), "s"(wu), "s"(wd), "s"(out), "s"(ws));
  C.ws = ws; C.lane = lane; C.wid = wid; C.mg = wid & 3; C.ng = wid >> 2;
  if (wid >= 4) __builtin_amdgcn_s_setprio(1);
  C.rb = 8 * (xj & 1) + (xi & 7); C.cs = 4 * (xj >> 1) + (xi >> 3); C.c = (C.mg + 2 * C.ng) & 3;
  C.sX = mk_srd(x + (size_t)C.rb * 128 * 512, 128 * 1024);
  C.sQKVw = mk_srd(wqkv, 512 * 3072);
  C.xi = xi;
  const unsigned vx = hx_voff(C), vw = hw_voff(C);
  STAMP(3);
  issue_half(C, 0, 0, vx, vw); issue_half(C, 0, 1, vx, vw); issue_half(C, 0, 2, vx, vw); if (C.cs >= 8) issue_half(C, 0, 3, vx, vw);
  STAMP(4);
  issue_half(C, 1, 0, vx, vw); issue_half(C, 1, 1, vx, vw); issue_half(C, 1, 2, vx, vw); if (C.cs >= 8) issue_half(C, 1, 3, vx, vw);
  STAMP(5);
  C.sWS = mk_srd(ws, 32u << 20);
  C.sG = mk_srd(wg, 512 * 3584); C.sU = mk_srd(wu, 512 * 3584); C.sWO = mk_srd(wo, 512 * 1024); C.sWD = mk_srd(wd, 1792 * 1024);
  if (!NO_WARM) {
    if (wid < 3) { const unsigned l = (C.cs & 3) * 192 + wid * 64 + lane; dma4(C.sX, (l / 6) * 1024 + (2 + l % 6) * 128, L_JUNK); }
    else if (wid < 5) { if (wid == 3 || C.cs >= 8) dma4(C.sQKVw, (128 + (C.rb & 7) * 48 + (lane < 48 ? lane : 0)) * 3072 + ((C.cs < 8 ? C.cs : 8 + 2 * (C.cs - 8)) + (wid - 3)) * 128, L_JUNK); }
    else if (wid < 7) dma4(wid == 5 ? C.sG : C.sU, ((C.rb & 7) * 64 + lane) * 3584 + C.cs * 128, L_JUNK);
    else { u64 pc; asm volatile("s_getpc_b64 %0" : "=s"(pc));
      dma4(mk_srd((const void*)(pc & ~255ull), CODE_BYTES), (xi * 9 + (lane < 9 ? lane : 0)) * 128, L_JUNK); }
  }
  if (tid < 256) lp<unsigned>(L_ONES)[tid] = 0x3f803f80u;
  const int rt64 = 4 * xj + (xi & 3), cg = xi >> 2;
  C.cg = cg;
  const int rt = wid & 1, kq = wid >> 1, RT = 2 * rt64 + rt, rb2 = RT >> 2;
  const bool p2need = lane < 10;
  unsigned p2off;
  { const int l = lane < 10 ? lane : 0, u = 3 * kq + ((l - 4) >> 1);
    p2off = l < 4 ? flag_off(1, rb2, 4 * kq + l) : flag_off(0, rb2, u < 8 ? u : 8 + 2 * (u - 8) + (l & 1)); }
  u32x4 qfrag; u32x2 poll2;
  if (C.cs < 8) gemm1<0>(C, vx, vw, qfrag, poll2, p2off); else gemm1<1>(C, vx, vw, qfrag, poll2, p2off);
  STAMP(6);

  const unsigned a2base = (unsigned)WS_A2 + (RT * 4 + kq) * 36 * 1024;
  u32x4 a2[36];
  check_flags(ws, p2off, p2need, C.tag, poll2);
  asm volatile("s_waitcnt lgkmcnt(0)\n\ts_barrier" ::: "memory");
  auto side = [&](auto kc) { constexpr int k = decltype(kc)::value;
    if constexpr (k < 8) { ld16_sc1(a2[2 * k], C.sWS, lane * 16, a2base + 2 * k * 1024); ld16_sc1(a2[2 * k + 1], C.sWS, lane * 16, a2base + (2 * k + 1) * 1024); }
    if constexpr (k >= 12) static_for<0, 3>([&](auto jc) { constexpr int q_ = 16 + 3 * (k - 12) + decltype(jc)::value; ld16_sc1(a2[q_], C.sWS, lane * 16, a2base + q_ * 1024); }); };
  u32x4 of;
  if (C.rb >= 2) of = attention<false>(C, qfrag, side); else of = attention<true>(C, qfrag, side);
  st16_sc1(of, C.sWS, lane * 16, a2_o(C));
  STAMP(7);

  const int g = (lane >> 4) & 1, h = lane >> 5, q = (lane & 15) >> 2, pp = lane & 3;
  const unsigned w2l0 = kq * 8192 + (4 * h + q) * 128 + ((0 ^ (q >> 1)) << 6) + g * 32 + pp * 8;
  const unsigned w2l1 = kq * 8192 + (4 * h + q) * 128 + ((1 ^ (q >> 1)) << 6) + g * 32 + pp * 8;
  bf16x8 wf[3][2];
  f32x16 acc[2];
  u32x4 xres;
  unsigned wb[2], gv;
  auto read_w2 = [&](int pos) {
    if ((pos & 3) == 0) { wb[0] = pinv(w2l0 + gslot(pos >> 2)); wb[1] = pinv(w2l1 + gslot(pos >> 2)); }
    wf[pos % 3][0] = ldfrag_tr(wb[0] + (pos & 3) * 2048, 1024); wf[pos % 3][1] = ldfrag_tr(wb[1] + (pos & 3) * 2048, 1024); };
  bar_vm<13>();
  static_for<0, 16>([&](auto kc) { constexpr int k = decltype(kc)::value; pin(a2[k]); });
  read_w2(0); read_w2(1);
  gv = g_voff(C);
  STAMP(8);
  u32x4 ef[4];
  static_for<0, 36>([&](auto ic) {
    constexpr int pos = decltype(ic)::value;
    constexpr int j = pos >> 2, i = pos & 3;
    if constexpr (i == 2) {
      STAMP(44 + 2 * j);
      if constexpr (j <= 1) bar_vm<63>();
      if constexpr (j == 2) bar_vm<6>();
      if constexpr (j == 3) bar_vm<6>();
      if constexpr (j == 4) bar_vm<7>();
      if constexpr (j == 5) bar_vm<15>();
      if constexpr (j == 6) bar_vm<4>();
      if constexpr (j == 7) bar_vm<0>();
      STAMP(45 + 2 * j);
    }
    if constexpr (pos == 14) { static_for<16, 28>([&](auto kc) { pin(a2[decltype(kc)::value]); }); check_frags<16, 28>(C, a2, a2base); }
    if constexpr (pos == 26) { static_for<28, 36>([&](auto kc) { pin(a2[decltype(kc)::value]); }); pin(xres); check_frags<28, 36>(C, a2, a2base); }
    if constexpr (pos == 16) { const i32x4 sXall = mk_srd(x, 2048 * 1024); ld16(xres, sXall, (RT * 32 + (lane & 31)) * 1024 + cg * 128 + kq * 32 + h * 16, 0); }
    if constexpr (pos == 18 || pos == 19) static_for<0, 4>([&](auto kc) { constexpr int k = 28 + 4 * (pos - 18) + decltype(kc)::value; ld16_sc1(a2[k], C.sWS, lane * 16, a2base + k * 1024); });
    if constexpr (pos < 24) issue_g(C, 3 + (pos >> 2), pos & 3, gv);
    if constexpr (pos == 30) { if (b == 0 && wid == 7) {
#pragma unroll
        for (int k = 0; k < 4; ++k) { u32x2 t; ld8_sc1(t, C.sWS, flag_off(0, 0, 0) + (lane * 4 + k) * 128); ef[k][0] = t[0]; ef[k][1] = t[1]; } } }
    if constexpr (pos + 2 < 36) read_w2(pos + 2);
    const bf16x8 af = __builtin_bit_cast(bf16x8, a2[pos]);
    if constexpr (pos == 0) { acc[0] = MFMA32(wf[0][0], af, f32x16{}); acc[1] = MFMA32(wf[0][1], af, f32x16{}); }
    else { acc[0] = MFMA32(wf[pos % 3][0], af, acc[0]); acc[1] = MFMA32(wf[pos % 3][1], af, acc[1]); }
    SB();
  });
  STAMP(9);
#pragma unroll
  for (int d = 0; d < 4; ++d) if (d != kq) {
    const int ct = d >> 1, r0 = (d & 1) * 8;
    const unsigned a = R2 + ((rt * 4 + d) * 4 + kq) * 2048 + lane * 16;
    *lp<f32x4>(a) = f32x4{acc[ct][r0], acc[ct][r0 + 1], acc[ct][r0 + 2], acc[ct][r0 + 3]};
    *lp<f32x4>(a + 1024) = f32x4{acc[ct][r0 + 4], acc[ct][r0 + 5], acc[ct][r0 + 6], acc[ct][r0 + 7]};
  }
  float s[8];
#pragma unroll
  for (int e = 0; e < 8; ++e) { const float lo = (kq & 1) ? acc[0][8 + e] : acc[0][e], hi = (kq & 1) ? acc[1][8 + e] : acc[1][e]; s[e] = (kq & 2) ? hi : lo; }
  asm volatile("s_waitcnt lgkmcnt(0)\n\ts_barrier" ::: "memory");
#pragma unroll
  for (int d = 0; d < 4; ++d) if (d != kq) {
    const unsigned a = R2 + ((rt * 4 + kq) * 4 + d) * 2048 + lane * 16;
    const f32x4 u = *lp<f32x4>(a), v = *lp<f32x4>(a + 1024);
#pragma unroll
    for (int e = 0; e < 4; ++e) { s[e] += u[e]; s[4 + e] += v[e]; }
  }
  {
    unsigned u0 = __float_as_uint(s[0]), u1 = __float_as_uint(s[1]), u2 = __float_as_uint(s[2]), u3 = __float_as_uint(s[3]);
    unsigned v0 = __float_as_uint(s[4]), v1 = __float_as_uint(s[5]), v2 = __float_as_uint(s[6]), v3 = __float_as_uint(s[7]);
    auto r0 = __builtin_amdgcn_permlane32_swap(u0, v0, false, false); auto r1 = __builtin_amdgcn_permlane32_swap(u1, v1, false, false);
    auto r2 = __builtin_amdgcn_permlane32_swap(u2, v2, false, false); auto r3 = __builtin_amdgcn_permlane32_swap(u3, v3, false, false);
    const float c0 = __uint_as_float(r0[0]), c1 = __uint_as_float(r1[0]), c2 = __uint_as_float(r2[0]), c3 = __uint_as_float(r3[0]);
    const float c4 = __uint_as_float(r0[1]), c5 = __uint_as_float(r1[1]), c6 = __uint_as_float(r2[1]), c7 = __uint_as_float(r3[1]);
    u32x4 f;
    f[0] = cvtpk(c0 + bflo(xres[0]), c1 + bfhi(xres[0])); f[1] = cvtpk(c2 + bflo(xres[1]), c3 + bfhi(xres[1]));
    f[2] = cvtpk(c4 + bflo(xres[2]), c5 + bfhi(xres[2])); f[3] = cvtpk(c6 + bflo(xres[3]), c7 + bfhi(xres[3]));
    const i32x4 sO = mk_srd(out, 2048 * 1024);
    st16(f, sO, (RT * 32 + (lane & 31)) * 1024 + cg * 128 + kq * 32 + h * 16, 0);
  }
  if (b == 0 && wid == 7) {
    wait_vm<0>();
    asm volatile("" : "+v"(ef[0]), "+v"(ef[1]), "+v"(ef[2]), "+v"(ef[3]));
    bool ok = true;
#pragma unroll
    for (int k = 0; k < 4; ++k) ok = ok && ef[k][0] == (unsigned)C.tag && ef[k][1] == (unsigned)(C.tag >> 32);
    if (!__all(ok)) {
#pragma unroll 1
      for (int k = 0; k < 4; ++k) spin_flags(ws, flag_off(0, 0, 0) + (lane * 4 + k) * 128, true, C.tag);
    }
    if (lane == 0) __hip_atomic_store((u64*)(ws + WS_EPOCH), C.tag, __ATOMIC_RELAXED, __HIP_MEMORY_SCOPE_AGENT);
  }
  STAMP(60);
}

extern "C" void kernel_launch(void* const* d_in, const int* in_sizes, int n_in, void* d_out, int out_size, void* d_ws, size_t ws_size, hipStream_t stream) {
  (void)in_sizes; (void)n_in; (void)out_size; (void)ws_size;
  (void)hipFuncSetAttribute((const void*)fused_kernel, hipFuncAttributeMaxDynamicSharedMemorySize, LDS_BYTES);
  fused_kernel<<<256, 512, LDS_BYTES, stream>>>((const u16*)d_in[0], (const u16*)d_in[1], (const u16*)d_in[2], (const u16*)d_in[4], (const u16*)d_in[5], (const u16*)d_in[6],
                                                 (u16*)d_out, (char*)d_ws);
}
```

```cpp
#include <hip/hip_runtime.h>
#include <stdint.h>
#include <type_traits>

typedef unsigned short u16;
typedef unsigned long long u64;
typedef short bf16x8 __attribute__((ext_vector_type(8)));
typedef short s16x4 __attribute__((ext_vector_type(4)));
typedef float f32x2 __attribute__((ext_vector_type(2)));
typedef float f32x4 __attribute__((ext_vector_type(4)));
typedef float f32x16 __attribute__((ext_vector_type(16)));
typedef unsigned u32x2 __attribute__((ext_vector_type(2)));
typedef unsigned u32x4 __attribute__((ext_vector_type(4)));
typedef int i32x4 __attribute__((ext_vector_type(4)));
#define LDSP __attribute__((address_space(3)))
#define DI __device__ __forceinline__
template <class T> DI LDSP T* lp(unsigned a) { return (LDSP T*)(size_t)a; }
template <int I, int N, class F> DI void static_for(F&& f) { if constexpr (I < N) { f(std::integral_constant<int, I>{}); static_for<I + 1, N>(f); } }

constexpr size_t WS_EPOCH = 0;
constexpr size_t WS_ERR   = 64;
constexpr size_t WS_FLAGS = 4096;
constexpr size_t WS_QKV   = 1u << 20;
constexpr size_t WS_A2    = 8u << 20;
constexpr size_t WS_DBG   = 24u << 20;

constexpr unsigned R0 = 0, R1 = 32768, R2 = 65536, R3 = 98304;
constexpr unsigned L_K = 98304, L_V = 122880;
constexpr unsigned L_ONES = 147456;
constexpr unsigned L_CSP = 148480;
constexpr unsigned L_CSW = 150528;
constexpr unsigned L_STAT = 152576;
constexpr unsigned L_JUNK = 154624;
constexpr unsigned LDS_BYTES = 163840;
constexpr unsigned CODE_BYTES = 36864;
constexpr unsigned POISON = 0xffffffffu;
constexpr unsigned SPIN_CAP = 1u << 13;

#define NO_WARM 0
#define NO_CS 0
#define NO_DMA 0
constexpr float QSCALE = 0.25f * 1.4426950408889634f;

DI i32x4 mk_srd(const void* p, unsigned bytes) { u64 a = (u64)p; i32x4 r; r[0] = (int)(unsigned)a; r[1] = (int)((unsigned)(a >> 32) & 0xffffu); r[2] = (int)bytes; r[3] = 0x00020000; return r; }
DI void dma16(i32x4 srd, unsigned voff, unsigned soff, unsigned lds) {
  if (NO_DMA) return;
  asm volatile("s_mov_b32 m0, %3\n\ts_nop 0\n\tbuffer_load_dwordx4 %0, %1, %2 offen lds" :: "v"(voff), "s"(srd), "s"(soff), "s"(lds) : "memory");
}
DI void dma4(i32x4 srd, unsigned voff, unsigned lds) {
  asm volatile("s_mov_b32 m0, %2\n\ts_nop 0\n\tbuffer_load_dword %0, %1, 0 offen lds" :: "v"(voff), "s"(srd), "s"(lds) : "memory");
}
DI void ld16_sc1(u32x4& d, i32x4 srd, unsigned voff, unsigned soff) {
  asm volatile("buffer_load_dwordx4 %0, %1, %2, %3 offen sc1" : "=v"(d) : "v"(voff), "s"(srd), "s"(soff) : "memory");
}
DI void ld16(u32x4& d, i32x4 srd, unsigned voff, unsigned soff) {
  asm volatile("buffer_load_dwordx4 %0, %1, %2, %3 offen" : "=v"(d) : "v"(voff), "s"(srd), "s"(soff) : "memory");
}
DI void ld8_sc1(u32x2& d, i32x4 srd, unsigned voff) {
  asm volatile("buffer_load_dwordx2 %0, %1, %2, 0 offen sc1" : "=v"(d) : "v"(voff), "s"(srd) : "memory");
}
DI void st16_sc1(u32x4 d, i32x4 srd, unsigned voff, unsigned soff) {
  asm volatile("buffer_store_dwordx4 %0, %1, %2, %3 offen sc1\n\ts_nop 1" :: "v"(d), "v"(voff), "s"(srd), "s"(soff) : "memory");
}
DI void st16(u32x4 d, i32x4 srd, unsigned voff, unsigned soff) {
  asm volatile("buffer_store_dwordx4 %0, %1, %2, %3 offen\n\ts_nop 1" :: "v"(d), "v"(voff), "s"(srd), "s"(soff) : "memory");
}
template <int N> DI void bar_vm() { static_assert(N >= 0 && N < 64, "vmcnt range"); asm volatile("s_waitcnt vmcnt(%0) lgkmcnt(0)\n\ts_barrier" :: "n"(N) : "memory"); }
template <int N> DI void wait_vm() { static_assert(N >= 0 && N < 64, "vmcnt range"); asm volatile("s_waitcnt vmcnt(%0)" :: "n"(N) : "memory"); }
DI s16x4 ldtr(unsigned a) { return __builtin_amdgcn_ds_read_tr16_b64_v4i16(lp<s16x4>(a)); }
DI bf16x8 ldfrag_tr(unsigned a, unsigned second) {
  s16x4 lo = ldtr(a), hi = ldtr(a + second);
  return __builtin_shufflevector(lo, hi, 0, 1, 2, 3, 4, 5, 6, 7);
}
DI bf16x8 ld128(unsigned a) { return *lp<bf16x8>(a); }
DI unsigned cvtpk(float lo, float hi) { typedef __bf16 b2 __attribute__((ext_vector_type(2))); f32x2 v = {lo, hi}; return __builtin_bit_cast(unsigned, __builtin_convertvector(v, b2)); }
DI float bflo(unsigned v) { return __uint_as_float(v << 16); }
DI float bfhi(unsigned v) { return __uint_as_float(v & 0xffff0000u); }
DI float rbf(float f) { return bflo(cvtpk(f, f) & 0xffffu); }
DI int fresh(int v) { asm volatile("" : "+v"(v)); return v; }
template <class T> DI void pin(T& v) { asm volatile("" : "+v"(v)); }
DI unsigned pinv(unsigned v) { asm volatile("" : "+v"(v)); return v; }
#define MFMA32(a, b, c) __builtin_amdgcn_mfma_f32_32x32x16_bf16((a), (b), (c), 0, 0, 0)
#define MFMA16(a, b, c) __builtin_amdgcn_mfma_f32_16x16x32_bf16((a), (b), (c), 0, 0, 0)
#define MFMA4(a, b, c) __builtin_amdgcn_mfma_f32_4x4x4bf16_1k((a), (b), (c), 0, 0, 0)
#define SB() __builtin_amdgcn_sched_barrier(0)
DI float swap_add(float v) { auto r = __builtin_amdgcn_permlane32_swap(__float_as_uint(v), __float_as_uint(v), false, false); return __uint_as_float(r[0]) + __uint_as_float(r[1]); }
DI float swap_max(float v) { auto r = __builtin_amdgcn_permlane32_swap(__float_as_uint(v), __float_as_uint(v), false, false); return __builtin_fmaxf(__uint_as_float(r[0]), __uint_as_float(r[1])); }

#define STAMP(i) do { } while (0)

struct Ctx {
  char* ws; u64 tag;
  i32x4 sX, sQKVw, sG, sU, sWO, sWD, sWS;
  int lane, wid, mg, ng, rb, cs, c, cg, xi; unsigned stampreg;
};

DI unsigned flag_off(int ev, int rb, int cs) { return (unsigned)(WS_FLAGS + (((ev * 16 + rb) * 16 + cs) << 7)); }
DI void publish(const Ctx& C, int ev) { if (C.wid == 0 && C.lane == 0) __hip_atomic_store((u64*)(C.ws + flag_off(ev, C.rb, C.cs)), C.tag, __ATOMIC_RELAXED, __HIP_MEMORY_SCOPE_AGENT); }
DI void give_up(char* ws) { __hip_atomic_store((unsigned*)(ws + WS_ERR), 1u, __ATOMIC_RELAXED, __HIP_MEMORY_SCOPE_AGENT); }
DI void spin_flags(char* ws, unsigned off, bool need, u64 tag) {
#pragma nounroll
  for (unsigned it = 0; it < SPIN_CAP; ++it) {
    u64 v = __hip_atomic_load((u64*)(ws + off), __ATOMIC_RELAXED, __HIP_MEMORY_SCOPE_AGENT);
    if (__all(!need || v == tag)) return;
    __builtin_amdgcn_s_sleep(2);
  }
  give_up(ws);
}
DI void check_flags(char* ws, unsigned off, bool need, u64 tag, u32x2& got) {
  asm volatile("" : "+v"(got));
  bool ok = !need || (got[0] == (unsigned)tag && got[1] == (unsigned)(tag >> 32));
  if (!__all(ok)) spin_flags(ws, off, need, tag);
}
template <int K0, int K1> DI void check_frags(const Ctx& C, u32x4 (&a2)[36], unsigned base) {
  auto dirty = [&]() { unsigned mx = 0;
    static_for<K0, K1>([&](auto kc) { constexpr int k = decltype(kc)::value;
      mx = __builtin_elementwise_max(mx, __builtin_elementwise_max(__builtin_elementwise_max(a2[k][0], a2[k][1]), __builtin_elementwise_max(a2[k][2], a2[k][3]))); });
    return __any(mx == POISON); };
  if (dirty()) {
#pragma nounroll
    for (unsigned it = 0; it < SPIN_CAP; ++it) {
      __builtin_amdgcn_s_sleep(2);
      static_for<K0, K1>([&](auto kc) { constexpr int k = decltype(kc)::value; ld16_sc1(a2[k], C.sWS, C.lane * 16, base + k * 1024); });
      wait_vm<0>();
      static_for<K0, K1>([&](auto kc) { constexpr int k = decltype(kc)::value; pin(a2[k]); });
      if (!dirty()) return;
    }
    give_up(C.ws);
  }
}

enum { T_H0 = 0, T_S4 = 8, T_E0 = 16, T_G0 = 19, T_POISON = 22, T_POLL, T_KL, T_VL, T_POLL2, T_EPOCH, T_WARM, T_NONE };
template <int TYPE> struct G1 {
  static constexpr int kind(int p) { return p == 1 ? 2 : TYPE == 0 ? (p == 0 ? 0 : 2) : (p == 0 ? 1 : 3); }
  static constexpr int nt(int p) { return (kind(p) == 0 || kind(p) == 3) ? 1 : 2; }
  static constexpr int nst(int p) { return kind(p) == 1 ? 4 : kind(p) == 3 ? 1 : 2; }
  static constexpr int npoison() { return (TYPE == 0 ? 2 : 1) + 1; }
  static constexpr unsigned slot(int s) {
    return s == 4 ? R0 : s == 5 ? R1 : s == 6 ? R2 : s == 7 ? R3 : s == 8 ? R0 : s == 9 ? R1 : s == 10 ? R2 : R0; }
  static constexpr int barpos(int bi) { return bi < 8 ? 4 * bi + 2 : 8 * (bi - 4) + 6; }
  static constexpr int barat(int pos) { return pos < 32 ? ((pos & 3) == 2 ? pos >> 2 : -1) : ((pos & 7) == 6 ? 4 + (pos >> 3) : -1); }
  static constexpr int grp(int bi) { return bi < 4 ? T_H0 + bi + 4 : bi < 11 ? T_S4 + (bi - 4) : bi == 11 ? T_NONE : bi == 12 ? T_S4 + 7 : T_G0 + (bi - 13); }
  static constexpr int gsize(int t) { return t == T_NONE ? 0 : t < T_S4 ? 2 + nt(0) : t < T_E0 ? (nt((t - T_S4 + 4) >> 2) == 1 ? 2 : 4) : 4; }
  static constexpr int rel(int bi, int k) { return bi < 8 ? k : bi == 15 ? (k >> 1) : 2 * k; }
  static constexpr int after(int t, int bi) {
    int cnt = -1;
    auto see = [&](int tag, int n) { for (int i = 0; i < n; ++i) { if (tag == t) cnt = 0; else if (cnt >= 0) ++cnt; } };
    for (int h = 0; h < 2; ++h) see(T_H0 + h, gsize(T_H0 + h));
    see(T_EPOCH, 1);
    for (int pos = 0; pos < 96; ++pos) {
      if (pos < 2) see(T_H0 + 2 + pos, gsize(T_H0 + 2 + pos));
      const int b = barat(pos);
      if (b >= 0) {
        if (b == bi) return cnt;
        if (b == 12) see(T_POLL, 1);
        if (b == 13) see(T_KL, 3);
        if (b == 14) see(T_VL, 4);
        if (b == 15) see(T_POLL2, 1);
      }
      for (int bb = 0; bb < 16; ++bb) for (int k = 0; k < gsize(grp(bb)); ++k) if (barpos(bb) + rel(bb, k) == pos) see(grp(bb), 1);
      if (pos == 50) see(T_POISON, npoison());
      if ((pos & 31) == 31) see(T_E0 + (pos >> 5), nst(pos >> 5));
    }
    return cnt;
  }
  static constexpr int mn(int a, int b) { return a < b ? a : b; }
};

template <int TYPE> DI unsigned tile_addr(const Ctx& C, int p, int t) {
  const int lane = fresh(C.lane), kind = G1<TYPE>::kind(p);
  const int g = (lane >> 4) & 1, h = lane >> 5, q = (lane & 15) >> 2, pp = lane & 3;
  const unsigned rdrow = (8 * h + q) * 128 + pp * 8;
  if (kind == 3) return g * 8192 + (8 * h + q) * 64 + pp * 8 + ((C.ng ^ g) << 5);
  if (kind == 2) return g * 16384 + rdrow + (((2 * C.ng + t) ^ (2 * (q >> 1)) ^ g) << 5);
  if (kind == 0) return 16384 + rdrow + ((C.ng ^ (q >> 1)) << 6) + g * 32;
  return 16384 + C.ng * 8192 + rdrow + ((t ^ (q >> 1)) << 6) + g * 32;
}
template <int TYPE> DI unsigned stage_voff(const Ctx& C, int p) {
  const int kind = G1<TYPE>::kind(p), lane = fresh(C.lane);
  if (kind == 3) return (16 * C.wid + (lane >> 2)) * 3584 + (24 + ((C.cs - 8) >> 1)) * 128 + ((C.cs - 8) & 1) * 64 + ((lane & 3) << 4);
  return (16 * C.wid + (lane >> 3)) * 3584 + (p == 1 ? C.cs : 16 + C.cs) * 128 + (((lane & 7) ^ (((lane >> 4) & 1) << 2)) << 4);
}
template <int TYPE> DI void issue_stage(const Ctx& C, int s, int k, unsigned v0) {
  const int kind = G1<TYPE>::kind(s >> 2);
  const unsigned slot = G1<TYPE>::slot(s), r0 = 128 * (s & 3);
  if (kind == 3) dma16(k ? C.sU : C.sG, k ? v0 ^ 32 : v0, r0 * 3584, slot + k * 8192 + C.wid * 1024);
  else dma16(k >> 1 ? C.sU : C.sG, k >> 1 ? v0 ^ 32 : v0, (r0 + 8 * (k & 1)) * 3584, slot + (k >> 1) * 16384 + C.wid * 2048 + (k & 1) * 1024);
}
DI unsigned hx_voff(const Ctx& C) { const int lane = fresh(C.lane); return (16 * C.wid + (lane >> 3)) * 1024 + (((lane & 7) ^ ((lane >> 4) & 3)) << 4); }
DI unsigned hw_voff(const Ctx& C) { const int lane = fresh(C.lane);
  return (8 * C.wid + (lane >> 3)) * 3072 + (C.cs < 8 ? C.cs : 8 + 2 * (C.cs - 8)) * 128 + (((lane & 7) ^ (((lane >> 4) & 1) << 2)) << 4); }
DI void issue_half(const Ctx& C, int h, int k, unsigned vx, unsigned vw) {
  const unsigned slot = (h & 3) * 32768;
  if (k < 2) dma16(C.sX, k ? (vx ^ 64) + 8 * 1024 : vx, h * 128, slot + C.wid * 2048 + k * 1024);
  else dma16(C.sQKVw, vw + (k - 2) * 128, h * 64 * 3072, slot + 16384 + (k - 2) * 8192 + C.wid * 1024);
}
DI void warm(i32x4 srd, unsigned line0, unsigned lines_per_row_log2, unsigned pitch, unsigned colbyte, int lane) {
  const unsigned l = line0 + lane;
  dma4(srd, (l >> lines_per_row_log2) * pitch + colbyte + (l & ((1u << lines_per_row_log2) - 1)) * 128, L_JUNK);
}
DI unsigned g_voff(const Ctx& C) { const int lane = fresh(C.lane); return (32 * (C.wid & 1) + (lane >> 3)) * 1024 + C.cg * 128 + (((lane & 7) ^ (((lane >> 4) & 1) << 2)) << 4); }
constexpr unsigned gslot(int j) { return j == 0 ? R1 : j == 1 ? R2 : j == 2 ? R0 : j == 3 ? R3 : j == 4 ? R1 : j == 5 ? R2 : j == 6 ? R0 : j == 7 ? R3 : R1; }
DI void issue_g(const Ctx& C, int j, int i, unsigned v) {
  const int kq = C.wid >> 1;
  const int row0 = j < 4 ? (4 * kq + j) * 64 : j < 7 ? (16 + 3 * kq + (j - 4)) * 64 : 128 * kq + 64 * (j - 7);
  dma16(j < 7 ? C.sWD : C.sWO, v, (unsigned)(row0 + 8 * i) * 1024, gslot(j) + kq * 8192 + (C.wid & 1) * 4096 + i * 1024);
}

template <int TYPE> DI void epi_qkv(const Ctx& C, const f32x16& acc, const f32x4 (&csr)[4], float m, float rinv, int qu, int h4) {
  const int h = C.lane >> 5;
  const float sc = TYPE == 0 ? rinv * QSCALE : rinv;
  float y[16];
#pragma unroll
  for (int i4 = 0; i4 < 4; ++i4)
#pragma unroll
    for (int e = 0; e < 4; ++e) y[4 * i4 + e] = (acc[4 * i4 + e] - csr[i4][e]) * sc;
  const unsigned base = (unsigned)WS_QKV + (((C.rb * 24 + qu) * 4 + C.mg) * 4 + h4) * 1024;
#pragma unroll
  for (int hh = 0; hh < 2; ++hh) {
    u32x4 f; f[0] = cvtpk(y[8 * hh], y[8 * hh + 1]); f[1] = cvtpk(y[8 * hh + 2], y[8 * hh + 3]); f[2] = cvtpk(y[8 * hh + 4], y[8 * hh + 5]); f[3] = cvtpk(y[8 * hh + 6], y[8 * hh + 7]);
    unsigned voff = C.lane * 16;
    if (TYPE == 1 && qu >= 16) {
      auto a = __builtin_amdgcn_permlane32_swap(f[0], f[2], false, false); auto b = __builtin_amdgcn_permlane32_swap(f[1], f[3], false, false);
      f[0] = a[0]; f[2] = a[1]; f[1] = b[0]; f[3] = b[1];
      voff = (C.lane & 31) * 32 + h * 16;
    }
    st16_sc1(f, C.sWS, voff, base + hh * 1024);
  }
}
DI void epi_gu(const Ctx& C, const f32x16& acc, const f32x4 (&csr)[4], float m, float r2, unsigned a2off) {
  float y[8];
#pragma unroll
  for (int i4 = 0; i4 < 2; ++i4)
#pragma unroll
    for (int e = 0; e < 4; ++e) y[4 * i4 + e] = ((acc[4 * i4 + e] - csr[i4][e]) * r2) * (acc[8 + 4 * i4 + e] - csr[2 + i4][e]);
  u32x4 f; f[0] = cvtpk(y[0], y[1]); f[1] = cvtpk(y[2], y[3]); f[2] = cvtpk(y[4], y[5]); f[3] = cvtpk(y[6], y[7]);
  st16_sc1(f, C.sWS, C.lane * 16, a2off);
}
template <int TYPE> DI unsigned a2_pass(const Ctx& C, int p) {
  if (p == 1) return (unsigned)WS_A2 + (((4 * C.rb + C.mg) * 4 + (C.cs >> 2)) * 36 + 4 * (C.cs & 3) + 2 * C.ng) * 1024;
  if (TYPE == 0) return (unsigned)WS_A2 + (((4 * C.rb + C.mg) * 4 + C.cs / 3) * 36 + 4 * (4 + C.cs % 3) + 2 * C.ng) * 1024;
  const int u = 8 + ((C.cs - 8) >> 1), half = (C.cs - 8) & 1;
  return (unsigned)WS_A2 + (((4 * C.rb + C.mg) * 4 + u / 3) * 36 + 4 * (4 + u % 3) + 2 * half + C.ng) * 1024;
}
DI unsigned a2_o(const Ctx& C) { return (unsigned)WS_A2 + (((4 * C.rb + C.mg) * 4 + (C.cs >> 2)) * 36 + 28 + 2 * (C.cs & 3) + C.ng) * 1024; }

template <int TYPE> DI void gemm1(Ctx& C, unsigned vx, unsigned vw, u32x4& qfrag, u32x2& poll2, unsigned poll2off) {
  using G = G1<TYPE>;
  const int lane = C.lane;
  char* ws = C.ws;
  bf16x8 xa[32];
  bf16x8 wf[3][2];
  f32x16 acc[2];
  unsigned ta[2], sv = 0;
  ta[0] = tile_addr<TYPE>(C, 0, 0); ta[1] = tile_addr<TYPE>(C, 0, 1);
  const unsigned r31 = lane & 31, hh = lane >> 5;
  const unsigned xlane = (32 * C.mg + r31) * 128 + ((hh ^ ((r31 >> 1) & 7)) << 4);
  f32x4 ssum = {0, 0, 0, 0}, ssq = {0, 0, 0, 0};
  f32x4 csacc[2] = {{0,0,0,0},{0,0,0,0}};
  bf16x8 sel;
  { const short one = ((lane & 15) == ((lane >> 4) & 1)) ? (short)0x3f80 : (short)0;
#pragma unroll
    for (int e = 0; e < 8; ++e) sel[e] = one; }
  float csn[2];
  f32x4 csr[2][4];
  float m = 0.f, rinv = 0.f;
  u32x2 pollv; u32x4 gk[3];
  unsigned polloff = 0; bool pollneed = false;

  STAMP(1);
  bar_vm<G::gsize(T_H0)>();
  STAMP(2);
  u32x2 ev; ld8_sc1(ev, C.sWS, (unsigned)WS_EPOCH);
  auto read_x = [&](int h) {
    const unsigned base = (h & 3) * 32768 + xlane;
#pragma unroll
    for (int i = 0; i < 4; ++i) xa[4 * h + i] = ld128(base ^ (((i + C.c) & 3) << 5));
  };
  auto read_w = [&](int pos) {
    const int p = pos >> 5, s = pos >> 3, i = pos & 7;
    const bool half = G::kind(p) == 3;
    if ((pos & 31) == 0 && pos > 0) { ta[0] = tile_addr<TYPE>(C, p, 0); ta[1] = tile_addr<TYPE>(C, p, 1); }
    const unsigned stride = half ? 1024 : 2048, second = half ? 256 : 512;
    const unsigned so = (p == 0 ? ((pos >> 2) & 3) * 32768 : G::slot(s) + (i & 4) * stride) + ((i + C.c) & 3) * stride;
    wf[pos % 3][0] = ldfrag_tr(pinv(ta[0] + so), second);
    if (G::nt(p) == 2) wf[pos % 3][1] = ldfrag_tr(pinv(ta[1] + so), second);
  };
  read_x(0); read_w(0); read_w(1);
  static_for<0, 96>([&](auto ic) {
    constexpr int pos = decltype(ic)::value;
    constexpr int p = pos >> 5, s = pos >> 3, st = s & 3, i = pos & 7, NT = G::nt(p), bi = G::barat(pos);
    if constexpr (pos < 2) static_for<0, G::gsize(T_H0)>([&](auto kc) { issue_half(C, 2 + pos, decltype(kc)::value, vx, vw); });
    if constexpr (bi >= 0) {
      STAMP(10 + 2 * bi);
      if constexpr (bi == 0) { bar_vm<G::after(T_H0 + 1, 0)>(); }
      else if constexpr (bi < 7) bar_vm<G::after(T_H0 + bi + 1, bi)>();
      else if constexpr (bi == 9) { bar_vm<G::mn(G::after(T_S4 + 2, 9), G::after(T_E0, 9))>(); publish(C, 0); }
      else if constexpr (bi == 13) { static_assert(G::after(T_E0 + 1, 13) <= G::after(T_POISON, 13), ""); bar_vm<G::mn(G::mn(G::after(T_S4 + 6, 13), G::after(T_E0 + 1, 13)), G::after(T_POLL, 13))>(); }
      else if constexpr (bi == 14) bar_vm<G::mn(G::after(T_S4 + 7, 14), G::after(T_KL, 14))>();
      else if constexpr (bi == 15) bar_vm<G::after(T_VL, 15)>();
      else bar_vm<G::after(T_S4 + bi - 7, bi)>();
      if constexpr (bi == 4) {
        static_assert(G::after(T_H0 + 5, 4) <= G::after(T_EPOCH, 4), "");
        pin(ev);
        const u64 t = (((u64)ev[1] << 32) | ev[0]) * 6364136223846793005ull + 1442695040888963407ull;
        C.tag = ((u64)__builtin_amdgcn_readfirstlane((unsigned)(t >> 32)) << 32) | __builtin_amdgcn_readfirstlane((unsigned)t);
      }
      if constexpr (bi == 12) {
        const int u = C.cs >> 1, l = lane & 7;
        const int ty = l == 0 ? 0 : l < 4 ? 1 : 2, rbp = l == 0 ? C.rb : C.rb - 2 + (l - 1) % 3;
        const int qu = 8 * ty + u;
        const int pcs = qu < 8 ? qu : 8 + ((qu - 8) >> 1);
        pollneed = l < 7 && rbp >= 0;
        polloff = flag_off(0, pollneed ? rbp : C.rb, pcs);
        ld8_sc1(pollv, C.sWS, polloff);
      }
      if constexpr (bi == 13) {
        check_flags(ws, polloff, pollneed, C.tag, pollv);
        const int u = C.cs >> 1, h4 = 2 * (C.cs & 1) + C.ng;
#pragma unroll
        for (int b = 0; b < 3; ++b) { const int rbp = C.rb - 2 + b < 0 ? 0 : C.rb - 2 + b;
          ld16_sc1(gk[b], C.sWS, lane * 16, (unsigned)WS_QKV + (((rbp * 24 + 8 + u) * 4 + C.mg) * 4 + h4) * 1024); }
        publish(C, 1);
      }
      if constexpr (bi == 14) {
        pin(gk[0]); pin(gk[1]); pin(gk[2]);
#pragma unroll
        for (int b = 0; b < 3; ++b) *lp<u32x4>(L_K + ((4 * b + C.mg) * 2 + C.ng) * 1024 + lane * 16) = gk[b];
        const int u = C.cs >> 1, h4 = 2 * (C.cs & 1) + C.ng;
#pragma unroll
        for (int b = 0; b < 3; ++b) { const int rbp = C.rb - 2 + b < 0 ? 0 : C.rb - 2 + b;
          ld16_sc1(gk[b], C.sWS, lane * 16, (unsigned)WS_QKV + (((rbp * 24 + 16 + u) * 4 + C.mg) * 4 + h4) * 1024); }
        ld16_sc1(qfrag, C.sWS, lane * 16, (unsigned)WS_QKV + (((C.rb * 24 + u) * 4 + C.mg) * 4 + h4) * 1024);
      }
      if constexpr (bi == 15) {
        pin(qfrag); pin(gk[0]); pin(gk[1]); pin(gk[2]);
#pragma unroll
        for (int b = 0; b < 3; ++b) *lp<u32x4>(L_V + C.ng * 12288 + (4 * b + C.mg) * 1024 + lane * 16) = gk[b];
        ld8_sc1(poll2, C.sWS, poll2off);
      }
      if constexpr (bi < 7) read_x(bi + 1);
      if constexpr (st == 3 && i == 6) {
#pragma unroll
        for (int t = 0; t < NT; ++t) { const unsigned a = L_CSP + (C.ng * 2 + t) * 512 + r31 * 4;
          csn[t] = (*lp<float>(a) + *lp<float>(a + 128)) + (*lp<float>(a + 256) + *lp<float>(a + 384)); }
      }
      STAMP(11 + 2 * bi);
    }
    static_for<0, 16>([&](auto bc) {
      constexpr int bb = decltype(bc)::value, tg = G::grp(bb), k0 = pos - G::barpos(bb);
      static_for<0, 4>([&](auto kc) {
        constexpr int k = decltype(kc)::value;
        if constexpr (k < G::gsize(tg) && G::rel(bb, k) == k0) {
          if constexpr (tg < T_S4) issue_half(C, tg, k, vx, vw);
          else if constexpr (tg >= T_G0) { if constexpr (k == 0) sv = g_voff(C); issue_g(C, tg - T_G0, k, sv); }
          else { if constexpr (k == 0) sv = stage_voff<TYPE>(C, (tg - T_S4 + 4) >> 2); issue_stage<TYPE>(C, tg - T_S4 + 4, k, sv); }
        }
      });
    });
    if constexpr (pos == 33 && !NO_WARM) {
      const int gu = TYPE == 0 ? 16 + C.cs : 24 + ((C.cs - 8) >> 1);
      if (C.wid < 2) warm(C.wid ? C.sU : C.sG, (C.rb & 7) * 64, 0, 3584, gu * 128, fresh(lane));
    }
    if constexpr (pos >= 39 && pos < 39 + 4 * 9 && ((pos - 39) & 3) == 0 && !NO_WARM) {
      constexpr int k = (pos - 39) >> 2;
      const unsigned l = (C.xi * 9 + k) * 64;
      if (C.wid == (k & 7)) warm(l < 4096 ? C.sWO : C.sWD, l < 4096 ? l : l - 4096, 3, 1024, 0, fresh(lane));
    }
    if constexpr (pos == 50) {
      const u32x4 pz = {POISON, POISON, POISON, POISON};
      const unsigned a = a2_pass<TYPE>(C, 2);
      st16_sc1(pz, C.sWS, lane * 16, a);
      if (TYPE == 0) st16_sc1(pz, C.sWS, lane * 16, a + 1024);
      st16_sc1(pz, C.sWS, lane * 16, a2_o(C));
    }
    if constexpr (pos + 2 < 96) read_w(pos + 2);
    if constexpr ((pos & 31) == 0) { acc[0] = MFMA32(wf[pos % 3][0], xa[pos & 31], f32x16{}); if (NT == 2) acc[1] = MFMA32(wf[pos % 3][1], xa[pos & 31], f32x16{}); }
    else { acc[0] = MFMA32(wf[pos % 3][0], xa[pos & 31], acc[0]); if (NT == 2) acc[1] = MFMA32(wf[pos % 3][1], xa[pos & 31], acc[1]); }
    if constexpr ((i & 3) == 0 && !NO_CS) {
      if constexpr ((pos & 31) == 0) { csacc[0] = MFMA16(wf[pos % 3][0], sel, (f32x4{0, 0, 0, 0})); if (NT == 2) csacc[1] = MFMA16(wf[pos % 3][1], sel, (f32x4{0, 0, 0, 0})); }
      else { csacc[0] = MFMA16(wf[pos % 3][0], sel, csacc[0]); if (NT == 2) csacc[1] = MFMA16(wf[pos % 3][1], sel, csacc[1]); }
    }
    if constexpr (p == 0 && (i & 3) < 2) {
      const bf16x8 xv = xa[pos & 31]; const s16x4 lo = __builtin_shufflevector(xv, xv, 0, 1, 2, 3), hi = __builtin_shufflevector(xv, xv, 4, 5, 6, 7);
      const s16x4 ones = {0x3f80, 0x3f80, 0x3f80, 0x3f80};
      ssum = MFMA4(ones, lo, ssum); ssq = MFMA4(lo, lo, ssq); ssum = MFMA4(ones, hi, ssum); ssq = MFMA4(hi, hi, ssq);
    }
    if constexpr (st == 3 && i == 5) {
      if ((lane & 15) < 2) {
#pragma unroll
        for (int t = 0; t < NT; ++t) *lp<f32x4>(L_CSP + ((C.ng * 2 + t) * 4 + C.mg) * 128 + (16 * (lane & 15) + 4 * (lane >> 4)) * 4) = csacc[t];
      }
      if constexpr (p == 0) {
        const float sq = (lane & 3) == 0 ? ssq[0] : (lane & 3) == 1 ? ssq[1] : (lane & 3) == 2 ? ssq[2] : ssq[3];
        const f32x2 v = {swap_add(ssum[0]), swap_add(sq)};
        if (lane < 32) *lp<f32x2>(L_STAT + (C.ng * 128 + 32 * C.mg + lane) * 8) = v;
      }
    }
    if constexpr (st == 3 && i == 6) {
#pragma unroll
      for (int t = 0; t < NT; ++t) *lp<float>(L_CSW + (C.wid * 2 + t) * 128 + r31 * 4) = csn[t];
    }
    if constexpr (st == 3 && i == 6 && p == 0) {
      const f32x2 a = *lp<f32x2>(L_STAT + (32 * C.mg + r31) * 8), b = *lp<f32x2>(L_STAT + (128 + 32 * C.mg + r31) * 8);
      const float mean = (a[0] + b[0]) * (1.f / 512.f);
      const float var = __builtin_fmaxf((a[1] + b[1]) * (1.f / 512.f) - mean * mean, 0.f);
      m = rbf(mean);
      rinv = __builtin_amdgcn_rcpf(rbf(__builtin_amdgcn_sqrtf(rbf(rbf(var) + 1e-5f))));
      pin(m); pin(rinv);
    }
    if constexpr (st == 3 && i == 7) {
#pragma unroll
      for (int t = 0; t < NT; ++t)
#pragma unroll
        for (int i4 = 0; i4 < 4; ++i4) { csr[t][i4] = *lp<f32x4>(L_CSW + (C.wid * 2 + t) * 128 + (8 * i4 + 4 * hh) * 4) * m; pin(csr[t][i4]); }
    }
    SB();
    if constexpr ((pos & 31) == 31) {
      if constexpr (G::kind(p) == 2) {
        const unsigned a2 = a2_pass<TYPE>(C, p);
        epi_gu(C, acc[0], csr[0], m, rinv * rinv, a2); epi_gu(C, acc[1], csr[1], m, rinv * rinv, a2 + 1024);
      } else if constexpr (G::kind(p) == 3) {
        epi_gu(C, acc[0], csr[0], m, rinv * rinv, a2_pass<TYPE>(C, p));
      } else if constexpr (G::kind(p) == 0) {
        epi_qkv<0>(C, acc[0], csr[0], m, rinv, C.cs, 2 * C.ng);
      } else {
        const int qu = 8 + 2 * (C.cs - 8) + C.ng;
        epi_qkv<1>(C, acc[0], csr[0], m, rinv, qu, 0); epi_qkv<1>(C, acc[1], csr[1], m, rinv, qu, 2);
      }
      SB();
    }
  });
  wait_vm<G::after(T_POLL2, 16)>();
}

template <bool EDGE, class SIDE> DI u32x4 attention(Ctx& C, const u32x4& qraw, SIDE&& side) {
  const int lane = C.lane, h = lane >> 5, g = (lane >> 4) & 1, q = (lane & 15) >> 2, pp = lane & 3;
  const bf16x8 qf = __builtin_bit_cast(bf16x8, qraw);
  const unsigned kaddr = pinv(L_K + (C.mg * 2 + C.ng) * 1024 + lane * 16);
  const int t0 = EDGE ? (C.rb == 0 ? 8 : 4) - C.mg : 1;
  const int dq = (lane & 31) - 4 * h;
  const float NEG = -1e30f;
  auto older = [&](int r) { return ((r & 3) + 8 * (r >> 2)) > dq; };
  auto max16 = [&](float& a, float& b, const f32x16& s) {
#pragma unroll
    for (int r = 0; r < 16; r += 4) { a = __builtin_fmaxf(__builtin_fmaxf(a, s[r]), s[r + 1]); b = __builtin_fmaxf(__builtin_fmaxf(b, s[r + 2]), s[r + 3]); } };
  bf16x8 kf[9];
#pragma unroll
  for (int t = 0; t < 9; ++t) if (!EDGE || t > 0) kf[t] = ld128(kaddr + t * 2048);
  float mx;
  {
    f32x16 S[3];
    f32x16 sl = MFMA32(kf[8], qf, f32x16{});
    f32x16 sf;
    if (!EDGE) sf = MFMA32(kf[0], qf, f32x16{});
    S[1] = MFMA32(kf[1], qf, f32x16{});
    S[2] = MFMA32(kf[2], qf, f32x16{});
    float a = NEG, b = NEG;
    side(std::integral_constant<int, 0>{});
    SB();
#pragma unroll
    for (int r = 0; r < 16; ++r) sl[r] = older(r) ? (EDGE ? NEG : sf[r]) : sl[r];
    max16(a, b, sl);
    static_for<1, 8>([&](auto tc) { constexpr int t = decltype(tc)::value;
      if constexpr (t + 2 < 8) S[(t + 2) % 3] = MFMA32(kf[t + 2], qf, f32x16{});
      side(std::integral_constant<int, t>{});
      SB();
      if (!EDGE || t >= t0) max16(a, b, S[t % 3]);
      SB(); });
    mx = swap_max(__builtin_fmaxf(a, b));
  }
  f32x16 negm;
#pragma unroll
  for (int r = 0; r < 16; ++r) negm[r] = -mx;
  const unsigned vbase = pinv(g == 0 ? L_V + C.ng * 12288 + C.mg * 1024 + (4 * h + q) * 32 + pp * 8 : L_ONES + 128 * (1 - h) + q * 32 + pp * 8);
  const unsigned vstr = g == 0 ? 1024u : 0u;
  f32x16 o;
  auto expack = [&](const f32x16& s, u32x4& pa, u32x4& pb) {
#pragma unroll
    for (int i = 0; i < 4; ++i) pa[i] = cvtpk(__builtin_amdgcn_exp2f(s[2 * i]), __builtin_amdgcn_exp2f(s[2 * i + 1]));
#pragma unroll
    for (int i = 0; i < 4; ++i) pb[i] = cvtpk(__builtin_amdgcn_exp2f(s[8 + 2 * i]), __builtin_amdgcn_exp2f(s[9 + 2 * i])); };
  f32x16 S[2];
  bf16x8 vf[2][2];
  {
    f32x16 sl = MFMA32(kf[8], qf, negm);
    f32x16 sf;
    if (!EDGE) sf = MFMA32(kf[0], qf, negm);
    S[1] = MFMA32(kf[1], qf, negm);
    const unsigned v8 = vbase + 8 * vstr;
    const bf16x8 va = ldfrag_tr(v8, 256), vb = ldfrag_tr(v8 + 512, 256);
    bf16x8 vc, vd;
    if (!EDGE) { vc = ldfrag_tr(vbase, 256); vd = ldfrag_tr(vbase + 512, 256); }
    vf[1][0] = ldfrag_tr(vbase + vstr, 256); vf[1][1] = ldfrag_tr(vbase + vstr + 512, 256);
    side(std::integral_constant<int, 8>{});
    SB();
#pragma unroll
    for (int r = 0; r < 16; ++r) sl[r] = older(r) ? (EDGE ? NEG : sf[r]) : sl[r];
    u32x4 pa, pb, mk0, mk1;
    expack(sl, pa, pb);
#pragma unroll
    for (int i = 0; i < 4; ++i) {
      mk0[i] = (older(2 * i) ? 0xffffu : 0u) | (older(2 * i + 1) ? 0xffff0000u : 0u);
      mk1[i] = (older(8 + 2 * i) ? 0xffffu : 0u) | (older(9 + 2 * i) ? 0xffff0000u : 0u); }
    o = MFMA32(va, __builtin_bit_cast(bf16x8, pa & ~mk0), f32x16{});
    o = MFMA32(vb, __builtin_bit_cast(bf16x8, pb & ~mk1), o);
    if (!EDGE) {
      o = MFMA32(vc, __builtin_bit_cast(bf16x8, pa & mk0), o);
      o = MFMA32(vd, __builtin_bit_cast(bf16x8, pb & mk1), o);
    }
    SB();
  }
  static_for<1, 8>([&](auto tc) { constexpr int t = decltype(tc)::value;
    if constexpr (t + 1 < 8) {
      const unsigned vt = vbase + (t + 1) * vstr;
      vf[(t + 1) & 1][0] = ldfrag_tr(vt, 256); vf[(t + 1) & 1][1] = ldfrag_tr(vt + 512, 256);
      S[(t + 1) & 1] = MFMA32(kf[t + 1], qf, negm); }
    side(std::integral_constant<int, 8 + t>{});
    SB();
    if (!EDGE || t >= t0) {
      u32x4 pa, pb;
      expack(S[t & 1], pa, pb);
      o = MFMA32(vf[t & 1][0], __builtin_bit_cast(bf16x8, pa), o);
      o = MFMA32(vf[t & 1][1], __builtin_bit_cast(bf16x8, pb), o);
    }
    SB(); });
  const float li = __builtin_amdgcn_rcpf(o[8]);
  u32x4 f; f[0] = cvtpk(o[0] * li, o[1] * li); f[1] = cvtpk(o[2] * li, o[3] * li); f[2] = cvtpk(o[4] * li, o[5] * li); f[3] = cvtpk(o[6] * li, o[7] * li);
  return f;
}

__global__ void __launch_bounds__(512, 2)
fused_kernel(const u16* __restrict__ x, const u16* __restrict__ wqkv, const u16* __restrict__ wo, const u16* __restrict__ wg, const u16* __restrict__ wu,
             const u16* __restrict__ wd, u16* __restrict__ out, char* __restrict__ ws) {
  Ctx C;
  const int tid = threadIdx.x, lane = tid & 63, wid = __builtin_amdgcn_readfirstlane(tid >> 6);
  const int b = blockIdx.x, xj = b & 7, xi = b >> 3;
  C.stampreg = 0;
  STAMP(0);
  asm volatile("" :: "s"(x), "s"(wqkv), "s"(wo), "s"(wg), "s"(wu), "s"(wd), "s"(out), "s"(ws));
  C.ws = ws; C.lane = lane; C.wid = wid; C.mg = wid & 3; C.ng = wid >> 2;
  C.rb = 8 * (xj & 1) + (xi & 7); C.cs = 4 * (xj >> 1) + (xi >> 3); C.c = (C.mg + 2 * C.ng) & 3;
  C.sX = mk_srd(x + (size_t)C.rb * 128 * 512, 128 * 1024);
  C.sQKVw = mk_srd(wqkv, 512 * 3072);
  C.xi = xi;
  const unsigned vx = hx_voff(C), vw = hw_voff(C);
  STAMP(3);
  issue_half(C, 0, 0, vx, vw); issue_half(C, 0, 1, vx, vw); issue_half(C, 0, 2, vx, vw); if (C.cs >= 8) issue_half(C, 0, 3, vx, vw);
  STAMP(4);
  issue_half(C, 1, 0, vx, vw); issue_half(C, 1, 1, vx, vw); issue_half(C, 1, 2, vx, vw); if (C.cs >= 8) issue_half(C, 1, 3, vx, vw);
  STAMP(5);
  C.sWS = mk_srd(ws, 32u << 20);
  C.sG = mk_srd(wg, 512 * 3584); C.sU = mk_srd(wu, 512 * 3584); C.sWO = mk_srd(wo, 512 * 1024); C.sWD = mk_srd(wd, 1792 * 1024);
  if (wid == 7) { u64 pc; asm volatile("s_getpc_b64 %0" : "=s"(pc));
    dma4(mk_srd((const void*)(pc & ~255ull), CODE_BYTES), (xi * 9 + (lane < 9 ? lane : 0)) * 128, L_JUNK); }
  if (tid < 256) lp<unsigned>(L_ONES)[tid] = 0x3f803f80u;
  const int rt64 = 4 * xj + (xi & 3), cg = xi >> 2;
  C.cg = cg;
  const int rt = wid & 1, kq = wid >> 1, RT = 2 * rt64 + rt, rb2 = RT >> 2;
  const bool p2need = lane < 10;
  unsigned p2off;
  { const int l = lane < 10 ? lane : 0, u = 3 * kq + ((l - 4) >> 1);
    p2off = flag_off(1, rb2, l < 4 ? 4 * kq + l : u < 8 ? u : 8 + 2 * (u - 8) + (l & 1)); }
  u32x4 qfrag; u32x2 poll2;
  if (C.cs < 8) gemm1<0>(C, vx, vw, qfrag, poll2, p2off); else gemm1<1>(C, vx, vw, qfrag, poll2, p2off);
  STAMP(6);

  const unsigned a2base = (unsigned)WS_A2 + (RT * 4 + kq) * 36 * 1024;
  u32x4 a2[36];
  check_flags(ws, p2off, p2need, C.tag, poll2);
  asm volatile("s_waitcnt lgkmcnt(0)\n\ts_barrier" ::: "memory");
  auto side = [&](auto kc) { constexpr int k = decltype(kc)::value;
    if constexpr (k < 8) { ld16_sc1(a2[2 * k], C.sWS, lane * 16, a2base + 2 * k * 1024); ld16_sc1(a2[2 * k + 1], C.sWS, lane * 16, a2base + (2 * k + 1) * 1024); }
    if constexpr (k >= 12) static_for<0, 3>([&](auto jc) { constexpr int q_ = 16 + 3 * (k - 12) + decltype(jc)::value; ld16_sc1(a2[q_], C.sWS, lane * 16, a2base + q_ * 1024); }); };
  u32x4 of;
  if (C.rb >= 2) of = attention<false>(C, qfrag, side); else of = attention<true>(C, qfrag, side);
  st16_sc1(of, C.sWS, lane * 16, a2_o(C));
  STAMP(7);

  const int g = (lane >> 4) & 1, h = lane >> 5, q = (lane & 15) >> 2, pp = lane & 3;
  const unsigned w2l0 = kq * 8192 + (4 * h + q) * 128 + ((0 ^ (q >> 1)) << 6) + g * 32 + pp * 8;
  const unsigned w2l1 = kq * 8192 + (4 * h + q) * 128 + ((1 ^ (q >> 1)) << 6) + g * 32 + pp * 8;
  bf16x8 wf[3][2];
  f32x16 acc[2];
  u32x4 xres;
  unsigned wb[2], gv;
  auto read_w2 = [&](int pos) {
    if ((pos & 3) == 0) { wb[0] = pinv(w2l0 + gslot(pos >> 2)); wb[1] = pinv(w2l1 + gslot(pos >> 2)); }
    wf[pos % 3][0] = ldfrag_tr(wb[0] + (pos & 3) * 2048, 1024); wf[pos % 3][1] = ldfrag_tr(wb[1] + (pos & 3) * 2048, 1024); };
  bar_vm<13>();
  static_for<0, 16>([&](auto kc) { constexpr int k = decltype(kc)::value; pin(a2[k]); });
  read_w2(0); read_w2(1);
  gv = g_voff(C);
  STAMP(8);
  u32x4 ef[4];
  static_for<0, 36>([&](auto ic) {
    constexpr int pos = decltype(ic)::value;
    constexpr int j = pos >> 2, i = pos & 3;
    if constexpr (i == 2) {
      STAMP(44 + 2 * j);
      if constexpr (j <= 1) bar_vm<63>();
      if constexpr (j == 2) bar_vm<6>();
      if constexpr (j == 3) bar_vm<6>();
      if constexpr (j == 4) bar_vm<7>();
      if constexpr (j == 5) bar_vm<15>();
      if constexpr (j == 6) bar_vm<4>();
      if constexpr (j == 7) bar_vm<0>();
      STAMP(45 + 2 * j);
    }
    if constexpr (pos == 14) { static_for<16, 28>([&](auto kc) { pin(a2[decltype(kc)::value]); }); check_frags<16, 28>(C, a2, a2base); }
    if constexpr (pos == 26) { static_for<28, 36>([&](auto kc) { pin(a2[decltype(kc)::value]); }); pin(xres); check_frags<28, 36>(C, a2, a2base); }
    if constexpr (pos == 16) { const i32x4 sXall = mk_srd(x, 2048 * 1024); ld16(xres, sXall, (RT * 32 + (lane & 31)) * 1024 + cg * 128 + kq * 32 + h * 16, 0); }
    if constexpr (pos == 18 || pos == 19) static_for<0, 4>([&](auto kc) { constexpr int k = 28 + 4 * (pos - 18) + decltype(kc)::value; ld16_sc1(a2[k], C.sWS, lane * 16, a2base + k * 1024); });
    if constexpr (pos < 24) issue_g(C, 3 + (pos >> 2), pos & 3, gv);
    if constexpr (pos == 30) { if (b == 0 && wid == 7) {
#pragma unroll
        for (int k = 0; k < 4; ++k) { u32x2 t; ld8_sc1(t, C.sWS, flag_off(0, 0, 0) + (lane * 4 + k) * 128); ef[k][0] = t[0]; ef[k][1] = t[1]; } } }
    if constexpr (pos + 2 < 36) read_w2(pos + 2);
    const bf16x8 af = __builtin_bit_cast(bf16x8, a2[pos]);
    if constexpr (pos == 0) { acc[0] = MFMA32(wf[0][0], af, f32x16{}); acc[1] = MFMA32(wf[0][1], af, f32x16{}); }
    else { acc[0] = MFMA32(wf[pos % 3][0], af, acc[0]); acc[1] = MFMA32(wf[pos % 3][1], af, acc[1]); }
    SB();
  });
  STAMP(9);
#pragma unroll
  for (int d = 0; d < 4; ++d) if (d != kq) {
    const int ct = d >> 1, r0 = (d & 1) * 8;
    const unsigned a = R2 + ((rt * 4 + d) * 4 + kq) * 2048 + lane * 16;
    *lp<f32x4>(a) = f32x4{acc[ct][r0], acc[ct][r0 + 1], acc[ct][r0 + 2], acc[ct][r0 + 3]};
    *lp<f32x4>(a + 1024) = f32x4{acc[ct][r0 + 4], acc[ct][r0 + 5], acc[ct][r0 + 6], acc[ct][r0 + 7]};
  }
  float s[8];
#pragma unroll
  for (int e = 0; e < 8; ++e) { const float lo = (kq & 1) ? acc[0][8 + e] : acc[0][e], hi = (kq & 1) ? acc[1][8 + e] : acc[1][e]; s[e] = (kq & 2) ? hi : lo; }
  asm volatile("s_waitcnt lgkmcnt(0)\n\ts_barrier" ::: "memory");
#pragma unroll
  for (int d = 0; d < 4; ++d) if (d != kq) {
    const unsigned a = R2 + ((rt * 4 + kq) * 4 + d) * 2048 + lane * 16;
    const f32x4 u = *lp<f32x4>(a), v = *lp<f32x4>(a + 1024);
#pragma unroll
    for (int e = 0; e < 4; ++e) { s[e] += u[e]; s[4 + e] += v[e]; }
  }
  {
    unsigned u0 = __float_as_uint(s[0]), u1 = __float_as_uint(s[1]), u2 = __float_as_uint(s[2]), u3 = __float_as_uint(s[3]);
    unsigned v0 = __float_as_uint(s[4]), v1 = __float_as_uint(s[5]), v2 = __float_as_uint(s[6]), v3 = __float_as_uint(s[7]);
    auto r0 = __builtin_amdgcn_permlane32_swap(u0, v0, false, false); auto r1 = __builtin_amdgcn_permlane32_swap(u1, v1, false, false);
    auto r2 = __builtin_amdgcn_permlane32_swap(u2, v2, false, false); auto r3 = __builtin_amdgcn_permlane32_swap(u3, v3, false, false);
    const float c0 = __uint_as_float(r0[0]), c1 = __uint_as_float(r1[0]), c2 = __uint_as_float(r2[0]), c3 = __uint_as_float(r3[0]);
    const float c4 = __uint_as_float(r0[1]), c5 = __uint_as_float(r1[1]), c6 = __uint_as_float(r2[1]), c7 = __uint_as_float(r3[1]);
    u32x4 f;
    f[0] = cvtpk(c0 + bflo(xres[0]), c1 + bfhi(xres[0])); f[1] = cvtpk(c2 + bflo(xres[1]), c3 + bfhi(xres[1]));
    f[2] = cvtpk(c4 + bflo(xres[2]), c5 + bfhi(xres[2])); f[3] = cvtpk(c6 + bflo(xres[3]), c7 + bfhi(xres[3]));
    const i32x4 sO = mk_srd(out, 2048 * 1024);
    st16(f, sO, (RT * 32 + (lane & 31)) * 1024 + cg * 128 + kq * 32 + h * 16, 0);
  }
  if (b == 0 && wid == 7) {
    wait_vm<0>();
    asm volatile("" : "+v"(ef[0]), "+v"(ef[1]), "+v"(ef[2]), "+v"(ef[3]));
    bool ok = true;
#pragma unroll
    for (int k = 0; k < 4; ++k) ok = ok && ef[k][0] == (unsigned)C.tag && ef[k][1] == (unsigned)(C.tag >> 32);
    if (!__all(ok)) {
#pragma unroll 1
      for (int k = 0; k < 4; ++k) spin_flags(ws, flag_off(0, 0, 0) + (lane * 4 + k) * 128, true, C.tag);
    }
    if (lane == 0) __hip_atomic_store((u64*)(ws + WS_EPOCH), C.tag, __ATOMIC_RELAXED, __HIP_MEMORY_SCOPE_AGENT);
  }
  STAMP(60);
}

extern "C" void kernel_launch(void* const* d_in, const int* in_sizes, int n_in, void* d_out, int out_size, void* d_ws, size_t ws_size, hipStream_t stream) {
  (void)in_sizes; (void)n_in; (void)out_size; (void)ws_size;
  (void)hipFuncSetAttribute((const void*)fused_kernel, hipFuncAttributeMaxDynamicSharedMemorySize, LDS_BYTES);
  fused_kernel<<<256, 512, LDS_BYTES, stream>>>((const u16*)d_in[0], (const u16*)d_in[1], (const u16*)d_in[2], (const u16*)d_in[4], (const u16*)d_in[5], (const u16*)d_in[6],
                                                 (u16*)d_out, (char*)d_ws);
}
```

```cpp
#include <hip/hip_runtime.h>
#include <stdint.h>
#include <type_traits>

typedef unsigned short u16;
typedef unsigned long long u64;
typedef short bf16x8 __attribute__((ext_vector_type(8)));
typedef short s16x4 __attribute__((ext_vector_type(4)));
typedef float f32x2 __attribute__((ext_vector_type(2)));
typedef float f32x4 __attribute__((ext_vector_type(4)));
typedef float f32x16 __attribute__((ext_vector_type(16)));
typedef unsigned u32x2 __attribute__((ext_vector_type(2)));
typedef unsigned u32x4 __attribute__((ext_vector_type(4)));
typedef int i32x4 __attribute__((ext_vector_type(4)));
#define LDSP __attribute__((address_space(3)))
#define DI __device__ __forceinline__
template <class T> DI LDSP T* lp(unsigned a) { return (LDSP T*)(size_t)a; }
template <int I, int N, class F> DI void static_for(F&& f) { if constexpr (I < N) { f(std::integral_constant<int, I>{}); static_for<I + 1, N>(f); } }

constexpr size_t WS_EPOCH = 0;
constexpr size_t WS_ERR   = 64;
constexpr size_t WS_FLAGS = 4096;
constexpr size_t WS_QKV   = 1u << 20;
constexpr size_t WS_A2    = 8u << 20;
constexpr size_t WS_DBG   = 24u << 20;

constexpr unsigned R0 = 0, R1 = 32768, R2 = 65536, R3 = 98304;
constexpr unsigned L_K = 98304, L_V = 122880;
constexpr unsigned L_ONES = 147456;
constexpr unsigned L_CSP = 148480;
constexpr unsigned L_CSW = 150528;
constexpr unsigned L_STAT = 152576;
constexpr unsigned L_JUNK = 154624;
constexpr unsigned LDS_BYTES = 163840;
constexpr unsigned CODE_BYTES = 36864;
constexpr unsigned POISON = 0xffffffffu;
constexpr unsigned SPIN_CAP = 1u << 13;

#define NO_WARM 0
#define NO_CS 0
#define NO_DMA 0
constexpr float QSCALE = 0.25f * 1.4426950408889634f;

DI i32x4 mk_srd(const void* p, unsigned bytes) { u64 a = (u64)p; i32x4 r; r[0] = (int)(unsigned)a; r[1] = (int)((unsigned)(a >> 32) & 0xffffu); r[2] = (int)bytes; r[3] = 0x00020000; return r; }
DI void dma16(i32x4 srd, unsigned voff, unsigned soff, unsigned lds) {
  if (NO_DMA) return;
  asm volatile("s_mov_b32 m0, %3\n\ts_nop 3\n\tbuffer_load_dwordx4 %0, %1, %2 offen lds" :: "v"(voff), "s"(srd), "s"(soff), "s"(lds) : "memory");
}
DI void dma4(i32x4 srd, unsigned voff, unsigned lds) {
  asm volatile("s_mov_b32 m0, %2\n\ts_nop 3\n\tbuffer_load_dword %0, %1, 0 offen lds" :: "v"(voff), "s"(srd), "s"(lds) : "memory");
}
DI void ld16_sc1(u32x4& d, i32x4 srd, unsigned voff, unsigned soff) {
  asm volatile("s_nop 4\n\tbuffer_load_dwordx4 %0, %1, %2, %3 offen sc1" : "=v"(d) : "v"(voff), "s"(srd), "s"(soff) : "memory");
}
DI void ld16(u32x4& d, i32x4 srd, unsigned voff, unsigned soff) {
  asm volatile("s_nop 4\n\tbuffer_load_dwordx4 %0, %1, %2, %3 offen" : "=v"(d) : "v"(voff), "s"(srd), "s"(soff) : "memory");
}
DI void ld8_sc1(u32x2& d, i32x4 srd, unsigned voff) {
  asm volatile("s_nop 4\n\tbuffer_load_dwordx2 %0, %1, %2, 0 offen sc1" : "=v"(d) : "v"(voff), "s"(srd) : "memory");
}
DI void st16_sc1(u32x4 d, i32x4 srd, unsigned voff, unsigned soff) {
  asm volatile("s_nop 4\n\tbuffer_store_dwordx4 %0, %1, %2, %3 offen sc1\n\ts_nop 1" :: "v"(d), "v"(voff), "s"(srd), "s"(soff) : "memory");
}
DI void st16(u32x4 d, i32x4 srd, unsigned voff, unsigned soff) {
  asm volatile("s_nop 4\n\tbuffer_store_dwordx4 %0, %1, %2, %3 offen\n\ts_nop 1" :: "v"(d), "v"(voff), "s"(srd), "s"(soff) : "memory");
}
template <int N> DI void bar_vm() { static_assert(N >= 0 && N < 64, "vmcnt range"); asm volatile("s_waitcnt vmcnt(%0) lgkmcnt(0)\n\ts_barrier" :: "n"(N) : "memory"); }
template <int N> DI void wait_vm() { static_assert(N >= 0 && N < 64, "vmcnt range"); asm volatile("s_waitcnt vmcnt(%0)" :: "n"(N) : "memory"); }
DI s16x4 ldtr(unsigned a) { return __builtin_amdgcn_ds_read_tr16_b64_v4i16(lp<s16x4>(a)); }
DI bf16x8 ldfrag_tr(unsigned a, unsigned second) {
  s16x4 lo = ldtr(a), hi = ldtr(a + second);
  return __builtin_shufflevector(lo, hi, 0, 1, 2, 3, 4, 5, 6, 7);
}
DI bf16x8 ld128(unsigned a) { return *lp<bf16x8>(a); }
DI unsigned cvtpk(float lo, float hi) { typedef __bf16 b2 __attribute__((ext_vector_type(2))); f32x2 v = {lo, hi}; return __builtin_bit_cast(unsigned, __builtin_convertvector(v, b2)); }
DI float bflo(unsigned v) { return __uint_as_float(v << 16); }
DI float bfhi(unsigned v) { return __uint_as_float(v & 0xffff0000u); }
DI float rbf(float f) { return bflo(cvtpk(f, f) & 0xffffu); }
DI int fresh(int v) { asm volatile("" : "+v"(v)); return v; }
template <class T> DI void pin(T& v) { asm volatile("" : "+v"(v)); }
DI unsigned pinv(unsigned v) { asm volatile("" : "+v"(v)); return v; }
#define MFMA32(a, b, c) __builtin_amdgcn_mfma_f32_32x32x16_bf16((a), (b), (c), 0, 0, 0)
#define MFMA16(a, b, c) __builtin_amdgcn_mfma_f32_16x16x32_bf16((a), (b), (c), 0, 0, 0)
#define MFMA4(a, b, c) __builtin_amdgcn_mfma_f32_4x4x4bf16_1k((a), (b), (c), 0, 0, 0)
#define SB() __builtin_amdgcn_sched_barrier(0)
DI float swap_add(float v) { auto r = __builtin_amdgcn_permlane32_swap(__float_as_uint(v), __float_as_uint(v), false, false); return __uint_as_float(r[0]) + __uint_as_float(r[1]); }
DI float swap_max(float v) { auto r = __builtin_amdgcn_permlane32_swap(__float_as_uint(v), __float_as_uint(v), false, false); return __builtin_fmaxf(__uint_as_float(r[0]), __uint_as_float(r[1])); }

#define STAMP(i) do { } while (0)

struct Ctx {
  char* ws; u64 tag;
  i32x4 sX, sQKVw, sG, sU, sWO, sWD, sWS;
  int lane, wid, mg, ng, rb, cs, c, cg, xi, kq, rb2; unsigned stampreg; u64 prt0, prtx;
};

DI unsigned flag_off(int ev, int rb, int cs) { return (unsigned)(WS_FLAGS + (((ev * 16 + rb) * 16 + cs) << 7)); }
DI void publish(const Ctx& C, int ev) { if (C.wid == 0 && C.lane == 0) __hip_atomic_store((u64*)(C.ws + flag_off(ev, C.rb, C.cs)), C.tag, __ATOMIC_RELAXED, __HIP_MEMORY_SCOPE_AGENT); }
DI void give_up(char* ws) { __hip_atomic_store((unsigned*)(ws + WS_ERR), 1u, __ATOMIC_RELAXED, __HIP_MEMORY_SCOPE_AGENT); }
DI void spin_flags(char* ws, unsigned off, bool need, u64 tag) {
#pragma nounroll
  for (unsigned it = 0; it < SPIN_CAP; ++it) {
    u64 v = __hip_atomic_load((u64*)(ws + off), __ATOMIC_RELAXED, __HIP_MEMORY_SCOPE_AGENT);
    if (__all(!need || v == tag)) return;
    __builtin_amdgcn_s_sleep(2);
  }
  give_up(ws);
}
DI void check_flags(char* ws, unsigned off, bool need, u64 tag, u32x2& got) {
  asm volatile("" : "+v"(got));
  bool ok = !need || (got[0] == (unsigned)tag && got[1] == (unsigned)(tag >> 32));
  if (!__all(ok)) spin_flags(ws, off, need, tag);
}
template <int K0, int K1> DI void check_frags(const Ctx& C, u32x4 (&a2)[36], unsigned base) {
  auto dirty = [&]() { unsigned mx = 0;
    static_for<K0, K1>([&](auto kc) { constexpr int k = decltype(kc)::value;
      mx = __builtin_elementwise_max(mx, __builtin_elementwise_max(__builtin_elementwise_max(a2[k][0], a2[k][1]), __builtin_elementwise_max(a2[k][2], a2[k][3]))); });
    return __any(mx == POISON); };
  if (dirty()) {
#pragma nounroll
    for (unsigned it = 0; it < SPIN_CAP; ++it) {
      __builtin_amdgcn_s_sleep(2);
      static_for<K0, K1>([&](auto kc) { constexpr int k = decltype(kc)::value; ld16_sc1(a2[k], C.sWS, C.lane * 16, base + k * 1024); });
      wait_vm<0>();
      static_for<K0, K1>([&](auto kc) { constexpr int k = decltype(kc)::value; pin(a2[k]); });
      if (!dirty()) return;
    }
    give_up(C.ws);
  }
}

enum { T_H0 = 0, T_S4 = 8, T_E0 = 16, T_G0 = 19, T_POISON = 22, T_POLL, T_KL, T_POLL2, T_QL, T_EPOCH, T_WARM, T_NONE };
template <int TYPE> struct G1 {
  static constexpr int kind(int p) { return p == 0 ? 0 : (p == 1 && TYPE == 1) ? 1 : 2; }
  static constexpr int nt(int p) { return p == 0 ? 1 : 2; }
  static constexpr int nst(int p) { return kind(p) == 1 ? 3 : 2; }
  static constexpr int npoison() { return 3; }
  static constexpr unsigned slot(int s) {
    return s == 4 ? R0 : s == 5 ? R1 : s == 6 ? R2 : s == 7 ? R3 : s == 8 ? R0 : s == 9 ? R1 : s == 10 ? R2 : R0; }
  static constexpr int barpos(int bi) { return bi < 8 ? 4 * bi + 2 : 8 * (bi - 4) + 6; }
  static constexpr int barat(int pos) { return pos < 32 ? ((pos & 3) == 2 ? pos >> 2 : -1) : ((pos & 7) == 6 ? 4 + (pos >> 3) : -1); }
  static constexpr int grp(int bi) { return bi < 4 ? T_H0 + bi + 4 : bi < 11 ? T_S4 + (bi - 4) : bi == 11 ? T_NONE : bi == 12 ? T_S4 + 7 : T_G0 + (bi - 13); }
  static constexpr int gsize(int t) { return t == T_NONE ? 0 : t < T_S4 ? 3 : 4; }
  static constexpr int rel(int bi, int k) { return bi < 8 ? k : bi == 15 ? (k >> 1) : 2 * k; }
  static constexpr int after(int t, int bi) {
    int cnt = -1;
    auto see = [&](int tag, int n) { for (int i = 0; i < n; ++i) { if (tag == t) cnt = 0; else if (cnt >= 0) ++cnt; } };
    for (int h = 0; h < 2; ++h) see(T_H0 + h, gsize(T_H0 + h));
    see(T_EPOCH, 1);
    for (int pos = 0; pos < 96; ++pos) {
      if (pos < 2) see(T_H0 + 2 + pos, gsize(T_H0 + 2 + pos));
      const int b = barat(pos);
      if (b >= 0) {
        if (b == bi) return cnt;
        if (b == 11) see(T_POLL, 1);
        if (b == 12) see(T_KL, 6);
        if (b == 14) see(T_POLL2, 1);
        if (b == 15) see(T_QL, 1);
      }
      for (int bb = 0; bb < 16; ++bb) for (int k = 0; k < gsize(grp(bb)); ++k) if (barpos(bb) + rel(bb, k) == pos) see(grp(bb), 1);
      if (pos == 50) see(T_POISON, npoison());
      if ((pos & 31) == 31) see(T_E0 + (pos >> 5), nst(pos >> 5));
    }
    return cnt;
  }
  static constexpr int mn(int a, int b) { return a < b ? a : b; }
};

template <int TYPE> DI unsigned tile_addr(const Ctx& C, int p, int t) {
  const int lane = fresh(C.lane), kind = G1<TYPE>::kind(p);
  const int g = (lane >> 4) & 1, h = lane >> 5, q = (lane & 15) >> 2, pp = lane & 3;
  const unsigned rdrow = (8 * h + q) * 128 + pp * 8;
  if (kind == 2) return g * 16384 + rdrow + (((2 * C.ng + t) ^ (2 * (q >> 1)) ^ g) << 5);
  if (kind == 0) return 16384 + rdrow + ((C.ng ^ (q >> 1)) << 6) + g * 32;
  if (t == 0) return rdrow + ((C.ng ^ (q >> 1)) << 6) + g * 32;
  return 16384 + g * 8192 + (8 * h + q) * 64 + pp * 8 + ((C.ng ^ g) << 5);
}
template <int TYPE> DI unsigned stage_voff(const Ctx& C, int p, int which) {
  const int kind = G1<TYPE>::kind(p), lane = fresh(C.lane);
  if (which == 1) return (16 * C.wid + (lane >> 2)) * 3584 + (8 + ((C.cs - 8) >> 1)) * 128 + ((C.cs - 8) & 1) * 64 + ((lane & 3) << 4);
  const unsigned row = 16 * C.wid + (lane >> 3), sw = ((lane & 7) ^ (((lane >> 4) & 1) << 2)) << 4;
  if (kind == 1) return row * 3072 + (C.cs - 8) * 128 + sw;
  return row * 3584 + (p == 1 ? C.cs : 12 + C.cs) * 128 + sw;
}
template <int TYPE> DI void issue_stage(const Ctx& C, int s, int k, unsigned v0) {
  const int kind = G1<TYPE>::kind(s >> 2);
  const unsigned slot = G1<TYPE>::slot(s), r0 = 128 * (s & 3);
  if (kind == 1) {
    if (k < 2) dma16(C.sQKVw, v0, (r0 + 8 * k) * 3072, slot + C.wid * 2048 + k * 1024);
    else dma16(k == 3 ? C.sU : C.sG, k == 3 ? v0 ^ 32 : v0, r0 * 3584, slot + 16384 + (k - 2) * 8192 + C.wid * 1024);
  } else dma16(k >> 1 ? C.sU : C.sG, k >> 1 ? v0 ^ 32 : v0, (r0 + 8 * (k & 1)) * 3584, slot + (k >> 1) * 16384 + C.wid * 2048 + (k & 1) * 1024);
}
DI unsigned hx_voff(const Ctx& C) { const int lane = fresh(C.lane); return (16 * C.wid + (lane >> 3)) * 1024 + (((lane & 7) ^ ((lane >> 4) & 3)) << 4); }
DI unsigned hw_voff(const Ctx& C) { const int lane = fresh(C.lane);
  return (8 * C.wid + (lane >> 3)) * 3072 + (8 + C.cs) * 128 + (((lane & 7) ^ (((lane >> 4) & 1) << 2)) << 4); }
DI void issue_half(const Ctx& C, int h, int k, unsigned vx, unsigned vw) {
  const unsigned slot = (h & 3) * 32768;
  if (k < 2) dma16(C.sX, k ? (vx ^ 64) + 8 * 1024 : vx, h * 128, slot + C.wid * 2048 + k * 1024);
  else dma16(C.sQKVw, vw + (k - 2) * 128, h * 64 * 3072, slot + 16384 + (k - 2) * 8192 + C.wid * 1024);
}
DI void warm(i32x4 srd, unsigned line0, unsigned lines_per_row_log2, unsigned pitch, unsigned colbyte, int lane) {
  const unsigned l = line0 + lane;
  dma4(srd, (l >> lines_per_row_log2) * pitch + colbyte + (l & ((1u << lines_per_row_log2) - 1)) * 128, L_JUNK);
}
DI unsigned g_voff(const Ctx& C) { const int lane = fresh(C.lane); return (32 * (C.wid & 1) + (lane >> 3)) * 1024 + C.cg * 128 + (((lane & 7) ^ (((lane >> 4) & 1) << 2)) << 4); }
constexpr unsigned gslot(int j) { return j == 0 ? R1 : j == 1 ? R2 : j == 2 ? R0 : j == 3 ? R3 : j == 4 ? R1 : j == 5 ? R2 : j == 6 ? R0 : j == 7 ? R3 : R1; }
DI void issue_g(const Ctx& C, int j, int i, unsigned v) {
  const int kq = C.wid >> 1;
  const int row0 = j < 3 ? (3 * kq + j) * 64 : j < 7 ? (12 + 4 * kq + (j - 3)) * 64 : 128 * kq + 64 * (j - 7);
  dma16(j < 7 ? C.sWD : C.sWO, v, (unsigned)(row0 + 8 * i) * 1024, gslot(j) + kq * 8192 + (C.wid & 1) * 4096 + i * 1024);
}

template <int WHAT> DI void epi_qkv(const Ctx& C, const f32x16& acc, const f32x4 (&csr)[4], float m, float rinv, int qu, int h4) {
  const int h = C.lane >> 5;
  const float sc = WHAT == 0 ? rinv * QSCALE : rinv;
  float y[16];
#pragma unroll
  for (int i4 = 0; i4 < 4; ++i4)
#pragma unroll
    for (int e = 0; e < 4; ++e) y[4 * i4 + e] = (acc[4 * i4 + e] - csr[i4][e]) * sc;
  const unsigned base = (unsigned)WS_QKV + (((C.rb * 24 + qu) * 4 + C.mg) * 4 + h4) * 1024;
#pragma unroll
  for (int hh = 0; hh < 2; ++hh) {
    u32x4 f; f[0] = cvtpk(y[8 * hh], y[8 * hh + 1]); f[1] = cvtpk(y[8 * hh + 2], y[8 * hh + 3]); f[2] = cvtpk(y[8 * hh + 4], y[8 * hh + 5]); f[3] = cvtpk(y[8 * hh + 6], y[8 * hh + 7]);
    unsigned voff = C.lane * 16;
    if (WHAT == 2) {
      auto a = __builtin_amdgcn_permlane32_swap(f[0], f[2], false, false); auto b = __builtin_amdgcn_permlane32_swap(f[1], f[3], false, false);
      f[0] = a[0]; f[2] = a[1]; f[1] = b[0]; f[3] = b[1];
      voff = (C.lane & 31) * 32 + h * 16;
    }
    st16_sc1(f, C.sWS, voff, base + hh * 1024);
  }
}
DI void epi_gu(const Ctx& C, const f32x16& acc, const f32x4 (&csr)[4], float m, float r2, unsigned a2off) {
  float y[8];
#pragma unroll
  for (int i4 = 0; i4 < 2; ++i4)
#pragma unroll
    for (int e = 0; e < 4; ++e) y[4 * i4 + e] = ((acc[4 * i4 + e] - csr[i4][e]) * r2) * (acc[8 + 4 * i4 + e] - csr[2 + i4][e]);
  u32x4 f; f[0] = cvtpk(y[0], y[1]); f[1] = cvtpk(y[2], y[3]); f[2] = cvtpk(y[4], y[5]); f[3] = cvtpk(y[6], y[7]);
  st16_sc1(f, C.sWS, C.lane * 16, a2off);
}
template <int TYPE> DI unsigned a2_pass(const Ctx& C, int p) {
  if (p == 2) return (unsigned)WS_A2 + (((4 * C.rb + C.mg) * 4 + (C.cs >> 2)) * 36 + 4 * (3 + (C.cs & 3)) + 2 * C.ng) * 1024;
  if (TYPE == 0) return (unsigned)WS_A2 + (((4 * C.rb + C.mg) * 4 + C.cs / 3) * 36 + 4 * (C.cs % 3) + 2 * C.ng) * 1024;
  const int u = 8 + ((C.cs - 8) >> 1), half = (C.cs - 8) & 1;
  return (unsigned)WS_A2 + (((4 * C.rb + C.mg) * 4 + u / 3) * 36 + 4 * (u % 3) + 2 * half + C.ng) * 1024;
}
DI unsigned a2_o(const Ctx& C) { return (unsigned)WS_A2 + (((4 * C.rb + C.mg) * 4 + (C.cs >> 2)) * 36 + 28 + 2 * (C.cs & 3) + C.ng) * 1024; }

template <int TYPE> DI void gemm1(Ctx& C, unsigned vx, unsigned vw, u32x4& qfrag) {
  using G = G1<TYPE>;
  const int lane = C.lane;
  char* ws = C.ws;
  bf16x8 xa[32];
  bf16x8 wf[3][2];
  f32x16 acc[2];
  unsigned ta[2], sv = 0;
  ta[0] = tile_addr<TYPE>(C, 0, 0); ta[1] = tile_addr<TYPE>(C, 0, 1);
  const unsigned r31 = fresh(lane) & 31, hh = fresh(lane) >> 5;
  const unsigned xlane = (32 * C.mg + r31) * 128 + ((hh ^ ((r31 >> 1) & 7)) << 4);
  f32x4 ssum = {0, 0, 0, 0}, ssq = {0, 0, 0, 0};
  f32x4 csacc[2] = {{0,0,0,0},{0,0,0,0}};
  bf16x8 sel;
  { const int lf = fresh(lane); const short one = ((lf & 15) == ((lf >> 4) & 1)) ? (short)0x3f80 : (short)0;
#pragma unroll
    for (int e = 0; e < 8; ++e) sel[e] = one; }
  float csn[2];
  f32x4 csr[2][4];
  float m = 0.f, rinv = 0.f;
  u32x2 pollv, poll2; u32x4 gk[6];
  unsigned polloff = 0, poll2off = 0; bool pollneed = false;

  STAMP(1);
  bar_vm<G::gsize(T_H0)>();
  STAMP(2); C.prtx = __builtin_amdgcn_s_memrealtime();
  u32x2 ev; ld8_sc1(ev, C.sWS, (unsigned)WS_EPOCH);
  auto read_x = [&](int h) {
    const unsigned base = (h & 3) * 32768 + xlane;
#pragma unroll
    for (int i = 0; i < 4; ++i) xa[4 * h + i] = ld128(base ^ (((i + C.c) & 3) << 5));
  };
  auto read_w = [&](int pos) {
    const int p = pos >> 5, s = pos >> 3, i = pos & 7;
    if ((pos & 31) == 0 && pos > 0) { ta[0] = tile_addr<TYPE>(C, p, 0); ta[1] = tile_addr<TYPE>(C, p, 1); }
    const unsigned sb = p == 0 ? ((pos >> 2) & 3) * 32768 : G::slot(s), ks = (p == 0 ? 0 : (i & 4)) + ((i + C.c) & 3);
    wf[pos % 3][0] = ldfrag_tr(pinv(ta[0] + sb + ks * 2048), 512);
    if (G::nt(p) == 2) { if (G::kind(p) == 1) wf[pos % 3][1] = ldfrag_tr(pinv(ta[1] + sb + ks * 1024), 256); else wf[pos % 3][1] = ldfrag_tr(pinv(ta[1] + sb + ks * 2048), 512); }
  };
  read_x(0); read_w(0); read_w(1);
  static_for<0, 96>([&](auto ic) {
    constexpr int pos = decltype(ic)::value;
    constexpr int p = pos >> 5, s = pos >> 3, st = s & 3, i = pos & 7, NT = G::nt(p), bi = G::barat(pos);
    if constexpr (pos < 2) static_for<0, G::gsize(T_H0)>([&](auto kc) { issue_half(C, 2 + pos, decltype(kc)::value, vx, vw); });
    if constexpr (bi >= 0) {
      STAMP(10 + 2 * bi);
      if constexpr (bi == 0) { bar_vm<G::after(T_H0 + 1, 0)>(); }
      else if constexpr (bi < 7) bar_vm<G::after(T_H0 + bi + 1, bi)>();
      else if constexpr (bi == 9) { bar_vm<G::mn(G::after(T_S4 + 2, 9), G::after(T_E0, 9))>(); publish(C, 0); }
      else if constexpr (bi == 12) bar_vm<G::mn(G::after(T_S4 + 5, 12), G::after(T_POLL, 12))>();
      else if constexpr (bi == 13) { static_assert(G::after(T_E0 + 1, 13) <= G::after(T_POISON, 13), ""); bar_vm<G::mn(G::after(T_S4 + 6, 13), G::after(T_E0 + 1, 13))>(); }
      else if constexpr (bi == 14) bar_vm<G::mn(G::after(T_S4 + 7, 14), G::after(T_KL, 14))>();
      else if constexpr (bi == 15) bar_vm<G::after(T_POLL2, 15)>();
      else bar_vm<G::after(T_S4 + bi - 7, bi)>();
      if constexpr (bi == 4) {
        static_assert(G::after(T_H0 + 5, 4) <= G::after(T_EPOCH, 4), "");
        pin(ev);
        const u64 t = (((u64)ev[1] << 32) | ev[0]) * 6364136223846793005ull + 1442695040888963407ull;
        C.tag = ((u64)__builtin_amdgcn_readfirstlane((unsigned)(t >> 32)) << 32) | __builtin_amdgcn_readfirstlane((unsigned)t);
      }
      if constexpr (bi == 11) {
        const int u = C.cs >> 1, l = fresh(lane) & 7;
        const int rbp = C.rb - 2 + l % 3;
        pollneed = l < 6 && rbp >= 0;
        polloff = flag_off(0, pollneed ? rbp : C.rb, l < 3 ? u : 8 + u);
        ld8_sc1(pollv, C.sWS, polloff);
      }
      if constexpr (bi == 12) {
        check_flags(ws, polloff, pollneed, C.tag, pollv);
        const int u = C.cs >> 1, h4 = 2 * (C.cs & 1) + C.ng;
#pragma unroll
        for (int b = 0; b < 6; ++b) { const int rbp = C.rb - 2 + b % 3 < 0 ? 0 : C.rb - 2 + b % 3;
          ld16_sc1(gk[b], C.sWS, lane * 16, (unsigned)WS_QKV + (((rbp * 24 + (b < 3 ? 8 : 16) + u) * 4 + C.mg) * 4 + h4) * 1024); }
      }
      if constexpr (bi == 13) publish(C, 1);
      if constexpr (bi == 14) {
#pragma unroll
        for (int b = 0; b < 6; ++b) pin(gk[b]);
#pragma unroll
        for (int b = 0; b < 3; ++b) { *lp<u32x4>(L_K + ((4 * b + C.mg) * 2 + C.ng) * 1024 + lane * 16) = gk[b];
                                      *lp<u32x4>(L_V + C.ng * 12288 + (4 * b + C.mg) * 1024 + lane * 16) = gk[3 + b]; }
        { const int ln = fresh(lane), l = ln < 11 ? ln : 0, uu = 3 * C.kq + (l >> 1);
          poll2off = l < 6 ? flag_off(1, C.rb2, uu < 8 ? uu : 8 + 2 * (uu - 8) + (l & 1)) : l < 10 ? flag_off(1, C.rb2, 4 * C.kq + (l - 6)) : flag_off(1, C.rb, 8 + (C.cs >> 1)); }
        ld8_sc1(poll2, C.sWS, poll2off);
      }
      if constexpr (bi == 15) {
        check_flags(ws, poll2off, lane < 11, C.tag, poll2);
        ld16_sc1(qfrag, C.sWS, lane * 16, (unsigned)WS_QKV + (((C.rb * 24 + (C.cs >> 1)) * 4 + C.mg) * 4 + 2 * (C.cs & 1) + C.ng) * 1024);
      }
      if constexpr (bi < 7) read_x(bi + 1);
      if constexpr (st == 3 && i == 6) {
#pragma unroll
        for (int t = 0; t < NT; ++t) { const unsigned a = L_CSP + (C.ng * 2 + t) * 512 + r31 * 4;
          csn[t] = (*lp<float>(a) + *lp<float>(a + 128)) + (*lp<float>(a + 256) + *lp<float>(a + 384)); }
      }
      STAMP(11 + 2 * bi);
    }
    static_for<0, 16>([&](auto bc) {
      constexpr int bb = decltype(bc)::value, tg = G::grp(bb), k0 = pos - G::barpos(bb);
      static_for<0, 4>([&](auto kc) {
        constexpr int k = decltype(kc)::value;
        if constexpr (k < G::gsize(tg) && G::rel(bb, k) == k0) {
          if constexpr (tg < T_S4) issue_half(C, tg, k, vx, vw);
          else if constexpr (tg >= T_G0) { if constexpr (k == 0) sv = g_voff(C); issue_g(C, tg - T_G0, k, sv); }
          else { constexpr int pp_ = (tg - T_S4 + 4) >> 2;
            if constexpr (k == 0) sv = stage_voff<TYPE>(C, pp_, 0);
            if constexpr (k == 2 && G::kind(pp_) == 1) sv = stage_voff<TYPE>(C, pp_, 1);
            issue_stage<TYPE>(C, tg - T_S4 + 4, k, sv); }
        }
      });
    });
    if constexpr (pos == 33 && !NO_WARM) {
      if (C.wid < 2) warm(C.wid ? C.sU : C.sG, (C.rb & 7) * 64, 0, 3584, (12 + C.cs) * 128, fresh(lane));
    }
    if constexpr (pos >= 39 && pos < 39 + 4 * 9 && ((pos - 39) & 3) == 0 && !NO_WARM) {
      constexpr int k = (pos - 39) >> 2;
      const unsigned l = (C.xi * 9 + k) * 64;
      if (C.wid == (k & 7)) warm(l < 4096 ? C.sWO : C.sWD, l < 4096 ? l : l - 4096, 3, 1024, 0, fresh(lane));
    }
    if constexpr (pos == 50) {
      const u32x4 pz = {POISON, POISON, POISON, POISON};
      const unsigned a = a2_pass<TYPE>(C, 2);
      st16_sc1(pz, C.sWS, lane * 16, a);
      st16_sc1(pz, C.sWS, lane * 16, a + 1024);
      st16_sc1(pz, C.sWS, lane * 16, a2_o(C));
    }
    if constexpr (pos + 2 < 96) read_w(pos + 2);
    if constexpr ((pos & 31) == 0) { acc[0] = MFMA32(wf[pos % 3][0], xa[pos & 31], f32x16{}); if (NT == 2) acc[1] = MFMA32(wf[pos % 3][1], xa[pos & 31], f32x16{}); }
    else { acc[0] = MFMA32(wf[pos % 3][0], xa[pos & 31], acc[0]); if (NT == 2) acc[1] = MFMA32(wf[pos % 3][1], xa[pos & 31], acc[1]); }
    if constexpr ((i & 3) == 0 && !NO_CS) {
      if constexpr ((pos & 31) == 0) { csacc[0] = MFMA16(wf[pos % 3][0], sel, (f32x4{0, 0, 0, 0})); if (NT == 2) csacc[1] = MFMA16(wf[pos % 3][1], sel, (f32x4{0, 0, 0, 0})); }
      else { csacc[0] = MFMA16(wf[pos % 3][0], sel, csacc[0]); if (NT == 2) csacc[1] = MFMA16(wf[pos % 3][1], sel, csacc[1]); }
    }
    if constexpr (p == 0 && (i & 3) < 2) {
      const bf16x8 xv = xa[pos & 31]; const s16x4 lo = __builtin_shufflevector(xv, xv, 0, 1, 2, 3), hi = __builtin_shufflevector(xv, xv, 4, 5, 6, 7);
      const s16x4 ones = {0x3f80, 0x3f80, 0x3f80, 0x3f80};
      ssum = MFMA4(ones, lo, ssum); ssq = MFMA4(lo, lo, ssq); ssum = MFMA4(ones, hi, ssum); ssq = MFMA4(hi, hi, ssq);
    }
    if constexpr (st == 3 && i == 5) {
      if ((lane & 15) < 2) {
#pragma unroll
        for (int t = 0; t < NT; ++t) *lp<f32x4>(L_CSP + ((C.ng * 2 + t) * 4 + C.mg) * 128 + (16 * (lane & 15) + 4 * (lane >> 4)) * 4) = csacc[t];
      }
      if constexpr (p == 0) {
        const float sq = (lane & 3) == 0 ? ssq[0] : (lane & 3) == 1 ? ssq[1] : (lane & 3) == 2 ? ssq[2] : ssq[3];
        const f32x2 v = {swap_add(ssum[0]), swap_add(sq)};
        if (lane < 32) *lp<f32x2>(L_STAT + (C.ng * 128 + 32 * C.mg + lane) * 8) = v;
      }
    }
    if constexpr (st == 3 && i == 6) {
#pragma unroll
      for (int t = 0; t < NT; ++t) *lp<float>(L_CSW + (C.wid * 2 + t) * 128 + r31 * 4) = csn[t];
    }
    if constexpr (st == 3 && i == 6 && p == 0) {
      const f32x2 a = *lp<f32x2>(L_STAT + (32 * C.mg + r31) * 8), b = *lp<f32x2>(L_STAT + (128 + 32 * C.mg + r31) * 8);
      const float mean = (a[0] + b[0]) * (1.f / 512.f);
      const float var = __builtin_fmaxf((a[1] + b[1]) * (1.f / 512.f) - mean * mean, 0.f);
      m = rbf(mean);
      rinv = __builtin_amdgcn_rcpf(rbf(__builtin_amdgcn_sqrtf(rbf(rbf(var) + 1e-5f))));
      pin(m); pin(rinv);
    }
    if constexpr (st == 3 && i == 7) {
#pragma unroll
      for (int t = 0; t < NT; ++t)
#pragma unroll
        for (int i4 = 0; i4 < 4; ++i4) { csr[t][i4] = *lp<f32x4>(L_CSW + (C.wid * 2 + t) * 128 + (8 * i4 + 4 * hh) * 4) * m; pin(csr[t][i4]); }
    }
    SB();
    if constexpr ((pos & 31) == 31) {
      if constexpr (G::kind(p) == 2) {
        const unsigned a2 = a2_pass<TYPE>(C, p);
        epi_gu(C, acc[0], csr[0], m, rinv * rinv, a2); epi_gu(C, acc[1], csr[1], m, rinv * rinv, a2 + 1024);
      } else if constexpr (G::kind(p) == 1) {
        epi_qkv<0>(C, acc[0], csr[0], m, rinv, C.cs - 8, 2 * C.ng);
        epi_gu(C, acc[1], csr[1], m, rinv * rinv, a2_pass<TYPE>(C, p));
      } else {
        epi_qkv<1 + TYPE>(C, acc[0], csr[0], m, rinv, 8 + C.cs, 2 * C.ng);
      }
      SB();
    }
  });
  wait_vm<G::after(T_QL, 16)>();
  pin(qfrag);
}

template <bool EDGE, class SIDE> DI u32x4 attention(Ctx& C, const u32x4& qraw, SIDE&& side) {
  const int lane = C.lane, h = lane >> 5, g = (lane >> 4) & 1, q = (lane & 15) >> 2, pp = lane & 3;
  const bf16x8 qf = __builtin_bit_cast(bf16x8, qraw);
  const unsigned kaddr = pinv(L_K + (C.mg * 2 + C.ng) * 1024 + lane * 16);
  const int t0 = EDGE ? (C.rb == 0 ? 8 : 4) - C.mg : 1;
  const int dq = (lane & 31) - 4 * h;
  const float NEG = -1e30f;
  auto older = [&](int r) { return ((r & 3) + 8 * (r >> 2)) > dq; };
  auto max16 = [&](float& a, float& b, const f32x16& s) {
#pragma unroll
    for (int r = 0; r < 16; r += 4) { a = __builtin_fmaxf(__builtin_fmaxf(a, s[r]), s[r + 1]); b = __builtin_fmaxf(__builtin_fmaxf(b, s[r + 2]), s[r + 3]); } };
  bf16x8 kf[9];
#pragma unroll
  for (int t = 0; t < 9; ++t) if (!EDGE || t > 0) kf[t] = ld128(kaddr + t * 2048);
  float mx;
  {
    f32x16 S[3];
    f32x16 sl = MFMA32(kf[8], qf, f32x16{});
    f32x16 sf;
    if (!EDGE) sf = MFMA32(kf[0], qf, f32x16{});
    S[1] = MFMA32(kf[1], qf, f32x16{});
    S[2] = MFMA32(kf[2], qf, f32x16{});
    float a = NEG, b = NEG;
    side(std::integral_constant<int, 0>{});
    SB();
#pragma unroll
    for (int r = 0; r < 16; ++r) sl[r] = older(r) ? (EDGE ? NEG : sf[r]) : sl[r];
    max16(a, b, sl);
    static_for<1, 8>([&](auto tc) { constexpr int t = decltype(tc)::value;
      if constexpr (t + 2 < 8) S[(t + 2) % 3] = MFMA32(kf[t + 2], qf, f32x16{});
      side(std::integral_constant<int, t>{});
      SB();
      if (!EDGE || t >= t0) max16(a, b, S[t % 3]);
      SB(); });
    mx = swap_max(__builtin_fmaxf(a, b));
  }
  f32x16 negm;
#pragma unroll
  for (int r = 0; r < 16; ++r) negm[r] = -mx;
  const unsigned vbase = pinv(g == 0 ? L_V + C.ng * 12288 + C.mg * 1024 + (4 * h + q) * 32 + pp * 8 : L_ONES + 128 * (1 - h) + q * 32 + pp * 8);
  const unsigned vstr = g == 0 ? 1024u : 0u;
  f32x16 o;
  auto expack = [&](const f32x16& s, u32x4& pa, u32x4& pb) {
#pragma unroll
    for (int i = 0; i < 4; ++i) pa[i] = cvtpk(__builtin_amdgcn_exp2f(s[2 * i]), __builtin_amdgcn_exp2f(s[2 * i + 1]));
#pragma unroll
    for (int i = 0; i < 4; ++i) pb[i] = cvtpk(__builtin_amdgcn_exp2f(s[8 + 2 * i]), __builtin_amdgcn_exp2f(s[9 + 2 * i])); };
  f32x16 S[2];
  bf16x8 vf[2][2];
  {
    f32x16 sl = MFMA32(kf[8], qf, negm);
    f32x16 sf;
    if (!EDGE) sf = MFMA32(kf[0], qf, negm);
    S[1] = MFMA32(kf[1], qf, negm);
    const unsigned v8 = vbase + 8 * vstr;
    const bf16x8 va = ldfrag_tr(v8, 256), vb = ldfrag_tr(v8 + 512, 256);
    bf16x8 vc, vd;
    if (!EDGE) { vc = ldfrag_tr(vbase, 256); vd = ldfrag_tr(vbase + 512, 256); }
    vf[1][0] = ldfrag_tr(vbase + vstr, 256); vf[1][1] = ldfrag_tr(vbase + vstr + 512, 256);
    side(std::integral_constant<int, 8>{});
    SB();
#pragma unroll
    for (int r = 0; r < 16; ++r) sl[r] = older(r) ? (EDGE ? NEG : sf[r]) : sl[r];
    u32x4 pa, pb, mk0, mk1;
    expack(sl, pa, pb);
#pragma unroll
    for (int i = 0; i < 4; ++i) {
      mk0[i] = (older(2 * i) ? 0xffffu : 0u) | (older(2 * i + 1) ? 0xffff0000u : 0u);
      mk1[i] = (older(8 + 2 * i) ? 0xffffu : 0u) | (older(9 + 2 * i) ? 0xffff0000u : 0u); }
    o = MFMA32(va, __builtin_bit_cast(bf16x8, pa & ~mk0), f32x16{});
    o = MFMA32(vb, __builtin_bit_cast(bf16x8, pb & ~mk1), o);
    if (!EDGE) {
      o = MFMA32(vc, __builtin_bit_cast(bf16x8, pa & mk0), o);
      o = MFMA32(vd, __builtin_bit_cast(bf16x8, pb & mk1), o);
    }
    SB();
  }
  static_for<1, 8>([&](auto tc) { constexpr int t = decltype(tc)::value;
    if constexpr (t + 1 < 8) {
      const unsigned vt = vbase + (t + 1) * vstr;
      vf[(t + 1) & 1][0] = ldfrag_tr(vt, 256); vf[(t + 1) & 1][1] = ldfrag_tr(vt + 512, 256);
      S[(t + 1) & 1] = MFMA32(kf[t + 1], qf, negm); }
    side(std::integral_constant<int, 8 + t>{});
    SB();
    if (!EDGE || t >= t0) {
      u32x4 pa, pb;
      expack(S[t & 1], pa, pb);
      o = MFMA32(vf[t & 1][0], __builtin_bit_cast(bf16x8, pa), o);
      o = MFMA32(vf[t & 1][1], __builtin_bit_cast(bf16x8, pb), o);
    }
    SB(); });
  const float li = __builtin_amdgcn_rcpf(o[8]);
  u32x4 f; f[0] = cvtpk(o[0] * li, o[1] * li); f[1] = cvtpk(o[2] * li, o[3] * li); f[2] = cvtpk(o[4] * li, o[5] * li); f[3] = cvtpk(o[6] * li, o[7] * li);
  return f;
}

__global__ void __launch_bounds__(512, 2)
fused_kernel(const u16* __restrict__ x, const u16* __restrict__ wqkv, const u16* __restrict__ wo, const u16* __restrict__ wg, const u16* __restrict__ wu,
             const u16* __restrict__ wd, u16* __restrict__ out, char* __restrict__ ws) {
  Ctx C;
  const int tid = threadIdx.x, lane = tid & 63, wid = __builtin_amdgcn_readfirstlane(tid >> 6);
  const int b = blockIdx.x, xj = b & 7, xi = b >> 3;
  C.stampreg = 0;
  STAMP(0);
  C.prt0 = __builtin_amdgcn_s_memrealtime(); C.prtx = C.prt0;
  asm volatile("" :: "s"(x), "s"(wqkv), "s"(wo), "s"(wg), "s"(wu), "s"(wd), "s"(out), "s"(ws));
  C.ws = ws; C.lane = lane; C.wid = wid; C.mg = wid & 3; C.ng = wid >> 2;
  C.rb = 8 * (xj & 1) + (xi & 7); C.cs = 4 * (xj >> 1) + (xi >> 3); C.c = (C.mg + 2 * C.ng) & 3;
  C.sX = mk_srd(x + (size_t)C.rb * 128 * 512, 128 * 1024);
  C.sQKVw = mk_srd(wqkv, 512 * 3072);
  C.xi = xi;
  const unsigned vx = hx_voff(C), vw = hw_voff(C);
  STAMP(3);
  issue_half(C, 0, 0, vx, vw); issue_half(C, 0, 1, vx, vw); issue_half(C, 0, 2, vx, vw);
  STAMP(4);
  issue_half(C, 1, 0, vx, vw); issue_half(C, 1, 1, vx, vw); issue_half(C, 1, 2, vx, vw);
  STAMP(5);
  C.sWS = mk_srd(ws, 32u << 20);
  C.sG = mk_srd(wg, 512 * 3584); C.sU = mk_srd(wu, 512 * 3584); C.sWO = mk_srd(wo, 512 * 1024); C.sWD = mk_srd(wd, 1792 * 1024);
  if (wid == 7) { u64 pc; asm volatile("s_getpc_b64 %0" : "=s"(pc));
    dma4(mk_srd((const void*)(pc & ~255ull), CODE_BYTES), (xi * 9 + (lane < 9 ? lane : 0)) * 128, L_JUNK); }
  if (tid < 256) lp<unsigned>(L_ONES)[tid] = 0x3f803f80u;
  const int rt64 = 4 * xj + (xi & 3), cg = xi >> 2;
  C.cg = cg;
  const int rt = wid & 1, kq = wid >> 1, RT = 2 * rt64 + rt, rb2 = RT >> 2;
  C.kq = kq; C.rb2 = rb2;
  u32x4 qfrag;
  if (C.cs < 8) gemm1<0>(C, vx, vw, qfrag); else gemm1<1>(C, vx, vw, qfrag);
  STAMP(6);

  const unsigned a2base = (unsigned)WS_A2 + (RT * 4 + kq) * 36 * 1024;
  u32x4 a2[36];
  auto side = [&](auto kc) { constexpr int k = decltype(kc)::value;
    if constexpr (k < 6) { ld16_sc1(a2[2 * k], C.sWS, lane * 16, a2base + 2 * k * 1024); ld16_sc1(a2[2 * k + 1], C.sWS, lane * 16, a2base + (2 * k + 1) * 1024); }
    if constexpr (k >= 12) static_for<0, 4>([&](auto jc) { constexpr int q_ = 12 + 4 * (k - 12) + decltype(jc)::value; ld16_sc1(a2[q_], C.sWS, lane * 16, a2base + q_ * 1024); }); };
  u32x4 of;
  if (C.rb >= 2) of = attention<false>(C, qfrag, side); else of = attention<true>(C, qfrag, side);
  st16_sc1(of, C.sWS, lane * 16, a2_o(C));
  STAMP(7);

  const int g = (lane >> 4) & 1, h = lane >> 5, q = (lane & 15) >> 2, pp = lane & 3;
  const unsigned w2l0 = kq * 8192 + (4 * h + q) * 128 + ((0 ^ (q >> 1)) << 6) + g * 32 + pp * 8;
  const unsigned w2l1 = kq * 8192 + (4 * h + q) * 128 + ((1 ^ (q >> 1)) << 6) + g * 32 + pp * 8;
  bf16x8 wf[3][2];
  f32x16 acc[2];
  u32x4 xres;
  unsigned wb[2], gv;
  auto read_w2 = [&](int pos) {
    if ((pos & 3) == 0) { wb[0] = pinv(w2l0 + gslot(pos >> 2)); wb[1] = pinv(w2l1 + gslot(pos >> 2)); }
    wf[pos % 3][0] = ldfrag_tr(wb[0] + (pos & 3) * 2048, 1024); wf[pos % 3][1] = ldfrag_tr(wb[1] + (pos & 3) * 2048, 1024); };
  bar_vm<17>();
  static_for<0, 12>([&](auto kc) { constexpr int k = decltype(kc)::value; pin(a2[k]); });
  read_w2(0); read_w2(1);
  gv = g_voff(C);
  STAMP(8);
  u32x4 ef[4];
  static_for<0, 36>([&](auto ic) {
    constexpr int pos = decltype(ic)::value;
    constexpr int j = pos >> 2, i = pos & 3;
    if constexpr (i == 2) {
      STAMP(44 + 2 * j);
      if constexpr (j <= 1) bar_vm<63>();
      if constexpr (j == 2) bar_vm<6>();
      if constexpr (j == 3) bar_vm<6>();
      if constexpr (j == 4) bar_vm<7>();
      if constexpr (j == 5) bar_vm<15>();
      if constexpr (j == 6) bar_vm<4>();
      if constexpr (j == 7) bar_vm<0>();
      STAMP(45 + 2 * j);
    }
    if constexpr (pos == 10) { static_for<12, 28>([&](auto kc) { pin(a2[decltype(kc)::value]); }); check_frags<12, 28>(C, a2, a2base); }
    if constexpr (pos == 26) { static_for<28, 36>([&](auto kc) { pin(a2[decltype(kc)::value]); }); pin(xres); check_frags<28, 36>(C, a2, a2base); }
    if constexpr (pos == 16) { const i32x4 sXall = mk_srd(x, 2048 * 1024); ld16(xres, sXall, (RT * 32 + (lane & 31)) * 1024 + cg * 128 + kq * 32 + h * 16, 0); }
    if constexpr (pos == 18 || pos == 19) static_for<0, 4>([&](auto kc) { constexpr int k = 28 + 4 * (pos - 18) + decltype(kc)::value; ld16_sc1(a2[k], C.sWS, lane * 16, a2base + k * 1024); });
    if constexpr (pos < 24) issue_g(C, 3 + (pos >> 2), pos & 3, gv);
    if constexpr (pos == 30) { if (b == 0 && wid == 7) {
#pragma unroll
        for (int k = 0; k < 4; ++k) { u32x2 t; ld8_sc1(t, C.sWS, flag_off(0, 0, 0) + (lane * 4 + k) * 128); ef[k][0] = t[0]; ef[k][1] = t[1]; } } }
    if constexpr (pos + 2 < 36) read_w2(pos + 2);
    const bf16x8 af = __builtin_bit_cast(bf16x8, a2[pos]);
    if constexpr (pos == 0) { acc[0] = MFMA32(wf[0][0], af, f32x16{}); acc[1] = MFMA32(wf[0][1], af, f32x16{}); }
    else { acc[0] = MFMA32(wf[pos % 3][0], af, acc[0]); acc[1] = MFMA32(wf[pos % 3][1], af, acc[1]); }
    SB();
  });
  STAMP(9);
#pragma unroll
  for (int d = 0; d < 4; ++d) if (d != kq) {
    const int ct = d >> 1, r0 = (d & 1) * 8;
    const unsigned a = R2 + ((rt * 4 + d) * 4 + kq) * 2048 + lane * 16;
    *lp<f32x4>(a) = f32x4{acc[ct][r0], acc[ct][r0 + 1], acc[ct][r0 + 2], acc[ct][r0 + 3]};
    *lp<f32x4>(a + 1024) = f32x4{acc[ct][r0 + 4], acc[ct][r0 + 5], acc[ct][r0 + 6], acc[ct][r0 + 7]};
  }
  float s[8];
#pragma unroll
  for (int e = 0; e < 8; ++e) { const float lo = (kq & 1) ? acc[0][8 + e] : acc[0][e], hi = (kq & 1) ? acc[1][8 + e] : acc[1][e]; s[e] = (kq & 2) ? hi : lo; }
  asm volatile("s_waitcnt lgkmcnt(0)\n\ts_barrier" ::: "memory");
#pragma unroll
  for (int d = 0; d < 4; ++d) if (d != kq) {
    const unsigned a = R2 + ((rt * 4 + kq) * 4 + d) * 2048 + lane * 16;
    const f32x4 u = *lp<f32x4>(a), v = *lp<f32x4>(a + 1024);
#pragma unroll
    for (int e = 0; e < 4; ++e) { s[e] += u[e]; s[4 + e] += v[e]; }
  }
  {
    unsigned u0 = __float_as_uint(s[0]), u1 = __float_as_uint(s[1]), u2 = __float_as_uint(s[2]), u3 = __float_as_uint(s[3]);
    unsigned v0 = __float_as_uint(s[4]), v1 = __float_as_uint(s[5]), v2 = __float_as_uint(s[6]), v3 = __float_as_uint(s[7]);
    auto r0 = __builtin_amdgcn_permlane32_swap(u0, v0, false, false); auto r1 = __builtin_amdgcn_permlane32_swap(u1, v1, false, false);
    auto r2 = __builtin_amdgcn_permlane32_swap(u2, v2, false, false); auto r3 = __builtin_amdgcn_permlane32_swap(u3, v3, false, false);
    const float c0 = __uint_as_float(r0[0]), c1 = __uint_as_float(r1[0]), c2 = __uint_as_float(r2[0]), c3 = __uint_as_float(r3[0]);
    const float c4 = __uint_as_float(r0[1]), c5 = __uint_as_float(r1[1]), c6 = __uint_as_float(r2[1]), c7 = __uint_as_float(r3[1]);
    u32x4 f;
    f[0] = cvtpk(c0 + bflo(xres[0]), c1 + bfhi(xres[0])); f[1] = cvtpk(c2 + bflo(xres[1]), c3 + bfhi(xres[1]));
    f[2] = cvtpk(c4 + bflo(xres[2]), c5 + bfhi(xres[2])); f[3] = cvtpk(c6 + bflo(xres[3]), c7 + bfhi(xres[3]));
    const i32x4 sO = mk_srd(out, 2048 * 1024);
    st16(f, sO, (RT * 32 + (lane & 31)) * 1024 + cg * 128 + kq * 32 + h * 16, 0);
  }
  if (b == 0 && wid == 7) {
    wait_vm<0>();
    asm volatile("" : "+v"(ef[0]), "+v"(ef[1]), "+v"(ef[2]), "+v"(ef[3]));
    bool ok = true;
#pragma unroll
    for (int k = 0; k < 4; ++k) ok = ok && ef[k][0] == (unsigned)C.tag && ef[k][1] == (unsigned)(C.tag >> 32);
    if (!__all(ok)) {
#pragma unroll 1
      for (int k = 0; k < 4; ++k) spin_flags(ws, flag_off(0, 0, 0) + (lane * 4 + k) * 128, true, C.tag);
    }
    if (lane == 0) __hip_atomic_store((u64*)(ws + WS_EPOCH), C.tag, __ATOMIC_RELAXED, __HIP_MEMORY_SCOPE_AGENT);
  }
  STAMP(60);

  if (b == 100 && wid == 0) { const u64 e = __builtin_amdgcn_s_memrealtime(); const u64 until = e + 2 * (C.prtx - C.prt0); while (__builtin_amdgcn_s_memrealtime() < until) __builtin_amdgcn_s_sleep(1); }
}

extern "C" void kernel_launch(void* const* d_in, const int* in_sizes, int n_in, void* d_out, int out_size, void* d_ws, size_t ws_size, hipStream_t stream) {
  (void)in_sizes; (void)n_in; (void)out_size; (void)ws_size;
  (void)hipFuncSetAttribute((const void*)fused_kernel, hipFuncAttributeMaxDynamicSharedMemorySize, LDS_BYTES);
  fused_kernel<<<256, 512, LDS_BYTES, stream>>>((const u16*)d_in[0], (const u16*)d_in[1], (const u16*)d_in[2], (const u16*)d_in[4], (const u16*)d_in[5], (const u16*)d_in[6],
                                                 (u16*)d_out, (char*)d_ws);
}
```

```cpp
#include <hip/hip_runtime.h>
#include <stdint.h>
#include <type_traits>

typedef unsigned short u16;
typedef unsigned long long u64;
typedef short bf16x8 __attribute__((ext_vector_type(8)));
typedef short s16x4 __attribute__((ext_vector_type(4)));
typedef float f32x2 __attribute__((ext_vector_type(2)));
typedef float f32x4 __attribute__((ext_vector_type(4)));
typedef float f32x16 __attribute__((ext_vector_type(16)));
typedef unsigned u32x2 __attribute__((ext_vector_type(2)));
typedef unsigned u32x4 __attribute__((ext_vector_type(4)));
typedef int i32x4 __attribute__((ext_vector_type(4)));
#define LDSP __attribute__((address_space(3)))
#define DI __device__ __forceinline__
template <class T> DI LDSP T* lp(unsigned a) { return (LDSP T*)(size_t)a; }
template <int I, int N, class F> DI void static_for(F&& f) { if constexpr (I < N) { f(std::integral_constant<int, I>{}); static_for<I + 1, N>(f); } }

constexpr size_t WS_EPOCH = 0;
constexpr size_t WS_ERR   = 64;
constexpr size_t WS_FLAGS = 4096;
constexpr size_t WS_QKV   = 1u << 20;
constexpr size_t WS_A2    = 8u << 20;
constexpr size_t WS_DBG   = 24u << 20;

constexpr unsigned R0 = 0, R1 = 32768, R2 = 65536, R3 = 98304;
constexpr unsigned L_K = 98304, L_V = 122880;
constexpr unsigned L_ONES = 147456;
constexpr unsigned L_CSP = 148480;
constexpr unsigned L_CSW = 150528;
constexpr unsigned L_STAT = 152576;
constexpr unsigned L_JUNK = 154624;
constexpr unsigned LDS_BYTES = 163840;
constexpr unsigned CODE_BYTES = 36864;
constexpr unsigned POISON = 0xffffffffu;
constexpr unsigned SPIN_CAP = 1u << 13;

#define NO_WARM 0
#define NO_CS 0
#define NO_DMA 0
constexpr float QSCALE = 0.25f * 1.4426950408889634f;

DI i32x4 mk_srd(const void* p, unsigned bytes) { u64 a = (u64)p; i32x4 r; r[0] = (int)(unsigned)a; r[1] = (int)((unsigned)(a >> 32) & 0xffffu); r[2] = (int)bytes; r[3] = 0x00020000; return r; }
DI void dma16(i32x4 srd, unsigned voff, unsigned soff, unsigned lds) {
  if (NO_DMA) return;
  asm volatile("s_mov_b32 m0, %3\n\ts_nop 3\n\tbuffer_load_dwordx4 %0, %1, %2 offen lds" :: "v"(voff), "s"(srd), "s"(soff), "s"(lds) : "memory");
}
DI void dma4(i32x4 srd, unsigned voff, unsigned lds) {
  asm volatile("s_mov_b32 m0, %2\n\ts_nop 3\n\tbuffer_load_dword %0, %1, 0 offen lds" :: "v"(voff), "s"(srd), "s"(lds) : "memory");
}
DI void ld16_sc1(u32x4& d, i32x4 srd, unsigned voff, unsigned soff) {
  asm volatile("s_nop 4\n\tbuffer_load_dwordx4 %0, %1, %2, %3 offen sc1" : "=v"(d) : "v"(voff), "s"(srd), "s"(soff) : "memory");
}
DI void ld16(u32x4& d, i32x4 srd, unsigned voff, unsigned soff) {
  asm volatile("s_nop 4\n\tbuffer_load_dwordx4 %0, %1, %2, %3 offen" : "=v"(d) : "v"(voff), "s"(srd), "s"(soff) : "memory");
}
DI void ld8_sc1(u32x2& d, i32x4 srd, unsigned voff) {
  asm volatile("s_nop 4\n\tbuffer_load_dwordx2 %0, %1, %2, 0 offen sc1" : "=v"(d) : "v"(voff), "s"(srd) : "memory");
}
DI void st16_sc1(u32x4 d, i32x4 srd, unsigned voff, unsigned soff) {
  asm volatile("s_nop 4\n\tbuffer_store_dwordx4 %0, %1, %2, %3 offen sc1\n\ts_nop 1" :: "v"(d), "v"(voff), "s"(srd), "s"(soff) : "memory");
}
DI void st16(u32x4 d, i32x4 srd, unsigned voff, unsigned soff) {
  asm volatile("s_nop 4\n\tbuffer_store_dwordx4 %0, %1, %2, %3 offen\n\ts_nop 1" :: "v"(d), "v"(voff), "s"(srd), "s"(soff) : "memory");
}
template <int N> DI void bar_vm() { static_assert(N >= 0 && N < 64, "vmcnt range"); asm volatile("s_waitcnt vmcnt(%0) lgkmcnt(0)\n\ts_barrier" :: "n"(N) : "memory"); }
template <int N> DI void wait_vm() { static_assert(N >= 0 && N < 64, "vmcnt range"); asm volatile("s_waitcnt vmcnt(%0)" :: "n"(N) : "memory"); }
DI s16x4 ldtr(unsigned a) { return __builtin_amdgcn_ds_read_tr16_b64_v4i16(lp<s16x4>(a)); }
DI bf16x8 ldfrag_tr(unsigned a, unsigned second) {
  s16x4 lo = ldtr(a), hi = ldtr(a + second);
  return __builtin_shufflevector(lo, hi, 0, 1, 2, 3, 4, 5, 6, 7);
}
DI bf16x8 ld128(unsigned a) { return *lp<bf16x8>(a); }
DI unsigned cvtpk(float lo, float hi) { typedef __bf16 b2 __attribute__((ext_vector_type(2))); f32x2 v = {lo, hi}; return __builtin_bit_cast(unsigned, __builtin_convertvector(v, b2)); }
DI float bflo(unsigned v) { return __uint_as_float(v << 16); }
DI float bfhi(unsigned v) { return __uint_as_float(v & 0xffff0000u); }
DI float rbf(float f) { return bflo(cvtpk(f, f) & 0xffffu); }
DI int fresh(int v) { asm volatile("" : "+v"(v)); return v; }
template <class T> DI void pin(T& v) { asm volatile("" : "+v"(v)); }
DI unsigned pinv(unsigned v) { asm volatile("" : "+v"(v)); return v; }
#define MFMA32(a, b, c) __builtin_amdgcn_mfma_f32_32x32x16_bf16((a), (b), (c), 0, 0, 0)
#define MFMA16(a, b, c) __builtin_amdgcn_mfma_f32_16x16x32_bf16((a), (b), (c), 0, 0, 0)
#define MFMA4(a, b, c) __builtin_amdgcn_mfma_f32_4x4x4bf16_1k((a), (b), (c), 0, 0, 0)
#define SB() __builtin_amdgcn_sched_barrier(0)
DI float swap_add(float v) { auto r = __builtin_amdgcn_permlane32_swap(__float_as_uint(v), __float_as_uint(v), false, false); return __uint_as_float(r[0]) + __uint_as_float(r[1]); }
DI float swap_max(float v) { auto r = __builtin_amdgcn_permlane32_swap(__float_as_uint(v), __float_as_uint(v), false, false); return __builtin_fmaxf(__uint_as_float(r[0]), __uint_as_float(r[1])); }

#define STAMP(i) do { } while (0)

struct Ctx {
  char* ws; u64 tag;
  i32x4 sX, sQKVw, sG, sU, sWO, sWD, sWS;
  int lane, wid, mg, ng, rb, cs, c, cg, xi, kq, rb2; unsigned stampreg; u64 prt0, prtx;
};

DI unsigned flag_off(int ev, int rb, int cs) { return (unsigned)(WS_FLAGS + (((ev * 16 + rb) * 16 + cs) << 7)); }
DI void publish(const Ctx& C, int ev) { if (C.wid == 0 && C.lane == 0) __hip_atomic_store((u64*)(C.ws + flag_off(ev, C.rb, C.cs)), C.tag, __ATOMIC_RELAXED, __HIP_MEMORY_SCOPE_AGENT); }
DI void give_up(char* ws) { __hip_atomic_store((unsigned*)(ws + WS_ERR), 1u, __ATOMIC_RELAXED, __HIP_MEMORY_SCOPE_AGENT); }
DI void spin_flags(char* ws, unsigned off, bool need, u64 tag) {
#pragma nounroll
  for (unsigned it = 0; it < SPIN_CAP; ++it) {
    u64 v = __hip_atomic_load((u64*)(ws + off), __ATOMIC_RELAXED, __HIP_MEMORY_SCOPE_AGENT);
    if (__all(!need || v == tag)) return;
    __builtin_amdgcn_s_sleep(2);
  }
  give_up(ws);
}
DI void check_flags(char* ws, unsigned off, bool need, u64 tag, u32x2& got) {
  asm volatile("" : "+v"(got));
  bool ok = !need || (got[0] == (unsigned)tag && got[1] == (unsigned)(tag >> 32));
  if (!__all(ok)) spin_flags(ws, off, need, tag);
}
template <int K0, int K1> DI void check_frags(const Ctx& C, u32x4 (&a2)[36], unsigned base) {
  auto dirty = [&]() { unsigned mx = 0;
    static_for<K0, K1>([&](auto kc) { constexpr int k = decltype(kc)::value;
      mx = __builtin_elementwise_max(mx, __builtin_elementwise_max(__builtin_elementwise_max(a2[k][0], a2[k][1]), __builtin_elementwise_max(a2[k][2], a2[k][3]))); });
    return __any(mx == POISON); };
  if (dirty()) {
#pragma nounroll
    for (unsigned it = 0; it < SPIN_CAP; ++it) {
      __builtin_amdgcn_s_sleep(2);
      static_for<K0, K1>([&](auto kc) { constexpr int k = decltype(kc)::value; ld16_sc1(a2[k], C.sWS, C.lane * 16, base + k * 1024); });
      wait_vm<0>();
      static_for<K0, K1>([&](auto kc) { constexpr int k = decltype(kc)::value; pin(a2[k]); });
      if (!dirty()) return;
    }
    give_up(C.ws);
  }
}

enum { T_H0 = 0, T_S4 = 8, T_E0 = 16, T_G0 = 19, T_POISON = 22, T_POLL, T_KL, T_POLL2, T_QL, T_QP, T_EPOCH, T_WARM, T_NONE };
template <int TYPE> struct G1 {
  static constexpr int kind(int p) { return p == 0 ? 0 : (p == 1 && TYPE == 1) ? 1 : 2; }
  static constexpr int nt(int p) { return p == 0 ? 1 : 2; }
  static constexpr int nst(int p) { return kind(p) == 1 ? 3 : 2; }
  static constexpr int npoison() { return 3; }
  static constexpr unsigned slot(int s) {
    return s == 4 ? R0 : s == 5 ? R1 : s == 6 ? R2 : s == 7 ? R3 : s == 8 ? R0 : s == 9 ? R1 : s == 10 ? R2 : R0; }
  static constexpr int warmat(int pos) { return (pos >= 39 && pos < 39 + 4 * 9 && ((pos - 39) & 3) == 0) ? (pos - 39) >> 2 : -1; }
  static constexpr int barpos(int bi) { return bi < 8 ? 4 * bi + 2 : 8 * (bi - 4) + 6; }
  static constexpr int barat(int pos) { return pos < 32 ? ((pos & 3) == 2 ? pos >> 2 : -1) : ((pos & 7) == 6 ? 4 + (pos >> 3) : -1); }
  static constexpr int grp(int bi) { return bi < 4 ? T_H0 + bi + 4 : bi < 11 ? T_S4 + (bi - 4) : bi == 11 ? T_NONE : bi == 12 ? T_S4 + 7 : T_G0 + (bi - 13); }
  static constexpr int gsize(int t) { return t == T_NONE ? 0 : t < T_S4 ? 3 : 4; }
  static constexpr int rel(int bi, int k) { return bi < 8 ? k : bi == 15 ? (k >> 1) : 2 * k; }
  static constexpr int after(int t, int bi) {
    int cnt = -1;
    auto see = [&](int tag, int n) { for (int i = 0; i < n; ++i) { if (tag == t) cnt = 0; else if (cnt >= 0) ++cnt; } };
    for (int h = 0; h < 2; ++h) see(T_H0 + h, gsize(T_H0 + h));
    see(T_EPOCH, 1);
    for (int pos = 0; pos < 96; ++pos) {
      if (pos < 2) see(T_H0 + 2 + pos, gsize(T_H0 + 2 + pos));
      const int b = barat(pos);
      if (b >= 0) {
        if (b == bi) return cnt;
        if (b == 12) see(T_KL, 6);
        if (b == 14) { see(T_POLL2, 1); see(T_QL, 1); }
      }
      for (int bb = 0; bb < 16; ++bb) for (int k = 0; k < gsize(grp(bb)); ++k) if (barpos(bb) + rel(bb, k) == pos) see(grp(bb), 1);
      if (!NO_WARM && pos == 35) see(T_WARM, 1);
      if (!NO_WARM && warmat(pos) >= 0) see(T_WARM, 1);
      if (pos == 32 && TYPE == 1) see(T_QP, 2);
      if (pos == 58) see(T_POLL, 1);
      if (pos == 50) see(T_POISON, npoison());
      if ((pos & 31) == 31) see(T_E0 + (pos >> 5), nst(pos >> 5));
    }
    return cnt;
  }
  static constexpr int mn(int a, int b) { return a < b ? a : b; }
};

template <int TYPE> DI unsigned tile_addr(const Ctx& C, int p, int t) {
  const int lane = fresh(C.lane), kind = G1<TYPE>::kind(p);
  const int g = (lane >> 4) & 1, h = lane >> 5, q = (lane & 15) >> 2, pp = lane & 3;
  const unsigned rdrow = (8 * h + q) * 128 + pp * 8;
  if (kind == 2) return g * 16384 + rdrow + (((2 * C.ng + t) ^ (2 * (q >> 1)) ^ g) << 5);
  if (kind == 0) return 16384 + rdrow + ((C.ng ^ (q >> 1)) << 6) + g * 32;
  if (t == 0) return rdrow + ((C.ng ^ (q >> 1)) << 6) + g * 32;
  return 16384 + g * 8192 + (8 * h + q) * 64 + pp * 8 + ((C.ng ^ g) << 5);
}
template <int TYPE> DI unsigned stage_voff(const Ctx& C, int p, int which) {
  const int kind = G1<TYPE>::kind(p), lane = fresh(C.lane);
  if (which == 1) return (16 * C.wid + (lane >> 2)) * 3584 + (8 + ((C.cs - 8) >> 1)) * 128 + ((C.cs - 8) & 1) * 64 + ((lane & 3) << 4);
  const unsigned row = 16 * C.wid + (lane >> 3), sw = ((lane & 7) ^ (((lane >> 4) & 1) << 2)) << 4;
  if (kind == 1) return row * 3072 + (C.cs - 8) * 128 + sw;
  return row * 3584 + (p == 1 ? C.cs : 12 + C.cs) * 128 + sw;
}
template <int TYPE> DI void issue_stage(const Ctx& C, int s, int k, unsigned v0) {
  const int kind = G1<TYPE>::kind(s >> 2);
  const unsigned slot = G1<TYPE>::slot(s), r0 = 128 * (s & 3);
  if (kind == 1) {
    if (k < 2) dma16(C.sQKVw, v0, (r0 + 8 * k) * 3072, slot + C.wid * 2048 + k * 1024);
    else dma16(k == 3 ? C.sU : C.sG, k == 3 ? v0 ^ 32 : v0, r0 * 3584, slot + 16384 + (k - 2) * 8192 + C.wid * 1024);
  } else dma16(k >> 1 ? C.sU : C.sG, k >> 1 ? v0 ^ 32 : v0, (r0 + 8 * (k & 1)) * 3584, slot + (k >> 1) * 16384 + C.wid * 2048 + (k & 1) * 1024);
}
DI unsigned hx_voff(const Ctx& C) { const int lane = fresh(C.lane); return (16 * C.wid + (lane >> 3)) * 1024 + (((lane & 7) ^ ((lane >> 4) & 3)) << 4); }
DI unsigned hw_voff(const Ctx& C) { const int lane = fresh(C.lane);
  return (8 * C.wid + (lane >> 3)) * 3072 + (8 + C.cs) * 128 + (((lane & 7) ^ (((lane >> 4) & 1) << 2)) << 4); }
DI void issue_half(const Ctx& C, int h, int k, unsigned vx, unsigned vw) {
  const unsigned slot = (h & 3) * 32768;
  if (k < 2) dma16(C.sX, k ? (vx ^ 64) + 8 * 1024 : vx, h * 128, slot + C.wid * 2048 + k * 1024);
  else dma16(C.sQKVw, vw + (k - 2) * 128, h * 64 * 3072, slot + 16384 + (k - 2) * 8192 + C.wid * 1024);
}
DI void warm(i32x4 srd, unsigned line0, unsigned lines_per_row_log2, unsigned pitch, unsigned colbyte, int lane) {
  const unsigned l = line0 + lane;
  dma4(srd, (l >> lines_per_row_log2) * pitch + colbyte + (l & ((1u << lines_per_row_log2) - 1)) * 128, L_JUNK);
}
DI unsigned g_voff(const Ctx& C) { const int lane = fresh(C.lane); return (32 * (C.wid & 1) + (lane >> 3)) * 1024 + C.cg * 128 + (((lane & 7) ^ (((lane >> 4) & 1) << 2)) << 4); }
constexpr unsigned gslot(int j) { return j == 0 ? R1 : j == 1 ? R2 : j == 2 ? R0 : j == 3 ? R3 : j == 4 ? R1 : j == 5 ? R2 : j == 6 ? R0 : j == 7 ? R3 : R1; }
DI void issue_g(const Ctx& C, int j, int i, unsigned v) {
  const int kq = C.wid >> 1;
  const int row0 = j < 3 ? (3 * kq + j) * 64 : j < 7 ? (12 + 4 * kq + (j - 3)) * 64 : 128 * kq + 64 * (j - 7);
  dma16(j < 7 ? C.sWD : C.sWO, v, (unsigned)(row0 + 8 * i) * 1024, gslot(j) + kq * 8192 + (C.wid & 1) * 4096 + i * 1024);
}

template <int WHAT> DI void epi_qkv(const Ctx& C, const f32x16& acc, const f32x4 (&csr)[4], float m, float rinv, int qu, int h4) {
  const int h = C.lane >> 5;
  const float sc = WHAT == 0 ? rinv * QSCALE : rinv;
  float y[16];
#pragma unroll
  for (int i4 = 0; i4 < 4; ++i4)
#pragma unroll
    for (int e = 0; e < 4; ++e) y[4 * i4 + e] = (acc[4 * i4 + e] - csr[i4][e]) * sc;
  const unsigned base = (unsigned)WS_QKV + (((C.rb * 24 + qu) * 4 + C.mg) * 4 + h4) * 1024;
#pragma unroll
  for (int hh = 0; hh < 2; ++hh) {
    u32x4 f; f[0] = cvtpk(y[8 * hh], y[8 * hh + 1]); f[1] = cvtpk(y[8 * hh + 2], y[8 * hh + 3]); f[2] = cvtpk(y[8 * hh + 4], y[8 * hh + 5]); f[3] = cvtpk(y[8 * hh + 6], y[8 * hh + 7]);
    unsigned voff = C.lane * 16;
    if (WHAT == 2) {
      auto a = __builtin_amdgcn_permlane32_swap(f[0], f[2], false, false); auto b = __builtin_amdgcn_permlane32_swap(f[1], f[3], false, false);
      f[0] = a[0]; f[2] = a[1]; f[1] = b[0]; f[3] = b[1];
      voff = (C.lane & 31) * 32 + h * 16;
    }
    st16_sc1(f, C.sWS, voff, base + hh * 1024);
  }
}
DI void epi_gu(const Ctx& C, const f32x16& acc, const f32x4 (&csr)[4], float m, float r2, unsigned a2off) {
  float y[8];
#pragma unroll
  for (int i4 = 0; i4 < 2; ++i4)
#pragma unroll
    for (int e = 0; e < 4; ++e) y[4 * i4 + e] = ((acc[4 * i4 + e] - csr[i4][e]) * r2) * (acc[8 + 4 * i4 + e] - csr[2 + i4][e]);
  u32x4 f; f[0] = cvtpk(y[0], y[1]); f[1] = cvtpk(y[2], y[3]); f[2] = cvtpk(y[4], y[5]); f[3] = cvtpk(y[6], y[7]);
  st16_sc1(f, C.sWS, C.lane * 16, a2off);
}
template <int TYPE> DI unsigned a2_pass(const Ctx& C, int p) {
  if (p == 2) return (unsigned)WS_A2 + (((4 * C.rb + C.mg) * 4 + (C.cs >> 2)) * 36 + 4 * (3 + (C.cs & 3)) + 2 * C.ng) * 1024;
  if (TYPE == 0) return (unsigned)WS_A2 + (((4 * C.rb + C.mg) * 4 + C.cs / 3) * 36 + 4 * (C.cs % 3) + 2 * C.ng) * 1024;
  const int u = 8 + ((C.cs - 8) >> 1), half = (C.cs - 8) & 1;
  return (unsigned)WS_A2 + (((4 * C.rb + C.mg) * 4 + u / 3) * 36 + 4 * (u % 3) + 2 * half + C.ng) * 1024;
}
DI unsigned a2_o(const Ctx& C) { return (unsigned)WS_A2 + (((4 * C.rb + C.mg) * 4 + (C.cs >> 2)) * 36 + 28 + 2 * (C.cs & 3) + C.ng) * 1024; }

template <int TYPE> DI void gemm1(Ctx& C, unsigned vx, unsigned vw, u32x4& qfrag) {
  using G = G1<TYPE>;
  const int lane = C.lane;
  char* ws = C.ws;
  bf16x8 xa[32];
  bf16x8 wf[3][2];
  f32x16 acc[2];
  unsigned ta[2], sv = 0;
  ta[0] = tile_addr<TYPE>(C, 0, 0); ta[1] = tile_addr<TYPE>(C, 0, 1);
  const unsigned r31 = fresh(lane) & 31, hh = fresh(lane) >> 5;
  const unsigned xlane = (32 * C.mg + r31) * 128 + ((hh ^ ((r31 >> 1) & 7)) << 4);
  f32x4 ssum = {0, 0, 0, 0}, ssq = {0, 0, 0, 0};
  f32x4 csacc[2] = {{0,0,0,0},{0,0,0,0}};
  bf16x8 sel;
  { const int lf = fresh(lane); const short one = ((lf & 15) == ((lf >> 4) & 1)) ? (short)0x3f80 : (short)0;
#pragma unroll
    for (int e = 0; e < 8; ++e) sel[e] = one; }
  float csn[2];
  f32x4 csr[2][4];
  float m = 0.f, rinv = 0.f;
  u32x2 pollv, poll2; u32x4 gk[6];
  unsigned polloff = 0, poll2off = 0; bool pollneed = false;
  const unsigned qoff = (unsigned)WS_QKV + (((C.rb * 24 + (C.cs >> 1)) * 4 + C.mg) * 4 + 2 * (C.cs & 1) + C.ng) * 1024;

  STAMP(1);
  bar_vm<G::gsize(T_H0)>();
  STAMP(2);
  u32x2 ev; ld8_sc1(ev, C.sWS, (unsigned)WS_EPOCH);
  auto read_x = [&](int h) {
    const unsigned base = (h & 3) * 32768 + xlane;
#pragma unroll
    for (int i = 0; i < 4; ++i) xa[4 * h + i] = ld128(base ^ (((i + C.c) & 3) << 5));
  };
  auto read_w = [&](int pos) {
    const int p = pos >> 5, s = pos >> 3, i = pos & 7;
    if ((pos & 31) == 0 && pos > 0) { ta[0] = tile_addr<TYPE>(C, p, 0); ta[1] = tile_addr<TYPE>(C, p, 1); }
    const unsigned sb = p == 0 ? ((pos >> 2) & 3) * 32768 : G::slot(s), ks = (p == 0 ? 0 : (i & 4)) + ((i + C.c) & 3);
    wf[pos % 3][0] = ldfrag_tr(pinv(ta[0] + sb + ks * 2048), 512);
    if (G::nt(p) == 2) { if (G::kind(p) == 1) wf[pos % 3][1] = ldfrag_tr(pinv(ta[1] + sb + ks * 1024), 256); else wf[pos % 3][1] = ldfrag_tr(pinv(ta[1] + sb + ks * 2048), 512); }
  };
  read_x(0); read_w(0); read_w(1);
  static_for<0, 96>([&](auto ic) {
    constexpr int pos = decltype(ic)::value;
    constexpr int p = pos >> 5, s = pos >> 3, st = s & 3, i = pos & 7, NT = G::nt(p), bi = G::barat(pos);
    if constexpr (pos < 2) static_for<0, G::gsize(T_H0)>([&](auto kc) { issue_half(C, 2 + pos, decltype(kc)::value, vx, vw); });
    if constexpr (bi >= 0) {
      STAMP(10 + 2 * bi);
      if constexpr (bi == 0) { bar_vm<G::after(T_H0 + 1, 0)>(); }
      else if constexpr (bi < 7) bar_vm<G::after(T_H0 + bi + 1, bi)>();
      else if constexpr (bi == 9) { bar_vm<G::mn(G::mn(G::after(T_S4 + 2, 9), G::after(T_E0, 9)), TYPE == 1 ? G::after(T_QP, 9) : 63)>(); publish(C, 0); }
      else if constexpr (bi == 12) bar_vm<G::mn(G::after(T_S4 + 5, 12), G::after(T_POLL, 12))>();
      else if constexpr (bi == 13) { static_assert(G::after(T_E0 + 1, 13) <= G::after(T_POISON, 13), ""); bar_vm<G::mn(G::after(T_S4 + 6, 13), G::after(T_E0 + 1, 13))>(); }
      else if constexpr (bi == 14) bar_vm<G::mn(G::after(T_S4 + 7, 14), G::after(T_KL, 14))>();
      else if constexpr (bi == 15) bar_vm<63>();
      else bar_vm<G::after(T_S4 + bi - 7, bi)>();
      if constexpr (bi == 4) {
        static_assert(G::after(T_H0 + 5, 4) <= G::after(T_EPOCH, 4), "");
        pin(ev);
        const u64 t = (((u64)ev[1] << 32) | ev[0]) * 6364136223846793005ull + 1442695040888963407ull;
        C.tag = ((u64)__builtin_amdgcn_readfirstlane((unsigned)(t >> 32)) << 32) | __builtin_amdgcn_readfirstlane((unsigned)t);
      }
      if constexpr (bi == 12) {
        check_flags(ws, polloff, pollneed, C.tag, pollv);
        const int u = C.cs >> 1, h4 = 2 * (C.cs & 1) + C.ng;
#pragma unroll
        for (int b = 0; b < 6; ++b) { const int rbp = C.rb - 2 + b % 3 < 0 ? 0 : C.rb - 2 + b % 3;
          ld16_sc1(gk[b], C.sWS, lane * 16, (unsigned)WS_QKV + (((rbp * 24 + (b < 3 ? 8 : 16) + u) * 4 + C.mg) * 4 + h4) * 1024); }
      }
      if constexpr (bi == 13) publish(C, 1);
      if constexpr (bi == 14) {
#pragma unroll
        for (int b = 0; b < 6; ++b) pin(gk[b]);
#pragma unroll
        for (int b = 0; b < 3; ++b) { *lp<u32x4>(L_K + ((4 * b + C.mg) * 2 + C.ng) * 1024 + lane * 16) = gk[b];
                                      *lp<u32x4>(L_V + C.ng * 12288 + (4 * b + C.mg) * 1024 + lane * 16) = gk[3 + b]; }
        { const int ln = fresh(lane), l = ln < 10 ? ln : 0, uu = 3 * C.kq + (l >> 1);
          poll2off = flag_off(1, C.rb2, l < 6 ? (uu < 8 ? uu : 8 + 2 * (uu - 8) + (l & 1)) : 4 * C.kq + (l - 6)); }
        ld8_sc1(poll2, C.sWS, poll2off);
        ld16_sc1(qfrag, C.sWS, lane * 16, qoff);
      }
      if constexpr (bi < 7) read_x(bi + 1);
      if constexpr (st == 3 && i == 6) {
#pragma unroll
        for (int t = 0; t < NT; ++t) { const unsigned a = L_CSP + (C.ng * 2 + t) * 512 + r31 * 4;
          csn[t] = (*lp<float>(a) + *lp<float>(a + 128)) + (*lp<float>(a + 256) + *lp<float>(a + 384)); }
      }
      STAMP(11 + 2 * bi);
      if constexpr (bi == 7) C.prtx = __builtin_amdgcn_s_memrealtime();
    }
    static_for<0, 16>([&](auto bc) {
      constexpr int bb = decltype(bc)::value, tg = G::grp(bb), k0 = pos - G::barpos(bb);
      static_for<0, 4>([&](auto kc) {
        constexpr int k = decltype(kc)::value;
        if constexpr (k < G::gsize(tg) && G::rel(bb, k) == k0) {
          if constexpr (tg < T_S4) issue_half(C, tg, k, vx, vw);
          else if constexpr (tg >= T_G0) { if constexpr (k == 0) sv = g_voff(C); issue_g(C, tg - T_G0, k, sv); }
          else { constexpr int pp_ = (tg - T_S4 + 4) >> 2;
            if constexpr (k == 0) sv = stage_voff<TYPE>(C, pp_, 0);
            if constexpr (k == 2 && G::kind(pp_) == 1) sv = stage_voff<TYPE>(C, pp_, 1);
            issue_stage<TYPE>(C, tg - T_S4 + 4, k, sv); }
        }
      });
    });
    if constexpr (pos == 35 && !NO_WARM) {
      const int lf = fresh(lane);
      dma4(C.wid < 4 ? C.sG : C.sU, ((C.rb & 7) * 64 + (C.wid & 3) * 16 + (lf & 15)) * 3584 + (12 + C.cs) * 128, L_JUNK);
    }
    if constexpr (G::warmat(pos) >= 0 && !NO_WARM) {
      const unsigned l = (C.xi * 9 + G::warmat(pos)) * 64 + C.wid * 8 + (fresh(lane) & 7);
      dma4(l < 4096 ? C.sWO : C.sWD, (l < 4096 ? l : l - 4096) * 128, L_JUNK);
    }
    if constexpr (pos == 32 && TYPE == 1) {
      const u32x4 pz = {POISON, POISON, POISON, POISON};
      const unsigned a = (unsigned)WS_QKV + (((C.rb * 24 + (C.cs - 8)) * 4 + C.mg) * 4 + 2 * C.ng) * 1024;
      st16_sc1(pz, C.sWS, lane * 16, a); st16_sc1(pz, C.sWS, lane * 16, a + 1024);
    }
    if constexpr (pos == 58) {
      const int u = C.cs >> 1, l = fresh(lane) & 7;
      const int rbp = C.rb - 2 + l % 3;
      pollneed = l < 6 && rbp >= 0;
      polloff = flag_off(0, pollneed ? rbp : C.rb, l < 3 ? u : 8 + u);
      ld8_sc1(pollv, C.sWS, polloff);
    }
    if constexpr (pos == 50) {
      const u32x4 pz = {POISON, POISON, POISON, POISON};
      const unsigned a = a2_pass<TYPE>(C, 2);
      st16_sc1(pz, C.sWS, lane * 16, a);
      st16_sc1(pz, C.sWS, lane * 16, a + 1024);
      st16_sc1(pz, C.sWS, lane * 16, a2_o(C));
    }
    if constexpr (pos + 2 < 96) read_w(pos + 2);
    if constexpr ((pos & 31) == 0) { acc[0] = MFMA32(wf[pos % 3][0], xa[pos & 31], f32x16{}); if (NT == 2) acc[1] = MFMA32(wf[pos % 3][1], xa[pos & 31], f32x16{}); }
    else { acc[0] = MFMA32(wf[pos % 3][0], xa[pos & 31], acc[0]); if (NT == 2) acc[1] = MFMA32(wf[pos % 3][1], xa[pos & 31], acc[1]); }
    if constexpr ((i & 3) == 0 && !NO_CS) {
      if constexpr ((pos & 31) == 0) { csacc[0] = MFMA16(wf[pos % 3][0], sel, (f32x4{0, 0, 0, 0})); if (NT == 2) csacc[1] = MFMA16(wf[pos % 3][1], sel, (f32x4{0, 0, 0, 0})); }
      else { csacc[0] = MFMA16(wf[pos % 3][0], sel, csacc[0]); if (NT == 2) csacc[1] = MFMA16(wf[pos % 3][1], sel, csacc[1]); }
    }
    if constexpr (p == 0 && (i & 3) < 2) {
      const bf16x8 xv = xa[pos & 31]; const s16x4 lo = __builtin_shufflevector(xv, xv, 0, 1, 2, 3), hi = __builtin_shufflevector(xv, xv, 4, 5, 6, 7);
      const s16x4 ones = {0x3f80, 0x3f80, 0x3f80, 0x3f80};
      ssum = MFMA4(ones, lo, ssum); ssq = MFMA4(lo, lo, ssq); ssum = MFMA4(ones, hi, ssum); ssq = MFMA4(hi, hi, ssq);
    }
    if constexpr (st == 3 && i == 5) {
      if ((lane & 15) < 2) {
#pragma unroll
        for (int t = 0; t < NT; ++t) *lp<f32x4>(L_CSP + ((C.ng * 2 + t) * 4 + C.mg) * 128 + (16 * (lane & 15) + 4 * (lane >> 4)) * 4) = csacc[t];
      }
      if constexpr (p == 0) {
        const float sq = (lane & 3) == 0 ? ssq[0] : (lane & 3) == 1 ? ssq[1] : (lane & 3) == 2 ? ssq[2] : ssq[3];
        const f32x2 v = {swap_add(ssum[0]), swap_add(sq)};
        if (lane < 32) *lp<f32x2>(L_STAT + (C.ng * 128 + 32 * C.mg + lane) * 8) = v;
      }
    }
    if constexpr (st == 3 && i == 6) {
#pragma unroll
      for (int t = 0; t < NT; ++t) *lp<float>(L_CSW + (C.wid * 2 + t) * 128 + r31 * 4) = csn[t];
    }
    if constexpr (st == 3 && i == 6 && p == 0) {
      const f32x2 a = *lp<f32x2>(L_STAT + (32 * C.mg + r31) * 8), b = *lp<f32x2>(L_STAT + (128 + 32 * C.mg + r31) * 8);
      const float mean = (a[0] + b[0]) * (1.f / 512.f);
      const float var = __builtin_fmaxf((a[1] + b[1]) * (1.f / 512.f) - mean * mean, 0.f);
      m = rbf(mean);
      rinv = __builtin_amdgcn_rcpf(rbf(__builtin_amdgcn_sqrtf(rbf(rbf(var) + 1e-5f))));
      pin(m); pin(rinv);
    }
    if constexpr (st == 3 && i == 7) {
#pragma unroll
      for (int t = 0; t < NT; ++t)
#pragma unroll
        for (int i4 = 0; i4 < 4; ++i4) { csr[t][i4] = *lp<f32x4>(L_CSW + (C.wid * 2 + t) * 128 + (8 * i4 + 4 * hh) * 4) * m; pin(csr[t][i4]); }
    }
    SB();
    if constexpr ((pos & 31) == 31) {
      if constexpr (G::kind(p) == 2) {
        const unsigned a2 = a2_pass<TYPE>(C, p);
        epi_gu(C, acc[0], csr[0], m, rinv * rinv, a2); epi_gu(C, acc[1], csr[1], m, rinv * rinv, a2 + 1024);
      } else if constexpr (G::kind(p) == 1) {
        epi_qkv<0>(C, acc[0], csr[0], m, rinv, C.cs - 8, 2 * C.ng);
        epi_gu(C, acc[1], csr[1], m, rinv * rinv, a2_pass<TYPE>(C, p));
      } else {
        epi_qkv<1 + TYPE>(C, acc[0], csr[0], m, rinv, 8 + C.cs, 2 * C.ng);
      }
      SB();
    }
  });
  wait_vm<G::after(T_QL, 16)>();
  pin(qfrag);
  check_flags(ws, poll2off, lane < 10, C.tag, poll2);
  { auto dirty = [&]() { return __any(__builtin_elementwise_max(__builtin_elementwise_max(qfrag[0], qfrag[1]), __builtin_elementwise_max(qfrag[2], qfrag[3])) == POISON); };
    if (dirty()) {
#pragma nounroll
      for (unsigned it = 0; it < SPIN_CAP; ++it) {
        __builtin_amdgcn_s_sleep(2);
        ld16_sc1(qfrag, C.sWS, lane * 16, qoff); wait_vm<0>(); pin(qfrag);
        if (!dirty()) break;
        if (it == SPIN_CAP - 1) give_up(ws);
      } } }
}

template <bool EDGE, class SIDE> DI u32x4 attention(Ctx& C, const u32x4& qraw, SIDE&& side) {
  const int lane = C.lane, h = lane >> 5, g = (lane >> 4) & 1, q = (lane & 15) >> 2, pp = lane & 3;
  const bf16x8 qf = __builtin_bit_cast(bf16x8, qraw);
  const unsigned kaddr = pinv(L_K + (C.mg * 2 + C.ng) * 1024 + lane * 16);
  const int t0 = EDGE ? (C.rb == 0 ? 8 : 4) - C.mg : 1;
  const int dq = (lane & 31) - 4 * h;
  const float NEG = -1e30f;
  auto older = [&](int r) { return ((r & 3) + 8 * (r >> 2)) > dq; };
  auto max16 = [&](float& a, float& b, const f32x16& s) {
#pragma unroll
    for (int r = 0; r < 16; r += 4) { a = __builtin_fmaxf(__builtin_fmaxf(a, s[r]), s[r + 1]); b = __builtin_fmaxf(__builtin_fmaxf(b, s[r + 2]), s[r + 3]); } };
  bf16x8 kf[9];
#pragma unroll
  for (int t = 0; t < 9; ++t) if (!EDGE || t > 0) kf[t] = ld128(kaddr + t * 2048);
  float mx;
  {
    f32x16 S[3];
    f32x16 sl = MFMA32(kf[8], qf, f32x16{});
    f32x16 sf;
    if (!EDGE) sf = MFMA32(kf[0], qf, f32x16{});
    S[1] = MFMA32(kf[1], qf, f32x16{});
    S[2] = MFMA32(kf[2], qf, f32x16{});
    float a = NEG, b = NEG;
    side(std::integral_constant<int, 0>{});
    SB();
#pragma unroll
    for (int r = 0; r < 16; ++r) sl[r] = older(r) ? (EDGE ? NEG : sf[r]) : sl[r];
    max16(a, b, sl);
    static_for<1, 8>([&](auto tc) { constexpr int t = decltype(tc)::value;
      if constexpr (t + 2 < 8) S[(t + 2) % 3] = MFMA32(kf[t + 2], qf, f32x16{});
      side(std::integral_constant<int, t>{});
      SB();
      if (!EDGE || t >= t0) max16(a, b, S[t % 3]);
      SB(); });
    mx = swap_max(__builtin_fmaxf(a, b));
  }
  f32x16 negm;
#pragma unroll
  for (int r = 0; r < 16; ++r) negm[r] = -mx;
  const unsigned vbase = pinv(g == 0 ? L_V + C.ng * 12288 + C.mg * 1024 + (4 * h + q) * 32 + pp * 8 : L_ONES + 128 * (1 - h) + q * 32 + pp * 8);
  const unsigned vstr = g == 0 ? 1024u : 0u;
  f32x16 o;
  auto expack = [&](const f32x16& s, u32x4& pa, u32x4& pb) {
#pragma unroll
    for (int i = 0; i < 4; ++i) pa[i] = cvtpk(__builtin_amdgcn_exp2f(s[2 * i]), __builtin_amdgcn_exp2f(s[2 * i + 1]));
#pragma unroll
    for (int i = 0; i < 4; ++i) pb[i] = cvtpk(__builtin_amdgcn_exp2f(s[8 + 2 * i]), __builtin_amdgcn_exp2f(s[9 + 2 * i])); };
  f32x16 S[2];
  bf16x8 vf[2][2];
  {
    f32x16 sl = MFMA32(kf[8], qf, negm);
    f32x16 sf;
    if (!EDGE) sf = MFMA32(kf[0], qf, negm);
    S[1] = MFMA32(kf[1], qf, negm);
    const unsigned v8 = vbase + 8 * vstr;
    const bf16x8 va = ldfrag_tr(v8, 256), vb = ldfrag_tr(v8 + 512, 256);
    bf16x8 vc, vd;
    if (!EDGE) { vc = ldfrag_tr(vbase, 256); vd = ldfrag_tr(vbase + 512, 256); }
    vf[1][0] = ldfrag_tr(vbase + vstr, 256); vf[1][1] = ldfrag_tr(vbase + vstr + 512, 256);
    side(std::integral_constant<int, 8>{});
    SB();
#pragma unroll
    for (int r = 0; r < 16; ++r) sl[r] = older(r) ? (EDGE ? NEG : sf[r]) : sl[r];
    u32x4 pa, pb, mk0, mk1;
    expack(sl, pa, pb);
#pragma unroll
    for (int i = 0; i < 4; ++i) {
      mk0[i] = (older(2 * i) ? 0xffffu : 0u) | (older(2 * i + 1) ? 0xffff0000u : 0u);
      mk1[i] = (older(8 + 2 * i) ? 0xffffu : 0u) | (older(9 + 2 * i) ? 0xffff0000u : 0u); }
    o = MFMA32(va, __builtin_bit_cast(bf16x8, pa & ~mk0), f32x16{});
    o = MFMA32(vb, __builtin_bit_cast(bf16x8, pb & ~mk1), o);
    if (!EDGE) {
      o = MFMA32(vc, __builtin_bit_cast(bf16x8, pa & mk0), o);
      o = MFMA32(vd, __builtin_bit_cast(bf16x8, pb & mk1), o);
    }
    SB();
  }
  static_for<1, 8>([&](auto tc) { constexpr int t = decltype(tc)::value;
    if constexpr (t + 1 < 8) {
      const unsigned vt = vbase + (t + 1) * vstr;
      vf[(t + 1) & 1][0] = ldfrag_tr(vt, 256); vf[(t + 1) & 1][1] = ldfrag_tr(vt + 512, 256);
      S[(t + 1) & 1] = MFMA32(kf[t + 1], qf, negm); }
    side(std::integral_constant<int, 8 + t>{});
    SB();
    if (!EDGE || t >= t0) {
      u32x4 pa, pb;
      expack(S[t & 1], pa, pb);
      o = MFMA32(vf[t & 1][0], __builtin_bit_cast(bf16x8, pa), o);
      o = MFMA32(vf[t & 1][1], __builtin_bit_cast(bf16x8, pb), o);
    }
    SB(); });
  const float li = __builtin_amdgcn_rcpf(o[8]);
  u32x4 f; f[0] = cvtpk(o[0] * li, o[1] * li); f[1] = cvtpk(o[2] * li, o[3] * li); f[2] = cvtpk(o[4] * li, o[5] * li); f[3] = cvtpk(o[6] * li, o[7] * li);
  return f;
}

__global__ void __launch_bounds__(512, 2)
fused_kernel(const u16* __restrict__ x, const u16* __restrict__ wqkv, const u16* __restrict__ wo, const u16* __restrict__ wg, const u16* __restrict__ wu,
             const u16* __restrict__ wd, u16* __restrict__ out, char* __restrict__ ws) {
  Ctx C;
  const int tid = threadIdx.x, lane = tid & 63, wid = __builtin_amdgcn_readfirstlane(tid >> 6);
  const int b = blockIdx.x, xj = b & 7, xi = b >> 3;
  C.stampreg = 0;
  STAMP(0);
  C.prt0 = __builtin_amdgcn_s_memrealtime(); C.prtx = C.prt0;
  asm volatile("" :: "s"(x), "s"(wqkv), "s"(wo), "s"(wg), "s"(wu), "s"(wd), "s"(out), "s"(ws));
  C.ws = ws; C.lane = lane; C.wid = wid; C.mg = wid & 3; C.ng = wid >> 2;
  C.rb = 8 * (xj & 1) + (xi & 7); C.cs = 4 * (xj >> 1) + (xi >> 3); C.c = (C.mg + 2 * C.ng) & 3;
  C.sX = mk_srd(x + (size_t)C.rb * 128 * 512, 128 * 1024);
  C.sQKVw = mk_srd(wqkv, 512 * 3072);
  C.xi = xi;
  const unsigned vx = hx_voff(C), vw = hw_voff(C);
  STAMP(3);
  issue_half(C, 0, 0, vx, vw); issue_half(C, 0, 1, vx, vw); issue_half(C, 0, 2, vx, vw);
  STAMP(4);
  issue_half(C, 1, 0, vx, vw); issue_half(C, 1, 1, vx, vw); issue_half(C, 1, 2, vx, vw);
  STAMP(5);
  C.sWS = mk_srd(ws, 32u << 20);
  C.sG = mk_srd(wg, 512 * 3584); C.sU = mk_srd(wu, 512 * 3584); C.sWO = mk_srd(wo, 512 * 1024); C.sWD = mk_srd(wd, 1792 * 1024);
  if (wid == 7) { u64 pc; asm volatile("s_getpc_b64 %0" : "=s"(pc));
    dma4(mk_srd((const void*)(pc & ~255ull), CODE_BYTES), (xi * 9 + (lane < 9 ? lane : 0)) * 128, L_JUNK); }
  if (tid < 256) lp<unsigned>(L_ONES)[tid] = 0x3f803f80u;
  const int rt64 = 4 * xj + (xi & 3), cg = xi >> 2;
  C.cg = cg;
  const int rt = wid & 1, kq = wid >> 1, RT = 2 * rt64 + rt, rb2 = RT >> 2;
  C.kq = kq; C.rb2 = rb2;
  u32x4 qfrag;
  if (C.cs < 8) gemm1<0>(C, vx, vw, qfrag); else gemm1<1>(C, vx, vw, qfrag);
  STAMP(6);

  const unsigned a2base = (unsigned)WS_A2 + (RT * 4 + kq) * 36 * 1024;
  u32x4 a2[36];
  auto side = [&](auto kc) { constexpr int k = decltype(kc)::value;
    if constexpr (k < 6) { ld16_sc1(a2[2 * k], C.sWS, lane * 16, a2base + 2 * k * 1024); ld16_sc1(a2[2 * k + 1], C.sWS, lane * 16, a2base + (2 * k + 1) * 1024); }
    if constexpr (k >= 12) static_for<0, 4>([&](auto jc) { constexpr int q_ = 12 + 4 * (k - 12) + decltype(jc)::value; ld16_sc1(a2[q_], C.sWS, lane * 16, a2base + q_ * 1024); }); };
  u32x4 of;
  if (C.rb >= 2) of = attention<false>(C, qfrag, side); else of = attention<true>(C, qfrag, side);
  st16_sc1(of, C.sWS, lane * 16, a2_o(C));
  STAMP(7);

  const int g = (lane >> 4) & 1, h = lane >> 5, q = (lane & 15) >> 2, pp = lane & 3;
  const unsigned w2l0 = kq * 8192 + (4 * h + q) * 128 + ((0 ^ (q >> 1)) << 6) + g * 32 + pp * 8;
  const unsigned w2l1 = kq * 8192 + (4 * h + q) * 128 + ((1 ^ (q >> 1)) << 6) + g * 32 + pp * 8;
  bf16x8 wf[3][2];
  f32x16 acc[2];
  u32x4 xres;
  unsigned wb[2], gv;
  auto read_w2 = [&](int pos) {
    if ((pos & 3) == 0) { wb[0] = pinv(w2l0 + gslot(pos >> 2)); wb[1] = pinv(w2l1 + gslot(pos >> 2)); }
    wf[pos % 3][0] = ldfrag_tr(wb[0] + (pos & 3) * 2048, 1024); wf[pos % 3][1] = ldfrag_tr(wb[1] + (pos & 3) * 2048, 1024); };
  bar_vm<17>();
  static_for<0, 12>([&](auto kc) { constexpr int k = decltype(kc)::value; pin(a2[k]); });
  read_w2(0); read_w2(1);
  gv = g_voff(C);
  STAMP(8);
  u32x4 ef[4];
  static_for<0, 36>([&](auto ic) {
    constexpr int pos = decltype(ic)::value;
    constexpr int j = pos >> 2, i = pos & 3;
    if constexpr (i == 2) {
      STAMP(44 + 2 * j);
      if constexpr (j <= 1) bar_vm<63>();
      if constexpr (j == 2) bar_vm<6>();
      if constexpr (j == 3) bar_vm<6>();
      if constexpr (j == 4) bar_vm<7>();
      if constexpr (j == 5) bar_vm<15>();
      if constexpr (j == 6) bar_vm<4>();
      if constexpr (j == 7) bar_vm<0>();
      STAMP(45 + 2 * j);
    }
    if constexpr (pos == 10) { static_for<12, 28>([&](auto kc) { pin(a2[decltype(kc)::value]); }); check_frags<12, 28>(C, a2, a2base); }
    if constexpr (pos == 26) { static_for<28, 36>([&](auto kc) { pin(a2[decltype(kc)::value]); }); pin(xres); check_frags<28, 36>(C, a2, a2base); }
    if constexpr (pos == 16) { const i32x4 sXall = mk_srd(x, 2048 * 1024); ld16(xres, sXall, (RT * 32 + (lane & 31)) * 1024 + cg * 128 + kq * 32 + h * 16, 0); }
    if constexpr (pos == 18 || pos == 19) static_for<0, 4>([&](auto kc) { constexpr int k = 28 + 4 * (pos - 18) + decltype(kc)::value; ld16_sc1(a2[k], C.sWS, lane * 16, a2base + k * 1024); });
    if constexpr (pos < 24) issue_g(C, 3 + (pos >> 2), pos & 3, gv);
    if constexpr (pos == 30) { if (b == 0 && wid == 7) {
#pragma unroll
        for (int k = 0; k < 4; ++k) { u32x2 t; ld8_sc1(t, C.sWS, flag_off(0, 0, 0) + (lane * 4 + k) * 128); ef[k][0] = t[0]; ef[k][1] = t[1]; } } }
    if constexpr (pos + 2 < 36) read_w2(pos + 2);
    const bf16x8 af = __builtin_bit_cast(bf16x8, a2[pos]);
    if constexpr (pos == 0) { acc[0] = MFMA32(wf[0][0], af, f32x16{}); acc[1] = MFMA32(wf[0][1], af, f32x16{}); }
    else { acc[0] = MFMA32(wf[pos % 3][0], af, acc[0]); acc[1] = MFMA32(wf[pos % 3][1], af, acc[1]); }
    SB();
  });
  STAMP(9);
#pragma unroll
  for (int d = 0; d < 4; ++d) if (d != kq) {
    const int ct = d >> 1, r0 = (d & 1) * 8;
    const unsigned a = R2 + ((rt * 4 + d) * 4 + kq) * 2048 + lane * 16;
    *lp<f32x4>(a) = f32x4{acc[ct][r0], acc[ct][r0 + 1], acc[ct][r0 + 2], acc[ct][r0 + 3]};
    *lp<f32x4>(a + 1024) = f32x4{acc[ct][r0 + 4], acc[ct][r0 + 5], acc[ct][r0 + 6], acc[ct][r0 + 7]};
  }
  float s[8];
#pragma unroll
  for (int e = 0; e < 8; ++e) { const float lo = (kq & 1) ? acc[0][8 + e] : acc[0][e], hi = (kq & 1) ? acc[1][8 + e] : acc[1][e]; s[e] = (kq & 2) ? hi : lo; }
  asm volatile("s_waitcnt lgkmcnt(0)\n\ts_barrier" ::: "memory");
#pragma unroll
  for (int d = 0; d < 4; ++d) if (d != kq) {
    const unsigned a = R2 + ((rt * 4 + kq) * 4 + d) * 2048 + lane * 16;
    const f32x4 u = *lp<f32x4>(a), v = *lp<f32x4>(a + 1024);
#pragma unroll
    for (int e = 0; e < 4; ++e) { s[e] += u[e]; s[4 + e] += v[e]; }
  }
  {
    unsigned u0 = __float_as_uint(s[0]), u1 = __float_as_uint(s[1]), u2 = __float_as_uint(s[2]), u3 = __float_as_uint(s[3]);
    unsigned v0 = __float_as_uint(s[4]), v1 = __float_as_uint(s[5]), v2 = __float_as_uint(s[6]), v3 = __float_as_uint(s[7]);
    auto r0 = __builtin_amdgcn_permlane32_swap(u0, v0, false, false); auto r1 = __builtin_amdgcn_permlane32_swap(u1, v1, false, false);
    auto r2 = __builtin_amdgcn_permlane32_swap(u2, v2, false, false); auto r3 = __builtin_amdgcn_permlane32_swap(u3, v3, false, false);
    const float c0 = __uint_as_float(r0[0]), c1 = __uint_as_float(r1[0]), c2 = __uint_as_float(r2[0]), c3 = __uint_as_float(r3[0]);
    const float c4 = __uint_as_float(r0[1]), c5 = __uint_as_float(r1[1]), c6 = __uint_as_float(r2[1]), c7 = __uint_as_float(r3[1]);
    u32x4 f;
    f[0] = cvtpk(c0 + bflo(xres[0]), c1 + bfhi(xres[0])); f[1] = cvtpk(c2 + bflo(xres[1]), c3 + bfhi(xres[1]));
    f[2] = cvtpk(c4 + bflo(xres[2]), c5 + bfhi(xres[2])); f[3] = cvtpk(c6 + bflo(xres[3]), c7 + bfhi(xres[3]));
    const i32x4 sO = mk_srd(out, 2048 * 1024);
    st16(f, sO, (RT * 32 + (lane & 31)) * 1024 + cg * 128 + kq * 32 + h * 16, 0);
  }
  if (b == 0 && wid == 7) {
    wait_vm<0>();
    asm volatile("" : "+v"(ef[0]), "+v"(ef[1]), "+v"(ef[2]), "+v"(ef[3]));
    bool ok = true;
#pragma unroll
    for (int k = 0; k < 4; ++k) ok = ok && ef[k][0] == (unsigned)C.tag && ef[k][1] == (unsigned)(C.tag >> 32);
    if (!__all(ok)) {
#pragma unroll 1
      for (int k = 0; k < 4; ++k) spin_flags(ws, flag_off(0, 0, 0) + (lane * 4 + k) * 128, true, C.tag);
    }
    if (lane == 0) __hip_atomic_store((u64*)(ws + WS_EPOCH), C.tag, __ATOMIC_RELAXED, __HIP_MEMORY_SCOPE_AGENT);
  }
  STAMP(60);
  if (b == 100 && wid == 0) { const u64 e = __builtin_amdgcn_s_memrealtime(); const u64 until = e + 2 * (C.prtx - C.prt0); while (__builtin_amdgcn_s_memrealtime() < until) __builtin_amdgcn_s_sleep(1); }
}

extern "C" void kernel_launch(void* const* d_in, const int* in_sizes, int n_in, void* d_out, int out_size, void* d_ws, size_t ws_size, hipStream_t stream) {
  (void)in_sizes; (void)n_in; (void)out_size; (void)ws_size;
  (void)hipFuncSetAttribute((const void*)fused_kernel, hipFuncAttributeMaxDynamicSharedMemorySize, LDS_BYTES);
  fused_kernel<<<256, 512, LDS_BYTES, stream>>>((const u16*)d_in[0], (const u16*)d_in[1], (const u16*)d_in[2], (const u16*)d_in[4], (const u16*)d_in[5], (const u16*)d_in[6],
                                                 (u16*)d_out, (char*)d_ws);
}
```

```cpp
#include <hip/hip_runtime.h>
#include <stdint.h>
#include <type_traits>

typedef unsigned short u16;
typedef unsigned long long u64;
typedef short bf16x8 __attribute__((ext_vector_type(8)));
typedef short s16x4 __attribute__((ext_vector_type(4)));
typedef float f32x2 __attribute__((ext_vector_type(2)));
typedef float f32x4 __attribute__((ext_vector_type(4)));
typedef float f32x16 __attribute__((ext_vector_type(16)));
typedef unsigned u32x2 __attribute__((ext_vector_type(2)));
typedef unsigned u32x4 __attribute__((ext_vector_type(4)));
typedef int i32x4 __attribute__((ext_vector_type(4)));
#define LDSP __attribute__((address_space(3)))
#define DI __device__ __forceinline__
template <class T> DI LDSP T* lp(unsigned a) { return (LDSP T*)(size_t)a; }
template <int I, int N, class F> DI void static_for(F&& f) { if constexpr (I < N) { f(std::integral_constant<int, I>{}); static_for<I + 1, N>(f); } }

constexpr size_t WS_EPOCH = 0;
constexpr size_t WS_ERR   = 64;
constexpr size_t WS_FLAGS = 4096;
constexpr size_t WS_GS    = 512u << 10;
constexpr size_t WS_GC    = 576u << 10;
constexpr size_t WS_QKV   = 1u << 20;
constexpr size_t WS_A2    = 8u << 20;
constexpr size_t WS_DBG   = 24u << 20;

constexpr unsigned R0 = 0, R1 = 32768, R2 = 65536, R3 = 98304;
constexpr unsigned L_K = 98304, L_V = 122880;
constexpr unsigned L_ONES = 147456;
constexpr unsigned L_CSP = 148480;
constexpr unsigned L_CS = 150528;
constexpr unsigned L_JUNK = 154624;
constexpr unsigned LDS_BYTES = 163840;
constexpr unsigned CODE_BYTES = 36864;
constexpr unsigned POISON = 0xffffffffu;
constexpr unsigned SPIN_CAP = 1u << 13;

#define NO_WARM 0
#define NO_CS 0
#define NO_DMA 0
constexpr float QSCALE = 0.25f * 1.4426950408889634f;

DI i32x4 mk_srd(const void* p, unsigned bytes) { u64 a = (u64)p; i32x4 r; r[0] = (int)(unsigned)a; r[1] = (int)((unsigned)(a >> 32) & 0xffffu); r[2] = (int)bytes; r[3] = 0x00020000; return r; }
DI void dma16(i32x4 srd, unsigned voff, unsigned soff, unsigned lds) {
  if (NO_DMA) return;
  asm volatile("s_mov_b32 m0, %3\n\ts_nop 3\n\tbuffer_load_dwordx4 %0, %1, %2 offen lds" :: "v"(voff), "s"(srd), "s"(soff), "s"(lds) : "memory");
}
DI void dma4(i32x4 srd, unsigned voff, unsigned lds) {
  asm volatile("s_mov_b32 m0, %2\n\ts_nop 3\n\tbuffer_load_dword %0, %1, 0 offen lds" :: "v"(voff), "s"(srd), "s"(lds) : "memory");
}
DI void ld16_sc1(u32x4& d, i32x4 srd, unsigned voff, unsigned soff) {
  asm volatile("s_nop 4\n\tbuffer_load_dwordx4 %0, %1, %2, %3 offen sc1" : "=v"(d) : "v"(voff), "s"(srd), "s"(soff) : "memory");
}
DI void ld16(u32x4& d, i32x4 srd, unsigned voff, unsigned soff) {
  asm volatile("s_nop 4\n\tbuffer_load_dwordx4 %0, %1, %2, %3 offen" : "=v"(d) : "v"(voff), "s"(srd), "s"(soff) : "memory");
}
DI void ld8_sc1(u32x2& d, i32x4 srd, unsigned voff) {
  asm volatile("s_nop 4\n\tbuffer_load_dwordx2 %0, %1, %2, 0 offen sc1" : "=v"(d) : "v"(voff), "s"(srd) : "memory");
}
DI void st16_sc1(u32x4 d, i32x4 srd, unsigned voff, unsigned soff) {
  asm volatile("s_nop 4\n\tbuffer_store_dwordx4 %0, %1, %2, %3 offen sc1\n\ts_nop 1" :: "v"(d), "v"(voff), "s"(srd), "s"(soff) : "memory");
}
DI void st16(u32x4 d, i32x4 srd, unsigned voff, unsigned soff) {
  asm volatile("s_nop 4\n\tbuffer_store_dwordx4 %0, %1, %2, %3 offen\n\ts_nop 1" :: "v"(d), "v"(voff), "s"(srd), "s"(soff) : "memory");
}
template <int N> DI void bar_vm() { static_assert(N >= 0 && N < 64, "vmcnt range"); asm volatile("s_waitcnt vmcnt(%0) lgkmcnt(0)\n\ts_barrier" :: "n"(N) : "memory"); }
template <int N> DI void wait_vm() { static_assert(N >= 0 && N < 64, "vmcnt range"); asm volatile("s_waitcnt vmcnt(%0)" :: "n"(N) : "memory"); }
DI s16x4 ldtr(unsigned a) { return __builtin_amdgcn_ds_read_tr16_b64_v4i16(lp<s16x4>(a)); }
DI bf16x8 ldfrag_tr(unsigned a, unsigned second) {
  s16x4 lo = ldtr(a), hi = ldtr(a + second);
  return __builtin_shufflevector(lo, hi, 0, 1, 2, 3, 4, 5, 6, 7);
}
DI bf16x8 ld128(unsigned a) { return *lp<bf16x8>(a); }
DI unsigned cvtpk(float lo, float hi) { typedef __bf16 b2 __attribute__((ext_vector_type(2))); f32x2 v = {lo, hi}; return __builtin_bit_cast(unsigned, __builtin_convertvector(v, b2)); }
DI float bflo(unsigned v) { return __uint_as_float(v << 16); }
DI float bfhi(unsigned v) { return __uint_as_float(v & 0xffff0000u); }
DI float rbf(float f) { return bflo(cvtpk(f, f) & 0xffffu); }
DI int fresh(int v) { asm volatile("" : "+v"(v)); return v; }
template <class T> DI void pin(T& v) { asm volatile("" : "+v"(v)); }
DI unsigned pinv(unsigned v) { asm volatile("" : "+v"(v)); return v; }
#define MFMA32(a, b, c) __builtin_amdgcn_mfma_f32_32x32x16_bf16((a), (b), (c), 0, 0, 0)
#define MFMA16(a, b, c) __builtin_amdgcn_mfma_f32_16x16x32_bf16((a), (b), (c), 0, 0, 0)
#define MFMA4(a, b, c) __builtin_amdgcn_mfma_f32_4x4x4bf16_1k((a), (b), (c), 0, 0, 0)
#define SB() __builtin_amdgcn_sched_barrier(0)
DI float swap_add(float v) { auto r = __builtin_amdgcn_permlane32_swap(__float_as_uint(v), __float_as_uint(v), false, false); return __uint_as_float(r[0]) + __uint_as_float(r[1]); }
template <class T> DI float ror_add(float v, T) { return v + __int_as_float(__builtin_amdgcn_update_dpp(0, __float_as_int(v), T::value, 0xf, 0xf, true)); }
DI float swap16_add(float v) { auto r = __builtin_amdgcn_permlane16_swap(__float_as_uint(v), __float_as_uint(v), false, false); return __uint_as_float(r[0]) + __uint_as_float(r[1]); }
template <int C> using IC = std::integral_constant<int, C>;
DI float wave_sum(float v) { v = ror_add(v, IC<0x121>{}); v = ror_add(v, IC<0x122>{}); v = ror_add(v, IC<0x124>{}); v = ror_add(v, IC<0x128>{}); return swap_add(swap16_add(v)); }
DI float swap_max(float v) { auto r = __builtin_amdgcn_permlane32_swap(__float_as_uint(v), __float_as_uint(v), false, false); return __builtin_fmaxf(__uint_as_float(r[0]), __uint_as_float(r[1])); }

#define STAMP(i) do { } while (0)

struct Ctx {
  char* ws; u64 tag;
  i32x4 sX, sXall, sQKVw, sG, sU, sWO, sWD, sWS;
  int lane, wid, mg, ng, rb, cs, cg, xi, kq, rb2, bid; unsigned stampreg;
};

DI unsigned flag_off(int ev, int rb, int cs) { return (unsigned)(WS_FLAGS + (((ev * 16 + rb) * 16 + cs) << 7)); }
DI void publish(const Ctx& C, int ev) { if (C.wid == 0 && C.lane == 0) __hip_atomic_store((u64*)(C.ws + flag_off(ev, C.rb, C.cs)), C.tag, __ATOMIC_RELAXED, __HIP_MEMORY_SCOPE_AGENT); }
DI void give_up(char* ws) { __hip_atomic_store((unsigned*)(ws + WS_ERR), 1u, __ATOMIC_RELAXED, __HIP_MEMORY_SCOPE_AGENT); }
DI void spin_flags(char* ws, unsigned off, bool need, u64 tag) {
#pragma nounroll
  for (unsigned it = 0; it < SPIN_CAP; ++it) {
    u64 v = __hip_atomic_load((u64*)(ws + off), __ATOMIC_RELAXED, __HIP_MEMORY_SCOPE_AGENT);
    if (__all(!need || v == tag)) return;
    __builtin_amdgcn_s_sleep(2);
  }
  give_up(ws);
}
DI void check_flags(char* ws, unsigned off, bool need, u64 tag, u32x2& got) {
  asm volatile("" : "+v"(got));
  bool ok = !need || (got[0] == (unsigned)tag && got[1] == (unsigned)(tag >> 32));
  if (!__all(ok)) spin_flags(ws, off, need, tag);
}
template <int K0, int K1> DI void check_frags(const Ctx& C, u32x4 (&a2)[36], unsigned base) {
  auto dirty = [&]() { unsigned mx = 0;
    static_for<K0, K1>([&](auto kc) { constexpr int k = decltype(kc)::value;
      mx = __builtin_elementwise_max(mx, __builtin_elementwise_max(__builtin_elementwise_max(a2[k][0], a2[k][1]), __builtin_elementwise_max(a2[k][2], a2[k][3]))); });
    return __any(mx == POISON); };
  if (dirty()) {
#pragma nounroll
    for (unsigned it = 0; it < SPIN_CAP; ++it) {
      __builtin_amdgcn_s_sleep(2);
      static_for<K0, K1>([&](auto kc) { constexpr int k = decltype(kc)::value; ld16_sc1(a2[k], C.sWS, C.lane * 16, base + k * 1024); });
      wait_vm<0>();
      static_for<K0, K1>([&](auto kc) { constexpr int k = decltype(kc)::value; pin(a2[k]); });
      if (!dirty()) return;
    }
    give_up(C.ws);
  }
}

enum { T_H0 = 0, T_S4 = 8, T_E0 = 16, T_G0 = 19, T_POISON = 22, T_POLL, T_KL, T_POLL2, T_QL, T_QP, T_EPOCH, T_CSL, T_GR, T_WARM, T_NONE };
template <int TYPE> struct G1 {
  static constexpr int kind(int p) { return p == 0 ? 0 : (p == 1 && TYPE == 1) ? 1 : 2; }
  static constexpr int nt(int p) { return p == 0 ? 1 : 2; }
  static constexpr int nst(int p) { return kind(p) == 1 ? 3 : 2; }
  static constexpr int npoison() { return 3; }
  static constexpr unsigned slot(int s) {
    return s == 4 ? R0 : s == 5 ? R1 : s == 6 ? R2 : s == 7 ? R3 : s == 8 ? R0 : s == 9 ? R1 : s == 10 ? R2 : R0; }
  static constexpr int warmat(int pos) { return (pos >= 39 && pos < 39 + 4 * 9 && ((pos - 39) & 3) == 0) ? (pos - 39) >> 2 : -1; }
  static constexpr int barpos(int bi) { return bi < 8 ? 4 * bi + 2 : 8 * (bi - 4) + 6; }
  static constexpr int barat(int pos) { return pos < 32 ? ((pos & 3) == 2 ? pos >> 2 : -1) : ((pos & 7) == 6 ? 4 + (pos >> 3) : -1); }
  static constexpr int grp(int bi) { return bi < 4 ? T_H0 + bi + 4 : bi < 11 ? T_S4 + (bi - 4) : bi == 11 ? T_NONE : bi == 12 ? T_S4 + 7 : T_G0 + (bi - 13); }
  static constexpr int gsize(int t) { return t == T_NONE ? 0 : t < T_S4 ? 3 : 4; }
  static constexpr int rel(int bi, int k) { return bi < 8 ? k : bi == 15 ? (k >> 1) : 2 * k; }
  static constexpr int after(int t, int bi) {
    int cnt = -1;
    auto see = [&](int tag, int n) { for (int i = 0; i < n; ++i) { if (tag == t) cnt = 0; else if (cnt >= 0) ++cnt; } };
    for (int h = 0; h < 2; ++h) see(T_H0 + h, gsize(T_H0 + h));
    see(T_EPOCH, 2); see(T_CSL, 4);
    if (bi == -1) return cnt;
    for (int pos = 0; pos < 96; ++pos) {
      if (pos < 2) see(T_H0 + 2 + pos, gsize(T_H0 + 2 + pos));
      const int b = barat(pos);
      if (b >= 0) {
        if (b == bi) return cnt;
        if (b == 12) see(T_KL, 6);
        if (b == 14) { see(T_POLL2, 1); see(T_QL, 1); }
      }
      for (int bb = 0; bb < 16; ++bb) for (int k = 0; k < gsize(grp(bb)); ++k) if (barpos(bb) + rel(bb, k) == pos) see(grp(bb), 1);
      if (!NO_WARM && pos == 35) see(T_WARM, 1);
      if (!NO_WARM && warmat(pos) >= 0) see(T_WARM, 1);
      if (pos == 32 && TYPE == 1) see(T_QP, 2);
      if (pos == 18) see(T_GR, 2);
      if (pos == 58) see(T_POLL, 1);
      if (pos == 50) see(T_POISON, npoison());
      if ((pos & 31) == 31) see(T_E0 + (pos >> 5), nst(pos >> 5));
    }
    return cnt;
  }
  static constexpr int mn(int a, int b) { return a < b ? a : b; }
};

template <int TYPE> DI unsigned tile_addr(const Ctx& C, int p, int t) {
  const int lane = fresh(C.lane), kind = G1<TYPE>::kind(p);
  const int g = (lane >> 4) & 1, h = lane >> 5, q = (lane & 15) >> 2, pp = lane & 3;
  const unsigned rdrow = (8 * h + q) * 128 + pp * 8;
  if (kind == 2) return g * 16384 + rdrow + (((2 * C.ng + t) ^ (2 * (q >> 1)) ^ g) << 5);
  if (kind == 0) return 16384 + rdrow + ((C.ng ^ (q >> 1)) << 6) + g * 32;
  if (t == 0) return rdrow + ((C.ng ^ (q >> 1)) << 6) + g * 32;
  return 16384 + g * 8192 + (8 * h + q) * 64 + pp * 8 + ((C.ng ^ g) << 5);
}
template <int TYPE> DI unsigned stage_voff(const Ctx& C, int p, int which) {
  const int kind = G1<TYPE>::kind(p), lane = fresh(C.lane);
  if (which == 1) return (16 * C.wid + (lane >> 2)) * 3584 + (8 + ((C.cs - 8) >> 1)) * 128 + ((C.cs - 8) & 1) * 64 + ((lane & 3) << 4);
  const unsigned row = 16 * C.wid + (lane >> 3), sw = ((lane & 7) ^ (((lane >> 4) & 1) << 2)) << 4;
  if (kind == 1) return row * 3072 + (C.cs - 8) * 128 + sw;
  return row * 3584 + (p == 1 ? C.cs : 12 + C.cs) * 128 + sw;
}
template <int TYPE> DI void issue_stage(const Ctx& C, int s, int k, unsigned v0) {
  const int kind = G1<TYPE>::kind(s >> 2);
  const unsigned slot = G1<TYPE>::slot(s), r0 = 128 * (s & 3);
  if (kind == 1) {
    if (k < 2) dma16(C.sQKVw, v0, (r0 + 8 * k) * 3072, slot + C.wid * 2048 + k * 1024);
    else dma16(k == 3 ? C.sU : C.sG, k == 3 ? v0 ^ 32 : v0, r0 * 3584, slot + 16384 + (k - 2) * 8192 + C.wid * 1024);
  } else dma16(k >> 1 ? C.sU : C.sG, k >> 1 ? v0 ^ 32 : v0, (r0 + 8 * (k & 1)) * 3584, slot + (k >> 1) * 16384 + C.wid * 2048 + (k & 1) * 1024);
}
DI unsigned hx_voff(const Ctx& C) { const int lane = fresh(C.lane); return (16 * C.wid + (lane >> 3)) * 1024 + (((lane & 7) ^ ((lane >> 4) & 3)) << 4); }
DI unsigned hw_voff(const Ctx& C) { const int lane = fresh(C.lane);
  return (8 * C.wid + (lane >> 3)) * 3072 + (8 + C.cs) * 128 + (((lane & 7) ^ (((lane >> 4) & 1) << 2)) << 4); }
DI void issue_half(const Ctx& C, int h, int k, unsigned vx, unsigned vw) {
  const unsigned slot = (h & 3) * 32768;
  if (k < 2) dma16(C.sX, k ? (vx ^ 64) + 8 * 1024 : vx, h * 128, slot + C.wid * 2048 + k * 1024);
  else dma16(C.sQKVw, vw + (k - 2) * 128, h * 64 * 3072, slot + 16384 + (k - 2) * 8192 + C.wid * 1024);
}
DI void warm(i32x4 srd, unsigned line0, unsigned lines_per_row_log2, unsigned pitch, unsigned colbyte, int lane) {
  const unsigned l = line0 + lane;
  dma4(srd, (l >> lines_per_row_log2) * pitch + colbyte + (l & ((1u << lines_per_row_log2) - 1)) * 128, L_JUNK);
}
DI unsigned g_voff(const Ctx& C) { const int lane = fresh(C.lane); return (32 * (C.wid & 1) + (lane >> 3)) * 1024 + C.cg * 128 + (((lane & 7) ^ (((lane >> 4) & 1) << 2)) << 4); }
constexpr unsigned gslot(int j) { return j == 0 ? R1 : j == 1 ? R2 : j == 2 ? R0 : j == 3 ? R3 : j == 4 ? R1 : j == 5 ? R2 : j == 6 ? R0 : j == 7 ? R3 : R1; }
DI void issue_g(const Ctx& C, int j, int i, unsigned v) {
  const int kq = C.wid >> 1;
  const int row0 = j < 3 ? (3 * kq + j) * 64 : j < 7 ? (12 + 4 * kq + (j - 3)) * 64 : 128 * kq + 64 * (j - 7);
  dma16(j < 7 ? C.sWD : C.sWO, v, (unsigned)(row0 + 8 * i) * 1024, gslot(j) + kq * 8192 + (C.wid & 1) * 4096 + i * 1024);
}

template <int WHAT> DI void epi_qkv(const Ctx& C, const f32x16& acc, const f32x4 (&csr)[4], float m, float rinv, int qu, int h4) {
  const int h = C.lane >> 5;
  const float sc = WHAT == 0 ? rinv * QSCALE : rinv;
  float y[16];
#pragma unroll
  for (int i4 = 0; i4 < 4; ++i4)
#pragma unroll
    for (int e = 0; e < 4; ++e) y[4 * i4 + e] = (acc[4 * i4 + e] - csr[i4][e]) * sc;
  const unsigned base = (unsigned)WS_QKV + (((C.rb * 24 + qu) * 4 + C.mg) * 4 + h4) * 1024;
#pragma unroll
  for (int hh = 0; hh < 2; ++hh) {
    u32x4 f; f[0] = cvtpk(y[8 * hh], y[8 * hh + 1]); f[1] = cvtpk(y[8 * hh + 2], y[8 * hh + 3]); f[2] = cvtpk(y[8 * hh + 4], y[8 * hh + 5]); f[3] = cvtpk(y[8 * hh + 6], y[8 * hh + 7]);
    unsigned voff = C.lane * 16;
    if (WHAT == 2) {
      auto a = __builtin_amdgcn_permlane32_swap(f[0], f[2], false, false); auto b = __builtin_amdgcn_permlane32_swap(f[1], f[3], false, false);
      f[0] = a[0]; f[2] = a[1]; f[1] = b[0]; f[3] = b[1];
      voff = (C.lane & 31) * 32 + h * 16;
    }
    st16_sc1(f, C.sWS, voff, base + hh * 1024);
  }
}
DI void epi_gu(const Ctx& C, const f32x16& acc, const f32x4 (&csr)[4], float m, float r2, unsigned a2off) {
  float y[8];
#pragma unroll
  for (int i4 = 0; i4 < 2; ++i4)
#pragma unroll
    for (int e = 0; e < 4; ++e) y[4 * i4 + e] = ((acc[4 * i4 + e] - csr[i4][e]) * r2) * (acc[8 + 4 * i4 + e] - csr[2 + i4][e]);
  u32x4 f; f[0] = cvtpk(y[0], y[1]); f[1] = cvtpk(y[2], y[3]); f[2] = cvtpk(y[4], y[5]); f[3] = cvtpk(y[6], y[7]);
  st16_sc1(f, C.sWS, C.lane * 16, a2off);
}
template <int TYPE> DI unsigned a2_pass(const Ctx& C, int p) {
  if (p == 2) return (unsigned)WS_A2 + (((4 * C.rb + C.mg) * 4 + (C.cs >> 2)) * 36 + 4 * (3 + (C.cs & 3)) + 2 * C.ng) * 1024;
  if (TYPE == 0) return (unsigned)WS_A2 + (((4 * C.rb + C.mg) * 4 + C.cs / 3) * 36 + 4 * (C.cs % 3) + 2 * C.ng) * 1024;
  const int u = 8 + ((C.cs - 8) >> 1), half = (C.cs - 8) & 1;
  return (unsigned)WS_A2 + (((4 * C.rb + C.mg) * 4 + u / 3) * 36 + 4 * (u % 3) + 2 * half + C.ng) * 1024;
}
DI unsigned a2_o(const Ctx& C) { return (unsigned)WS_A2 + (((4 * C.rb + C.mg) * 4 + (C.cs >> 2)) * 36 + 28 + 2 * (C.cs & 3) + C.ng) * 1024; }

template <int TYPE> DI void gemm1(Ctx& C, unsigned vx, unsigned vw, u32x4& qfrag) {
  using G = G1<TYPE>;
  const int lane = C.lane;
  char* ws = C.ws;
  bf16x8 xa[32];
  bf16x8 wf[3][2];
  f32x16 acc[2];
  unsigned ta[2], sv = 0;
  ta[0] = pinv(tile_addr<TYPE>(C, 0, 0)); ta[1] = pinv(tile_addr<TYPE>(C, 0, 1));
  unsigned th[2];
  th[0] = ta[0] + 65536; th[1] = ta[1] + 65536;
  const unsigned r31 = fresh(lane) & 31, hh = fresh(lane) >> 5;
  const unsigned xlane = (32 * C.mg + r31) * 128 + ((hh ^ ((r31 >> 1) & 7)) << 4);
  f32x4 csr[2][4];
  float m = 0.f, rinv = 0.f;
  u32x2 pollv, poll2, gs; u32x4 gk[6], gc;
  unsigned polloff = 0, poll2off = 0, gsoff = 0, gcoff = 0; bool pollneed = false;
  const unsigned qoff = (unsigned)WS_QKV + (((C.rb * 24 + (C.cs >> 1)) * 4 + C.mg) * 4 + 2 * (C.cs & 1) + C.ng) * 1024;

  u32x2 ev; ld8_sc1(ev, C.sWS, (unsigned)WS_EPOCH);
  u32x4 srow, cl[4];
  ld16(srow, C.sXall, (8 * C.bid + C.wid) * 1024 + fresh(lane) * 16, 0);
  { const int pan = C.bid >> 1, lf = fresh(lane);
    const unsigned pitch = pan < 24 ? 3072 : 3584, col = (pan < 24 ? pan : pan < 52 ? pan - 24 : pan - 52) * 128;
    i32x4 sp;
#pragma unroll
    for (int e = 0; e < 4; ++e) sp[e] = pan < 24 ? C.sQKVw[e] : pan < 52 ? C.sG[e] : C.sU[e];
    const unsigned v = (256 * (C.bid & 1) + 32 * C.wid + (lf >> 3)) * pitch + col + (lf & 7) * 16 + (pan < 80 ? 0u : 0x40000000u);
#pragma unroll
    for (int k = 0; k < 4; ++k) ld16(cl[k], sp, v, 8 * k * pitch); }
  STAMP(1);
  bar_vm<G::after(T_H0, -1)>();
  STAMP(2);
  auto read_x = [&](int h) {
    const unsigned base = (h & 3) * 32768 + xlane;
#pragma unroll
    for (int i = 0; i < 4; ++i) xa[4 * h + i] = ld128(base ^ (i << 5));
  };
  auto read_w = [&](int pos) {
    const int p = pos >> 5, s = pos >> 3, i = pos & 7;
    if ((pos & 31) == 0 && pos > 0) { ta[0] = pinv(tile_addr<TYPE>(C, p, 0)); ta[1] = pinv(tile_addr<TYPE>(C, p, 1)); th[0] = pinv(ta[0] + 65536); th[1] = pinv(ta[1] + 65536); }
    const unsigned sb = p == 0 ? ((pos >> 2) & 3) * 32768 : G::slot(s), ks = p == 0 ? (i & 3) : i;
    const unsigned b0 = (sb & 65536) ? th[0] : ta[0], b1 = (sb & 65536) ? th[1] : ta[1], so = sb & 65535;
    wf[pos % 3][0] = ldfrag_tr(b0 + so + ks * 2048, 512);
    if (G::nt(p) == 2) { if (G::kind(p) == 1) wf[pos % 3][1] = ldfrag_tr(b1 + so + ks * 1024, 256); else wf[pos % 3][1] = ldfrag_tr(b1 + so + ks * 2048, 512); }
  };
  read_x(0); read_w(0); read_w(1);
  static_for<0, 96>([&](auto ic) {
    constexpr int pos = decltype(ic)::value;
    constexpr int p = pos >> 5, s = pos >> 3, st = s & 3, i = pos & 7, NT = G::nt(p), bi = G::barat(pos);
    if constexpr (pos < 2) static_for<0, G::gsize(T_H0)>([&](auto kc) { issue_half(C, 2 + pos, decltype(kc)::value, vx, vw); });
    if constexpr (bi >= 0) {
      STAMP(10 + 2 * bi);
      if constexpr (bi == 6) bar_vm<G::mn(G::after(T_H0 + 7, 6), G::after(T_GR, 6))>();
      else if constexpr (bi < 7) bar_vm<G::after(T_H0 + bi + 1, bi)>();
      else if constexpr (bi == 9) { bar_vm<G::mn(G::mn(G::after(T_S4 + 2, 9), G::after(T_E0, 9)), TYPE == 1 ? G::after(T_QP, 9) : 63)>(); publish(C, 0); }
      else if constexpr (bi == 12) bar_vm<G::mn(G::after(T_S4 + 5, 12), G::after(T_POLL, 12))>();
      else if constexpr (bi == 13) { static_assert(G::after(T_E0 + 1, 13) <= G::after(T_POISON, 13), ""); bar_vm<G::mn(G::after(T_S4 + 6, 13), G::after(T_E0 + 1, 13))>(); }
      else if constexpr (bi == 14) bar_vm<G::mn(G::after(T_S4 + 7, 14), G::after(T_KL, 14))>();
      else if constexpr (bi == 15) bar_vm<63>();
      else bar_vm<G::after(T_S4 + bi - 7, bi)>();
      if constexpr (bi == 1) {
        static_assert(G::after(T_H0 + 2, 1) <= G::after(T_CSL, 1), "");
        pin(ev); pin(srow); pin(cl[0]); pin(cl[1]); pin(cl[2]); pin(cl[3]);
        { const u64 t = (((u64)ev[1] << 32) | ev[0]) * 6364136223846793005ull + 1442695040888963407ull;
          C.tag = ((u64)__builtin_amdgcn_readfirstlane((unsigned)(t >> 32)) << 32) | __builtin_amdgcn_readfirstlane((unsigned)t); }
        float xv[8];
#pragma unroll
        for (int e = 0; e < 4; ++e) { xv[2 * e] = bflo(srow[e]); xv[2 * e + 1] = bfhi(srow[e]); }
        const float mean = wave_sum(((xv[0] + xv[1]) + (xv[2] + xv[3])) + ((xv[4] + xv[5]) + (xv[6] + xv[7]))) * (1.f / 512.f);
        float q2 = 0.f;
#pragma unroll
        for (int e = 0; e < 8; ++e) { const float d = xv[e] - mean; q2 += d * d; }
        const float var = wave_sum(q2) * (1.f / 512.f);
        const float sd = __builtin_amdgcn_sqrtf(rbf(rbf(var) + 1e-5f));
        if (lane == 0) __hip_atomic_store((u64*)(ws + WS_GS + (8 * C.bid + C.wid) * 8), ((u64)(unsigned)C.tag << 32) | cvtpk(mean, sd), __ATOMIC_RELAXED, __HIP_MEMORY_SCOPE_AGENT);
        float cz[8];
#pragma unroll
        for (int e = 0; e < 4; ++e) { cz[2 * e] = (bflo(cl[0][e]) + bflo(cl[1][e])) + (bflo(cl[2][e]) + bflo(cl[3][e])); cz[2 * e + 1] = (bfhi(cl[0][e]) + bfhi(cl[1][e])) + (bfhi(cl[2][e]) + bfhi(cl[3][e])); }
#pragma unroll
        for (int e = 0; e < 8; ++e) cz[e] = swap_add(swap16_add(ror_add(cz[e], IC<0x128>{})));
        if (lane < 8) { *lp<f32x4>(L_CSP + C.wid * 256 + lane * 32) = f32x4{cz[0], cz[1], cz[2], cz[3]}; *lp<f32x4>(L_CSP + C.wid * 256 + lane * 32 + 16) = f32x4{cz[4], cz[5], cz[6], cz[7]}; }
      }
      if constexpr (bi == 2) {
        if (C.wid == 0 && C.bid < 160) {
          float t = 0.f;
#pragma unroll
          for (int w = 0; w < 8; ++w) t += *lp<float>(L_CSP + w * 256 + lane * 4);
          __hip_atomic_store((u64*)(ws + WS_GC + ((C.bid >> 1) * 64 + lane) * 16 + (C.bid & 1) * 8), ((u64)(unsigned)C.tag << 32) | __float_as_uint(t), __ATOMIC_RELAXED, __HIP_MEMORY_SCOPE_AGENT);
        }
      }
      if constexpr (bi == 6) {
        pin(gs); pin(gc);
        if (!__all(gs[1] == (unsigned)C.tag && gc[1] == (unsigned)C.tag && gc[3] == (unsigned)C.tag)) {
#pragma nounroll
          for (unsigned it = 0; it < SPIN_CAP; ++it) {
            __builtin_amdgcn_s_sleep(2);
            ld8_sc1(gs, C.sWS, gsoff); ld16_sc1(gc, C.sWS, gcoff, 0); wait_vm<0>(); pin(gs); pin(gc);
            if (__all(gs[1] == (unsigned)C.tag && gc[1] == (unsigned)C.tag && gc[3] == (unsigned)C.tag)) break;
            if (it == SPIN_CAP - 1) give_up(ws);
          }
        }
        m = bflo(gs[0]); rinv = __builtin_amdgcn_rcpf(bfhi(gs[0]));
        *lp<float>(L_CS + (C.wid < 6 ? C.wid : 5) * 256 + lane * 4) = __uint_as_float(gc[0]) + __uint_as_float(gc[2]);
      }
      if constexpr (bi == 12) {
        check_flags(ws, polloff, pollneed, C.tag, pollv);
        const int u = C.cs >> 1, h4 = 2 * (C.cs & 1) + C.ng;
#pragma unroll
        for (int b = 0; b < 6; ++b) { const int rbp = C.rb - 2 + b % 3 < 0 ? 0 : C.rb - 2 + b % 3;
          ld16_sc1(gk[b], C.sWS, lane * 16, (unsigned)WS_QKV + (((rbp * 24 + (b < 3 ? 8 : 16) + u) * 4 + C.mg) * 4 + h4) * 1024); }
      }
      if constexpr (bi == 13) publish(C, 1);
      if constexpr (bi == 14) {
#pragma unroll
        for (int b = 0; b < 6; ++b) pin(gk[b]);
#pragma unroll
        for (int b = 0; b < 3; ++b) { *lp<u32x4>(L_K + ((4 * b + C.mg) * 2 + C.ng) * 1024 + lane * 16) = gk[b];
                                      *lp<u32x4>(L_V + C.ng * 12288 + (4 * b + C.mg) * 1024 + lane * 16) = gk[3 + b]; }
        { const int ln = fresh(lane), l = ln < 10 ? ln : 0, uu = 3 * C.kq + (l >> 1);
          poll2off = flag_off(1, C.rb2, l < 6 ? (uu < 8 ? uu : 8 + 2 * (uu - 8) + (l & 1)) : 4 * C.kq + (l - 6)); }
        ld8_sc1(poll2, C.sWS, poll2off);
        ld16_sc1(qfrag, C.sWS, lane * 16, qoff);
      }
      if constexpr (bi < 7) read_x(bi + 1);
      STAMP(11 + 2 * bi);
    }
    static_for<0, 16>([&](auto bc) {
      constexpr int bb = decltype(bc)::value, tg = G::grp(bb), k0 = pos - G::barpos(bb);
      static_for<0, 4>([&](auto kc) {
        constexpr int k = decltype(kc)::value;
        if constexpr (k < G::gsize(tg) && G::rel(bb, k) == k0) {
          if constexpr (tg < T_S4) issue_half(C, tg, k, vx, vw);
          else if constexpr (tg >= T_G0) { if constexpr (k == 0) sv = g_voff(C); issue_g(C, tg - T_G0, k, sv); }
          else { constexpr int pp_ = (tg - T_S4 + 4) >> 2;
            if constexpr (k == 0) sv = stage_voff<TYPE>(C, pp_, 0);
            if constexpr (k == 2 && G::kind(pp_) == 1) sv = stage_voff<TYPE>(C, pp_, 1);
            issue_stage<TYPE>(C, tg - T_S4 + 4, k, sv); }
        }
      });
    });
    if constexpr (pos == 35 && !NO_WARM) {
      const int lf = fresh(lane);
      dma4(C.wid < 4 ? C.sG : C.sU, ((C.rb & 7) * 64 + (C.wid & 3) * 16 + (lf & 15)) * 3584 + (12 + C.cs) * 128, L_JUNK);
    }
    if constexpr (G::warmat(pos) >= 0 && !NO_WARM) {
      const unsigned l = (C.xi * 9 + G::warmat(pos)) * 64 + C.wid * 8 + (fresh(lane) & 7);
      dma4(l < 4096 ? C.sWO : C.sWD, (l < 4096 ? l : l - 4096) * 128, L_JUNK);
    }
    if constexpr (pos == 32 && TYPE == 1) {
      const u32x4 pz = {POISON, POISON, POISON, POISON};
      const unsigned a = (unsigned)WS_QKV + (((C.rb * 24 + (C.cs - 8)) * 4 + C.mg) * 4 + 2 * C.ng) * 1024;
      st16_sc1(pz, C.sWS, lane * 16, a); st16_sc1(pz, C.sWS, lane * 16, a + 1024);
    }
    if constexpr (pos == 18) {
      const int lf = fresh(lane), w = C.wid < 6 ? C.wid : 5;
      const int guh = 8 + ((C.cs - 8) >> 1);
      const int pan = w == 0 ? 8 + C.cs : w >= 4 ? (w == 4 ? 24 : 52) + 12 + C.cs
                    : TYPE == 0 ? (w == 1 ? 24 + C.cs : 52 + C.cs) : (w == 1 ? C.cs - 8 : w == 2 ? 24 + guh : 52 + guh);
      gsoff = (unsigned)WS_GS + (C.rb * 128 + 32 * C.mg + (lf & 31)) * 8; gcoff = (unsigned)WS_GC + (pan * 64 + lf) * 16;
      ld8_sc1(gs, C.sWS, gsoff); ld16_sc1(gc, C.sWS, gcoff, 0);
    }
    if constexpr (pos == 58) {
      const int u = C.cs >> 1, l = fresh(lane) & 7;
      const int rbp = C.rb - 2 + l % 3;
      pollneed = l < 6 && rbp >= 0;
      polloff = flag_off(0, pollneed ? rbp : C.rb, l < 3 ? u : 8 + u);
      ld8_sc1(pollv, C.sWS, polloff);
    }
    if constexpr (pos == 50) {
      const u32x4 pz = {POISON, POISON, POISON, POISON};
      const unsigned a = a2_pass<TYPE>(C, 2);
      st16_sc1(pz, C.sWS, lane * 16, a);
      st16_sc1(pz, C.sWS, lane * 16, a + 1024);
      st16_sc1(pz, C.sWS, lane * 16, a2_o(C));
    }
    if constexpr (pos + 2 < 96) read_w(pos + 2);
    if constexpr ((pos & 31) == 0) { acc[0] = MFMA32(wf[pos % 3][0], xa[pos & 31], f32x16{}); if (NT == 2) acc[1] = MFMA32(wf[pos % 3][1], xa[pos & 31], f32x16{}); }
    else { acc[0] = MFMA32(wf[pos % 3][0], xa[pos & 31], acc[0]); if (NT == 2) acc[1] = MFMA32(wf[pos % 3][1], xa[pos & 31], acc[1]); }
    if constexpr (st == 3 && i == 7) {
      static_for<0, NT>([&](auto tc) { constexpr int t = decltype(tc)::value;
        const bool gu = G::kind(p) == 2 || (G::kind(p) == 1 && t == 1);
        const int tl = G::kind(p) == 2 ? 2 * C.ng + t : 2 * ((C.cs - 8) & 1) + C.ng;
        const unsigned s0 = L_CS + (p == 0 ? 0 : p == 2 ? 4 : (G::kind(p) == 1 ? 1 + t : 1)) * 256;
#pragma unroll
        for (int i4 = 0; i4 < 4; ++i4) {
          const unsigned a = gu ? s0 + (i4 >> 1) * 256 + (16 * tl + 8 * (i4 & 1) + 4 * hh) * 4 : s0 + (32 * C.ng + 8 * i4 + 4 * hh) * 4;
          csr[t][i4] = *lp<f32x4>(a) * m; pin(csr[t][i4]); } });
    }
    SB();
    if constexpr ((pos & 31) == 31) {
      if constexpr (G::kind(p) == 2) {
        const unsigned a2 = a2_pass<TYPE>(C, p);
        epi_gu(C, acc[0], csr[0], m, rinv * rinv, a2); epi_gu(C, acc[1], csr[1], m, rinv * rinv, a2 + 1024);
      } else if constexpr (G::kind(p) == 1) {
        epi_qkv<0>(C, acc[0], csr[0], m, rinv, C.cs - 8, 2 * C.ng);
        epi_gu(C, acc[1], csr[1], m, rinv * rinv, a2_pass<TYPE>(C, p));
      } else {
        epi_qkv<1 + TYPE>(C, acc[0], csr[0], m, rinv, 8 + C.cs, 2 * C.ng);
      }
      SB();
    }
  });
  wait_vm<G::after(T_QL, 16)>();
  pin(qfrag);
  check_flags(ws, poll2off, lane < 10, C.tag, poll2);
  { auto dirty = [&]() { return __any(__builtin_elementwise_max(__builtin_elementwise_max(qfrag[0], qfrag[1]), __builtin_elementwise_max(qfrag[2], qfrag[3])) == POISON); };
    if (dirty()) {
#pragma nounroll
      for (unsigned it = 0; it < SPIN_CAP; ++it) {
        __builtin_amdgcn_s_sleep(2);
        ld16_sc1(qfrag, C.sWS, lane * 16, qoff); wait_vm<0>(); pin(qfrag);
        if (!dirty()) break;
        if (it == SPIN_CAP - 1) give_up(ws);
      } } }
}

template <bool EDGE, class SIDE> DI u32x4 attention(Ctx& C, const u32x4& qraw, SIDE&& side) {
  const int lane = C.lane, h = lane >> 5, g = (lane >> 4) & 1, q = (lane & 15) >> 2, pp = lane & 3;
  const bf16x8 qf = __builtin_bit_cast(bf16x8, qraw);
  const unsigned kaddr = pinv(L_K + (C.mg * 2 + C.ng) * 1024 + lane * 16);
  const int t0 = EDGE ? (C.rb == 0 ? 8 : 4) - C.mg : 1;
  const int dq = (lane & 31) - 4 * h;
  const float NEG = -1e30f;
  auto older = [&](int r) { return ((r & 3) + 8 * (r >> 2)) > dq; };
  auto max16 = [&](float& a, float& b, const f32x16& s) {
#pragma unroll
    for (int r = 0; r < 16; r += 4) { a = __builtin_fmaxf(__builtin_fmaxf(a, s[r]), s[r + 1]); b = __builtin_fmaxf(__builtin_fmaxf(b, s[r + 2]), s[r + 3]); } };
  bf16x8 kf[9];
#pragma unroll
  for (int t = 0; t < 9; ++t) if (!EDGE || t > 0) kf[t] = ld128(kaddr + t * 2048);
  float mx;
  {
    f32x16 S[3];
    f32x16 sl = MFMA32(kf[8], qf, f32x16{});
    f32x16 sf;
    if (!EDGE) sf = MFMA32(kf[0], qf, f32x16{});
    S[1] = MFMA32(kf[1], qf, f32x16{});
    S[2] = MFMA32(kf[2], qf, f32x16{});
    float a = NEG, b = NEG;
    side(std::integral_constant<int, 0>{});
    SB();
#pragma unroll
    for (int r = 0; r < 16; ++r) sl[r] = older(r) ? (EDGE ? NEG : sf[r]) : sl[r];
    max16(a, b, sl);
    static_for<1, 8>([&](auto tc) { constexpr int t = decltype(tc)::value;
      if constexpr (t + 2 < 8) S[(t + 2) % 3] = MFMA32(kf[t + 2], qf, f32x16{});
      side(std::integral_constant<int, t>{});
      SB();
      if (!EDGE || t >= t0) max16(a, b, S[t % 3]);
      SB(); });
    mx = swap_max(__builtin_fmaxf(a, b));
  }
  f32x16 negm;
#pragma unroll
  for (int r = 0; r < 16; ++r) negm[r] = -mx;
  const unsigned vbase = pinv(g == 0 ? L_V + C.ng * 12288 + C.mg * 1024 + (4 * h + q) * 32 + pp * 8 : L_ONES + 128 * (1 - h) + q * 32 + pp * 8);
  const unsigned vstr = g == 0 ? 1024u : 0u;
  f32x16 o;
  auto expack = [&](const f32x16& s, u32x4& pa, u32x4& pb) {
#pragma unroll
    for (int i = 0; i < 4; ++i) pa[i] = cvtpk(__builtin_amdgcn_exp2f(s[2 * i]), __builtin_amdgcn_exp2f(s[2 * i + 1]));
#pragma unroll
    for (int i = 0; i < 4; ++i) pb[i] = cvtpk(__builtin_amdgcn_exp2f(s[8 + 2 * i]), __builtin_amdgcn_exp2f(s[9 + 2 * i])); };
  f32x16 S[2];
  bf16x8 vf[2][2];
  {
    f32x16 sl = MFMA32(kf[8], qf, negm);
    f32x16 sf;
    if (!EDGE) sf = MFMA32(kf[0], qf, negm);
    S[1] = MFMA32(kf[1], qf, negm);
    const unsigned v8 = vbase + 8 * vstr;
    const bf16x8 va = ldfrag_tr(v8, 256), vb = ldfrag_tr(v8 + 512, 256);
    bf16x8 vc, vd;
    if (!EDGE) { vc = ldfrag_tr(vbase, 256); vd = ldfrag_tr(vbase + 512, 256); }
    vf[1][0] = ldfrag_tr(vbase + vstr, 256); vf[1][1] = ldfrag_tr(vbase + vstr + 512, 256);
    side(std::integral_constant<int, 8>{});
    SB();
#pragma unroll
    for (int r = 0; r < 16; ++r) sl[r] = older(r) ? (EDGE ? NEG : sf[r]) : sl[r];
    u32x4 pa, pb, mk0, mk1;
    expack(sl, pa, pb);
#pragma unroll
    for (int i = 0; i < 4; ++i) {
      mk0[i] = (older(2 * i) ? 0xffffu : 0u) | (older(2 * i + 1) ? 0xffff0000u : 0u);
      mk1[i] = (older(8 + 2 * i) ? 0xffffu : 0u) | (older(9 + 2 * i) ? 0xffff0000u : 0u); }
    o = MFMA32(va, __builtin_bit_cast(bf16x8, pa & ~mk0), f32x16{});
    o = MFMA32(vb, __builtin_bit_cast(bf16x8, pb & ~mk1), o);
    if (!EDGE) {
      o = MFMA32(vc, __builtin_bit_cast(bf16x8, pa & mk0), o);
      o = MFMA32(vd, __builtin_bit_cast(bf16x8, pb & mk1), o);
    }
    SB();
  }
  static_for<1, 8>([&](auto tc) { constexpr int t = decltype(tc)::value;
    if constexpr (t + 1 < 8) {
      const unsigned vt = vbase + (t + 1) * vstr;
      vf[(t + 1) & 1][0] = ldfrag_tr(vt, 256); vf[(t + 1) & 1][1] = ldfrag_tr(vt + 512, 256);
      S[(t + 1) & 1] = MFMA32(kf[t + 1], qf, negm); }
    side(std::integral_constant<int, 8 + t>{});
    SB();
    if (!EDGE || t >= t0) {
      u32x4 pa, pb;
      expack(S[t & 1], pa, pb);
      o = MFMA32(vf[t & 1][0], __builtin_bit_cast(bf16x8, pa), o);
      o = MFMA32(vf[t & 1][1], __builtin_bit_cast(bf16x8, pb), o);
    }
    SB(); });
  const float li = __builtin_amdgcn_rcpf(o[8]);
  u32x4 f; f[0] = cvtpk(o[0] * li, o[1] * li); f[1] = cvtpk(o[2] * li, o[3] * li); f[2] = cvtpk(o[4] * li, o[5] * li); f[3] = cvtpk(o[6] * li, o[7] * li);
  return f;
}

__global__ void __launch_bounds__(512, 2)
fused_kernel(const u16* __restrict__ x, const u16* __restrict__ wqkv, const u16* __restrict__ wo, const u16* __restrict__ wg, const u16* __restrict__ wu,
             const u16* __restrict__ wd, u16* __restrict__ out, char* __restrict__ ws) {
  Ctx C;
  const int tid = threadIdx.x, lane = tid & 63, wid = __builtin_amdgcn_readfirstlane(tid >> 6);
  const int b = blockIdx.x, xj = b & 7, xi = b >> 3;
  C.stampreg = 0;
  STAMP(0);
  asm volatile("" :: "s"(x), "s"(wqkv), "s"(wo), "s"(wg), "s"(wu), "s"(wd), "s"(out), "s"(ws));
  C.ws = ws; C.lane = lane; C.wid = wid; C.mg = wid & 3; C.ng = wid >> 2;
  C.rb = 8 * (xj & 1) + (xi & 7); C.cs = 4 * (xj >> 1) + (xi >> 3); C.bid = b;
  C.sX = mk_srd(x + (size_t)C.rb * 128 * 512, 128 * 1024);
  C.sQKVw = mk_srd(wqkv, 512 * 3072);
  C.xi = xi;
  const unsigned vx = hx_voff(C), vw = hw_voff(C);
  STAMP(3);
  issue_half(C, 0, 0, vx, vw); issue_half(C, 0, 1, vx, vw); issue_half(C, 0, 2, vx, vw);
  STAMP(4);
  issue_half(C, 1, 0, vx, vw); issue_half(C, 1, 1, vx, vw); issue_half(C, 1, 2, vx, vw);
  STAMP(5);
  C.sWS = mk_srd(ws, 32u << 20); C.sXall = mk_srd(x, 2048 * 1024);
  C.sG = mk_srd(wg, 512 * 3584); C.sU = mk_srd(wu, 512 * 3584); C.sWO = mk_srd(wo, 512 * 1024); C.sWD = mk_srd(wd, 1792 * 1024);
  if (wid == 7) { u64 pc; asm volatile("s_getpc_b64 %0" : "=s"(pc));
    dma4(mk_srd((const void*)(pc & ~255ull), CODE_BYTES), (xi * 9 + (lane < 9 ? lane : 0)) * 128, L_JUNK); }
  if (tid < 256) lp<unsigned>(L_ONES)[tid] = 0x3f803f80u;
  const int rt64 = 4 * xj + (xi & 3), cg = xi >> 2;
  C.cg = cg;
  const int rt = wid & 1, kq = wid >> 1, RT = 2 * rt64 + rt, rb2 = RT >> 2;
  C.kq = kq; C.rb2 = rb2;
  u32x4 qfrag;
  if (C.cs < 8) gemm1<0>(C, vx, vw, qfrag); else gemm1<1>(C, vx, vw, qfrag);
  STAMP(6);

  const unsigned a2base = (unsigned)WS_A2 + (RT * 4 + kq) * 36 * 1024;
  u32x4 a2[36];
  auto side = [&](auto kc) { constexpr int k = decltype(kc)::value;
    if constexpr (k < 6) { ld16_sc1(a2[2 * k], C.sWS, lane * 16, a2base + 2 * k * 1024); ld16_sc1(a2[2 * k + 1], C.sWS, lane * 16, a2base + (2 * k + 1) * 1024); }
    if constexpr (k >= 12) static_for<0, 4>([&](auto jc) { constexpr int q_ = 12 + 4 * (k - 12) + decltype(jc)::value; ld16_sc1(a2[q_], C.sWS, lane * 16, a2base + q_ * 1024); }); };
  u32x4 of;
  if (C.rb >= 2) of = attention<false>(C, qfrag, side); else of = attention<true>(C, qfrag, side);
  st16_sc1(of, C.sWS, lane * 16, a2_o(C));
  STAMP(7);

  const int g = (lane >> 4) & 1, h = lane >> 5, q = (lane & 15) >> 2, pp = lane & 3;
  const unsigned w2l0 = kq * 8192 + (4 * h + q) * 128 + ((0 ^ (q >> 1)) << 6) + g * 32 + pp * 8;
  const unsigned w2l1 = kq * 8192 + (4 * h + q) * 128 + ((1 ^ (q >> 1)) << 6) + g * 32 + pp * 8;
  bf16x8 wf[3][2];
  f32x16 acc[2];
  u32x4 xres;
  unsigned wb[2], gv;
  auto read_w2 = [&](int pos) {
    if ((pos & 3) == 0) { wb[0] = pinv(w2l0 + gslot(pos >> 2)); wb[1] = pinv(w2l1 + gslot(pos >> 2)); }
    wf[pos % 3][0] = ldfrag_tr(wb[0] + (pos & 3) * 2048, 1024); wf[pos % 3][1] = ldfrag_tr(wb[1] + (pos & 3) * 2048, 1024); };
  bar_vm<17>();
  static_for<0, 12>([&](auto kc) { constexpr int k = decltype(kc)::value; pin(a2[k]); });
  read_w2(0); read_w2(1);
  gv = g_voff(C);
  STAMP(8);
  u32x4 ef[4];
  static_for<0, 36>([&](auto ic) {
    constexpr int pos = decltype(ic)::value;
    constexpr int j = pos >> 2, i = pos & 3;
    if constexpr (i == 2) {
      STAMP(44 + 2 * j);
      if constexpr (j <= 1) bar_vm<63>();
      if constexpr (j == 2) bar_vm<6>();
      if constexpr (j == 3) bar_vm<6>();
      if constexpr (j == 4) bar_vm<7>();
      if constexpr (j == 5) bar_vm<15>();
      if constexpr (j == 6) bar_vm<4>();
      if constexpr (j == 7) bar_vm<0>();
      STAMP(45 + 2 * j);
    }
    if constexpr (pos == 10) { static_for<12, 28>([&](auto kc) { pin(a2[decltype(kc)::value]); }); check_frags<12, 28>(C, a2, a2base); }
    if constexpr (pos == 26) { static_for<28, 36>([&](auto kc) { pin(a2[decltype(kc)::value]); }); pin(xres); check_frags<28, 36>(C, a2, a2base); }
    if constexpr (pos == 16) { ld16(xres, C.sXall, (RT * 32 + (lane & 31)) * 1024 + cg * 128 + kq * 32 + h * 16, 0); }
    if constexpr (pos == 18 || pos == 19) static_for<0, 4>([&](auto kc) { constexpr int k = 28 + 4 * (pos - 18) + decltype(kc)::value; ld16_sc1(a2[k], C.sWS, lane * 16, a2base + k * 1024); });
    if constexpr (pos < 24) issue_g(C, 3 + (pos >> 2), pos & 3, gv);
    if constexpr (pos == 30) { if (b == 0 && wid == 7) {
#pragma unroll
        for (int k = 0; k < 4; ++k) { u32x2 t; ld8_sc1(t, C.sWS, flag_off(0, 0, 0) + (lane * 4 + k) * 128); ef[k][0] = t[0]; ef[k][1] = t[1]; } } }
    if constexpr (pos + 2 < 36) read_w2(pos + 2);
    const bf16x8 af = __builtin_bit_cast(bf16x8, a2[pos]);
    if constexpr (pos == 0) { acc[0] = MFMA32(wf[0][0], af, f32x16{}); acc[1] = MFMA32(wf[0][1], af, f32x16{}); }
    else { acc[0] = MFMA32(wf[pos % 3][0], af, acc[0]); acc[1] = MFMA32(wf[pos % 3][1], af, acc[1]); }
    SB();
  });
  STAMP(9);
#pragma unroll
  for (int d = 0; d < 4; ++d) if (d != kq) {
    const int ct = d >> 1, r0 = (d & 1) * 8;
    const unsigned a = R2 + ((rt * 4 + d) * 4 + kq) * 2048 + lane * 16;
    *lp<f32x4>(a) = f32x4{acc[ct][r0], acc[ct][r0 + 1], acc[ct][r0 + 2], acc[ct][r0 + 3]};
    *lp<f32x4>(a + 1024) = f32x4{acc[ct][r0 + 4], acc[ct][r0 + 5], acc[ct][r0 + 6], acc[ct][r0 + 7]};
  }
  float s[8];
#pragma unroll
  for (int e = 0; e < 8; ++e) { const float lo = (kq & 1) ? acc[0][8 + e] : acc[0][e], hi = (kq & 1) ? acc[1][8 + e] : acc[1][e]; s[e] = (kq & 2) ? hi : lo; }
  asm volatile("s_waitcnt lgkmcnt(0)\n\ts_barrier" ::: "memory");
#pragma unroll
  for (int d = 0; d < 4; ++d) if (d != kq) {
    const unsigned a = R2 + ((rt * 4 + kq) * 4 + d) * 2048 + lane * 16;
    const f32x4 u = *lp<f32x4>(a), v = *lp<f32x4>(a + 1024);
#pragma unroll
    for (int e = 0; e < 4; ++e) { s[e] += u[e]; s[4 + e] += v[e]; }
  }
  {
    unsigned u0 = __float_as_uint(s[0]), u1 = __float_as_uint(s[1]), u2 = __float_as_uint(s[2]), u3 = __float_as_uint(s[3]);
    unsigned v0 = __float_as_uint(s[4]), v1 = __float_as_uint(s[5]), v2 = __float_as_uint(s[6]), v3 = __float_as_uint(s[7]);
    auto r0 = __builtin_amdgcn_permlane32_swap(u0, v0, false, false); auto r1 = __builtin_amdgcn_permlane32_swap(u1, v1, false, false);
    auto r2 = __builtin_amdgcn_permlane32_swap(u2, v2, false, false); auto r3 = __builtin_amdgcn_permlane32_swap(u3, v3, false, false);
    const float c0 = __uint_as_float(r0[0]), c1 = __uint_as_float(r1[0]), c2 = __uint_as_float(r2[0]), c3 = __uint_as_float(r3[0]);
    const float c4 = __uint_as_float(r0[1]), c5 = __uint_as_float(r1[1]), c6 = __uint_as_float(r2[1]), c7 = __uint_as_float(r3[1]);
    u32x4 f;
    f[0] = cvtpk(c0 + bflo(xres[0]), c1 + bfhi(xres[0])); f[1] = cvtpk(c2 + bflo(xres[1]), c3 + bfhi(xres[1]));
    f[2] = cvtpk(c4 + bflo(xres[2]), c5 + bfhi(xres[2])); f[3] = cvtpk(c6 + bflo(xres[3]), c7 + bfhi(xres[3]));
    const i32x4 sO = mk_srd(out, 2048 * 1024);
    st16(f, sO, (RT * 32 + (lane & 31)) * 1024 + cg * 128 + kq * 32 + h * 16, 0);
  }
  if (b == 0 && wid == 7) {
    wait_vm<0>();
    asm volatile("" : "+v"(ef[0]), "+v"(ef[1]), "+v"(ef[2]), "+v"(ef[3]));
    bool ok = true;
#pragma unroll
    for (int k = 0; k < 4; ++k) ok = ok && ef[k][0] == (unsigned)C.tag && ef[k][1] == (unsigned)(C.tag >> 32);
    if (!__all(ok)) {
#pragma unroll 1
      for (int k = 0; k < 4; ++k) spin_flags(ws, flag_off(0, 0, 0) + (lane * 4 + k) * 128, true, C.tag);
    }
    if (lane == 0) __hip_atomic_store((u64*)(ws + WS_EPOCH), C.tag, __ATOMIC_RELAXED, __HIP_MEMORY_SCOPE_AGENT);
  }
  STAMP(60);
}

extern "C" void kernel_launch(void* const* d_in, const int* in_sizes, int n_in, void* d_out, int out_size, void* d_ws, size_t ws_size, hipStream_t stream) {
  (void)in_sizes; (void)n_in; (void)out_size; (void)ws_size;
  (void)hipFuncSetAttribute((const void*)fused_kernel, hipFuncAttributeMaxDynamicSharedMemorySize, LDS_BYTES);
  fused_kernel<<<256, 512, LDS_BYTES, stream>>>((const u16*)d_in[0], (const u16*)d_in[1], (const u16*)d_in[2], (const u16*)d_in[4], (const u16*)d_in[5], (const u16*)d_in[6],
                                                 (u16*)d_out, (char*)d_ws);
}
```

```cpp
#include <hip/hip_runtime.h>
#include <stdint.h>
#include <type_traits>

typedef unsigned short u16;
typedef unsigned long long u64;
typedef short bf16x8 __attribute__((ext_vector_type(8)));
typedef short s16x4 __attribute__((ext_vector_type(4)));
typedef float f32x2 __attribute__((ext_vector_type(2)));
typedef float f32x4 __attribute__((ext_vector_type(4)));
typedef float f32x16 __attribute__((ext_vector_type(16)));
typedef unsigned u32x2 __attribute__((ext_vector_type(2)));
typedef unsigned u32x4 __attribute__((ext_vector_type(4)));
typedef int i32x4 __attribute__((ext_vector_type(4)));
#define LDSP __attribute__((address_space(3)))
#define DI __device__ __forceinline__
template <class T> DI LDSP T* lp(unsigned a) { return (LDSP T*)(size_t)a; }
template <int I, int N, class F> DI void static_for(F&& f) { if constexpr (I < N) { f(std::integral_constant<int, I>{}); static_for<I + 1, N>(f); } }

constexpr size_t WS_EPOCH = 0;
constexpr size_t WS_ERR   = 64;
constexpr size_t WS_FLAGS = 4096;
constexpr size_t WS_GS    = 512u << 10;
constexpr size_t WS_GC    = 576u << 10;
constexpr size_t WS_QKV   = 1u << 20;
constexpr size_t WS_A2    = 8u << 20;
constexpr size_t WS_DBG   = 24u << 20;

constexpr unsigned R0 = 0, R1 = 32768, R2 = 65536, R3 = 98304;
constexpr unsigned L_K = 98304, L_V = 122880;
constexpr unsigned L_ONES = 147456;
constexpr unsigned L_CSP = 148480;
constexpr unsigned L_CS = 150528;
constexpr unsigned L_JUNK = 154624;
constexpr unsigned LDS_BYTES = 163840;
constexpr unsigned CODE_BYTES = 36864;
constexpr unsigned POISON = 0xffffffffu;
constexpr unsigned SPIN_CAP = 1u << 13;

#define NO_WARM 0
#define NO_CS 0
#define NO_DMA 0
constexpr float QSCALE = 0.25f * 1.4426950408889634f;

DI i32x4 mk_srd(const void* p, unsigned bytes) { u64 a = (u64)p; i32x4 r; r[0] = (int)(unsigned)a; r[1] = (int)((unsigned)(a >> 32) & 0xffffu); r[2] = (int)bytes; r[3] = 0x00020000; return r; }
DI void dma16(i32x4 srd, unsigned voff, unsigned soff, unsigned lds) {
  if (NO_DMA) return;
  asm volatile("s_mov_b32 m0, %3\n\ts_nop 3\n\tbuffer_load_dwordx4 %0, %1, %2 offen lds" :: "v"(voff), "s"(srd), "s"(soff), "s"(lds) : "memory");
}
DI void dma4(i32x4 srd, unsigned voff, unsigned lds) {
  asm volatile("s_mov_b32 m0, %2\n\ts_nop 3\n\tbuffer_load_dword %0, %1, 0 offen lds" :: "v"(voff), "s"(srd), "s"(lds) : "memory");
}
DI void ld16_sc1(u32x4& d, i32x4 srd, unsigned voff, unsigned soff) {
  asm volatile("s_nop 4\n\tbuffer_load_dwordx4 %0, %1, %2, %3 offen sc1" : "=v"(d) : "v"(voff), "s"(srd), "s"(soff) : "memory");
}
DI void ld16(u32x4& d, i32x4 srd, unsigned voff, unsigned soff) {
  asm volatile("s_nop 4\n\tbuffer_load_dwordx4 %0, %1, %2, %3 offen" : "=v"(d) : "v"(voff), "s"(srd), "s"(soff) : "memory");
}
DI void ld8_sc1(u32x2& d, i32x4 srd, unsigned voff) {
  asm volatile("s_nop 4\n\tbuffer_load_dwordx2 %0, %1, %2, 0 offen sc1" : "=v"(d) : "v"(voff), "s"(srd) : "memory");
}
DI void st16_sc1(u32x4 d, i32x4 srd, unsigned voff, unsigned soff) {
  asm volatile("s_nop 4\n\tbuffer_store_dwordx4 %0, %1, %2, %3 offen sc1\n\ts_nop 1" :: "v"(d), "v"(voff), "s"(srd), "s"(soff) : "memory");
}
DI void st16(u32x4 d, i32x4 srd, unsigned voff, unsigned soff) {
  asm volatile("s_nop 4\n\tbuffer_store_dwordx4 %0, %1, %2, %3 offen\n\ts_nop 1" :: "v"(d), "v"(voff), "s"(srd), "s"(soff) : "memory");
}
template <int N> DI void bar_vm() { static_assert(N >= 0 && N < 64, "vmcnt range"); asm volatile("s_waitcnt vmcnt(%0) lgkmcnt(0)\n\ts_barrier" :: "n"(N) : "memory"); }
template <int N> DI void wait_vm() { static_assert(N >= 0 && N < 64, "vmcnt range"); asm volatile("s_waitcnt vmcnt(%0)" :: "n"(N) : "memory"); }
DI s16x4 ldtr(unsigned a) { return __builtin_amdgcn_ds_read_tr16_b64_v4i16(lp<s16x4>(a)); }
DI bf16x8 ldfrag_tr(unsigned a, unsigned second) {
  s16x4 lo = ldtr(a), hi = ldtr(a + second);
  return __builtin_shufflevector(lo, hi, 0, 1, 2, 3, 4, 5, 6, 7);
}
DI bf16x8 ld128(unsigned a) { return *lp<bf16x8>(a); }
DI unsigned cvtpk(float lo, float hi) { typedef __bf16 b2 __attribute__((ext_vector_type(2))); f32x2 v = {lo, hi}; return __builtin_bit_cast(unsigned, __builtin_convertvector(v, b2)); }
DI float bflo(unsigned v) { return __uint_as_float(v << 16); }
DI float bfhi(unsigned v) { return __uint_as_float(v & 0xffff0000u); }
DI float rbf(float f) { return bflo(cvtpk(f, f) & 0xffffu); }
DI int fresh(int v) { asm volatile("" : "+v"(v)); return v; }
template <class T> DI void pin(T& v) { asm volatile("" : "+v"(v)); }
DI unsigned pinv(unsigned v) { asm volatile("" : "+v"(v)); return v; }
#define MFMA32(a, b, c) __builtin_amdgcn_mfma_f32_32x32x16_bf16((a), (b), (c), 0, 0, 0)
#define MFMA16(a, b, c) __builtin_amdgcn_mfma_f32_16x16x32_bf16((a), (b), (c), 0, 0, 0)
#define MFMA4(a, b, c) __builtin_amdgcn_mfma_f32_4x4x4bf16_1k((a), (b), (c), 0, 0, 0)
#define SB() __builtin_amdgcn_sched_barrier(0)
DI float swap_add(float v) { auto r = __builtin_amdgcn_permlane32_swap(__float_as_uint(v), __float_as_uint(v), false, false); return __uint_as_float(r[0]) + __uint_as_float(r[1]); }
template <class T> DI float ror_add(float v, T) { return v + __int_as_float(__builtin_amdgcn_update_dpp(0, __float_as_int(v), T::value, 0xf, 0xf, true)); }
DI float swap16_add(float v) { auto r = __builtin_amdgcn_permlane16_swap(__float_as_uint(v), __float_as_uint(v), false, false); return __uint_as_float(r[0]) + __uint_as_float(r[1]); }
template <int C> using IC = std::integral_constant<int, C>;
DI float wave_sum(float v) { v = ror_add(v, IC<0x121>{}); v = ror_add(v, IC<0x122>{}); v = ror_add(v, IC<0x124>{}); v = ror_add(v, IC<0x128>{}); return swap_add(swap16_add(v)); }
DI float swap_max(float v) { auto r = __builtin_amdgcn_permlane32_swap(__float_as_uint(v), __float_as_uint(v), false, false); return __builtin_fmaxf(__uint_as_float(r[0]), __uint_as_float(r[1])); }

#define STAMP(i) do { } while (0)

struct Ctx {
  char* ws; u64 tag;
  i32x4 sX, sXall, sQKVw, sG, sU, sWO, sWD, sWS;
  int lane, wid, mg, ng, rb, cs, cg, xi, kq, rb2, bid; unsigned stampreg; u64 prt0, prtx;
};

DI unsigned flag_off(int ev, int rb, int cs) { return (unsigned)(WS_FLAGS + (((ev * 16 + rb) * 16 + cs) << 7)); }
DI void publish(const Ctx& C, int ev) { if (C.wid == 0 && C.lane == 0) __hip_atomic_store((u64*)(C.ws + flag_off(ev, C.rb, C.cs)), C.tag, __ATOMIC_RELAXED, __HIP_MEMORY_SCOPE_AGENT); }
DI void give_up(char* ws) { __hip_atomic_store((unsigned*)(ws + WS_ERR), 1u, __ATOMIC_RELAXED, __HIP_MEMORY_SCOPE_AGENT); }
DI void spin_flags(char* ws, unsigned off, bool need, u64 tag) {
#pragma nounroll
  for (unsigned it = 0; it < SPIN_CAP; ++it) {
    u64 v = __hip_atomic_load((u64*)(ws + off), __ATOMIC_RELAXED, __HIP_MEMORY_SCOPE_AGENT);
    if (__all(!need || v == tag)) return;
    __builtin_amdgcn_s_sleep(2);
  }
  give_up(ws);
}
DI void check_flags(char* ws, unsigned off, bool need, u64 tag, u32x2& got) {
  asm volatile("" : "+v"(got));
  bool ok = !need || (got[0] == (unsigned)tag && got[1] == (unsigned)(tag >> 32));
  if (!__all(ok)) spin_flags(ws, off, need, tag);
}
template <int K0, int K1> DI void check_frags(const Ctx& C, u32x4 (&a2)[36], unsigned base) {
  auto dirty = [&]() { unsigned mx = 0;
    static_for<K0, K1>([&](auto kc) { constexpr int k = decltype(kc)::value;
      mx = __builtin_elementwise_max(mx, __builtin_elementwise_max(__builtin_elementwise_max(a2[k][0], a2[k][1]), __builtin_elementwise_max(a2[k][2], a2[k][3]))); });
    return __any(mx == POISON); };
  if (dirty()) {
#pragma nounroll
    for (unsigned it = 0; it < SPIN_CAP; ++it) {
      __builtin_amdgcn_s_sleep(2);
      static_for<K0, K1>([&](auto kc) { constexpr int k = decltype(kc)::value; ld16_sc1(a2[k], C.sWS, C.lane * 16, base + k * 1024); });
      wait_vm<0>();
      static_for<K0, K1>([&](auto kc) { constexpr int k = decltype(kc)::value; pin(a2[k]); });
      if (!dirty()) return;
    }
    give_up(C.ws);
  }
}

enum { T_H0 = 0, T_S4 = 8, T_E0 = 16, T_G0 = 19, T_POISON = 22, T_POLL, T_KL, T_POLL2, T_QL, T_QP, T_EPOCH, T_CSL, T_GR, T_WARM, T_NONE };
template <int TYPE> struct G1 {
  static constexpr int kind(int p) { return p == 0 ? 0 : (p == 1 && TYPE == 1) ? 1 : 2; }
  static constexpr int nt(int p) { return p == 0 ? 1 : 2; }
  static constexpr int nst(int p) { return kind(p) == 1 ? 3 : 2; }
  static constexpr int npoison() { return 3; }
  static constexpr unsigned slot(int s) {
    return s == 4 ? R0 : s == 5 ? R1 : s == 6 ? R2 : s == 7 ? R3 : s == 8 ? R0 : s == 9 ? R1 : s == 10 ? R2 : R0; }
  static constexpr int warmat(int pos) { return (pos >= 39 && pos < 39 + 4 * 9 && ((pos - 39) & 3) == 0) ? (pos - 39) >> 2 : -1; }
  static constexpr int barpos(int bi) { return bi < 8 ? 4 * bi + 2 : 8 * (bi - 4) + 6; }
  static constexpr int barat(int pos) { return pos < 32 ? ((pos & 3) == 2 ? pos >> 2 : -1) : ((pos & 7) == 6 ? 4 + (pos >> 3) : -1); }
  static constexpr int grp(int bi) { return bi < 4 ? T_H0 + bi + 4 : bi < 11 ? T_S4 + (bi - 4) : bi == 11 ? T_NONE : bi == 12 ? T_S4 + 7 : T_G0 + (bi - 13); }
  static constexpr int gsize(int t) { return t == T_NONE ? 0 : t < T_S4 ? 3 : 4; }
  static constexpr int rel(int bi, int k) { return bi < 8 ? k : bi == 15 ? (k >> 1) : 2 * k; }
  static constexpr int after(int t, int bi) {
    int cnt = -1;
    auto see = [&](int tag, int n) { for (int i = 0; i < n; ++i) { if (tag == t) cnt = 0; else if (cnt >= 0) ++cnt; } };
    for (int h = 0; h < 2; ++h) see(T_H0 + h, gsize(T_H0 + h));
    see(T_EPOCH, 2); see(T_CSL, 4);
    if (bi == -1) return cnt;
    for (int pos = 0; pos < 96; ++pos) {
      if (pos < 2) see(T_H0 + 2 + pos, gsize(T_H0 + 2 + pos));
      const int b = barat(pos);
      if (b >= 0) {
        if (b == bi) return cnt;
        if (b == 12) see(T_KL, 6);
        if (b == 14) { see(T_POLL2, 1); see(T_QL, 1); }
      }
      for (int bb = 0; bb < 16; ++bb) for (int k = 0; k < gsize(grp(bb)); ++k) if (barpos(bb) + rel(bb, k) == pos) see(grp(bb), 1);
      if (!NO_WARM && pos == 35) see(T_WARM, 1);
      if (!NO_WARM && warmat(pos) >= 0) see(T_WARM, 1);
      if (pos == 32 && TYPE == 1) see(T_QP, 2);
      if (pos == 18) see(T_GR, 2);
      if (pos == 58) see(T_POLL, 1);
      if (pos == 50) see(T_POISON, npoison());
      if ((pos & 31) == 31) see(T_E0 + (pos >> 5), nst(pos >> 5));
    }
    return cnt;
  }
  static constexpr int mn(int a, int b) { return a < b ? a : b; }
};

template <int TYPE> DI unsigned tile_addr(const Ctx& C, int p, int t) {
  const int lane = fresh(C.lane), kind = G1<TYPE>::kind(p);
  const int g = (lane >> 4) & 1, h = lane >> 5, q = (lane & 15) >> 2, pp = lane & 3;
  const unsigned rdrow = (8 * h + q) * 128 + pp * 8;
  if (kind == 2) return g * 16384 + rdrow + (((2 * C.ng + t) ^ (2 * (q >> 1)) ^ g) << 5);
  if (kind == 0) return 16384 + rdrow + ((C.ng ^ (q >> 1)) << 6) + g * 32;
  if (t == 0) return rdrow + ((C.ng ^ (q >> 1)) << 6) + g * 32;
  return 16384 + g * 8192 + (8 * h + q) * 64 + pp * 8 + ((C.ng ^ g) << 5);
}
template <int TYPE> DI unsigned stage_voff(const Ctx& C, int p, int which) {
  const int kind = G1<TYPE>::kind(p), lane = fresh(C.lane);
  if (which == 1) return (16 * C.wid + (lane >> 2)) * 3584 + (8 + ((C.cs - 8) >> 1)) * 128 + ((C.cs - 8) & 1) * 64 + ((lane & 3) << 4);
  const unsigned row = 16 * C.wid + (lane >> 3), sw = ((lane & 7) ^ (((lane >> 4) & 1) << 2)) << 4;
  if (kind == 1) return row * 3072 + (C.cs - 8) * 128 + sw;
  return row * 3584 + (p == 1 ? C.cs : 12 + C.cs) * 128 + sw;
}
template <int TYPE> DI void issue_stage(const Ctx& C, int s, int k, unsigned v0) {
  const int kind = G1<TYPE>::kind(s >> 2);
  const unsigned slot = G1<TYPE>::slot(s), r0 = 128 * (s & 3);
  if (kind == 1) {
    if (k < 2) dma16(C.sQKVw, v0, (r0 + 8 * k) * 3072, slot + C.wid * 2048 + k * 1024);
    else dma16(k == 3 ? C.sU : C.sG, k == 3 ? v0 ^ 32 : v0, r0 * 3584, slot + 16384 + (k - 2) * 8192 + C.wid * 1024);
  } else dma16(k >> 1 ? C.sU : C.sG, k >> 1 ? v0 ^ 32 : v0, (r0 + 8 * (k & 1)) * 3584, slot + (k >> 1) * 16384 + C.wid * 2048 + (k & 1) * 1024);
}
DI unsigned hx_voff(const Ctx& C) { const int lane = fresh(C.lane); return (16 * C.wid + (lane >> 3)) * 1024 + (((lane & 7) ^ ((lane >> 4) & 3)) << 4); }
DI unsigned hw_voff(const Ctx& C) { const int lane = fresh(C.lane);
  return (8 * C.wid + (lane >> 3)) * 3072 + (8 + C.cs) * 128 + (((lane & 7) ^ (((lane >> 4) & 1) << 2)) << 4); }
DI void issue_half(const Ctx& C, int h, int k, unsigned vx, unsigned vw) {
  const unsigned slot = (h & 3) * 32768;
  if (k < 2) dma16(C.sX, k ? (vx ^ 64) + 8 * 1024 : vx, h * 128, slot + C.wid * 2048 + k * 1024);
  else dma16(C.sQKVw, vw + (k - 2) * 128, h * 64 * 3072, slot + 16384 + (k - 2) * 8192 + C.wid * 1024);
}
DI void warm(i32x4 srd, unsigned line0, unsigned lines_per_row_log2, unsigned pitch, unsigned colbyte, int lane) {
  const unsigned l = line0 + lane;
  dma4(srd, (l >> lines_per_row_log2) * pitch + colbyte + (l & ((1u << lines_per_row_log2) - 1)) * 128, L_JUNK);
}
DI unsigned g_voff(const Ctx& C) { const int lane = fresh(C.lane); return (32 * (C.wid & 1) + (lane >> 3)) * 1024 + C.cg * 128 + (((lane & 7) ^ (((lane >> 4) & 1) << 2)) << 4); }
constexpr unsigned gslot(int j) { return j == 0 ? R1 : j == 1 ? R2 : j == 2 ? R0 : j == 3 ? R3 : j == 4 ? R1 : j == 5 ? R2 : j == 6 ? R0 : j == 7 ? R3 : R1; }
DI void issue_g(const Ctx& C, int j, int i, unsigned v) {
  const int kq = C.wid >> 1;
  const int row0 = j < 3 ? (3 * kq + j) * 64 : j < 7 ? (12 + 4 * kq + (j - 3)) * 64 : 128 * kq + 64 * (j - 7);
  dma16(j < 7 ? C.sWD : C.sWO, v, (unsigned)(row0 + 8 * i) * 1024, gslot(j) + kq * 8192 + (C.wid & 1) * 4096 + i * 1024);
}

template <int WHAT> DI void epi_qkv(const Ctx& C, const f32x16& acc, const f32x4 (&csr)[4], float m, float rinv, int qu, int h4) {
  const int h = C.lane >> 5;
  const float sc = WHAT == 0 ? rinv * QSCALE : rinv;
  float y[16];
#pragma unroll
  for (int i4 = 0; i4 < 4; ++i4)
#pragma unroll
    for (int e = 0; e < 4; ++e) y[4 * i4 + e] = (acc[4 * i4 + e] - csr[i4][e]) * sc;
  const unsigned base = (unsigned)WS_QKV + (((C.rb * 24 + qu) * 4 + C.mg) * 4 + h4) * 1024;
#pragma unroll
  for (int hh = 0; hh < 2; ++hh) {
    u32x4 f; f[0] = cvtpk(y[8 * hh], y[8 * hh + 1]); f[1] = cvtpk(y[8 * hh + 2], y[8 * hh + 3]); f[2] = cvtpk(y[8 * hh + 4], y[8 * hh + 5]); f[3] = cvtpk(y[8 * hh + 6], y[8 * hh + 7]);
    unsigned voff = C.lane * 16;
    if (WHAT == 2) {
      auto a = __builtin_amdgcn_permlane32_swap(f[0], f[2], false, false); auto b = __builtin_amdgcn_permlane32_swap(f[1], f[3], false, false);
      f[0] = a[0]; f[2] = a[1]; f[1] = b[0]; f[3] = b[1];
      voff = (C.lane & 31) * 32 + h * 16;
    }
    st16_sc1(f, C.sWS, voff, base + hh * 1024);
  }
}
DI void epi_gu(const Ctx& C, const f32x16& acc, const f32x4 (&csr)[4], float m, float r2, unsigned a2off) {
  float y[8];
#pragma unroll
  for (int i4 = 0; i4 < 2; ++i4)
#pragma unroll
    for (int e = 0; e < 4; ++e) y[4 * i4 + e] = ((acc[4 * i4 + e] - csr[i4][e]) * r2) * (acc[8 + 4 * i4 + e] - csr[2 + i4][e]);
  u32x4 f; f[0] = cvtpk(y[0], y[1]); f[1] = cvtpk(y[2], y[3]); f[2] = cvtpk(y[4], y[5]); f[3] = cvtpk(y[6], y[7]);
  st16_sc1(f, C.sWS, C.lane * 16, a2off);
}
template <int TYPE> DI unsigned a2_pass(const Ctx& C, int p) {
  if (p == 2) return (unsigned)WS_A2 + (((4 * C.rb + C.mg) * 4 + (C.cs >> 2)) * 36 + 4 * (3 + (C.cs & 3)) + 2 * C.ng) * 1024;
  if (TYPE == 0) return (unsigned)WS_A2 + (((4 * C.rb + C.mg) * 4 + C.cs / 3) * 36 + 4 * (C.cs % 3) + 2 * C.ng) * 1024;
  const int u = 8 + ((C.cs - 8) >> 1), half = (C.cs - 8) & 1;
  return (unsigned)WS_A2 + (((4 * C.rb + C.mg) * 4 + u / 3) * 36 + 4 * (u % 3) + 2 * half + C.ng) * 1024;
}
DI unsigned a2_o(const Ctx& C) { return (unsigned)WS_A2 + (((4 * C.rb + C.mg) * 4 + (C.cs >> 2)) * 36 + 28 + 2 * (C.cs & 3) + C.ng) * 1024; }

template <int TYPE> DI void gemm1(Ctx& C, unsigned vx, unsigned vw, u32x4& qfrag) {
  using G = G1<TYPE>;
  const int lane = C.lane;
  char* ws = C.ws;
  bf16x8 xa[32];
  bf16x8 wf[3][2];
  f32x16 acc[2];
  unsigned ta[2], sv = 0;
  ta[0] = pinv(tile_addr<TYPE>(C, 0, 0)); ta[1] = pinv(tile_addr<TYPE>(C, 0, 1));
  unsigned th[2];
  th[0] = ta[0] + 65536; th[1] = ta[1] + 65536;
  const unsigned r31 = fresh(lane) & 31, hh = fresh(lane) >> 5;
  const unsigned xlane = (32 * C.mg + r31) * 128 + ((hh ^ ((r31 >> 1) & 7)) << 4);
  f32x4 csr[2][4];
  float m = 0.f, rinv = 0.f;
  u32x2 pollv, poll2, gs; u32x4 gk[6], gc;
  unsigned polloff = 0, poll2off = 0, gsoff = 0, gcoff = 0; bool pollneed = false;
  const unsigned qoff = (unsigned)WS_QKV + (((C.rb * 24 + (C.cs >> 1)) * 4 + C.mg) * 4 + 2 * (C.cs & 1) + C.ng) * 1024;

  u32x2 ev; ld8_sc1(ev, C.sWS, (unsigned)WS_EPOCH);
  u32x4 srow, cl[4];
  ld16(srow, C.sXall, (8 * C.bid + C.wid) * 1024 + fresh(lane) * 16, 0);
  { const int pan = C.bid >> 1, lf = fresh(lane);
    const unsigned pitch = pan < 24 ? 3072 : 3584, col = (pan < 24 ? pan : pan < 52 ? pan - 24 : pan - 52) * 128;
    i32x4 sp;
#pragma unroll
    for (int e = 0; e < 4; ++e) sp[e] = pan < 24 ? C.sQKVw[e] : pan < 52 ? C.sG[e] : C.sU[e];
    const unsigned v = (256 * (C.bid & 1) + 32 * C.wid + (lf >> 3)) * pitch + col + (lf & 7) * 16 + (pan < 80 ? 0u : 0x40000000u);
#pragma unroll
    for (int k = 0; k < 4; ++k) ld16(cl[k], sp, v, 8 * k * pitch); }
  STAMP(1);
  bar_vm<G::after(T_H0, -1)>();
  STAMP(2);
  auto read_x = [&](int h) {
    const unsigned base = (h & 3) * 32768 + xlane;
#pragma unroll
    for (int i = 0; i < 4; ++i) xa[4 * h + i] = ld128(base ^ (i << 5));
  };
  auto read_w = [&](int pos) {
    const int p = pos >> 5, s = pos >> 3, i = pos & 7;
    if ((pos & 31) == 0 && pos > 0) { ta[0] = pinv(tile_addr<TYPE>(C, p, 0)); ta[1] = pinv(tile_addr<TYPE>(C, p, 1)); th[0] = pinv(ta[0] + 65536); th[1] = pinv(ta[1] + 65536); }
    const unsigned sb = p == 0 ? ((pos >> 2) & 3) * 32768 : G::slot(s), ks = p == 0 ? (i & 3) : i;
    const unsigned b0 = (sb & 65536) ? th[0] : ta[0], b1 = (sb & 65536) ? th[1] : ta[1], so = sb & 65535;
    wf[pos % 3][0] = ldfrag_tr(b0 + so + ks * 2048, 512);
    if (G::nt(p) == 2) { if (G::kind(p) == 1) wf[pos % 3][1] = ldfrag_tr(b1 + so + ks * 1024, 256); else wf[pos % 3][1] = ldfrag_tr(b1 + so + ks * 2048, 512); }
  };
  read_x(0); read_w(0); read_w(1);
  static_for<0, 96>([&](auto ic) {
    constexpr int pos = decltype(ic)::value;
    constexpr int p = pos >> 5, s = pos >> 3, st = s & 3, i = pos & 7, NT = G::nt(p), bi = G::barat(pos);
    if constexpr (pos < 2) static_for<0, G::gsize(T_H0)>([&](auto kc) { issue_half(C, 2 + pos, decltype(kc)::value, vx, vw); });
    if constexpr (bi >= 0) {
      STAMP(10 + 2 * bi);
      if constexpr (bi == 6) bar_vm<G::mn(G::after(T_H0 + 7, 6), G::after(T_GR, 6))>();
      else if constexpr (bi < 7) bar_vm<G::after(T_H0 + bi + 1, bi)>();
      else if constexpr (bi == 9) { bar_vm<G::mn(G::mn(G::after(T_S4 + 2, 9), G::after(T_E0, 9)), TYPE == 1 ? G::after(T_QP, 9) : 63)>(); publish(C, 0); }
      else if constexpr (bi == 12) bar_vm<G::mn(G::after(T_S4 + 5, 12), G::after(T_POLL, 12))>();
      else if constexpr (bi == 13) { static_assert(G::after(T_E0 + 1, 13) <= G::after(T_POISON, 13), ""); bar_vm<G::mn(G::after(T_S4 + 6, 13), G::after(T_E0 + 1, 13))>(); }
      else if constexpr (bi == 14) bar_vm<G::mn(G::after(T_S4 + 7, 14), G::after(T_KL, 14))>();
      else if constexpr (bi == 15) bar_vm<63>();
      else bar_vm<G::after(T_S4 + bi - 7, bi)>();
      if constexpr (bi == 1) {
        static_assert(G::after(T_H0 + 2, 1) <= G::after(T_CSL, 1), "");
        pin(ev); pin(srow); pin(cl[0]); pin(cl[1]); pin(cl[2]); pin(cl[3]);
        { const u64 t = (((u64)ev[1] << 32) | ev[0]) * 6364136223846793005ull + 1442695040888963407ull;
          C.tag = ((u64)__builtin_amdgcn_readfirstlane((unsigned)(t >> 32)) << 32) | __builtin_amdgcn_readfirstlane((unsigned)t); }
        float xv[8];
#pragma unroll
        for (int e = 0; e < 4; ++e) { xv[2 * e] = bflo(srow[e]); xv[2 * e + 1] = bfhi(srow[e]); }
        const float mean = wave_sum(((xv[0] + xv[1]) + (xv[2] + xv[3])) + ((xv[4] + xv[5]) + (xv[6] + xv[7]))) * (1.f / 512.f);
        float q2 = 0.f;
#pragma unroll
        for (int e = 0; e < 8; ++e) { const float d = xv[e] - mean; q2 += d * d; }
        const float var = wave_sum(q2) * (1.f / 512.f);
        const float sd = __builtin_amdgcn_sqrtf(rbf(rbf(var) + 1e-5f));
        if (lane == 0) __hip_atomic_store((u64*)(ws + WS_GS + (8 * C.bid + C.wid) * 8), ((u64)(unsigned)C.tag << 32) | cvtpk(mean, sd), __ATOMIC_RELAXED, __HIP_MEMORY_SCOPE_AGENT);
        float cz[8];
#pragma unroll
        for (int e = 0; e < 4; ++e) { cz[2 * e] = (bflo(cl[0][e]) + bflo(cl[1][e])) + (bflo(cl[2][e]) + bflo(cl[3][e])); cz[2 * e + 1] = (bfhi(cl[0][e]) + bfhi(cl[1][e])) + (bfhi(cl[2][e]) + bfhi(cl[3][e])); }
#pragma unroll
        for (int e = 0; e < 8; ++e) cz[e] = swap_add(swap16_add(ror_add(cz[e], IC<0x128>{})));
        if (lane < 8) { *lp<f32x4>(L_CSP + C.wid * 256 + lane * 32) = f32x4{cz[0], cz[1], cz[2], cz[3]}; *lp<f32x4>(L_CSP + C.wid * 256 + lane * 32 + 16) = f32x4{cz[4], cz[5], cz[6], cz[7]}; }
      }
      if constexpr (bi == 2) {
        if (C.wid == 0 && C.bid < 160) {
          float t = 0.f;
#pragma unroll
          for (int w = 0; w < 8; ++w) t += *lp<float>(L_CSP + w * 256 + lane * 4);
          __hip_atomic_store((u64*)(ws + WS_GC + ((C.bid >> 1) * 64 + lane) * 16 + (C.bid & 1) * 8), ((u64)(unsigned)C.tag << 32) | __float_as_uint(t), __ATOMIC_RELAXED, __HIP_MEMORY_SCOPE_AGENT);
        }
      }
      if constexpr (bi == 6) {
        pin(gs); pin(gc);
        if (!__all(gs[1] == (unsigned)C.tag && gc[1] == (unsigned)C.tag && gc[3] == (unsigned)C.tag)) {
#pragma nounroll
          for (unsigned it = 0; it < SPIN_CAP; ++it) {
            __builtin_amdgcn_s_sleep(2);
            ld8_sc1(gs, C.sWS, gsoff); ld16_sc1(gc, C.sWS, gcoff, 0); wait_vm<0>(); pin(gs); pin(gc);
            if (__all(gs[1] == (unsigned)C.tag && gc[1] == (unsigned)C.tag && gc[3] == (unsigned)C.tag)) break;
            if (it == SPIN_CAP - 1) give_up(ws);
          }
        }
        m = bflo(gs[0]); rinv = __builtin_amdgcn_rcpf(bfhi(gs[0]));
        *lp<float>(L_CS + (C.wid < 6 ? C.wid : 5) * 256 + lane * 4) = __uint_as_float(gc[0]) + __uint_as_float(gc[2]);
      }
      if constexpr (bi == 12) {
        check_flags(ws, polloff, pollneed, C.tag, pollv);
        const int u = C.cs >> 1, h4 = 2 * (C.cs & 1) + C.ng;
#pragma unroll
        for (int b = 0; b < 6; ++b) { const int rbp = C.rb - 2 + b % 3 < 0 ? 0 : C.rb - 2 + b % 3;
          ld16_sc1(gk[b], C.sWS, lane * 16, (unsigned)WS_QKV + (((rbp * 24 + (b < 3 ? 8 : 16) + u) * 4 + C.mg) * 4 + h4) * 1024); }
      }
      if constexpr (bi == 13) publish(C, 1);
      if constexpr (bi == 14) {
#pragma unroll
        for (int b = 0; b < 6; ++b) pin(gk[b]);
#pragma unroll
        for (int b = 0; b < 3; ++b) { *lp<u32x4>(L_K + ((4 * b + C.mg) * 2 + C.ng) * 1024 + lane * 16) = gk[b];
                                      *lp<u32x4>(L_V + C.ng * 12288 + (4 * b + C.mg) * 1024 + lane * 16) = gk[3 + b]; }
        { const int ln = fresh(lane), l = ln < 10 ? ln : 0, uu = 3 * C.kq + (l >> 1);
          poll2off = flag_off(1, C.rb2, l < 6 ? (uu < 8 ? uu : 8 + 2 * (uu - 8) + (l & 1)) : 4 * C.kq + (l - 6)); }
        ld8_sc1(poll2, C.sWS, poll2off);
        ld16_sc1(qfrag, C.sWS, lane * 16, qoff);
      }
      if constexpr (bi < 7) read_x(bi + 1);
      STAMP(11 + 2 * bi);
      if constexpr (bi == 7) C.prtx = __builtin_amdgcn_s_memrealtime();
    }
    static_for<0, 16>([&](auto bc) {
      constexpr int bb = decltype(bc)::value, tg = G::grp(bb), k0 = pos - G::barpos(bb);
      static_for<0, 4>([&](auto kc) {
        constexpr int k = decltype(kc)::value;
        if constexpr (k < G::gsize(tg) && G::rel(bb, k) == k0) {
          if constexpr (tg < T_S4) issue_half(C, tg, k, vx, vw);
          else if constexpr (tg >= T_G0) { if constexpr (k == 0) sv = g_voff(C); issue_g(C, tg - T_G0, k, sv); }
          else { constexpr int pp_ = (tg - T_S4 + 4) >> 2;
            if constexpr (k == 0) sv = stage_voff<TYPE>(C, pp_, 0);
            if constexpr (k == 2 && G::kind(pp_) == 1) sv = stage_voff<TYPE>(C, pp_, 1);
            issue_stage<TYPE>(C, tg - T_S4 + 4, k, sv); }
        }
      });
    });
    if constexpr (pos == 35 && !NO_WARM) {
      const int lf = fresh(lane);
      dma4(C.wid < 4 ? C.sG : C.sU, ((C.rb & 7) * 64 + (C.wid & 3) * 16 + (lf & 15)) * 3584 + (12 + C.cs) * 128, L_JUNK);
    }
    if constexpr (G::warmat(pos) >= 0 && !NO_WARM) {
      const unsigned l = (C.xi * 9 + G::warmat(pos)) * 64 + C.wid * 8 + (fresh(lane) & 7);
      dma4(l < 4096 ? C.sWO : C.sWD, (l < 4096 ? l : l - 4096) * 128, L_JUNK);
    }
    if constexpr (pos == 32 && TYPE == 1) {
      const u32x4 pz = {POISON, POISON, POISON, POISON};
      const unsigned a = (unsigned)WS_QKV + (((C.rb * 24 + (C.cs - 8)) * 4 + C.mg) * 4 + 2 * C.ng) * 1024;
      st16_sc1(pz, C.sWS, lane * 16, a); st16_sc1(pz, C.sWS, lane * 16, a + 1024);
    }
    if constexpr (pos == 18) {
      const int lf = fresh(lane), w = C.wid < 6 ? C.wid : 5;
      const int guh = 8 + ((C.cs - 8) >> 1);
      const int pan = w == 0 ? 8 + C.cs : w >= 4 ? (w == 4 ? 24 : 52) + 12 + C.cs
                    : TYPE == 0 ? (w == 1 ? 24 + C.cs : 52 + C.cs) : (w == 1 ? C.cs - 8 : w == 2 ? 24 + guh : 52 + guh);
      gsoff = (unsigned)WS_GS + (C.rb * 128 + 32 * C.mg + (lf & 31)) * 8; gcoff = (unsigned)WS_GC + (pan * 64 + lf) * 16;
      ld8_sc1(gs, C.sWS, gsoff); ld16_sc1(gc, C.sWS, gcoff, 0);
    }
    if constexpr (pos == 58) {
      const int u = C.cs >> 1, l = fresh(lane) & 7;
      const int rbp = C.rb - 2 + l % 3;
      pollneed = l < 6 && rbp >= 0;
      polloff = flag_off(0, pollneed ? rbp : C.rb, l < 3 ? u : 8 + u);
      ld8_sc1(pollv, C.sWS, polloff);
    }
    if constexpr (pos == 50) {
      const u32x4 pz = {POISON, POISON, POISON, POISON};
      const unsigned a = a2_pass<TYPE>(C, 2);
      st16_sc1(pz, C.sWS, lane * 16, a);
      st16_sc1(pz, C.sWS, lane * 16, a + 1024);
      st16_sc1(pz, C.sWS, lane * 16, a2_o(C));
    }
    if constexpr (pos + 2 < 96) read_w(pos + 2);
    if constexpr ((pos & 31) == 0) { acc[0] = MFMA32(wf[pos % 3][0], xa[pos & 31], f32x16{}); if (NT == 2) acc[1] = MFMA32(wf[pos % 3][1], xa[pos & 31], f32x16{}); }
    else { acc[0] = MFMA32(wf[pos % 3][0], xa[pos & 31], acc[0]); if (NT == 2) acc[1] = MFMA32(wf[pos % 3][1], xa[pos & 31], acc[1]); }
    if constexpr (st == 3 && i == 7) {
      static_for<0, NT>([&](auto tc) { constexpr int t = decltype(tc)::value;
        const bool gu = G::kind(p) == 2 || (G::kind(p) == 1 && t == 1);
        const int tl = G::kind(p) == 2 ? 2 * C.ng + t : 2 * ((C.cs - 8) & 1) + C.ng;
        const unsigned s0 = L_CS + (p == 0 ? 0 : p == 2 ? 4 : (G::kind(p) == 1 ? 1 + t : 1)) * 256;
#pragma unroll
        for (int i4 = 0; i4 < 4; ++i4) {
          const unsigned a = gu ? s0 + (i4 >> 1) * 256 + (16 * tl + 8 * (i4 & 1) + 4 * hh) * 4 : s0 + (32 * C.ng + 8 * i4 + 4 * hh) * 4;
          csr[t][i4] = *lp<f32x4>(a) * m; pin(csr[t][i4]); } });
    }
    SB();
    if constexpr ((pos & 31) == 31) {
      if constexpr (G::kind(p) == 2) {
        const unsigned a2 = a2_pass<TYPE>(C, p);
        epi_gu(C, acc[0], csr[0], m, rinv * rinv, a2); epi_gu(C, acc[1], csr[1], m, rinv * rinv, a2 + 1024);
      } else if constexpr (G::kind(p) == 1) {
        epi_qkv<0>(C, acc[0], csr[0], m, rinv, C.cs - 8, 2 * C.ng);
        epi_gu(C, acc[1], csr[1], m, rinv * rinv, a2_pass<TYPE>(C, p));
      } else {
        epi_qkv<1 + TYPE>(C, acc[0], csr[0], m, rinv, 8 + C.cs, 2 * C.ng);
      }
      SB();
    }
  });
  wait_vm<G::after(T_QL, 16)>();
  pin(qfrag);
  check_flags(ws, poll2off, lane < 10, C.tag, poll2);
  { auto dirty = [&]() { return __any(__builtin_elementwise_max(__builtin_elementwise_max(qfrag[0], qfrag[1]), __builtin_elementwise_max(qfrag[2], qfrag[3])) == POISON); };
    if (dirty()) {
#pragma nounroll
      for (unsigned it = 0; it < SPIN_CAP; ++it) {
        __builtin_amdgcn_s_sleep(2);
        ld16_sc1(qfrag, C.sWS, lane * 16, qoff); wait_vm<0>(); pin(qfrag);
        if (!dirty()) break;
        if (it == SPIN_CAP - 1) give_up(ws);
      } } }
}

template <bool EDGE, class SIDE> DI u32x4 attention(Ctx& C, const u32x4& qraw, SIDE&& side) {
  const int lane = C.lane, h = lane >> 5, g = (lane >> 4) & 1, q = (lane & 15) >> 2, pp = lane & 3;
  const bf16x8 qf = __builtin_bit_cast(bf16x8, qraw);
  const unsigned kaddr = pinv(L_K + (C.mg * 2 + C.ng) * 1024 + lane * 16);
  const int t0 = EDGE ? (C.rb == 0 ? 8 : 4) - C.mg : 1;
  const int dq = (lane & 31) - 4 * h;
  const float NEG = -1e30f;
  auto older = [&](int r) { return ((r & 3) + 8 * (r >> 2)) > dq; };
  auto max16 = [&](float& a, float& b, const f32x16& s) {
#pragma unroll
    for (int r = 0; r < 16; r += 4) { a = __builtin_fmaxf(__builtin_fmaxf(a, s[r]), s[r + 1]); b = __builtin_fmaxf(__builtin_fmaxf(b, s[r + 2]), s[r + 3]); } };
  bf16x8 kf[9];
#pragma unroll
  for (int t = 0; t < 9; ++t) if (!EDGE || t > 0) kf[t] = ld128(kaddr + t * 2048);
  float mx;
  {
    f32x16 S[3];
    f32x16 sl = MFMA32(kf[8], qf, f32x16{});
    f32x16 sf;
    if (!EDGE) sf = MFMA32(kf[0], qf, f32x16{});
    S[1] = MFMA32(kf[1], qf, f32x16{});
    S[2] = MFMA32(kf[2], qf, f32x16{});
    float a = NEG, b = NEG;
    side(std::integral_constant<int, 0>{});
    SB();
#pragma unroll
    for (int r = 0; r < 16; ++r) sl[r] = older(r) ? (EDGE ? NEG : sf[r]) : sl[r];
    max16(a, b, sl);
    static_for<1, 8>([&](auto tc) { constexpr int t = decltype(tc)::value;
      if constexpr (t + 2 < 8) S[(t + 2) % 3] = MFMA32(kf[t + 2], qf, f32x16{});
      side(std::integral_constant<int, t>{});
      SB();
      if (!EDGE || t >= t0) max16(a, b, S[t % 3]);
      SB(); });
    mx = swap_max(__builtin_fmaxf(a, b));
  }
  f32x16 negm;
#pragma unroll
  for (int r = 0; r < 16; ++r) negm[r] = -mx;
  const unsigned vbase = pinv(g == 0 ? L_V + C.ng * 12288 + C.mg * 1024 + (4 * h + q) * 32 + pp * 8 : L_ONES + 128 * (1 - h) + q * 32 + pp * 8);
  const unsigned vstr = g == 0 ? 1024u : 0u;
  f32x16 o;
  auto expack = [&](const f32x16& s, u32x4& pa, u32x4& pb) {
#pragma unroll
    for (int i = 0; i < 4; ++i) pa[i] = cvtpk(__builtin_amdgcn_exp2f(s[2 * i]), __builtin_amdgcn_exp2f(s[2 * i + 1]));
#pragma unroll
    for (int i = 0; i < 4; ++i) pb[i] = cvtpk(__builtin_amdgcn_exp2f(s[8 + 2 * i]), __builtin_amdgcn_exp2f(s[9 + 2 * i])); };
  f32x16 S[2];
  bf16x8 vf[2][2];
  {
    f32x16 sl = MFMA32(kf[8], qf, negm);
    f32x16 sf;
    if (!EDGE) sf = MFMA32(kf[0], qf, negm);
    S[1] = MFMA32(kf[1], qf, negm);
    const unsigned v8 = vbase + 8 * vstr;
    const bf16x8 va = ldfrag_tr(v8, 256), vb = ldfrag_tr(v8 + 512, 256);
    bf16x8 vc, vd;
    if (!EDGE) { vc = ldfrag_tr(vbase, 256); vd = ldfrag_tr(vbase + 512, 256); }
    vf[1][0] = ldfrag_tr(vbase + vstr, 256); vf[1][1] = ldfrag_tr(vbase + vstr + 512, 256);
    side(std::integral_constant<int, 8>{});
    SB();
#pragma unroll
    for (int r = 0; r < 16; ++r) sl[r] = older(r) ? (EDGE ? NEG : sf[r]) : sl[r];
    u32x4 pa, pb, mk0, mk1;
    expack(sl, pa, pb);
#pragma unroll
    for (int i = 0; i < 4; ++i) {
      mk0[i] = (older(2 * i) ? 0xffffu : 0u) | (older(2 * i + 1) ? 0xffff0000u : 0u);
      mk1[i] = (older(8 + 2 * i) ? 0xffffu : 0u) | (older(9 + 2 * i) ? 0xffff0000u : 0u); }
    o = MFMA32(va, __builtin_bit_cast(bf16x8, pa & ~mk0), f32x16{});
    o = MFMA32(vb, __builtin_bit_cast(bf16x8, pb & ~mk1), o);
    if (!EDGE) {
      o = MFMA32(vc, __builtin_bit_cast(bf16x8, pa & mk0), o);
      o = MFMA32(vd, __builtin_bit_cast(bf16x8, pb & mk1), o);
    }
    SB();
  }
  static_for<1, 8>([&](auto tc) { constexpr int t = decltype(tc)::value;
    if constexpr (t + 1 < 8) {
      const unsigned vt = vbase + (t + 1) * vstr;
      vf[(t + 1) & 1][0] = ldfrag_tr(vt, 256); vf[(t + 1) & 1][1] = ldfrag_tr(vt + 512, 256);
      S[(t + 1) & 1] = MFMA32(kf[t + 1], qf, negm); }
    side(std::integral_constant<int, 8 + t>{});
    SB();
    if (!EDGE || t >= t0) {
      u32x4 pa, pb;
      expack(S[t & 1], pa, pb);
      o = MFMA32(vf[t & 1][0], __builtin_bit_cast(bf16x8, pa), o);
      o = MFMA32(vf[t & 1][1], __builtin_bit_cast(bf16x8, pb), o);
    }
    SB(); });
  const float li = __builtin_amdgcn_rcpf(o[8]);
  u32x4 f; f[0] = cvtpk(o[0] * li, o[1] * li); f[1] = cvtpk(o[2] * li, o[3] * li); f[2] = cvtpk(o[4] * li, o[5] * li); f[3] = cvtpk(o[6] * li, o[7] * li);
  return f;
}

__global__ void __launch_bounds__(512, 2)
fused_kernel(const u16* __restrict__ x, const u16* __restrict__ wqkv, const u16* __restrict__ wo, const u16* __restrict__ wg, const u16* __restrict__ wu,
             const u16* __restrict__ wd, u16* __restrict__ out, char* __restrict__ ws) {
  Ctx C;
  const int tid = threadIdx.x, lane = tid & 63, wid = __builtin_amdgcn_readfirstlane(tid >> 6);
  const int b = blockIdx.x, xj = b & 7, xi = b >> 3;
  C.stampreg = 0;
  STAMP(0);
  C.prt0 = __builtin_amdgcn_s_memrealtime(); C.prtx = C.prt0;
  asm volatile("" :: "s"(x), "s"(wqkv), "s"(wo), "s"(wg), "s"(wu), "s"(wd), "s"(out), "s"(ws));
  C.ws = ws; C.lane = lane; C.wid = wid; C.mg = wid & 3; C.ng = wid >> 2;
  C.rb = 8 * (xj & 1) + (xi & 7); C.cs = 4 * (xj >> 1) + (xi >> 3); C.bid = b;
  C.sX = mk_srd(x + (size_t)C.rb * 128 * 512, 128 * 1024);
  C.sQKVw = mk_srd(wqkv, 512 * 3072);
  C.xi = xi;
  const unsigned vx = hx_voff(C), vw = hw_voff(C);
  STAMP(3);
  issue_half(C, 0, 0, vx, vw); issue_half(C, 0, 1, vx, vw); issue_half(C, 0, 2, vx, vw);
  STAMP(4);
  issue_half(C, 1, 0, vx, vw); issue_half(C, 1, 1, vx, vw); issue_half(C, 1, 2, vx, vw);
  STAMP(5);
  C.sWS = mk_srd(ws, 32u << 20); C.sXall = mk_srd(x, 2048 * 1024);
  C.sG = mk_srd(wg, 512 * 3584); C.sU = mk_srd(wu, 512 * 3584); C.sWO = mk_srd(wo, 512 * 1024); C.sWD = mk_srd(wd, 1792 * 1024);
  if (wid == 7) { u64 pc; asm volatile("s_getpc_b64 %0" : "=s"(pc));
    dma4(mk_srd((const void*)(pc & ~255ull), CODE_BYTES), (xi * 9 + (lane < 9 ? lane : 0)) * 128, L_JUNK); }
  if (tid < 256) lp<unsigned>(L_ONES)[tid] = 0x3f803f80u;
  const int rt64 = 4 * xj + (xi & 3), cg = xi >> 2;
  C.cg = cg;
  const int rt = wid & 1, kq = wid >> 1, RT = 2 * rt64 + rt, rb2 = RT >> 2;
  C.kq = kq; C.rb2 = rb2;
  u32x4 qfrag;
  if (C.cs < 8) gemm1<0>(C, vx, vw, qfrag); else gemm1<1>(C, vx, vw, qfrag);
  STAMP(6);

  const unsigned a2base = (unsigned)WS_A2 + (RT * 4 + kq) * 36 * 1024;
  u32x4 a2[36];
  auto side = [&](auto kc) { constexpr int k = decltype(kc)::value;
    if constexpr (k < 6) { ld16_sc1(a2[2 * k], C.sWS, lane * 16, a2base + 2 * k * 1024); ld16_sc1(a2[2 * k + 1], C.sWS, lane * 16, a2base + (2 * k + 1) * 1024); }
    if constexpr (k >= 12) static_for<0, 4>([&](auto jc) { constexpr int q_ = 12 + 4 * (k - 12) + decltype(jc)::value; ld16_sc1(a2[q_], C.sWS, lane * 16, a2base + q_ * 1024); }); };
  u32x4 of;
  if (C.rb >= 2) of = attention<false>(C, qfrag, side); else of = attention<true>(C, qfrag, side);
  st16_sc1(of, C.sWS, lane * 16, a2_o(C));
  STAMP(7);

  const int g = (lane >> 4) & 1, h = lane >> 5, q = (lane & 15) >> 2, pp = lane & 3;
  const unsigned w2l0 = kq * 8192 + (4 * h + q) * 128 + ((0 ^ (q >> 1)) << 6) + g * 32 + pp * 8;
  const unsigned w2l1 = kq * 8192 + (4 * h + q) * 128 + ((1 ^ (q >> 1)) << 6) + g * 32 + pp * 8;
  bf16x8 wf[3][2];
  f32x16 acc[2];
  u32x4 xres;
  unsigned wb[2], gv;
  auto read_w2 = [&](int pos) {
    if ((pos & 3) == 0) { wb[0] = pinv(w2l0 + gslot(pos >> 2)); wb[1] = pinv(w2l1 + gslot(pos >> 2)); }
    wf[pos % 3][0] = ldfrag_tr(wb[0] + (pos & 3) * 2048, 1024); wf[pos % 3][1] = ldfrag_tr(wb[1] + (pos & 3) * 2048, 1024); };
  bar_vm<17>();
  static_for<0, 12>([&](auto kc) { constexpr int k = decltype(kc)::value; pin(a2[k]); });
  read_w2(0); read_w2(1);
  gv = g_voff(C);
  STAMP(8);
  u32x4 ef[4];
  static_for<0, 36>([&](auto ic) {
    constexpr int pos = decltype(ic)::value;
    constexpr int j = pos >> 2, i = pos & 3;
    if constexpr (i == 2) {
      STAMP(44 + 2 * j);
      if constexpr (j <= 1) bar_vm<63>();
      if constexpr (j == 2) bar_vm<6>();
      if constexpr (j == 3) bar_vm<6>();
      if constexpr (j == 4) bar_vm<7>();
      if constexpr (j == 5) bar_vm<15>();
      if constexpr (j == 6) bar_vm<4>();
      if constexpr (j == 7) bar_vm<0>();
      STAMP(45 + 2 * j);
    }
    if constexpr (pos == 10) { static_for<12, 28>([&](auto kc) { pin(a2[decltype(kc)::value]); }); check_frags<12, 28>(C, a2, a2base); }
    if constexpr (pos == 26) { static_for<28, 36>([&](auto kc) { pin(a2[decltype(kc)::value]); }); pin(xres); check_frags<28, 36>(C, a2, a2base); }
    if constexpr (pos == 16) { ld16(xres, C.sXall, (RT * 32 + (lane & 31)) * 1024 + cg * 128 + kq * 32 + h * 16, 0); }
    if constexpr (pos == 18 || pos == 19) static_for<0, 4>([&](auto kc) { constexpr int k = 28 + 4 * (pos - 18) + decltype(kc)::value; ld16_sc1(a2[k], C.sWS, lane * 16, a2base + k * 1024); });
    if constexpr (pos < 24) issue_g(C, 3 + (pos >> 2), pos & 3, gv);
    if constexpr (pos == 30) { if (b == 0 && wid == 7) {
#pragma unroll
        for (int k = 0; k < 4; ++k) { u32x2 t; ld8_sc1(t, C.sWS, flag_off(0, 0, 0) + (lane * 4 + k) * 128); ef[k][0] = t[0]; ef[k][1] = t[1]; } } }
    if constexpr (pos + 2 < 36) read_w2(pos + 2);
    const bf16x8 af = __builtin_bit_cast(bf16x8, a2[pos]);
    if constexpr (pos == 0) { acc[0] = MFMA32(wf[0][0], af, f32x16{}); acc[1] = MFMA32(wf[0][1], af, f32x16{}); }
    else { acc[0] = MFMA32(wf[pos % 3][0], af, acc[0]); acc[1] = MFMA32(wf[pos % 3][1], af, acc[1]); }
    SB();
  });
  STAMP(9);
#pragma unroll
  for (int d = 0; d < 4; ++d) if (d != kq) {
    const int ct = d >> 1, r0 = (d & 1) * 8;
    const unsigned a = R2 + ((rt * 4 + d) * 4 + kq) * 2048 + lane * 16;
    *lp<f32x4>(a) = f32x4{acc[ct][r0], acc[ct][r0 + 1], acc[ct][r0 + 2], acc[ct][r0 + 3]};
    *lp<f32x4>(a + 1024) = f32x4{acc[ct][r0 + 4], acc[ct][r0 + 5], acc[ct][r0 + 6], acc[ct][r0 + 7]};
  }
  float s[8];
#pragma unroll
  for (int e = 0; e < 8; ++e) { const float lo = (kq & 1) ? acc[0][8 + e] : acc[0][e], hi = (kq & 1) ? acc[1][8 + e] : acc[1][e]; s[e] = (kq & 2) ? hi : lo; }
  asm volatile("s_waitcnt lgkmcnt(0)\n\ts_barrier" ::: "memory");
#pragma unroll
  for (int d = 0; d < 4; ++d) if (d != kq) {
    const unsigned a = R2 + ((rt * 4 + kq) * 4 + d) * 2048 + lane * 16;
    const f32x4 u = *lp<f32x4>(a), v = *lp<f32x4>(a + 1024);
#pragma unroll
    for (int e = 0; e < 4; ++e) { s[e] += u[e]; s[4 + e] += v[e]; }
  }
  {
    unsigned u0 = __float_as_uint(s[0]), u1 = __float_as_uint(s[1]), u2 = __float_as_uint(s[2]), u3 = __float_as_uint(s[3]);
    unsigned v0 = __float_as_uint(s[4]), v1 = __float_as_uint(s[5]), v2 = __float_as_uint(s[6]), v3 = __float_as_uint(s[7]);
    auto r0 = __builtin_amdgcn_permlane32_swap(u0, v0, false, false); auto r1 = __builtin_amdgcn_permlane32_swap(u1, v1, false, false);
    auto r2 = __builtin_amdgcn_permlane32_swap(u2, v2, false, false); auto r3 = __builtin_amdgcn_permlane32_swap(u3, v3, false, false);
    const float c0 = __uint_as_float(r0[0]), c1 = __uint_as_float(r1[0]), c2 = __uint_as_float(r2[0]), c3 = __uint_as_float(r3[0]);
    const float c4 = __uint_as_float(r0[1]), c5 = __uint_as_float(r1[1]), c6 = __uint_as_float(r2[1]), c7 = __uint_as_float(r3[1]);
    u32x4 f;
    f[0] = cvtpk(c0 + bflo(xres[0]), c1 + bfhi(xres[0])); f[1] = cvtpk(c2 + bflo(xres[1]), c3 + bfhi(xres[1]));
    f[2] = cvtpk(c4 + bflo(xres[2]), c5 + bfhi(xres[2])); f[3] = cvtpk(c6 + bflo(xres[3]), c7 + bfhi(xres[3]));
    const i32x4 sO = mk_srd(out, 2048 * 1024);
    st16(f, sO, (RT * 32 + (lane & 31)) * 1024 + cg * 128 + kq * 32 + h * 16, 0);
  }
  if (b == 0 && wid == 7) {
    wait_vm<0>();
    asm volatile("" : "+v"(ef[0]), "+v"(ef[1]), "+v"(ef[2]), "+v"(ef[3]));
    bool ok = true;
#pragma unroll
    for (int k = 0; k < 4; ++k) ok = ok && ef[k][0] == (unsigned)C.tag && ef[k][1] == (unsigned)(C.tag >> 32);
    if (!__all(ok)) {
#pragma unroll 1
      for (int k = 0; k < 4; ++k) spin_flags(ws, flag_off(0, 0, 0) + (lane * 4 + k) * 128, true, C.tag);
    }
    if (lane == 0) __hip_atomic_store((u64*)(ws + WS_EPOCH), C.tag, __ATOMIC_RELAXED, __HIP_MEMORY_SCOPE_AGENT);
  }
  STAMP(60);
  if (b == 100 && wid == 0) { const u64 e = __builtin_amdgcn_s_memrealtime(); const u64 until = e + 2 * (C.prtx - C.prt0); while (__builtin_amdgcn_s_memrealtime() < until) __builtin_amdgcn_s_sleep(1); }
}

extern "C" void kernel_launch(void* const* d_in, const int* in_sizes, int n_in, void* d_out, int out_size, void* d_ws, size_t ws_size, hipStream_t stream) {
  (void)in_sizes; (void)n_in; (void)out_size; (void)ws_size;
  (void)hipFuncSetAttribute((const void*)fused_kernel, hipFuncAttributeMaxDynamicSharedMemorySize, LDS_BYTES);
  fused_kernel<<<256, 512, LDS_BYTES, stream>>>((const u16*)d_in[0], (const u16*)d_in[1], (const u16*)d_in[2], (const u16*)d_in[4], (const u16*)d_in[5], (const u16*)d_in[6],
                                                 (u16*)d_out, (char*)d_ws);
}
```

```cpp
#include <hip/hip_runtime.h>
#include <stdint.h>
#include <type_traits>

typedef unsigned short u16;
typedef unsigned long long u64;
typedef short bf16x8 __attribute__((ext_vector_type(8)));
typedef short s16x4 __attribute__((ext_vector_type(4)));
typedef float f32x2 __attribute__((ext_vector_type(2)));
typedef float f32x4 __attribute__((ext_vector_type(4)));
typedef float f32x16 __attribute__((ext_vector_type(16)));
typedef unsigned u32x2 __attribute__((ext_vector_type(2)));
typedef unsigned u32x4 __attribute__((ext_vector_type(4)));
typedef int i32x4 __attribute__((ext_vector_type(4)));
#define LDSP __attribute__((address_space(3)))
#define DI __device__ __forceinline__
template <class T> DI LDSP T* lp(unsigned a) { return (LDSP T*)(size_t)a; }
template <int I, int N, class F> DI void static_for(F&& f) { if constexpr (I < N) { f(std::integral_constant<int, I>{}); static_for<I + 1, N>(f); } }

constexpr size_t WS_EPOCH = 0;
constexpr size_t WS_ERR   = 64;
constexpr size_t WS_FLAGS = 4096;
constexpr size_t WS_GS    = 512u << 10;
constexpr size_t WS_GC    = 576u << 10;
constexpr size_t WS_QKV   = 1u << 20;
constexpr size_t WS_A2    = 8u << 20;
constexpr size_t WS_DBG   = 24u << 20;

constexpr unsigned R0 = 0, R1 = 32768, R2 = 65536, R3 = 98304;
constexpr unsigned L_K = 98304, L_V = 122880;
constexpr unsigned L_ONES = 147456;
constexpr unsigned L_CSP = 148480;
constexpr unsigned L_CS = 150528;
constexpr unsigned L_JUNK = 154624;
constexpr unsigned LDS_BYTES = 163840;
constexpr unsigned CODE_BYTES = 36864;
constexpr unsigned POISON = 0xffffffffu;
constexpr unsigned SPIN_CAP = 1u << 13;

#define NO_WARM 0
#define NO_CS 0
#define NO_DMA 0
constexpr float QSCALE = 0.25f * 1.4426950408889634f;

DI i32x4 mk_srd(const void* p, unsigned bytes) { u64 a = (u64)p; i32x4 r; r[0] = (int)(unsigned)a; r[1] = (int)((unsigned)(a >> 32) & 0xffffu); r[2] = (int)bytes; r[3] = 0x00020000; return r; }
DI void dma16(i32x4 srd, unsigned voff, unsigned soff, unsigned lds) {
  if (NO_DMA) return;
  asm volatile("s_mov_b32 m0, %3\n\ts_nop 3\n\tbuffer_load_dwordx4 %0, %1, %2 offen lds" :: "v"(voff), "s"(srd), "s"(soff), "s"(lds) : "memory");
}
DI void dma4(i32x4 srd, unsigned voff, unsigned lds) {
  asm volatile("s_mov_b32 m0, %2\n\ts_nop 3\n\tbuffer_load_dword %0, %1, 0 offen lds" :: "v"(voff), "s"(srd), "s"(lds) : "memory");
}
DI void ld16_sc1(u32x4& d, i32x4 srd, unsigned voff, unsigned soff) {
  asm volatile("s_nop 4\n\tbuffer_load_dwordx4 %0, %1, %2, %3 offen sc1" : "=v"(d) : "v"(voff), "s"(srd), "s"(soff) : "memory");
}
DI void ld16(u32x4& d, i32x4 srd, unsigned voff, unsigned soff) {
  asm volatile("s_nop 4\n\tbuffer_load_dwordx4 %0, %1, %2, %3 offen" : "=v"(d) : "v"(voff), "s"(srd), "s"(soff) : "memory");
}
DI void ld8_sc1(u32x2& d, i32x4 srd, unsigned voff) {
  asm volatile("s_nop 4\n\tbuffer_load_dwordx2 %0, %1, %2, 0 offen sc1" : "=v"(d) : "v"(voff), "s"(srd) : "memory");
}
DI void st16_sc1(u32x4 d, i32x4 srd, unsigned voff, unsigned soff) {
  asm volatile("s_nop 4\n\tbuffer_store_dwordx4 %0, %1, %2, %3 offen sc1\n\ts_nop 1" :: "v"(d), "v"(voff), "s"(srd), "s"(soff) : "memory");
}
DI void st16(u32x4 d, i32x4 srd, unsigned voff, unsigned soff) {
  asm volatile("s_nop 4\n\tbuffer_store_dwordx4 %0, %1, %2, %3 offen\n\ts_nop 1" :: "v"(d), "v"(voff), "s"(srd), "s"(soff) : "memory");
}
template <int N> DI void bar_vm() { static_assert(N >= 0 && N < 64, "vmcnt range"); asm volatile("s_waitcnt vmcnt(%0) lgkmcnt(0)\n\ts_barrier" :: "n"(N) : "memory"); }
template <int N> DI void wait_vm() { static_assert(N >= 0 && N < 64, "vmcnt range"); asm volatile("s_waitcnt vmcnt(%0)" :: "n"(N) : "memory"); }
DI s16x4 ldtr(unsigned a) { return __builtin_amdgcn_ds_read_tr16_b64_v4i16(lp<s16x4>(a)); }
DI bf16x8 ldfrag_tr(unsigned a, unsigned second) {
  s16x4 lo = ldtr(a), hi = ldtr(a + second);
  return __builtin_shufflevector(lo, hi, 0, 1, 2, 3, 4, 5, 6, 7);
}
DI bf16x8 ld128(unsigned a) { return *lp<bf16x8>(a); }
DI unsigned cvtpk(float lo, float hi) { typedef __bf16 b2 __attribute__((ext_vector_type(2))); f32x2 v = {lo, hi}; return __builtin_bit_cast(unsigned, __builtin_convertvector(v, b2)); }
DI float bflo(unsigned v) { return __uint_as_float(v << 16); }
DI float bfhi(unsigned v) { return __uint_as_float(v & 0xffff0000u); }
DI float rbf(float f) { return bflo(cvtpk(f, f) & 0xffffu); }
DI int fresh(int v) { asm volatile("" : "+v"(v)); return v; }
template <class T> DI void pin(T& v) { asm volatile("" : "+v"(v)); }
DI unsigned pinv(unsigned v) { asm volatile("" : "+v"(v)); return v; }
#define MFMA32(a, b, c) __builtin_amdgcn_mfma_f32_32x32x16_bf16((a), (b), (c), 0, 0, 0)
#define MFMA16(a, b, c) __builtin_amdgcn_mfma_f32_16x16x32_bf16((a), (b), (c), 0, 0, 0)
#define MFMA4(a, b, c) __builtin_amdgcn_mfma_f32_4x4x4bf16_1k((a), (b), (c), 0, 0, 0)
#define SB() __builtin_amdgcn_sched_barrier(0)
DI float swap_add(float v) { auto r = __builtin_amdgcn_permlane32_swap(__float_as_uint(v), __float_as_uint(v), false, false); return __uint_as_float(r[0]) + __uint_as_float(r[1]); }
template <class T> DI float ror_add(float v, T) { return v + __int_as_float(__builtin_amdgcn_update_dpp(0, __float_as_int(v), T::value, 0xf, 0xf, true)); }
DI float swap16_add(float v) { auto r = __builtin_amdgcn_permlane16_swap(__float_as_uint(v), __float_as_uint(v), false, false); return __uint_as_float(r[0]) + __uint_as_float(r[1]); }
template <int C> using IC = std::integral_constant<int, C>;
DI float wave_sum(float v) { v = ror_add(v, IC<0x121>{}); v = ror_add(v, IC<0x122>{}); v = ror_add(v, IC<0x124>{}); v = ror_add(v, IC<0x128>{}); return swap_add(swap16_add(v)); }
DI float swap_max(float v) { auto r = __builtin_amdgcn_permlane32_swap(__float_as_uint(v), __float_as_uint(v), false, false); return __builtin_fmaxf(__uint_as_float(r[0]), __uint_as_float(r[1])); }

#define STAMP(i) do { } while (0)

struct Ctx {
  char* ws; u64 tag;
  i32x4 sX, sXall, sQKVw, sG, sU, sWO, sWD, sWS;
  int lane, wid, mg, ng, rb, cs, cg, xi, kq, rb2, bid; unsigned stampreg;
};

DI unsigned flag_off(int ev, int rb, int cs) { return (unsigned)(WS_FLAGS + (((ev * 16 + rb) * 16 + cs) << 7)); }
DI void publish(const Ctx& C, int ev) { if (C.wid == 0 && C.lane == 0) __hip_atomic_store((u64*)(C.ws + flag_off(ev, C.rb, C.cs)), C.tag, __ATOMIC_RELAXED, __HIP_MEMORY_SCOPE_AGENT); }
DI void give_up(char* ws) { __hip_atomic_store((unsigned*)(ws + WS_ERR), 1u, __ATOMIC_RELAXED, __HIP_MEMORY_SCOPE_AGENT); }
DI void spin_flags(char* ws, unsigned off, bool need, u64 tag) {
#pragma nounroll
  for (unsigned it = 0; it < SPIN_CAP; ++it) {
    u64 v = __hip_atomic_load((u64*)(ws + off), __ATOMIC_RELAXED, __HIP_MEMORY_SCOPE_AGENT);
    if (__all(!need || v == tag)) return;
    __builtin_amdgcn_s_sleep(2);
  }
  give_up(ws);
}
DI void check_flags(char* ws, unsigned off, bool need, u64 tag, u32x2& got) {
  asm volatile("" : "+v"(got));
  bool ok = !need || (got[0] == (unsigned)tag && got[1] == (unsigned)(tag >> 32));
  if (!__all(ok)) spin_flags(ws, off, need, tag);
}
template <int K0, int K1> DI void check_frags(const Ctx& C, u32x4 (&a2)[36], unsigned base) {
  auto dirty = [&]() { unsigned mx = 0;
    static_for<K0, K1>([&](auto kc) { constexpr int k = decltype(kc)::value;
      mx = __builtin_elementwise_max(mx, __builtin_elementwise_max(__builtin_elementwise_max(a2[k][0], a2[k][1]), __builtin_elementwise_max(a2[k][2], a2[k][3]))); });
    return __any(mx == POISON); };
  if (dirty()) {
#pragma nounroll
    for (unsigned it = 0; it < SPIN_CAP; ++it) {
      __builtin_amdgcn_s_sleep(2);
      static_for<K0, K1>([&](auto kc) { constexpr int k = decltype(kc)::value; ld16_sc1(a2[k], C.sWS, C.lane * 16, base + k * 1024); });
      wait_vm<0>();
      static_for<K0, K1>([&](auto kc) { constexpr int k = decltype(kc)::value; pin(a2[k]); });
      if (!dirty()) return;
    }
    give_up(C.ws);
  }
}

enum { T_H0 = 0, T_S4 = 8, T_E0 = 16, T_G0 = 19, T_POISON = 22, T_POLL, T_KL, T_POLL2, T_QL, T_QP, T_EPOCH, T_CSL, T_GR, T_WARM, T_NONE };
template <int TYPE> struct G1 {
  static constexpr int kind(int p) { return p == 0 ? 0 : (p == 1 && TYPE == 1) ? 1 : 2; }
  static constexpr int nt(int p) { return p == 0 ? 1 : 2; }
  static constexpr int nst(int p) { return kind(p) == 1 ? 3 : 2; }
  static constexpr int npoison() { return 3; }
  static constexpr unsigned slot(int s) {
    return s == 4 ? R0 : s == 5 ? R1 : s == 6 ? R2 : s == 7 ? R3 : s == 8 ? R0 : s == 9 ? R1 : s == 10 ? R2 : R0; }
  static constexpr int warmat(int pos) { return (pos >= 39 && pos < 39 + 4 * 9 && ((pos - 39) & 3) == 0) ? (pos - 39) >> 2 : -1; }
  static constexpr int barpos(int bi) { return bi < 8 ? 4 * bi + 2 : 8 * (bi - 4) + 6; }
  static constexpr int barat(int pos) { return pos < 32 ? ((pos & 3) == 2 ? pos >> 2 : -1) : ((pos & 7) == 6 ? 4 + (pos >> 3) : -1); }
  static constexpr int grp(int bi) { return bi < 4 ? T_H0 + bi + 4 : bi < 11 ? T_S4 + (bi - 4) : bi == 11 ? T_NONE : bi == 12 ? T_S4 + 7 : T_G0 + (bi - 13); }
  static constexpr int gsize(int t) { return t == T_NONE ? 0 : t < T_S4 ? 3 : 4; }
  static constexpr int rel(int bi, int k) { return bi < 8 ? k : bi == 15 ? (k >> 1) : 2 * k; }
  static constexpr int after(int t, int bi) {
    int cnt = -1;
    auto see = [&](int tag, int n) { for (int i = 0; i < n; ++i) { if (tag == t) cnt = 0; else if (cnt >= 0) ++cnt; } };
    see(T_H0, gsize(T_H0)); see(T_EPOCH, 2); see(T_CSL, 4); see(T_H0 + 1, gsize(T_H0 + 1));
    if (bi == -1) return cnt;
    for (int pos = 0; pos < 96; ++pos) {
      if (pos < 2) see(T_H0 + 2 + pos, gsize(T_H0 + 2 + pos));
      const int b = barat(pos);
      if (b >= 0) {
        if (b == bi) return cnt;
        if (b == 12) see(T_KL, 6);
        if (b == 14) { see(T_POLL2, 1); see(T_QL, 1); }
      }
      for (int bb = 0; bb < 16; ++bb) for (int k = 0; k < gsize(grp(bb)); ++k) if (barpos(bb) + rel(bb, k) == pos) see(grp(bb), 1);
      if (pos == 32 && TYPE == 1) see(T_QP, 2);
      if (pos == 22) see(T_GR, 2);
      if (pos == 58) see(T_POLL, 1);
      if (pos == 50) see(T_POISON, npoison());
      if ((pos & 31) == 31) see(T_E0 + (pos >> 5), nst(pos >> 5));
    }
    return cnt;
  }
  static constexpr int mn(int a, int b) { return a < b ? a : b; }
};

template <int TYPE> DI unsigned tile_addr(const Ctx& C, int p, int t) {
  const int lane = fresh(C.lane), kind = G1<TYPE>::kind(p);
  const int g = (lane >> 4) & 1, h = lane >> 5, q = (lane & 15) >> 2, pp = lane & 3;
  const unsigned rdrow = (8 * h + q) * 128 + pp * 8;
  if (kind == 2) return g * 16384 + rdrow + (((2 * C.ng + t) ^ (2 * (q >> 1)) ^ g) << 5);
  if (kind == 0) return 16384 + rdrow + ((C.ng ^ (q >> 1)) << 6) + g * 32;
  if (t == 0) return rdrow + ((C.ng ^ (q >> 1)) << 6) + g * 32;
  return 16384 + g * 8192 + (8 * h + q) * 64 + pp * 8 + ((C.ng ^ g) << 5);
}
template <int TYPE> DI unsigned stage_voff(const Ctx& C, int p, int which) {
  const int kind = G1<TYPE>::kind(p), lane = fresh(C.lane);
  if (which == 1) return (16 * C.wid + (lane >> 2)) * 3584 + (8 + ((C.cs - 8) >> 1)) * 128 + ((C.cs - 8) & 1) * 64 + ((lane & 3) << 4);
  const unsigned row = 16 * C.wid + (lane >> 3), sw = ((lane & 7) ^ (((lane >> 4) & 1) << 2)) << 4;
  if (kind == 1) return row * 3072 + (C.cs - 8) * 128 + sw;
  return row * 3584 + (p == 1 ? C.cs : 12 + C.cs) * 128 + sw;
}
template <int TYPE> DI void issue_stage(const Ctx& C, int s, int k, unsigned v0) {
  const int kind = G1<TYPE>::kind(s >> 2);
  const unsigned slot = G1<TYPE>::slot(s), r0 = 128 * (s & 3);
  if (kind == 1) {
    if (k < 2) dma16(C.sQKVw, v0, (r0 + 8 * k) * 3072, slot + C.wid * 2048 + k * 1024);
    else dma16(k == 3 ? C.sU : C.sG, k == 3 ? v0 ^ 32 : v0, r0 * 3584, slot + 16384 + (k - 2) * 8192 + C.wid * 1024);
  } else dma16(k >> 1 ? C.sU : C.sG, k >> 1 ? v0 ^ 32 : v0, (r0 + 8 * (k & 1)) * 3584, slot + (k >> 1) * 16384 + C.wid * 2048 + (k & 1) * 1024);
}
DI unsigned hx_voff(const Ctx& C) { const int lane = fresh(C.lane); return (16 * C.wid + (lane >> 3)) * 1024 + (((lane & 7) ^ ((lane >> 4) & 3)) << 4); }
DI unsigned hw_voff(const Ctx& C) { const int lane = fresh(C.lane);
  return (8 * C.wid + (lane >> 3)) * 3072 + (8 + C.cs) * 128 + (((lane & 7) ^ (((lane >> 4) & 1) << 2)) << 4); }
DI void issue_half(const Ctx& C, int h, int k, unsigned vx, unsigned vw) {
  const unsigned slot = (h & 3) * 32768;
  if (k < 2) dma16(C.sX, k ? (vx ^ 64) + 8 * 1024 : vx, h * 128, slot + C.wid * 2048 + k * 1024);
  else dma16(C.sQKVw, vw + (k - 2) * 128, h * 64 * 3072, slot + 16384 + (k - 2) * 8192 + C.wid * 1024);
}
DI void warm(i32x4 srd, unsigned line0, unsigned lines_per_row_log2, unsigned pitch, unsigned colbyte, int lane) {
  const unsigned l = line0 + lane;
  dma4(srd, (l >> lines_per_row_log2) * pitch + colbyte + (l & ((1u << lines_per_row_log2) - 1)) * 128, L_JUNK);
}
DI unsigned g_voff(const Ctx& C) { const int lane = fresh(C.lane); return (32 * (C.wid & 1) + (lane >> 3)) * 1024 + C.cg * 128 + (((lane & 7) ^ (((lane >> 4) & 1) << 2)) << 4); }
constexpr unsigned gslot(int j) { return j == 0 ? R1 : j == 1 ? R2 : j == 2 ? R0 : j == 3 ? R3 : j == 4 ? R1 : j == 5 ? R2 : j == 6 ? R0 : j == 7 ? R3 : R1; }
DI void issue_g(const Ctx& C, int j, int i, unsigned v) {
  const int kq = C.wid >> 1;
  const int row0 = j < 3 ? (3 * kq + j) * 64 : j < 7 ? (12 + 4 * kq + (j - 3)) * 64 : 128 * kq + 64 * (j - 7);
  dma16(j < 7 ? C.sWD : C.sWO, v, (unsigned)(row0 + 8 * i) * 1024, gslot(j) + kq * 8192 + (C.wid & 1) * 4096 + i * 1024);
}

template <int WHAT> DI void epi_qkv(const Ctx& C, const f32x16& acc, const f32x4 (&csr)[4], float m, float rinv, int qu, int h4) {
  const int h = C.lane >> 5;
  const float sc = WHAT == 0 ? rinv * QSCALE : rinv;
  float y[16];
#pragma unroll
  for (int i4 = 0; i4 < 4; ++i4)
#pragma unroll
    for (int e = 0; e < 4; ++e) y[4 * i4 + e] = (acc[4 * i4 + e] - csr[i4][e]) * sc;
  const unsigned base = (unsigned)WS_QKV + (((C.rb * 24 + qu) * 4 + C.mg) * 4 + h4) * 1024;
#pragma unroll
  for (int hh = 0; hh < 2; ++hh) {
    u32x4 f; f[0] = cvtpk(y[8 * hh], y[8 * hh + 1]); f[1] = cvtpk(y[8 * hh + 2], y[8 * hh + 3]); f[2] = cvtpk(y[8 * hh + 4], y[8 * hh + 5]); f[3] = cvtpk(y[8 * hh + 6], y[8 * hh + 7]);
    unsigned voff = C.lane * 16;
    if (WHAT == 2) {
      auto a = __builtin_amdgcn_permlane32_swap(f[0], f[2], false, false); auto b = __builtin_amdgcn_permlane32_swap(f[1], f[3], false, false);
      f[0] = a[0]; f[2] = a[1]; f[1] = b[0]; f[3] = b[1];
      voff = (C.lane & 31) * 32 + h * 16;
    }
    st16_sc1(f, C.sWS, voff, base + hh * 1024);
  }
}
DI void epi_gu(const Ctx& C, const f32x16& acc, const f32x4 (&csr)[4], float m, float r2, unsigned a2off) {
  float y[8];
#pragma unroll
  for (int i4 = 0; i4 < 2; ++i4)
#pragma unroll
    for (int e = 0; e < 4; ++e) y[4 * i4 + e] = ((acc[4 * i4 + e] - csr[i4][e]) * r2) * (acc[8 + 4 * i4 + e] - csr[2 + i4][e]);
  u32x4 f; f[0] = cvtpk(y[0], y[1]); f[1] = cvtpk(y[2], y[3]); f[2] = cvtpk(y[4], y[5]); f[3] = cvtpk(y[6], y[7]);
  st16_sc1(f, C.sWS, C.lane * 16, a2off);
}
template <int TYPE> DI unsigned a2_pass(const Ctx& C, int p) {
  if (p == 2) return (unsigned)WS_A2 + (((4 * C.rb + C.mg) * 4 + (C.cs >> 2)) * 36 + 4 * (3 + (C.cs & 3)) + 2 * C.ng) * 1024;
  if (TYPE == 0) return (unsigned)WS_A2 + (((4 * C.rb + C.mg) * 4 + C.cs / 3) * 36 + 4 * (C.cs % 3) + 2 * C.ng) * 1024;
  const int u = 8 + ((C.cs - 8) >> 1), half = (C.cs - 8) & 1;
  return (unsigned)WS_A2 + (((4 * C.rb + C.mg) * 4 + u / 3) * 36 + 4 * (u % 3) + 2 * half + C.ng) * 1024;
}
DI unsigned a2_o(const Ctx& C) { return (unsigned)WS_A2 + (((4 * C.rb + C.mg) * 4 + (C.cs >> 2)) * 36 + 28 + 2 * (C.cs & 3) + C.ng) * 1024; }

template <int TYPE> DI void gemm1(Ctx& C, unsigned vx, unsigned vw, u32x4& qfrag) {
  using G = G1<TYPE>;
  const int lane = C.lane;
  char* ws = C.ws;
  bf16x8 xa[32];
  bf16x8 wf[3][2];
  f32x16 acc[2];
  unsigned ta[2], sv = 0;
  ta[0] = pinv(tile_addr<TYPE>(C, 0, 0)); ta[1] = pinv(tile_addr<TYPE>(C, 0, 1));
  unsigned th[2];
  th[0] = ta[0] + 65536; th[1] = ta[1] + 65536;
  const unsigned r31 = fresh(lane) & 31, hh = fresh(lane) >> 5;
  const unsigned xlane = (32 * C.mg + r31) * 128 + ((hh ^ ((r31 >> 1) & 7)) << 4);
  f32x4 csr[2][4];
  float m = 0.f, rinv = 0.f;
  u32x2 pollv, poll2, gs; u32x4 gk[6], gc;
  unsigned polloff = 0, poll2off = 0, gsoff = 0, gcoff = 0; bool pollneed = false;
  const unsigned qoff = (unsigned)WS_QKV + (((C.rb * 24 + (C.cs >> 1)) * 4 + C.mg) * 4 + 2 * (C.cs & 1) + C.ng) * 1024;

  u32x2 ev; ld8_sc1(ev, C.sWS, (unsigned)WS_EPOCH);
  u32x4 srow, cl[4];
  ld16(srow, C.sXall, (8 * C.bid + C.wid) * 1024 + fresh(lane) * 16, 0);
  { const int pan = C.bid >> 1, lf = fresh(lane);
    const unsigned pitch = pan < 24 ? 3072 : 3584, col = (pan < 24 ? pan : pan < 52 ? pan - 24 : pan - 52) * 128;
    i32x4 sp;
#pragma unroll
    for (int e = 0; e < 4; ++e) sp[e] = pan < 24 ? C.sQKVw[e] : pan < 52 ? C.sG[e] : C.sU[e];
    const unsigned v = (256 * (C.bid & 1) + 32 * C.wid + (lf >> 3)) * pitch + col + (lf & 7) * 16 + (pan < 80 ? 0u : 0x40000000u);
#pragma unroll
    for (int k = 0; k < 4; ++k) ld16(cl[k], sp, v, 8 * k * pitch); }
  issue_half(C, 1, 0, vx, vw); issue_half(C, 1, 1, vx, vw); issue_half(C, 1, 2, vx, vw);
  STAMP(1);
  bar_vm<G::after(T_CSL, -1)>();
  STAMP(2);
  {
  pin(ev); pin(srow); pin(cl[0]); pin(cl[1]); pin(cl[2]); pin(cl[3]);
  { const u64 t = (((u64)ev[1] << 32) | ev[0]) * 6364136223846793005ull + 1442695040888963407ull;
    C.tag = ((u64)__builtin_amdgcn_readfirstlane((unsigned)(t >> 32)) << 32) | __builtin_amdgcn_readfirstlane((unsigned)t); }
  float xv[8];
#pragma unroll
  for (int e = 0; e < 4; ++e) { xv[2 * e] = bflo(srow[e]); xv[2 * e + 1] = bfhi(srow[e]); }
  const float mean = wave_sum(((xv[0] + xv[1]) + (xv[2] + xv[3])) + ((xv[4] + xv[5]) + (xv[6] + xv[7]))) * (1.f / 512.f);
  float q2 = 0.f;
#pragma unroll
  for (int e = 0; e < 8; ++e) { const float d = xv[e] - mean; q2 += d * d; }
  const float var = wave_sum(q2) * (1.f / 512.f);
  const float sd = __builtin_amdgcn_sqrtf(rbf(rbf(var) + 1e-5f));
  if (lane == 0) __hip_atomic_store((u64*)(ws + WS_GS + (8 * C.bid + C.wid) * 8), ((u64)(unsigned)C.tag << 32) | cvtpk(mean, sd), __ATOMIC_RELAXED, __HIP_MEMORY_SCOPE_AGENT);
  float cz[8];
#pragma unroll
  for (int e = 0; e < 4; ++e) { cz[2 * e] = (bflo(cl[0][e]) + bflo(cl[1][e])) + (bflo(cl[2][e]) + bflo(cl[3][e])); cz[2 * e + 1] = (bfhi(cl[0][e]) + bfhi(cl[1][e])) + (bfhi(cl[2][e]) + bfhi(cl[3][e])); }
#pragma unroll
  for (int e = 0; e < 8; ++e) cz[e] = swap_add(swap16_add(ror_add(cz[e], IC<0x128>{})));
  if (lane < 8) { *lp<f32x4>(L_CSP + C.wid * 256 + lane * 32) = f32x4{cz[0], cz[1], cz[2], cz[3]}; *lp<f32x4>(L_CSP + C.wid * 256 + lane * 32 + 16) = f32x4{cz[4], cz[5], cz[6], cz[7]}; }
  }
  auto read_x = [&](int h) {
    const unsigned base = (h & 3) * 32768 + xlane;
#pragma unroll
    for (int i = 0; i < 4; ++i) xa[4 * h + i] = ld128(base ^ (i << 5));
  };
  auto read_w = [&](int pos) {
    const int p = pos >> 5, s = pos >> 3, i = pos & 7;
    if ((pos & 31) == 0 && pos > 0) { ta[0] = pinv(tile_addr<TYPE>(C, p, 0)); ta[1] = pinv(tile_addr<TYPE>(C, p, 1)); th[0] = pinv(ta[0] + 65536); th[1] = pinv(ta[1] + 65536); }
    const unsigned sb = p == 0 ? ((pos >> 2) & 3) * 32768 : G::slot(s), ks = p == 0 ? (i & 3) : i;
    const unsigned b0 = (sb & 65536) ? th[0] : ta[0], b1 = (sb & 65536) ? th[1] : ta[1], so = sb & 65535;
    wf[pos % 3][0] = ldfrag_tr(b0 + so + ks * 2048, 512);
    if (G::nt(p) == 2) { if (G::kind(p) == 1) wf[pos % 3][1] = ldfrag_tr(b1 + so + ks * 1024, 256); else wf[pos % 3][1] = ldfrag_tr(b1 + so + ks * 2048, 512); }
  };
  read_x(0); read_w(0); read_w(1);
  static_for<0, 96>([&](auto ic) {
    constexpr int pos = decltype(ic)::value;
    constexpr int p = pos >> 5, s = pos >> 3, st = s & 3, i = pos & 7, NT = G::nt(p), bi = G::barat(pos);
    if constexpr (pos < 2) static_for<0, G::gsize(T_H0)>([&](auto kc) { issue_half(C, 2 + pos, decltype(kc)::value, vx, vw); });
    if constexpr (bi >= 0) {
      STAMP(10 + 2 * bi);
      if constexpr (bi < 7) bar_vm<G::after(T_H0 + bi + 1, bi)>();
      else if constexpr (bi == 7) bar_vm<G::mn(G::after(T_S4, 7), G::after(T_GR, 7))>();
      else if constexpr (bi == 9) { bar_vm<G::mn(G::mn(G::after(T_S4 + 2, 9), G::after(T_E0, 9)), TYPE == 1 ? G::after(T_QP, 9) : 63)>(); publish(C, 0); }
      else if constexpr (bi == 12) bar_vm<G::mn(G::after(T_S4 + 5, 12), G::after(T_POLL, 12))>();
      else if constexpr (bi == 13) { static_assert(G::after(T_E0 + 1, 13) <= G::after(T_POISON, 13), ""); bar_vm<G::mn(G::after(T_S4 + 6, 13), G::after(T_E0 + 1, 13))>(); }
      else if constexpr (bi == 14) bar_vm<G::mn(G::after(T_S4 + 7, 14), G::after(T_KL, 14))>();
      else if constexpr (bi == 15) bar_vm<63>();
      else bar_vm<G::after(T_S4 + bi - 7, bi)>();
      if constexpr (bi == 0) {
        if (C.wid == 0 && C.bid < 160) {
          float t = 0.f;
#pragma unroll
          for (int w = 0; w < 8; ++w) t += *lp<float>(L_CSP + w * 256 + lane * 4);
          __hip_atomic_store((u64*)(ws + WS_GC + ((C.bid >> 1) * 64 + lane) * 16 + (C.bid & 1) * 8), ((u64)(unsigned)C.tag << 32) | __float_as_uint(t), __ATOMIC_RELAXED, __HIP_MEMORY_SCOPE_AGENT);
        }
      }
      if constexpr (bi == 7) {
        pin(gs); pin(gc);
        if (!__all(gs[1] == (unsigned)C.tag && gc[1] == (unsigned)C.tag && gc[3] == (unsigned)C.tag)) {
#pragma nounroll
          for (unsigned it = 0; it < SPIN_CAP; ++it) {
            __builtin_amdgcn_s_sleep(2);
            ld8_sc1(gs, C.sWS, gsoff); ld16_sc1(gc, C.sWS, gcoff, 0); wait_vm<0>(); pin(gs); pin(gc);
            if (__all(gs[1] == (unsigned)C.tag && gc[1] == (unsigned)C.tag && gc[3] == (unsigned)C.tag)) break;
            if (it == SPIN_CAP - 1) give_up(ws);
          }
        }
        m = bflo(gs[0]); rinv = __builtin_amdgcn_rcpf(bfhi(gs[0]));
        *lp<float>(L_CS + (C.wid < 6 ? C.wid : 5) * 256 + lane * 4) = __uint_as_float(gc[0]) + __uint_as_float(gc[2]);
      }
      if constexpr (bi == 12) {
        check_flags(ws, polloff, pollneed, C.tag, pollv);
        const int u = C.cs >> 1, h4 = 2 * (C.cs & 1) + C.ng;
#pragma unroll
        for (int b = 0; b < 6; ++b) { const int rbp = C.rb - 2 + b % 3 < 0 ? 0 : C.rb - 2 + b % 3;
          ld16_sc1(gk[b], C.sWS, lane * 16, (unsigned)WS_QKV + (((rbp * 24 + (b < 3 ? 8 : 16) + u) * 4 + C.mg) * 4 + h4) * 1024); }
      }
      if constexpr (bi == 13) publish(C, 1);
      if constexpr (bi == 14) {
#pragma unroll
        for (int b = 0; b < 6; ++b) pin(gk[b]);
#pragma unroll
        for (int b = 0; b < 3; ++b) { *lp<u32x4>(L_K + ((4 * b + C.mg) * 2 + C.ng) * 1024 + lane * 16) = gk[b];
                                      *lp<u32x4>(L_V + C.ng * 12288 + (4 * b + C.mg) * 1024 + lane * 16) = gk[3 + b]; }
        { const int ln = fresh(lane), l = ln < 10 ? ln : 0, uu = 3 * C.kq + (l >> 1);
          poll2off = flag_off(1, C.rb2, l < 6 ? (uu < 8 ? uu : 8 + 2 * (uu - 8) + (l & 1)) : 4 * C.kq + (l - 6)); }
        ld8_sc1(poll2, C.sWS, poll2off);
        ld16_sc1(qfrag, C.sWS, lane * 16, qoff);
      }
      if constexpr (bi < 7) read_x(bi + 1);
      STAMP(11 + 2 * bi);
    }
    static_for<0, 16>([&](auto bc) {
      constexpr int bb = decltype(bc)::value, tg = G::grp(bb), k0 = pos - G::barpos(bb);
      static_for<0, 4>([&](auto kc) {
        constexpr int k = decltype(kc)::value;
        if constexpr (k < G::gsize(tg) && G::rel(bb, k) == k0) {
          if constexpr (tg < T_S4) issue_half(C, tg, k, vx, vw);
          else if constexpr (tg >= T_G0) { if constexpr (k == 0) sv = g_voff(C); issue_g(C, tg - T_G0, k, sv); }
          else { constexpr int pp_ = (tg - T_S4 + 4) >> 2;
            if constexpr (k == 0) sv = stage_voff<TYPE>(C, pp_, 0);
            if constexpr (k == 2 && G::kind(pp_) == 1) sv = stage_voff<TYPE>(C, pp_, 1);
            issue_stage<TYPE>(C, tg - T_S4 + 4, k, sv); }
        }
      });
    });
    if constexpr (pos == 32 && TYPE == 1) {
      const u32x4 pz = {POISON, POISON, POISON, POISON};
      const unsigned a = (unsigned)WS_QKV + (((C.rb * 24 + (C.cs - 8)) * 4 + C.mg) * 4 + 2 * C.ng) * 1024;
      st16_sc1(pz, C.sWS, lane * 16, a); st16_sc1(pz, C.sWS, lane * 16, a + 1024);
    }
    if constexpr (pos == 22) {
      const int lf = fresh(lane), w = C.wid < 6 ? C.wid : 5;
      const int guh = 8 + ((C.cs - 8) >> 1);
      const int pan = w == 0 ? 8 + C.cs : w >= 4 ? (w == 4 ? 24 : 52) + 12 + C.cs
                    : TYPE == 0 ? (w == 1 ? 24 + C.cs : 52 + C.cs) : (w == 1 ? C.cs - 8 : w == 2 ? 24 + guh : 52 + guh);
      gsoff = (unsigned)WS_GS + (C.rb * 128 + 32 * C.mg + (lf & 31)) * 8; gcoff = (unsigned)WS_GC + (pan * 64 + lf) * 16;
      ld8_sc1(gs, C.sWS, gsoff); ld16_sc1(gc, C.sWS, gcoff, 0);
    }
    if constexpr (pos == 58) {
      const int u = C.cs >> 1, l = fresh(lane) & 7;
      const int rbp = C.rb - 2 + l % 3;
      pollneed = l < 6 && rbp >= 0;
      polloff = flag_off(0, pollneed ? rbp : C.rb, l < 3 ? u : 8 + u);
      ld8_sc1(pollv, C.sWS, polloff);
    }
    if constexpr (pos == 50) {
      const u32x4 pz = {POISON, POISON, POISON, POISON};
      const unsigned a = a2_pass<TYPE>(C, 2);
      st16_sc1(pz, C.sWS, lane * 16, a);
      st16_sc1(pz, C.sWS, lane * 16, a + 1024);
      st16_sc1(pz, C.sWS, lane * 16, a2_o(C));
    }
    if constexpr (pos + 2 < 96) read_w(pos + 2);
    if constexpr ((pos & 31) == 0) { acc[0] = MFMA32(wf[pos % 3][0], xa[pos & 31], f32x16{}); if (NT == 2) acc[1] = MFMA32(wf[pos % 3][1], xa[pos & 31], f32x16{}); }
    else { acc[0] = MFMA32(wf[pos % 3][0], xa[pos & 31], acc[0]); if (NT == 2) acc[1] = MFMA32(wf[pos % 3][1], xa[pos & 31], acc[1]); }
    if constexpr (st == 3 && i == 7) {
      if constexpr (p == 0) asm volatile("s_waitcnt lgkmcnt(0)\n\ts_barrier" ::: "memory");
      static_for<0, NT>([&](auto tc) { constexpr int t = decltype(tc)::value;
        const bool gu = G::kind(p) == 2 || (G::kind(p) == 1 && t == 1);
        const int tl = G::kind(p) == 2 ? 2 * C.ng + t : 2 * ((C.cs - 8) & 1) + C.ng;
        const unsigned s0 = L_CS + (p == 0 ? 0 : p == 2 ? 4 : (G::kind(p) == 1 ? 1 + t : 1)) * 256;
#pragma unroll
        for (int i4 = 0; i4 < 4; ++i4) {
          const unsigned a = gu ? s0 + (i4 >> 1) * 256 + (16 * tl + 8 * (i4 & 1) + 4 * hh) * 4 : s0 + (32 * C.ng + 8 * i4 + 4 * hh) * 4;
          csr[t][i4] = *lp<f32x4>(a) * m; pin(csr[t][i4]); } });
    }
    SB();
    if constexpr ((pos & 31) == 31) {
      if constexpr (G::kind(p) == 2) {
        const unsigned a2 = a2_pass<TYPE>(C, p);
        epi_gu(C, acc[0], csr[0], m, rinv * rinv, a2); epi_gu(C, acc[1], csr[1], m, rinv * rinv, a2 + 1024);
      } else if constexpr (G::kind(p) == 1) {
        epi_qkv<0>(C, acc[0], csr[0], m, rinv, C.cs - 8, 2 * C.ng);
        epi_gu(C, acc[1], csr[1], m, rinv * rinv, a2_pass<TYPE>(C, p));
      } else {
        epi_qkv<1 + TYPE>(C, acc[0], csr[0], m, rinv, 8 + C.cs, 2 * C.ng);
      }
      SB();
    }
  });
  wait_vm<G::after(T_QL, 16)>();
  pin(qfrag);
  check_flags(ws, poll2off, lane < 10, C.tag, poll2);
  { auto dirty = [&]() { return __any(__builtin_elementwise_max(__builtin_elementwise_max(qfrag[0], qfrag[1]), __builtin_elementwise_max(qfrag[2], qfrag[3])) == POISON); };
    if (dirty()) {
#pragma nounroll
      for (unsigned it = 0; it < SPIN_CAP; ++it) {
        __builtin_amdgcn_s_sleep(2);
        ld16_sc1(qfrag, C.sWS, lane * 16, qoff); wait_vm<0>(); pin(qfrag);
        if (!dirty()) break;
        if (it == SPIN_CAP - 1) give_up(ws);
      } } }
}

template <bool EDGE, class SIDE> DI u32x4 attention(Ctx& C, const u32x4& qraw, SIDE&& side) {
  const int lane = C.lane, h = lane >> 5, g = (lane >> 4) & 1, q = (lane & 15) >> 2, pp = lane & 3;
  const bf16x8 qf = __builtin_bit_cast(bf16x8, qraw);
  const unsigned kaddr = pinv(L_K + (C.mg * 2 + C.ng) * 1024 + lane * 16);
  const int t0 = EDGE ? (C.rb == 0 ? 8 : 4) - C.mg : 1;
  const int dq = (lane & 31) - 4 * h;
  const float NEG = -1e30f;
  auto older = [&](int r) { return ((r & 3) + 8 * (r >> 2)) > dq; };
  auto max16 = [&](float& a, float& b, const f32x16& s) {
#pragma unroll
    for (int r = 0; r < 16; r += 4) { a = __builtin_fmaxf(__builtin_fmaxf(a, s[r]), s[r + 1]); b = __builtin_fmaxf(__builtin_fmaxf(b, s[r + 2]), s[r + 3]); } };
  bf16x8 kf[9];
#pragma unroll
  for (int t = 0; t < 9; ++t) if (!EDGE || t > 0) kf[t] = ld128(kaddr + t * 2048);
  float mx;
  {
    f32x16 S[3];
    f32x16 sl = MFMA32(kf[8], qf, f32x16{});
    f32x16 sf;
    if (!EDGE) sf = MFMA32(kf[0], qf, f32x16{});
    S[1] = MFMA32(kf[1], qf, f32x16{});
    S[2] = MFMA32(kf[2], qf, f32x16{});
    float a = NEG, b = NEG;
    side(std::integral_constant<int, 0>{});
    SB();
#pragma unroll
    for (int r = 0; r < 16; ++r) sl[r] = older(r) ? (EDGE ? NEG : sf[r]) : sl[r];
    max16(a, b, sl);
    static_for<1, 8>([&](auto tc) { constexpr int t = decltype(tc)::value;
      if constexpr (t + 2 < 8) S[(t + 2) % 3] = MFMA32(kf[t + 2], qf, f32x16{});
      side(std::integral_constant<int, t>{});
      SB();
      if (!EDGE || t >= t0) max16(a, b, S[t % 3]);
      SB(); });
    mx = swap_max(__builtin_fmaxf(a, b));
  }
  f32x16 negm;
#pragma unroll
  for (int r = 0; r < 16; ++r) negm[r] = -mx;
  const unsigned vbase = pinv(g == 0 ? L_V + C.ng * 12288 + C.mg * 1024 + (4 * h + q) * 32 + pp * 8 : L_ONES + 128 * (1 - h) + q * 32 + pp * 8);
  const unsigned vstr = g == 0 ? 1024u : 0u;
  f32x16 o;
  auto expack = [&](const f32x16& s, u32x4& pa, u32x4& pb) {
#pragma unroll
    for (int i = 0; i < 4; ++i) pa[i] = cvtpk(__builtin_amdgcn_exp2f(s[2 * i]), __builtin_amdgcn_exp2f(s[2 * i + 1]));
#pragma unroll
    for (int i = 0; i < 4; ++i) pb[i] = cvtpk(__builtin_amdgcn_exp2f(s[8 + 2 * i]), __builtin_amdgcn_exp2f(s[9 + 2 * i])); };
  f32x16 S[2];
  bf16x8 vf[2][2];
  {
    f32x16 sl = MFMA32(kf[8], qf, negm);
    f32x16 sf;
    if (!EDGE) sf = MFMA32(kf[0], qf, negm);
    S[1] = MFMA32(kf[1], qf, negm);
    const unsigned v8 = vbase + 8 * vstr;
    const bf16x8 va = ldfrag_tr(v8, 256), vb = ldfrag_tr(v8 + 512, 256);
    bf16x8 vc, vd;
    if (!EDGE) { vc = ldfrag_tr(vbase, 256); vd = ldfrag_tr(vbase + 512, 256); }
    vf[1][0] = ldfrag_tr(vbase + vstr, 256); vf[1][1] = ldfrag_tr(vbase + vstr + 512, 256);
    side(std::integral_constant<int, 8>{});
    SB();
#pragma unroll
    for (int r = 0; r < 16; ++r) sl[r] = older(r) ? (EDGE ? NEG : sf[r]) : sl[r];
    u32x4 pa, pb, mk0, mk1;
    expack(sl, pa, pb);
#pragma unroll
    for (int i = 0; i < 4; ++i) {
      mk0[i] = (older(2 * i) ? 0xffffu : 0u) | (older(2 * i + 1) ? 0xffff0000u : 0u);
      mk1[i] = (older(8 + 2 * i) ? 0xffffu : 0u) | (older(9 + 2 * i) ? 0xffff0000u : 0u); }
    o = MFMA32(va, __builtin_bit_cast(bf16x8, pa & ~mk0), f32x16{});
    o = MFMA32(vb, __builtin_bit_cast(bf16x8, pb & ~mk1), o);
    if (!EDGE) {
      o = MFMA32(vc, __builtin_bit_cast(bf16x8, pa & mk0), o);
      o = MFMA32(vd, __builtin_bit_cast(bf16x8, pb & mk1), o);
    }
    SB();
  }
  static_for<1, 8>([&](auto tc) { constexpr int t = decltype(tc)::value;
    if constexpr (t + 1 < 8) {
      const unsigned vt = vbase + (t + 1) * vstr;
      vf[(t + 1) & 1][0] = ldfrag_tr(vt, 256); vf[(t + 1) & 1][1] = ldfrag_tr(vt + 512, 256);
      S[(t + 1) & 1] = MFMA32(kf[t + 1], qf, negm); }
    side(std::integral_constant<int, 8 + t>{});
    SB();
    if (!EDGE || t >= t0) {
      u32x4 pa, pb;
      expack(S[t & 1], pa, pb);
      o = MFMA32(vf[t & 1][0], __builtin_bit_cast(bf16x8, pa), o);
      o = MFMA32(vf[t & 1][1], __builtin_bit_cast(bf16x8, pb), o);
    }
    SB(); });
  const float li = __builtin_amdgcn_rcpf(o[8]);
  u32x4 f; f[0] = cvtpk(o[0] * li, o[1] * li); f[1] = cvtpk(o[2] * li, o[3] * li); f[2] = cvtpk(o[4] * li, o[5] * li); f[3] = cvtpk(o[6] * li, o[7] * li);
  return f;
}

__global__ void __launch_bounds__(512, 2)
fused_kernel(const u16* __restrict__ x, const u16* __restrict__ wqkv, const u16* __restrict__ wo, const u16* __restrict__ wg, const u16* __restrict__ wu,
             const u16* __restrict__ wd, u16* __restrict__ out, char* __restrict__ ws) {
  Ctx C;
  const int tid = threadIdx.x, lane = tid & 63, wid = __builtin_amdgcn_readfirstlane(tid >> 6);
  const int b = blockIdx.x, xj = b & 7, xi = b >> 3;
  C.stampreg = 0;
  STAMP(0);
  asm volatile("" :: "s"(x), "s"(wqkv), "s"(wo), "s"(wg), "s"(wu), "s"(wd), "s"(out), "s"(ws));
  C.ws = ws; C.lane = lane; C.wid = wid; C.mg = wid & 3; C.ng = wid >> 2;
  C.rb = 8 * (xj & 1) + (xi & 7); C.cs = 4 * (xj >> 1) + (xi >> 3); C.bid = b;
  C.sX = mk_srd(x + (size_t)C.rb * 128 * 512, 128 * 1024);
  C.sQKVw = mk_srd(wqkv, 512 * 3072);
  C.xi = xi;
  const unsigned vx = hx_voff(C), vw = hw_voff(C);
  STAMP(3);
  issue_half(C, 0, 0, vx, vw); issue_half(C, 0, 1, vx, vw); issue_half(C, 0, 2, vx, vw);
  STAMP(4);
  STAMP(5);
  C.sWS = mk_srd(ws, 32u << 20); C.sXall = mk_srd(x, 2048 * 1024);
  C.sG = mk_srd(wg, 512 * 3584); C.sU = mk_srd(wu, 512 * 3584); C.sWO = mk_srd(wo, 512 * 1024); C.sWD = mk_srd(wd, 1792 * 1024);
  if (wid == 7) { u64 pc; asm volatile("s_getpc_b64 %0" : "=s"(pc));
    dma4(mk_srd((const void*)(pc & ~255ull), CODE_BYTES), (xi * 9 + (lane < 9 ? lane : 0)) * 128, L_JUNK); }
  if (tid < 256) lp<unsigned>(L_ONES)[tid] = 0x3f803f80u;
  const int rt64 = 4 * xj + (xi & 3), cg = xi >> 2;
  C.cg = cg;
  const int rt = wid & 1, kq = wid >> 1, RT = 2 * rt64 + rt, rb2 = RT >> 2;
  C.kq = kq; C.rb2 = rb2;
  u32x4 qfrag;
  if (C.cs < 8) gemm1<0>(C, vx, vw, qfrag); else gemm1<1>(C, vx, vw, qfrag);
  STAMP(6);

  const unsigned a2base = (unsigned)WS_A2 + (RT * 4 + kq) * 36 * 1024;
  u32x4 a2[36];
  auto side = [&](auto kc) { constexpr int k = decltype(kc)::value;
    if constexpr (k < 6) { ld16_sc1(a2[2 * k], C.sWS, lane * 16, a2base + 2 * k * 1024); ld16_sc1(a2[2 * k + 1], C.sWS, lane * 16, a2base + (2 * k + 1) * 1024); }
    if constexpr (k >= 6 && k < 12 && !NO_WARM) { const unsigned l = (xi * 6 + (k - 6)) * 64 + wid * 8 + (fresh(lane) & 7);
      dma4(l < 8192 ? C.sWD : C.sWO, (l < 8192 ? l + 768 * 8 : l - 8192) * 128, L_JUNK); }
    if constexpr (k >= 12) static_for<0, 4>([&](auto jc) { constexpr int q_ = 12 + 4 * (k - 12) + decltype(jc)::value; ld16_sc1(a2[q_], C.sWS, lane * 16, a2base + q_ * 1024); }); };
  u32x4 of;
  if (C.rb >= 2) of = attention<false>(C, qfrag, side); else of = attention<true>(C, qfrag, side);
  st16_sc1(of, C.sWS, lane * 16, a2_o(C));
  STAMP(7);

  const int g = (lane >> 4) & 1, h = lane >> 5, q = (lane & 15) >> 2, pp = lane & 3;
  const unsigned w2l0 = kq * 8192 + (4 * h + q) * 128 + ((0 ^ (q >> 1)) << 6) + g * 32 + pp * 8;
  const unsigned w2l1 = kq * 8192 + (4 * h + q) * 128 + ((1 ^ (q >> 1)) << 6) + g * 32 + pp * 8;
  bf16x8 wf[3][2];
  f32x16 acc[2];
  u32x4 xres;
  unsigned wb[2], gv;
  auto read_w2 = [&](int pos) {
    if ((pos & 3) == 0) { wb[0] = pinv(w2l0 + gslot(pos >> 2)); wb[1] = pinv(w2l1 + gslot(pos >> 2)); }
    wf[pos % 3][0] = ldfrag_tr(wb[0] + (pos & 3) * 2048, 1024); wf[pos % 3][1] = ldfrag_tr(wb[1] + (pos & 3) * 2048, 1024); };
  bar_vm<17 + (NO_WARM ? 0 : 6)>();
  static_for<0, 12>([&](auto kc) { constexpr int k = decltype(kc)::value; pin(a2[k]); });
  read_w2(0); read_w2(1);
  gv = g_voff(C);
  STAMP(8);
  u32x4 ef[4];
  static_for<0, 36>([&](auto ic) {
    constexpr int pos = decltype(ic)::value;
    constexpr int j = pos >> 2, i = pos & 3;
    if constexpr (i == 2) {
      STAMP(44 + 2 * j);
      if constexpr (j <= 1) bar_vm<63>();
      if constexpr (j == 2) bar_vm<6>();
      if constexpr (j == 3) bar_vm<6>();
      if constexpr (j == 4) bar_vm<7>();
      if constexpr (j == 5) bar_vm<15>();
      if constexpr (j == 6) bar_vm<4>();
      if constexpr (j == 7) bar_vm<0>();
      STAMP(45 + 2 * j);
    }
    if constexpr (pos == 10) { static_for<12, 28>([&](auto kc) { pin(a2[decltype(kc)::value]); }); check_frags<12, 28>(C, a2, a2base); }
    if constexpr (pos == 26) { static_for<28, 36>([&](auto kc) { pin(a2[decltype(kc)::value]); }); pin(xres); check_frags<28, 36>(C, a2, a2base); }
    if constexpr (pos == 16) { ld16(xres, C.sXall, (RT * 32 + (lane & 31)) * 1024 + cg * 128 + kq * 32 + h * 16, 0); }
    if constexpr (pos == 18 || pos == 19) static_for<0, 4>([&](auto kc) { constexpr int k = 28 + 4 * (pos - 18) + decltype(kc)::value; ld16_sc1(a2[k], C.sWS, lane * 16, a2base + k * 1024); });
    if constexpr (pos < 24) issue_g(C, 3 + (pos >> 2), pos & 3, gv);
    if constexpr (pos == 30) { if (b == 0 && wid == 7) {
#pragma unroll
        for (int k = 0; k < 4; ++k) { u32x2 t; ld8_sc1(t, C.sWS, flag_off(0, 0, 0) + (lane * 4 + k) * 128); ef[k][0] = t[0]; ef[k][1] = t[1]; } } }
    if constexpr (pos + 2 < 36) read_w2(pos + 2);
    const bf16x8 af = __builtin_bit_cast(bf16x8, a2[pos]);
    if constexpr (pos == 0) { acc[0] = MFMA32(wf[0][0], af, f32x16{}); acc[1] = MFMA32(wf[0][1], af, f32x16{}); }
    else { acc[0] = MFMA32(wf[pos % 3][0], af, acc[0]); acc[1] = MFMA32(wf[pos % 3][1], af, acc[1]); }
    SB();
  });
  STAMP(9);
#pragma unroll
  for (int d = 0; d < 4; ++d) if (d != kq) {
    const int ct = d >> 1, r0 = (d & 1) * 8;
    const unsigned a = R2 + ((rt * 4 + d) * 4 + kq) * 2048 + lane * 16;
    *lp<f32x4>(a) = f32x4{acc[ct][r0], acc[ct][r0 + 1], acc[ct][r0 + 2], acc[ct][r0 + 3]};
    *lp<f32x4>(a + 1024) = f32x4{acc[ct][r0 + 4], acc[ct][r0 + 5], acc[ct][r0 + 6], acc[ct][r0 + 7]};
  }
  float s[8];
#pragma unroll
  for (int e = 0; e < 8; ++e) { const float lo = (kq & 1) ? acc[0][8 + e] : acc[0][e], hi = (kq & 1) ? acc[1][8 + e] : acc[1][e]; s[e] = (kq & 2) ? hi : lo; }
  asm volatile("s_waitcnt lgkmcnt(0)\n\ts_barrier" ::: "memory");
#pragma unroll
  for (int d = 0; d < 4; ++d) if (d != kq) {
    const unsigned a = R2 + ((rt * 4 + kq) * 4 + d) * 2048 + lane * 16;
    const f32x4 u = *lp<f32x4>(a), v = *lp<f32x4>(a + 1024);
#pragma unroll
    for (int e = 0; e < 4; ++e) { s[e] += u[e]; s[4 + e] += v[e]; }
  }
  {
    unsigned u0 = __float_as_uint(s[0]), u1 = __float_as_uint(s[1]), u2 = __float_as_uint(s[2]), u3 = __float_as_uint(s[3]);
    unsigned v0 = __float_as_uint(s[4]), v1 = __float_as_uint(s[5]), v2 = __float_as_uint(s[6]), v3 = __float_as_uint(s[7]);
    auto r0 = __builtin_amdgcn_permlane32_swap(u0, v0, false, false); auto r1 = __builtin_amdgcn_permlane32_swap(u1, v1, false, false);
    auto r2 = __builtin_amdgcn_permlane32_swap(u2, v2, false, false); auto r3 = __builtin_amdgcn_permlane32_swap(u3, v3, false, false);
    const float c0 = __uint_as_float(r0[0]), c1 = __uint_as_float(r1[0]), c2 = __uint_as_float(r2[0]), c3 = __uint_as_float(r3[0]);
    const float c4 = __uint_as_float(r0[1]), c5 = __uint_as_float(r1[1]), c6 = __uint_as_float(r2[1]), c7 = __uint_as_float(r3[1]);
    u32x4 f;
    f[0] = cvtpk(c0 + bflo(xres[0]), c1 + bfhi(xres[0])); f[1] = cvtpk(c2 + bflo(xres[1]), c3 + bfhi(xres[1]));
    f[2] = cvtpk(c4 + bflo(xres[2]), c5 + bfhi(xres[2])); f[3] = cvtpk(c6 + bflo(xres[3]), c7 + bfhi(xres[3]));
    const i32x4 sO = mk_srd(out, 2048 * 1024);
    st16(f, sO, (RT * 32 + (lane & 31)) * 1024 + cg * 128 + kq * 32 + h * 16, 0);
  }
  if (b == 0 && wid == 7) {
    wait_vm<0>();
    asm volatile("" : "+v"(ef[0]), "+v"(ef[1]), "+v"(ef[2]), "+v"(ef[3]));
    bool ok = true;
#pragma unroll
    for (int k = 0; k < 4; ++k) ok = ok && ef[k][0] == (unsigned)C.tag && ef[k][1] == (unsigned)(C.tag >> 32);
    if (!__all(ok)) {
#pragma unroll 1
      for (int k = 0; k < 4; ++k) spin_flags(ws, flag_off(0, 0, 0) + (lane * 4 + k) * 128, true, C.tag);
    }
    if (lane == 0) __hip_atomic_store((u64*)(ws + WS_EPOCH), C.tag, __ATOMIC_RELAXED, __HIP_MEMORY_SCOPE_AGENT);
  }
  STAMP(60);
}

extern "C" void kernel_launch(void* const* d_in, const int* in_sizes, int n_in, void* d_out, int out_size, void* d_ws, size_t ws_size, hipStream_t stream) {
  (void)in_sizes; (void)n_in; (void)out_size; (void)ws_size;
  (void)hipFuncSetAttribute((const void*)fused_kernel, hipFuncAttributeMaxDynamicSharedMemorySize, LDS_BYTES);
  fused_kernel<<<256, 512, LDS_BYTES, stream>>>((const u16*)d_in[0], (const u16*)d_in[1], (const u16*)d_in[2], (const u16*)d_in[4], (const u16*)d_in[5], (const u16*)d_in[6],
                                                 (u16*)d_out, (char*)d_ws);
}
```

```cpp
#include <hip/hip_runtime.h>
#include <stdint.h>
#include <type_traits>

typedef unsigned short u16;
typedef unsigned long long u64;
typedef short bf16x8 __attribute__((ext_vector_type(8)));
typedef short s16x4 __attribute__((ext_vector_type(4)));
typedef float f32x2 __attribute__((ext_vector_type(2)));
typedef float f32x4 __attribute__((ext_vector_type(4)));
typedef float f32x16 __attribute__((ext_vector_type(16)));
typedef unsigned u32x2 __attribute__((ext_vector_type(2)));
typedef unsigned u32x4 __attribute__((ext_vector_type(4)));
typedef int i32x4 __attribute__((ext_vector_type(4)));
#define LDSP __attribute__((address_space(3)))
#define DI __device__ __forceinline__
template <class T> DI LDSP T* lp(unsigned a) { return (LDSP T*)(size_t)a; }
template <int I, int N, class F> DI void static_for(F&& f) { if constexpr (I < N) { f(std::integral_constant<int, I>{}); static_for<I + 1, N>(f); } }

constexpr size_t WS_EPOCH = 0;
constexpr size_t WS_ERR   = 64;
constexpr size_t WS_FLAGS = 4096;
constexpr size_t WS_GS    = 512u << 10;
constexpr size_t WS_GC    = 576u << 10;
constexpr size_t WS_QKV   = 1u << 20;
constexpr size_t WS_A2    = 8u << 20;
constexpr size_t WS_DBG   = 24u << 20;

constexpr unsigned R0 = 0, R1 = 32768, R2 = 65536, R3 = 98304;
constexpr unsigned L_K = 98304, L_V = 122880;
constexpr unsigned L_ONES = 147456;
constexpr unsigned L_CSP = 148480;
constexpr unsigned L_CS = 150528;
constexpr unsigned L_JUNK = 154624;
constexpr unsigned LDS_BYTES = 163840;
constexpr unsigned CODE_BYTES = 36864;
constexpr unsigned POISON = 0xffffffffu;
constexpr unsigned SPIN_CAP = 1u << 13;

#define NO_WARM 0
#define NO_CS 0
#define NO_DMA 0
constexpr float QSCALE = 0.25f * 1.4426950408889634f;

DI i32x4 mk_srd(const void* p, unsigned bytes) { u64 a = (u64)p; i32x4 r; r[0] = (int)(unsigned)a; r[1] = (int)((unsigned)(a >> 32) & 0xffffu); r[2] = (int)bytes; r[3] = 0x00020000; return r; }
DI void dma16(i32x4 srd, unsigned voff, unsigned soff, unsigned lds) {
  if (NO_DMA) return;
  asm volatile("s_mov_b32 m0, %3\n\ts_nop 3\n\tbuffer_load_dwordx4 %0, %1, %2 offen lds" :: "v"(voff), "s"(srd), "s"(soff), "s"(lds) : "memory");
}
DI void dma4(i32x4 srd, unsigned voff, unsigned lds) {
  asm volatile("s_mov_b32 m0, %2\n\ts_nop 3\n\tbuffer_load_dword %0, %1, 0 offen lds" :: "v"(voff), "s"(srd), "s"(lds) : "memory");
}
DI void ld16_sc1(u32x4& d, i32x4 srd, unsigned voff, unsigned soff) {
  asm volatile("s_nop 4\n\tbuffer_load_dwordx4 %0, %1, %2, %3 offen sc1" : "=v"(d) : "v"(voff), "s"(srd), "s"(soff) : "memory");
}
DI void ld16(u32x4& d, i32x4 srd, unsigned voff, unsigned soff) {
  asm volatile("s_nop 4\n\tbuffer_load_dwordx4 %0, %1, %2, %3 offen" : "=v"(d) : "v"(voff), "s"(srd), "s"(soff) : "memory");
}
DI void ld8_sc1(u32x2& d, i32x4 srd, unsigned voff) {
  asm volatile("s_nop 4\n\tbuffer_load_dwordx2 %0, %1, %2, 0 offen sc1" : "=v"(d) : "v"(voff), "s"(srd) : "memory");
}
DI void st16_sc1(u32x4 d, i32x4 srd, unsigned voff, unsigned soff) {
  asm volatile("s_nop 4\n\tbuffer_store_dwordx4 %0, %1, %2, %3 offen sc1\n\ts_nop 1" :: "v"(d), "v"(voff), "s"(srd), "s"(soff) : "memory");
}
DI void st16(u32x4 d, i32x4 srd, unsigned voff, unsigned soff) {
  asm volatile("s_nop 4\n\tbuffer_store_dwordx4 %0, %1, %2, %3 offen\n\ts_nop 1" :: "v"(d), "v"(voff), "s"(srd), "s"(soff) : "memory");
}
template <int N> DI void bar_vm() { static_assert(N >= 0 && N < 64, "vmcnt range"); asm volatile("s_waitcnt vmcnt(%0) lgkmcnt(0)\n\ts_barrier" :: "n"(N) : "memory"); }
template <int N> DI void wait_vm() { static_assert(N >= 0 && N < 64, "vmcnt range"); asm volatile("s_waitcnt vmcnt(%0)" :: "n"(N) : "memory"); }
DI s16x4 ldtr(unsigned a) { return __builtin_amdgcn_ds_read_tr16_b64_v4i16(lp<s16x4>(a)); }
DI bf16x8 ldfrag_tr(unsigned a, unsigned second) {
  s16x4 lo = ldtr(a), hi = ldtr(a + second);
  return __builtin_shufflevector(lo, hi, 0, 1, 2, 3, 4, 5, 6, 7);
}
DI bf16x8 ld128(unsigned a) { return *lp<bf16x8>(a); }
DI unsigned cvtpk(float lo, float hi) { typedef __bf16 b2 __attribute__((ext_vector_type(2))); f32x2 v = {lo, hi}; return __builtin_bit_cast(unsigned, __builtin_convertvector(v, b2)); }
DI float bflo(unsigned v) { return __uint_as_float(v << 16); }
DI float bfhi(unsigned v) { return __uint_as_float(v & 0xffff0000u); }
DI float rbf(float f) { return bflo(cvtpk(f, f) & 0xffffu); }
DI int fresh(int v) { asm volatile("" : "+v"(v)); return v; }
template <class T> DI void pin(T& v) { asm volatile("" : "+v"(v)); }
DI unsigned pinv(unsigned v) { asm volatile("" : "+v"(v)); return v; }
#define MFMA32(a, b, c) __builtin_amdgcn_mfma_f32_32x32x16_bf16((a), (b), (c), 0, 0, 0)
#define MFMA16(a, b, c) __builtin_amdgcn_mfma_f32_16x16x32_bf16((a), (b), (c), 0, 0, 0)
#define MFMA4(a, b, c) __builtin_amdgcn_mfma_f32_4x4x4bf16_1k((a), (b), (c), 0, 0, 0)
#define SB() __builtin_amdgcn_sched_barrier(0)
DI float swap_add(float v) { auto r = __builtin_amdgcn_permlane32_swap(__float_as_uint(v), __float_as_uint(v), false, false); return __uint_as_float(r[0]) + __uint_as_float(r[1]); }
template <class T> DI float ror_add(float v, T) { return v + __int_as_float(__builtin_amdgcn_update_dpp(0, __float_as_int(v), T::value, 0xf, 0xf, true)); }
DI float swap16_add(float v) { auto r = __builtin_amdgcn_permlane16_swap(__float_as_uint(v), __float_as_uint(v), false, false); return __uint_as_float(r[0]) + __uint_as_float(r[1]); }
template <int C> using IC = std::integral_constant<int, C>;
DI float wave_sum(float v) { v = ror_add(v, IC<0x121>{}); v = ror_add(v, IC<0x122>{}); v = ror_add(v, IC<0x124>{}); v = ror_add(v, IC<0x128>{}); return swap_add(swap16_add(v)); }
DI float swap_max(float v) { auto r = __builtin_amdgcn_permlane32_swap(__float_as_uint(v), __float_as_uint(v), false, false); return __builtin_fmaxf(__uint_as_float(r[0]), __uint_as_float(r[1])); }

#define STAMP(i) do { } while (0)

struct Ctx {
  char* ws; u64 tag;
  i32x4 sX, sXall, sQKVw, sG, sU, sWO, sWD, sWS;
  int lane, wid, mg, ng, rb, cs, cg, xi, kq, rb2, bid; unsigned stampreg; u64 prt0, prtx;
};

DI unsigned flag_off(int ev, int rb, int cs) { return (unsigned)(WS_FLAGS + (((ev * 16 + rb) * 16 + cs) << 7)); }
DI void publish(const Ctx& C, int ev) { if (C.wid == 0 && C.lane == 0) __hip_atomic_store((u64*)(C.ws + flag_off(ev, C.rb, C.cs)), C.tag, __ATOMIC_RELAXED, __HIP_MEMORY_SCOPE_AGENT); }
DI void give_up(char* ws) { __hip_atomic_store((unsigned*)(ws + WS_ERR), 1u, __ATOMIC_RELAXED, __HIP_MEMORY_SCOPE_AGENT); }
DI void spin_flags(char* ws, unsigned off, bool need, u64 tag) {
#pragma nounroll
  for (unsigned it = 0; it < SPIN_CAP; ++it) {
    u64 v = __hip_atomic_load((u64*)(ws + off), __ATOMIC_RELAXED, __HIP_MEMORY_SCOPE_AGENT);
    if (__all(!need || v == tag)) return;
    __builtin_amdgcn_s_sleep(2);
  }
  give_up(ws);
}
DI void check_flags(char* ws, unsigned off, bool need, u64 tag, u32x2& got) {
  asm volatile("" : "+v"(got));
  bool ok = !need || (got[0] == (unsigned)tag && got[1] == (unsigned)(tag >> 32));
  if (!__all(ok)) spin_flags(ws, off, need, tag);
}
template <int K0, int K1> DI void check_frags(const Ctx& C, u32x4 (&a2)[36], unsigned base) {
  auto dirty = [&]() { unsigned mx = 0;
    static_for<K0, K1>([&](auto kc) { constexpr int k = decltype(kc)::value;
      mx = __builtin_elementwise_max(mx, __builtin_elementwise_max(__builtin_elementwise_max(a2[k][0], a2[k][1]), __builtin_elementwise_max(a2[k][2], a2[k][3]))); });
    return __any(mx == POISON); };
  if (dirty()) {
#pragma nounroll
    for (unsigned it = 0; it < SPIN_CAP; ++it) {
      __builtin_amdgcn_s_sleep(2);
      static_for<K0, K1>([&](auto kc) { constexpr int k = decltype(kc)::value; ld16_sc1(a2[k], C.sWS, C.lane * 16, base + k * 1024); });
      wait_vm<0>();
      static_for<K0, K1>([&](auto kc) { constexpr int k = decltype(kc)::value; pin(a2[k]); });
      if (!dirty()) return;
    }
    give_up(C.ws);
  }
}

enum { T_H0 = 0, T_S4 = 8, T_E0 = 16, T_G0 = 19, T_POISON = 22, T_POLL, T_KL, T_POLL2, T_QL, T_QP, T_EPOCH, T_CSL, T_GR, T_WARM, T_NONE };
template <int TYPE> struct G1 {
  static constexpr int kind(int p) { return p == 0 ? 0 : (p == 1 && TYPE == 1) ? 1 : 2; }
  static constexpr int nt(int p) { return p == 0 ? 1 : 2; }
  static constexpr int nst(int p) { return kind(p) == 1 ? 3 : 2; }
  static constexpr int npoison() { return 3; }
  static constexpr unsigned slot(int s) {
    return s == 4 ? R0 : s == 5 ? R1 : s == 6 ? R2 : s == 7 ? R3 : s == 8 ? R0 : s == 9 ? R1 : s == 10 ? R2 : R0; }
  static constexpr int warmat(int pos) { return (pos >= 39 && pos < 39 + 4 * 9 && ((pos - 39) & 3) == 0) ? (pos - 39) >> 2 : -1; }
  static constexpr int barpos(int bi) { return bi < 8 ? 4 * bi + 2 : 8 * (bi - 4) + 6; }
  static constexpr int barat(int pos) { return pos < 32 ? ((pos & 3) == 2 ? pos >> 2 : -1) : ((pos & 7) == 6 ? 4 + (pos >> 3) : -1); }
  static constexpr int grp(int bi) { return bi < 4 ? T_H0 + bi + 4 : bi < 11 ? T_S4 + (bi - 4) : bi == 11 ? T_NONE : bi == 12 ? T_S4 + 7 : T_G0 + (bi - 13); }
  static constexpr int gsize(int t) { return t == T_NONE ? 0 : t < T_S4 ? 3 : 4; }
  static constexpr int rel(int bi, int k) { return bi < 8 ? k : bi == 15 ? (k >> 1) : 2 * k; }
  static constexpr int after(int t, int bi) {
    int cnt = -1;
    auto see = [&](int tag, int n) { for (int i = 0; i < n; ++i) { if (tag == t) cnt = 0; else if (cnt >= 0) ++cnt; } };
    see(T_H0, gsize(T_H0)); see(T_EPOCH, 2); see(T_H0 + 1, gsize(T_H0 + 1)); see(T_CSL, 4);
    if (bi == -1) return cnt;
    for (int pos = 0; pos < 96; ++pos) {
      if (pos < 2) see(T_H0 + 2 + pos, gsize(T_H0 + 2 + pos));
      const int b = barat(pos);
      if (b >= 0) {
        if (b == bi) return cnt;
        if (b == 12) see(T_KL, 6);
        if (b == 13) see(T_QL, 1);
        if (b == 14) see(T_POLL2, 1);
      }
      for (int bb = 0; bb < 16; ++bb) for (int k = 0; k < gsize(grp(bb)); ++k) if (barpos(bb) + rel(bb, k) == pos) see(grp(bb), 1);
      if (pos == 32 && TYPE == 1) see(T_QP, 2);
      if (pos == 22) see(T_GR, 2);
      if (pos == 58) see(T_POLL, 1);
      if (pos == 50) see(T_POISON, npoison());
      if ((pos & 31) == 31) see(T_E0 + (pos >> 5), nst(pos >> 5));
    }
    return cnt;
  }
  static constexpr int mn(int a, int b) { return a < b ? a : b; }
};

template <int TYPE> DI unsigned tile_addr(const Ctx& C, int p, int t) {
  const int lane = fresh(C.lane), kind = G1<TYPE>::kind(p);
  const int g = (lane >> 4) & 1, h = lane >> 5, q = (lane & 15) >> 2, pp = lane & 3;
  const unsigned rdrow = (8 * h + q) * 128 + pp * 8;
  if (kind == 2) return g * 16384 + rdrow + (((2 * C.ng + t) ^ (2 * (q >> 1)) ^ g) << 5);
  if (kind == 0) return 16384 + rdrow + ((C.ng ^ (q >> 1)) << 6) + g * 32;
  if (t == 0) return rdrow + ((C.ng ^ (q >> 1)) << 6) + g * 32;
  return 16384 + g * 8192 + (8 * h + q) * 64 + pp * 8 + ((C.ng ^ g) << 5);
}
template <int TYPE> DI unsigned stage_voff(const Ctx& C, int p, int which) {
  const int kind = G1<TYPE>::kind(p), lane = fresh(C.lane);
  if (which == 1) return (16 * C.wid + (lane >> 2)) * 3584 + (8 + ((C.cs - 8) >> 1)) * 128 + ((C.cs - 8) & 1) * 64 + ((lane & 3) << 4);
  const unsigned row = 16 * C.wid + (lane >> 3), sw = ((lane & 7) ^ (((lane >> 4) & 1) << 2)) << 4;
  if (kind == 1) return row * 3072 + (C.cs - 8) * 128 + sw;
  return row * 3584 + (p == 1 ? C.cs : 12 + C.cs) * 128 + sw;
}
template <int TYPE> DI void issue_stage(const Ctx& C, int s, int k, unsigned v0) {
  const int kind = G1<TYPE>::kind(s >> 2);
  const unsigned slot = G1<TYPE>::slot(s), r0 = 128 * (s & 3);
  if (kind == 1) {
    if (k < 2) dma16(C.sQKVw, v0, (r0 + 8 * k) * 3072, slot + C.wid * 2048 + k * 1024);
    else dma16(k == 3 ? C.sU : C.sG, k == 3 ? v0 ^ 32 : v0, r0 * 3584, slot + 16384 + (k - 2) * 8192 + C.wid * 1024);
  } else dma16(k >> 1 ? C.sU : C.sG, k >> 1 ? v0 ^ 32 : v0, (r0 + 8 * (k & 1)) * 3584, slot + (k >> 1) * 16384 + C.wid * 2048 + (k & 1) * 1024);
}
DI unsigned hx_voff(const Ctx& C) { const int lane = fresh(C.lane); return (16 * C.wid + (lane >> 3)) * 1024 + (((lane & 7) ^ ((lane >> 4) & 3)) << 4); }
DI unsigned hw_voff(const Ctx& C) { const int lane = fresh(C.lane);
  return (8 * C.wid + (lane >> 3)) * 3072 + (8 + C.cs) * 128 + (((lane & 7) ^ (((lane >> 4) & 1) << 2)) << 4); }
DI void issue_half(const Ctx& C, int h, int k, unsigned vx, unsigned vw) {
  const unsigned slot = (h & 3) * 32768;
  if (k < 2) dma16(C.sX, k ? (vx ^ 64) + 8 * 1024 : vx, h * 128, slot + C.wid * 2048 + k * 1024);
  else dma16(C.sQKVw, vw + (k - 2) * 128, h * 64 * 3072, slot + 16384 + (k - 2) * 8192 + C.wid * 1024);
}
DI void warm(i32x4 srd, unsigned line0, unsigned lines_per_row_log2, unsigned pitch, unsigned colbyte, int lane) {
  const unsigned l = line0 + lane;
  dma4(srd, (l >> lines_per_row_log2) * pitch + colbyte + (l & ((1u << lines_per_row_log2) - 1)) * 128, L_JUNK);
}
DI unsigned g_voff(const Ctx& C) { const int lane = fresh(C.lane); return (32 * (C.wid & 1) + (lane >> 3)) * 1024 + C.cg * 128 + (((lane & 7) ^ (((lane >> 4) & 1) << 2)) << 4); }
constexpr unsigned gslot(int j) { return j == 0 ? R1 : j == 1 ? R2 : j == 2 ? R0 : j == 3 ? R3 : j == 4 ? R1 : j == 5 ? R2 : j == 6 ? R0 : j == 7 ? R3 : R1; }
DI void issue_g(const Ctx& C, int j, int i, unsigned v) {
  const int kq = C.wid >> 1;
  const int row0 = j < 3 ? (3 * kq + j) * 64 : j < 7 ? (12 + 4 * kq + (j - 3)) * 64 : 128 * kq + 64 * (j - 7);
  dma16(j < 7 ? C.sWD : C.sWO, v, (unsigned)(row0 + 8 * i) * 1024, gslot(j) + kq * 8192 + (C.wid & 1) * 4096 + i * 1024);
}

template <int WHAT> DI void epi_qkv(const Ctx& C, const f32x16& acc, const f32x4 (&csr)[4], float m, float rinv, int qu, int h4) {
  const int h = C.lane >> 5;
  const float sc = WHAT == 0 ? rinv * QSCALE : rinv;
  float y[16];
#pragma unroll
  for (int i4 = 0; i4 < 4; ++i4)
#pragma unroll
    for (int e = 0; e < 4; ++e) y[4 * i4 + e] = (acc[4 * i4 + e] - csr[i4][e]) * sc;
  const unsigned base = (unsigned)WS_QKV + (((C.rb * 24 + qu) * 4 + C.mg) * 4 + h4) * 1024;
#pragma unroll
  for (int hh = 0; hh < 2; ++hh) {
    u32x4 f; f[0] = cvtpk(y[8 * hh], y[8 * hh + 1]); f[1] = cvtpk(y[8 * hh + 2], y[8 * hh + 3]); f[2] = cvtpk(y[8 * hh + 4], y[8 * hh + 5]); f[3] = cvtpk(y[8 * hh + 6], y[8 * hh + 7]);
    unsigned voff = C.lane * 16;
    if (WHAT == 2) {
      auto a = __builtin_amdgcn_permlane32_swap(f[0], f[2], false, false); auto b = __builtin_amdgcn_permlane32_swap(f[1], f[3], false, false);
      f[0] = a[0]; f[2] = a[1]; f[1] = b[0]; f[3] = b[1];
      voff = (C.lane & 31) * 32 + h * 16;
    }
    st16_sc1(f, C.sWS, voff, base + hh * 1024);
  }
}
DI void epi_gu(const Ctx& C, const f32x16& acc, const f32x4 (&csr)[4], float m, float r2, unsigned a2off) {
  float y[8];
#pragma unroll
  for (int i4 = 0; i4 < 2; ++i4)
#pragma unroll
    for (int e = 0; e < 4; ++e) y[4 * i4 + e] = ((acc[4 * i4 + e] - csr[i4][e]) * r2) * (acc[8 + 4 * i4 + e] - csr[2 + i4][e]);
  u32x4 f; f[0] = cvtpk(y[0], y[1]); f[1] = cvtpk(y[2], y[3]); f[2] = cvtpk(y[4], y[5]); f[3] = cvtpk(y[6], y[7]);
  st16_sc1(f, C.sWS, C.lane * 16, a2off);
}
template <int TYPE> DI unsigned a2_pass(const Ctx& C, int p) {
  if (p == 2) return (unsigned)WS_A2 + (((4 * C.rb + C.mg) * 4 + (C.cs >> 2)) * 36 + 4 * (3 + (C.cs & 3)) + 2 * C.ng) * 1024;
  if (TYPE == 0) return (unsigned)WS_A2 + (((4 * C.rb + C.mg) * 4 + C.cs / 3) * 36 + 4 * (C.cs % 3) + 2 * C.ng) * 1024;
  const int u = 8 + ((C.cs - 8) >> 1), half = (C.cs - 8) & 1;
  return (unsigned)WS_A2 + (((4 * C.rb + C.mg) * 4 + u / 3) * 36 + 4 * (u % 3) + 2 * half + C.ng) * 1024;
}
DI unsigned a2_o(const Ctx& C) { return (unsigned)WS_A2 + (((4 * C.rb + C.mg) * 4 + (C.cs >> 2)) * 36 + 28 + 2 * (C.cs & 3) + C.ng) * 1024; }

template <int TYPE> DI void gemm1(Ctx& C, unsigned vx, unsigned vw, u32x4& qfrag, u32x2& poll2, unsigned& poll2off) {
  using G = G1<TYPE>;
  const int lane = C.lane;
  char* ws = C.ws;
  bf16x8 xa[32];
  bf16x8 wf[3][2];
  f32x16 acc[2];
  unsigned ta[2], sv = 0;
  ta[0] = pinv(tile_addr<TYPE>(C, 0, 0)); ta[1] = pinv(tile_addr<TYPE>(C, 0, 1));
  unsigned th[2];
  th[0] = ta[0] + 65536; th[1] = ta[1] + 65536;
  const unsigned r31 = fresh(lane) & 31, hh = fresh(lane) >> 5;
  const unsigned xlane = (32 * C.mg + r31) * 128 + ((hh ^ ((r31 >> 1) & 7)) << 4);
  f32x4 csr[2][4];
  float m = 0.f, rinv = 0.f;
  u32x2 pollv, gs; u32x4 gk[6], gc;
  unsigned polloff = 0, gsoff = 0, gcoff = 0; bool pollneed = false;
  const unsigned qoff = (unsigned)WS_QKV + (((C.rb * 24 + (C.cs >> 1)) * 4 + C.mg) * 4 + 2 * (C.cs & 1) + C.ng) * 1024;

  u32x2 ev; ld8_sc1(ev, C.sWS, (unsigned)WS_EPOCH);
  u32x4 srow, cl[4];
  ld16(srow, C.sXall, (8 * C.bid + C.wid) * 1024 + fresh(lane) * 16, 0);
  issue_half(C, 1, 0, vx, vw); issue_half(C, 1, 1, vx, vw); issue_half(C, 1, 2, vx, vw);
  { const int pan = C.bid >> 1, lf = fresh(lane);
    const unsigned pitch = pan < 24 ? 3072 : 3584, col = (pan < 24 ? pan : pan < 52 ? pan - 24 : pan - 52) * 128;
    i32x4 sp;
#pragma unroll
    for (int e = 0; e < 4; ++e) sp[e] = pan < 24 ? C.sQKVw[e] : pan < 52 ? C.sG[e] : C.sU[e];
    const unsigned v = (256 * (C.bid & 1) + 32 * C.wid + (lf >> 3)) * pitch + col + (lf & 7) * 16 + (pan < 80 ? 0u : 0x40000000u);
#pragma unroll
    for (int k = 0; k < 4; ++k) ld16(cl[k], sp, v, 8 * k * pitch); }
  STAMP(1);
  bar_vm<G::after(T_EPOCH, -1)>();
  STAMP(2);
  {
  pin(ev); pin(srow);
  { const u64 t = (((u64)ev[1] << 32) | ev[0]) * 6364136223846793005ull + 1442695040888963407ull;
    C.tag = ((u64)__builtin_amdgcn_readfirstlane((unsigned)(t >> 32)) << 32) | __builtin_amdgcn_readfirstlane((unsigned)t); }
  float xv[8];
#pragma unroll
  for (int e = 0; e < 4; ++e) { xv[2 * e] = bflo(srow[e]); xv[2 * e + 1] = bfhi(srow[e]); }
  const float mean = wave_sum(((xv[0] + xv[1]) + (xv[2] + xv[3])) + ((xv[4] + xv[5]) + (xv[6] + xv[7]))) * (1.f / 512.f);
  float q2 = 0.f;
#pragma unroll
  for (int e = 0; e < 8; ++e) { const float d = xv[e] - mean; q2 += d * d; }
  const float var = wave_sum(q2) * (1.f / 512.f);
  const float sd = __builtin_amdgcn_sqrtf(rbf(rbf(var) + 1e-5f));
  if (lane == 0) __hip_atomic_store((u64*)(ws + WS_GS + (8 * C.bid + C.wid) * 8), ((u64)(unsigned)C.tag << 32) | cvtpk(mean, sd), __ATOMIC_RELAXED, __HIP_MEMORY_SCOPE_AGENT);
  }
  auto read_x = [&](int h) {
    const unsigned base = (h & 3) * 32768 + xlane;
#pragma unroll
    for (int i = 0; i < 4; ++i) xa[4 * h + i] = ld128(base ^ (i << 5));
  };
  auto read_w = [&](int pos) {
    const int p = pos >> 5, s = pos >> 3, i = pos & 7;
    if ((pos & 31) == 0 && pos > 0) { ta[0] = pinv(tile_addr<TYPE>(C, p, 0)); ta[1] = pinv(tile_addr<TYPE>(C, p, 1)); th[0] = pinv(ta[0] + 65536); th[1] = pinv(ta[1] + 65536); }
    const unsigned sb = p == 0 ? ((pos >> 2) & 3) * 32768 : G::slot(s), ks = p == 0 ? (i & 3) : i;
    const unsigned b0 = (sb & 65536) ? th[0] : ta[0], b1 = (sb & 65536) ? th[1] : ta[1], so = sb & 65535;
    wf[pos % 3][0] = ldfrag_tr(b0 + so + ks * 2048, 512);
    if (G::nt(p) == 2) { if (G::kind(p) == 1) wf[pos % 3][1] = ldfrag_tr(b1 + so + ks * 1024, 256); else wf[pos % 3][1] = ldfrag_tr(b1 + so + ks * 2048, 512); }
  };
  read_x(0); read_w(0); read_w(1);
  static_for<0, 96>([&](auto ic) {
    constexpr int pos = decltype(ic)::value;
    constexpr int p = pos >> 5, s = pos >> 3, st = s & 3, i = pos & 7, NT = G::nt(p), bi = G::barat(pos);
    if constexpr (pos < 2) static_for<0, G::gsize(T_H0)>([&](auto kc) { issue_half(C, 2 + pos, decltype(kc)::value, vx, vw); });
    if constexpr (bi >= 0) {
      STAMP(10 + 2 * bi);
      if constexpr (bi == 0) bar_vm<G::mn(G::after(T_H0 + 1, 0), G::after(T_CSL, 0))>();
      else if constexpr (bi < 7) bar_vm<G::after(T_H0 + bi + 1, bi)>();
      else if constexpr (bi == 7) bar_vm<G::mn(G::after(T_S4, 7), G::after(T_GR, 7))>();
      else if constexpr (bi == 9) { bar_vm<G::mn(G::mn(G::after(T_S4 + 2, 9), G::after(T_E0, 9)), TYPE == 1 ? G::after(T_QP, 9) : 63)>(); publish(C, 0); }
      else if constexpr (bi == 12) bar_vm<G::mn(G::after(T_S4 + 5, 12), G::after(T_POLL, 12))>();
      else if constexpr (bi == 13) { static_assert(G::after(T_E0 + 1, 13) <= G::after(T_POISON, 13), ""); bar_vm<G::mn(G::after(T_S4 + 6, 13), G::after(T_E0 + 1, 13))>(); }
      else if constexpr (bi == 14) bar_vm<G::mn(G::after(T_S4 + 7, 14), G::after(T_KL, 14))>();
      else if constexpr (bi == 15) bar_vm<63>();
      else bar_vm<G::after(T_S4 + bi - 7, bi)>();
      if constexpr (bi == 0) {
      pin(cl[0]); pin(cl[1]); pin(cl[2]); pin(cl[3]);
      float cz[8];
#pragma unroll
      for (int e = 0; e < 4; ++e) { cz[2 * e] = (bflo(cl[0][e]) + bflo(cl[1][e])) + (bflo(cl[2][e]) + bflo(cl[3][e])); cz[2 * e + 1] = (bfhi(cl[0][e]) + bfhi(cl[1][e])) + (bfhi(cl[2][e]) + bfhi(cl[3][e])); }
#pragma unroll
      for (int e = 0; e < 8; ++e) cz[e] = swap_add(swap16_add(ror_add(cz[e], IC<0x128>{})));
      if (lane < 8) { *lp<f32x4>(L_CSP + C.wid * 256 + lane * 32) = f32x4{cz[0], cz[1], cz[2], cz[3]}; *lp<f32x4>(L_CSP + C.wid * 256 + lane * 32 + 16) = f32x4{cz[4], cz[5], cz[6], cz[7]}; }
      }
      if constexpr (bi == 1) {
        if (C.wid == 0 && C.bid < 160) {
          float t = 0.f;
#pragma unroll
          for (int w = 0; w < 8; ++w) t += *lp<float>(L_CSP + w * 256 + lane * 4);
          __hip_atomic_store((u64*)(ws + WS_GC + ((C.bid >> 1) * 64 + lane) * 16 + (C.bid & 1) * 8), ((u64)(unsigned)C.tag << 32) | __float_as_uint(t), __ATOMIC_RELAXED, __HIP_MEMORY_SCOPE_AGENT);
        }
      }
      if constexpr (bi == 7) {
        pin(gs); pin(gc);
        if (!__all(gs[1] == (unsigned)C.tag && gc[1] == (unsigned)C.tag && gc[3] == (unsigned)C.tag)) {
#pragma nounroll
          for (unsigned it = 0; it < SPIN_CAP; ++it) {
            __builtin_amdgcn_s_sleep(2);
            ld8_sc1(gs, C.sWS, gsoff); ld16_sc1(gc, C.sWS, gcoff, 0); wait_vm<0>(); pin(gs); pin(gc);
            if (__all(gs[1] == (unsigned)C.tag && gc[1] == (unsigned)C.tag && gc[3] == (unsigned)C.tag)) break;
            if (it == SPIN_CAP - 1) give_up(ws);
          }
        }
        m = bflo(gs[0]); rinv = __builtin_amdgcn_rcpf(bfhi(gs[0]));
        *lp<float>(L_CS + (C.wid < 6 ? C.wid : 5) * 256 + lane * 4) = __uint_as_float(gc[0]) + __uint_as_float(gc[2]);
      }
      if constexpr (bi == 12) {
        check_flags(ws, polloff, pollneed, C.tag, pollv);
        const int u = C.cs >> 1, h4 = 2 * (C.cs & 1) + C.ng;
#pragma unroll
        for (int b = 0; b < 6; ++b) { const int rbp = C.rb - 2 + b % 3 < 0 ? 0 : C.rb - 2 + b % 3;
          ld16_sc1(gk[b], C.sWS, lane * 16, (unsigned)WS_QKV + (((rbp * 24 + (b < 3 ? 8 : 16) + u) * 4 + C.mg) * 4 + h4) * 1024); }
      }
      if constexpr (bi == 13) { publish(C, 1); ld16_sc1(qfrag, C.sWS, lane * 16, qoff); }
      if constexpr (bi == 14) {
#pragma unroll
        for (int b = 0; b < 6; ++b) pin(gk[b]);
#pragma unroll
        for (int b = 0; b < 3; ++b) { *lp<u32x4>(L_K + ((4 * b + C.mg) * 2 + C.ng) * 1024 + lane * 16) = gk[b];
                                      *lp<u32x4>(L_V + C.ng * 12288 + (4 * b + C.mg) * 1024 + lane * 16) = gk[3 + b]; }
        { const int ln = fresh(lane), l = ln < 10 ? ln : 0, uu = 3 * C.kq + (l >> 1);
          poll2off = flag_off(1, C.rb2, l < 6 ? (uu < 8 ? uu : 8 + 2 * (uu - 8) + (l & 1)) : 4 * C.kq + (l - 6)); }
        ld8_sc1(poll2, C.sWS, poll2off);
      }
      if constexpr (bi < 7) read_x(bi + 1);
      STAMP(11 + 2 * bi);
      if constexpr (bi == 11) C.prtx = __builtin_amdgcn_s_memrealtime();
    }
    static_for<0, 16>([&](auto bc) {
      constexpr int bb = decltype(bc)::value, tg = G::grp(bb), k0 = pos - G::barpos(bb);
      static_for<0, 4>([&](auto kc) {
        constexpr int k = decltype(kc)::value;
        if constexpr (k < G::gsize(tg) && G::rel(bb, k) == k0) {
          if constexpr (tg < T_S4) issue_half(C, tg, k, vx, vw);
          else if constexpr (tg >= T_G0) { if constexpr (k == 0) sv = g_voff(C); issue_g(C, tg - T_G0, k, sv); }
          else { constexpr int pp_ = (tg - T_S4 + 4) >> 2;
            if constexpr (k == 0) sv = stage_voff<TYPE>(C, pp_, 0);
            if constexpr (k == 2 && G::kind(pp_) == 1) sv = stage_voff<TYPE>(C, pp_, 1);
            issue_stage<TYPE>(C, tg - T_S4 + 4, k, sv); }
        }
      });
    });
    if constexpr (pos == 32 && TYPE == 1) {
      const u32x4 pz = {POISON, POISON, POISON, POISON};
      const unsigned a = (unsigned)WS_QKV + (((C.rb * 24 + (C.cs - 8)) * 4 + C.mg) * 4 + 2 * C.ng) * 1024;
      st16_sc1(pz, C.sWS, lane * 16, a); st16_sc1(pz, C.sWS, lane * 16, a + 1024);
    }
    if constexpr (pos == 22) {
      const int lf = fresh(lane), w = C.wid < 6 ? C.wid : 5;
      const int guh = 8 + ((C.cs - 8) >> 1);
      const int pan = w == 0 ? 8 + C.cs : w >= 4 ? (w == 4 ? 24 : 52) + 12 + C.cs
                    : TYPE == 0 ? (w == 1 ? 24 + C.cs : 52 + C.cs) : (w == 1 ? C.cs - 8 : w == 2 ? 24 + guh : 52 + guh);
      gsoff = (unsigned)WS_GS + (C.rb * 128 + 32 * C.mg + (lf & 31)) * 8; gcoff = (unsigned)WS_GC + (pan * 64 + lf) * 16;
      ld8_sc1(gs, C.sWS, gsoff); ld16_sc1(gc, C.sWS, gcoff, 0);
    }
    if constexpr (pos == 58) {
      const int u = C.cs >> 1, l = fresh(lane) & 7;
      const int rbp = C.rb - 2 + l % 3;
      pollneed = l < 6 && rbp >= 0;
      polloff = flag_off(0, pollneed ? rbp : C.rb, l < 3 ? u : 8 + u);
      ld8_sc1(pollv, C.sWS, polloff);
    }
    if constexpr (pos == 50) {
      const u32x4 pz = {POISON, POISON, POISON, POISON};
      const unsigned a = a2_pass<TYPE>(C, 2);
      st16_sc1(pz, C.sWS, lane * 16, a);
      st16_sc1(pz, C.sWS, lane * 16, a + 1024);
      st16_sc1(pz, C.sWS, lane * 16, a2_o(C));
    }
    if constexpr (pos + 2 < 96) read_w(pos + 2);
    if constexpr ((pos & 31) == 0) { acc[0] = MFMA32(wf[pos % 3][0], xa[pos & 31], f32x16{}); if (NT == 2) acc[1] = MFMA32(wf[pos % 3][1], xa[pos & 31], f32x16{}); }
    else { acc[0] = MFMA32(wf[pos % 3][0], xa[pos & 31], acc[0]); if (NT == 2) acc[1] = MFMA32(wf[pos % 3][1], xa[pos & 31], acc[1]); }
    if constexpr (st == 3 && i == 7) {
      if constexpr (p == 0) asm volatile("s_waitcnt lgkmcnt(0)\n\ts_barrier" ::: "memory");
      static_for<0, NT>([&](auto tc) { constexpr int t = decltype(tc)::value;
        const bool gu = G::kind(p) == 2 || (G::kind(p) == 1 && t == 1);
        const int tl = G::kind(p) == 2 ? 2 * C.ng + t : 2 * ((C.cs - 8) & 1) + C.ng;
        const unsigned s0 = L_CS + (p == 0 ? 0 : p == 2 ? 4 : (G::kind(p) == 1 ? 1 + t : 1)) * 256;
#pragma unroll
        for (int i4 = 0; i4 < 4; ++i4) {
          const unsigned a = gu ? s0 + (i4 >> 1) * 256 + (16 * tl + 8 * (i4 & 1) + 4 * hh) * 4 : s0 + (32 * C.ng + 8 * i4 + 4 * hh) * 4;
          csr[t][i4] = *lp<f32x4>(a) * m; pin(csr[t][i4]); } });
    }
    SB();
    if constexpr ((pos & 31) == 31) {
      if constexpr (G::kind(p) == 2) {
        const unsigned a2 = a2_pass<TYPE>(C, p);
        epi_gu(C, acc[0], csr[0], m, rinv * rinv, a2); epi_gu(C, acc[1], csr[1], m, rinv * rinv, a2 + 1024);
      } else if constexpr (G::kind(p) == 1) {
        epi_qkv<0>(C, acc[0], csr[0], m, rinv, C.cs - 8, 2 * C.ng);
        epi_gu(C, acc[1], csr[1], m, rinv * rinv, a2_pass<TYPE>(C, p));
      } else {
        epi_qkv<1 + TYPE>(C, acc[0], csr[0], m, rinv, 8 + C.cs, 2 * C.ng);
      }
      SB();
    }
  });
  static_assert(G::after(T_POLL2, 16) == 10, "attention counts on this");
  wait_vm<G::after(T_QL, 16)>();
  pin(qfrag);
  { auto dirty = [&]() { return __any(__builtin_elementwise_max(__builtin_elementwise_max(qfrag[0], qfrag[1]), __builtin_elementwise_max(qfrag[2], qfrag[3])) == POISON); };
    if (dirty()) {
#pragma nounroll
      for (unsigned it = 0; it < SPIN_CAP; ++it) {
        __builtin_amdgcn_s_sleep(2);
        ld16_sc1(qfrag, C.sWS, lane * 16, qoff); wait_vm<0>(); pin(qfrag);
        if (!dirty()) break;
        if (it == SPIN_CAP - 1) give_up(ws);
      } } }
}

template <bool EDGE, class SIDE> DI u32x4 attention(Ctx& C, const u32x4& qraw, SIDE&& side) {
  const int lane = C.lane, h = lane >> 5, g = (lane >> 4) & 1, q = (lane & 15) >> 2, pp = lane & 3;
  const bf16x8 qf = __builtin_bit_cast(bf16x8, qraw);
  const unsigned kaddr = pinv(L_K + (C.mg * 2 + C.ng) * 1024 + lane * 16);
  const int t0 = EDGE ? (C.rb == 0 ? 8 : 4) - C.mg : 1;
  const int dq = (lane & 31) - 4 * h;
  const float NEG = -1e30f;
  auto older = [&](int r) { return ((r & 3) + 8 * (r >> 2)) > dq; };
  auto max16 = [&](float& a, float& b, const f32x16& s) {
#pragma unroll
    for (int r = 0; r < 16; r += 4) { a = __builtin_fmaxf(__builtin_fmaxf(a, s[r]), s[r + 1]); b = __builtin_fmaxf(__builtin_fmaxf(b, s[r + 2]), s[r + 3]); } };
  bf16x8 kf[9];
#pragma unroll
  for (int t = 0; t < 9; ++t) if (!EDGE || t > 0) kf[t] = ld128(kaddr + t * 2048);
  float mx;
  {
    f32x16 S[3];
    f32x16 sl = MFMA32(kf[8], qf, f32x16{});
    f32x16 sf;
    if (!EDGE) sf = MFMA32(kf[0], qf, f32x16{});
    S[1] = MFMA32(kf[1], qf, f32x16{});
    S[2] = MFMA32(kf[2], qf, f32x16{});
    float a = NEG, b = NEG;
    side(std::integral_constant<int, 0>{});
    SB();
#pragma unroll
    for (int r = 0; r < 16; ++r) sl[r] = older(r) ? (EDGE ? NEG : sf[r]) : sl[r];
    max16(a, b, sl);
    static_for<1, 8>([&](auto tc) { constexpr int t = decltype(tc)::value;
      if constexpr (t + 2 < 8) S[(t + 2) % 3] = MFMA32(kf[t + 2], qf, f32x16{});
      side(std::integral_constant<int, t>{});
      SB();
      if (!EDGE || t >= t0) max16(a, b, S[t % 3]);
      SB(); });
    mx = swap_max(__builtin_fmaxf(a, b));
  }
  f32x16 negm;
#pragma unroll
  for (int r = 0; r < 16; ++r) negm[r] = -mx;
  const unsigned vbase = pinv(g == 0 ? L_V + C.ng * 12288 + C.mg * 1024 + (4 * h + q) * 32 + pp * 8 : L_ONES + 128 * (1 - h) + q * 32 + pp * 8);
  const unsigned vstr = g == 0 ? 1024u : 0u;
  f32x16 o;
  auto expack = [&](const f32x16& s, u32x4& pa, u32x4& pb) {
#pragma unroll
    for (int i = 0; i < 4; ++i) pa[i] = cvtpk(__builtin_amdgcn_exp2f(s[2 * i]), __builtin_amdgcn_exp2f(s[2 * i + 1]));
#pragma unroll
    for (int i = 0; i < 4; ++i) pb[i] = cvtpk(__builtin_amdgcn_exp2f(s[8 + 2 * i]), __builtin_amdgcn_exp2f(s[9 + 2 * i])); };
  f32x16 S[2];
  bf16x8 vf[2][2];
  {
    f32x16 sl = MFMA32(kf[8], qf, negm);
    f32x16 sf;
    if (!EDGE) sf = MFMA32(kf[0], qf, negm);
    S[1] = MFMA32(kf[1], qf, negm);
    const unsigned v8 = vbase + 8 * vstr;
    const bf16x8 va = ldfrag_tr(v8, 256), vb = ldfrag_tr(v8 + 512, 256);
    bf16x8 vc, vd;
    if (!EDGE) { vc = ldfrag_tr(vbase, 256); vd = ldfrag_tr(vbase + 512, 256); }
    vf[1][0] = ldfrag_tr(vbase + vstr, 256); vf[1][1] = ldfrag_tr(vbase + vstr + 512, 256);
    side(std::integral_constant<int, 8>{});
    SB();
#pragma unroll
    for (int r = 0; r < 16; ++r) sl[r] = older(r) ? (EDGE ? NEG : sf[r]) : sl[r];
    u32x4 pa, pb, mk0, mk1;
    expack(sl, pa, pb);
#pragma unroll
    for (int i = 0; i < 4; ++i) {
      mk0[i] = (older(2 * i) ? 0xffffu : 0u) | (older(2 * i + 1) ? 0xffff0000u : 0u);
      mk1[i] = (older(8 + 2 * i) ? 0xffffu : 0u) | (older(9 + 2 * i) ? 0xffff0000u : 0u); }
    o = MFMA32(va, __builtin_bit_cast(bf16x8, pa & ~mk0), f32x16{});
    o = MFMA32(vb, __builtin_bit_cast(bf16x8, pb & ~mk1), o);
    if (!EDGE) {
      o = MFMA32(vc, __builtin_bit_cast(bf16x8, pa & mk0), o);
      o = MFMA32(vd, __builtin_bit_cast(bf16x8, pb & mk1), o);
    }
    SB();
  }
  static_for<1, 8>([&](auto tc) { constexpr int t = decltype(tc)::value;
    if constexpr (t + 1 < 8) {
      const unsigned vt = vbase + (t + 1) * vstr;
      vf[(t + 1) & 1][0] = ldfrag_tr(vt, 256); vf[(t + 1) & 1][1] = ldfrag_tr(vt + 512, 256);
      S[(t + 1) & 1] = MFMA32(kf[t + 1], qf, negm); }
    side(std::integral_constant<int, 8 + t>{});
    SB();
    if (!EDGE || t >= t0) {
      u32x4 pa, pb;
      expack(S[t & 1], pa, pb);
      o = MFMA32(vf[t & 1][0], __builtin_bit_cast(bf16x8, pa), o);
      o = MFMA32(vf[t & 1][1], __builtin_bit_cast(bf16x8, pb), o);
    }
    SB(); });
  const float li = __builtin_amdgcn_rcpf(o[8]);
  u32x4 f; f[0] = cvtpk(o[0] * li, o[1] * li); f[1] = cvtpk(o[2] * li, o[3] * li); f[2] = cvtpk(o[4] * li, o[5] * li); f[3] = cvtpk(o[6] * li, o[7] * li);
  return f;
}

__global__ void __launch_bounds__(512, 2)
fused_kernel(const u16* __restrict__ x, const u16* __restrict__ wqkv, const u16* __restrict__ wo, const u16* __restrict__ wg, const u16* __restrict__ wu,
             const u16* __restrict__ wd, u16* __restrict__ out, char* __restrict__ ws) {
  Ctx C;
  const int tid = threadIdx.x, lane = tid & 63, wid = __builtin_amdgcn_readfirstlane(tid >> 6);
  const int b = blockIdx.x, xj = b & 7, xi = b >> 3;
  C.stampreg = 0;
  STAMP(0);
  C.prt0 = __builtin_amdgcn_s_memrealtime(); C.prtx = C.prt0;
  asm volatile("" :: "s"(x), "s"(wqkv), "s"(wo), "s"(wg), "s"(wu), "s"(wd), "s"(out), "s"(ws));
  C.ws = ws; C.lane = lane; C.wid = wid; C.mg = wid & 3; C.ng = wid >> 2;
  C.rb = 8 * (xj & 1) + (xi & 7); C.cs = 4 * (xj >> 1) + (xi >> 3); C.bid = b;
  C.sX = mk_srd(x + (size_t)C.rb * 128 * 512, 128 * 1024);
  C.sQKVw = mk_srd(wqkv, 512 * 3072);
  C.xi = xi;
  const unsigned vx = hx_voff(C), vw = hw_voff(C);
  STAMP(3);
  issue_half(C, 0, 0, vx, vw); issue_half(C, 0, 1, vx, vw); issue_half(C, 0, 2, vx, vw);
  STAMP(4);
  STAMP(5);
  C.sWS = mk_srd(ws, 32u << 20); C.sXall = mk_srd(x, 2048 * 1024);
  C.sG = mk_srd(wg, 512 * 3584); C.sU = mk_srd(wu, 512 * 3584); C.sWO = mk_srd(wo, 512 * 1024); C.sWD = mk_srd(wd, 1792 * 1024);
  if (wid == 7) { u64 pc; asm volatile("s_getpc_b64 %0" : "=s"(pc));
    dma4(mk_srd((const void*)(pc & ~255ull), CODE_BYTES), (xi * 9 + (lane < 9 ? lane : 0)) * 128, L_JUNK); }
  if (tid < 256) lp<unsigned>(L_ONES)[tid] = 0x3f803f80u;
  const int rt64 = 4 * xj + (xi & 3), cg = xi >> 2;
  C.cg = cg;
  const int rt = wid & 1, kq = wid >> 1, RT = 2 * rt64 + rt, rb2 = RT >> 2;
  C.kq = kq; C.rb2 = rb2;
  u32x4 qfrag; u32x2 poll2; unsigned poll2off;
  if (C.cs < 8) gemm1<0>(C, vx, vw, qfrag, poll2, poll2off); else gemm1<1>(C, vx, vw, qfrag, poll2, poll2off);
  STAMP(6);

  const unsigned a2base = (unsigned)WS_A2 + (RT * 4 + kq) * 36 * 1024;
  u32x4 a2[36];
  auto side = [&](auto kc) { constexpr int k = decltype(kc)::value;
    if constexpr (k == 1) { wait_vm<10>(); check_flags(ws, poll2off, lane < 10, C.tag, poll2); }
    if constexpr (k >= 1 && k < 7) { ld16_sc1(a2[2 * k - 2], C.sWS, lane * 16, a2base + (2 * k - 2) * 1024); ld16_sc1(a2[2 * k - 1], C.sWS, lane * 16, a2base + (2 * k - 1) * 1024); }
    if constexpr (k >= 12) static_for<0, 4>([&](auto jc) { constexpr int q_ = 12 + 4 * (k - 12) + decltype(jc)::value; ld16_sc1(a2[q_], C.sWS, lane * 16, a2base + q_ * 1024); });
    if constexpr (k >= 7 && k < 13 && !NO_WARM) { const unsigned l = (xi * 6 + (k - 7)) * 64 + wid * 8 + (fresh(lane) & 7);
      dma4(l < 8192 ? C.sWD : C.sWO, (l < 8192 ? l + 768 * 8 : l - 8192) * 128, L_JUNK); } };
  u32x4 of;
  if (C.rb >= 2) of = attention<false>(C, qfrag, side); else of = attention<true>(C, qfrag, side);
  st16_sc1(of, C.sWS, lane * 16, a2_o(C));
  STAMP(7);

  const int g = (lane >> 4) & 1, h = lane >> 5, q = (lane & 15) >> 2, pp = lane & 3;
  const unsigned w2l0 = kq * 8192 + (4 * h + q) * 128 + ((0 ^ (q >> 1)) << 6) + g * 32 + pp * 8;
  const unsigned w2l1 = kq * 8192 + (4 * h + q) * 128 + ((1 ^ (q >> 1)) << 6) + g * 32 + pp * 8;
  bf16x8 wf[3][2];
  f32x16 acc[2];
  u32x4 xres;
  unsigned wb[2], gv;
  auto read_w2 = [&](int pos) {
    if ((pos & 3) == 0) { wb[0] = pinv(w2l0 + gslot(pos >> 2)); wb[1] = pinv(w2l1 + gslot(pos >> 2)); }
    wf[pos % 3][0] = ldfrag_tr(wb[0] + (pos & 3) * 2048, 1024); wf[pos % 3][1] = ldfrag_tr(wb[1] + (pos & 3) * 2048, 1024); };
  bar_vm<17 + (NO_WARM ? 0 : 6)>();
  static_for<0, 12>([&](auto kc) { constexpr int k = decltype(kc)::value; pin(a2[k]); });
  read_w2(0); read_w2(1);
  gv = g_voff(C);
  STAMP(8);
  u32x4 ef[4];
  static_for<0, 36>([&](auto ic) {
    constexpr int pos = decltype(ic)::value;
    constexpr int j = pos >> 2, i = pos & 3;
    if constexpr (i == 2) {
      STAMP(44 + 2 * j);
      if constexpr (j <= 1) bar_vm<63>();
      if constexpr (j == 2) bar_vm<6>();
      if constexpr (j == 3) bar_vm<6>();
      if constexpr (j == 4) bar_vm<7>();
      if constexpr (j == 5) bar_vm<15>();
      if constexpr (j == 6) bar_vm<4>();
      if constexpr (j == 7) bar_vm<0>();
      STAMP(45 + 2 * j);
    }
    if constexpr (pos == 10) { static_for<12, 28>([&](auto kc) { pin(a2[decltype(kc)::value]); }); check_frags<12, 28>(C, a2, a2base); }
    if constexpr (pos == 26) { static_for<28, 36>([&](auto kc) { pin(a2[decltype(kc)::value]); }); pin(xres); check_frags<28, 36>(C, a2, a2base); }
    if constexpr (pos == 16) { ld16(xres, C.sXall, (RT * 32 + (lane & 31)) * 1024 + cg * 128 + kq * 32 + h * 16, 0); }
    if constexpr (pos == 18 || pos == 19) static_for<0, 4>([&](auto kc) { constexpr int k = 28 + 4 * (pos - 18) + decltype(kc)::value; ld16_sc1(a2[k], C.sWS, lane * 16, a2base + k * 1024); });
    if constexpr (pos < 24) issue_g(C, 3 + (pos >> 2), pos & 3, gv);
    if constexpr (pos == 30) { if (b == 0 && wid == 7) {
#pragma unroll
        for (int k = 0; k < 4; ++k) { u32x2 t; ld8_sc1(t, C.sWS, flag_off(0, 0, 0) + (lane * 4 + k) * 128); ef[k][0] = t[0]; ef[k][1] = t[1]; } } }
    if constexpr (pos + 2 < 36) read_w2(pos + 2);
    const bf16x8 af = __builtin_bit_cast(bf16x8, a2[pos]);
    if constexpr (pos == 0) { acc[0] = MFMA32(wf[0][0], af, f32x16{}); acc[1] = MFMA32(wf[0][1], af, f32x16{}); }
    else { acc[0] = MFMA32(wf[pos % 3][0], af, acc[0]); acc[1] = MFMA32(wf[pos % 3][1], af, acc[1]); }
    SB();
  });
  STAMP(9);
#pragma unroll
  for (int d = 0; d < 4; ++d) if (d != kq) {
    const int ct = d >> 1, r0 = (d & 1) * 8;
    const unsigned a = R2 + ((rt * 4 + d) * 4 + kq) * 2048 + lane * 16;
    *lp<f32x4>(a) = f32x4{acc[ct][r0], acc[ct][r0 + 1], acc[ct][r0 + 2], acc[ct][r0 + 3]};
    *lp<f32x4>(a + 1024) = f32x4{acc[ct][r0 + 4], acc[ct][r0 + 5], acc[ct][r0 + 6], acc[ct][r0 + 7]};
  }
  float s[8];
#pragma unroll
  for (int e = 0; e < 8; ++e) { const float lo = (kq & 1) ? acc[0][8 + e] : acc[0][e], hi = (kq & 1) ? acc[1][8 + e] : acc[1][e]; s[e] = (kq & 2) ? hi : lo; }
  asm volatile("s_waitcnt lgkmcnt(0)\n\ts_barrier" ::: "memory");
#pragma unroll
  for (int d = 0; d < 4; ++d) if (d != kq) {
    const unsigned a = R2 + ((rt * 4 + kq) * 4 + d) * 2048 + lane * 16;
    const f32x4 u = *lp<f32x4>(a), v = *lp<f32x4>(a + 1024);
#pragma unroll
    for (int e = 0; e < 4; ++e) { s[e] += u[e]; s[4 + e] += v[e]; }
  }
  {
    unsigned u0 = __float_as_uint(s[0]), u1 = __float_as_uint(s[1]), u2 = __float_as_uint(s[2]), u3 = __float_as_uint(s[3]);
    unsigned v0 = __float_as_uint(s[4]), v1 = __float_as_uint(s[5]), v2 = __float_as_uint(s[6]), v3 = __float_as_uint(s[7]);
    auto r0 = __builtin_amdgcn_permlane32_swap(u0, v0, false, false); auto r1 = __builtin_amdgcn_permlane32_swap(u1, v1, false, false);
    auto r2 = __builtin_amdgcn_permlane32_swap(u2, v2, false, false); auto r3 = __builtin_amdgcn_permlane32_swap(u3, v3, false, false);
    const float c0 = __uint_as_float(r0[0]), c1 = __uint_as_float(r1[0]), c2 = __uint_as_float(r2[0]), c3 = __uint_as_float(r3[0]);
    const float c4 = __uint_as_float(r0[1]), c5 = __uint_as_float(r1[1]), c6 = __uint_as_float(r2[1]), c7 = __uint_as_float(r3[1]);
    u32x4 f;
    f[0] = cvtpk(c0 + bflo(xres[0]), c1 + bfhi(xres[0])); f[1] = cvtpk(c2 + bflo(xres[1]), c3 + bfhi(xres[1]));
    f[2] = cvtpk(c4 + bflo(xres[2]), c5 + bfhi(xres[2])); f[3] = cvtpk(c6 + bflo(xres[3]), c7 + bfhi(xres[3]));
    const i32x4 sO = mk_srd(out, 2048 * 1024);
    st16(f, sO, (RT * 32 + (lane & 31)) * 1024 + cg * 128 + kq * 32 + h * 16, 0);
  }
  if (b == 0 && wid == 7) {
    wait_vm<0>();
    asm volatile("" : "+v"(ef[0]), "+v"(ef[1]), "+v"(ef[2]), "+v"(ef[3]));
    bool ok = true;
#pragma unroll
    for (int k = 0; k < 4; ++k) ok = ok && ef[k][0] == (unsigned)C.tag && ef[k][1] == (unsigned)(C.tag >> 32);
    if (!__all(ok)) {
#pragma unroll 1
      for (int k = 0; k < 4; ++k) spin_flags(ws, flag_off(0, 0, 0) + (lane * 4 + k) * 128, true, C.tag);
    }
    if (lane == 0) __hip_atomic_store((u64*)(ws + WS_EPOCH), C.tag, __ATOMIC_RELAXED, __HIP_MEMORY_SCOPE_AGENT);
  }
  STAMP(60);
  if (b == 100 && wid == 0) { const u64 e = __builtin_amdgcn_s_memrealtime(); const u64 until = e + 2 * (C.prtx - C.prt0); while (__builtin_amdgcn_s_memrealtime() < until) __builtin_amdgcn_s_sleep(1); }
}

extern "C" void kernel_launch(void* const* d_in, const int* in_sizes, int n_in, void* d_out, int out_size, void* d_ws, size_t ws_size, hipStream_t stream) {
  (void)in_sizes; (void)n_in; (void)out_size; (void)ws_size;
  (void)hipFuncSetAttribute((const void*)fused_kernel, hipFuncAttributeMaxDynamicSharedMemorySize, LDS_BYTES);
  fused_kernel<<<256, 512, LDS_BYTES, stream>>>((const u16*)d_in[0], (const u16*)d_in[1], (const u16*)d_in[2], (const u16*)d_in[4], (const u16*)d_in[5], (const u16*)d_in[6],
                                                 (u16*)d_out, (char*)d_ws);
}
```

```cpp
#include <hip/hip_runtime.h>
#include <stdint.h>
#include <type_traits>

typedef unsigned short u16;
typedef unsigned long long u64;
typedef short bf16x8 __attribute__((ext_vector_type(8)));
typedef short s16x4 __attribute__((ext_vector_type(4)));
typedef float f32x2 __attribute__((ext_vector_type(2)));
typedef float f32x4 __attribute__((ext_vector_type(4)));
typedef float f32x16 __attribute__((ext_vector_type(16)));
typedef unsigned u32x2 __attribute__((ext_vector_type(2)));
typedef unsigned u32x4 __attribute__((ext_vector_type(4)));
typedef int i32x4 __attribute__((ext_vector_type(4)));
#define LDSP __attribute__((address_space(3)))
#define DI __device__ __forceinline__
template <class T> DI LDSP T* lp(unsigned a) { return (LDSP T*)(size_t)a; }
template <int I, int N, class F> DI void static_for(F&& f) { if constexpr (I < N) { f(std::integral_constant<int, I>{}); static_for<I + 1, N>(f); } }

constexpr size_t WS_EPOCH = 0;
constexpr size_t WS_ERR   = 64;
constexpr size_t WS_FLAGS = 4096;
constexpr size_t WS_GS    = 512u << 10;
constexpr size_t WS_GC    = 576u << 10;
constexpr size_t WS_QKV   = 1u << 20;
constexpr size_t WS_A2    = 8u << 20;
constexpr size_t WS_DBG   = 24u << 20;

constexpr unsigned R0 = 0, R1 = 32768, R2 = 65536, R3 = 98304;
constexpr unsigned L_K = 98304, L_V = 122880;
constexpr unsigned L_ONES = 147456;
constexpr unsigned L_CSP = 148480;
constexpr unsigned L_CS = 150528;
constexpr unsigned L_JUNK = 154624;
constexpr unsigned LDS_BYTES = 163840;
constexpr unsigned CODE_BYTES = 36864;
constexpr unsigned POISON = 0xffffffffu;
constexpr unsigned SPIN_CAP = 1u << 13;

#define NO_WARM 0
#define NO_CS 0
#define NO_DMA 0
constexpr float QSCALE = 0.25f * 1.4426950408889634f;

DI i32x4 mk_srd(const void* p, unsigned bytes) { u64 a = (u64)p; i32x4 r; r[0] = (int)(unsigned)a; r[1] = (int)((unsigned)(a >> 32) & 0xffffu); r[2] = (int)bytes; r[3] = 0x00020000; return r; }
DI void dma16(i32x4 srd, unsigned voff, unsigned soff, unsigned lds) {
  if (NO_DMA) return;
  asm volatile("s_mov_b32 m0, %3\n\ts_nop 3\n\tbuffer_load_dwordx4 %0, %1, %2 offen lds" :: "v"(voff), "s"(srd), "s"(soff), "s"(lds) : "memory");
}
DI void dma4(i32x4 srd, unsigned voff, unsigned lds) {
  asm volatile("s_mov_b32 m0, %2\n\ts_nop 3\n\tbuffer_load_dword %0, %1, 0 offen lds" :: "v"(voff), "s"(srd), "s"(lds) : "memory");
}
DI void ld16_sc1(u32x4& d, i32x4 srd, unsigned voff, unsigned soff) {
  asm volatile("s_nop 4\n\tbuffer_load_dwordx4 %0, %1, %2, %3 offen sc1" : "=v"(d) : "v"(voff), "s"(srd), "s"(soff) : "memory");
}
DI void ld16(u32x4& d, i32x4 srd, unsigned voff, unsigned soff) {
  asm volatile("s_nop 4\n\tbuffer_load_dwordx4 %0, %1, %2, %3 offen" : "=v"(d) : "v"(voff), "s"(srd), "s"(soff) : "memory");
}
DI void ld8_sc1(u32x2& d, i32x4 srd, unsigned voff) {
  asm volatile("s_nop 4\n\tbuffer_load_dwordx2 %0, %1, %2, 0 offen sc1" : "=v"(d) : "v"(voff), "s"(srd) : "memory");
}
DI void st16_sc1(u32x4 d, i32x4 srd, unsigned voff, unsigned soff) {
  asm volatile("s_nop 4\n\tbuffer_store_dwordx4 %0, %1, %2, %3 offen sc1\n\ts_nop 1" :: "v"(d), "v"(voff), "s"(srd), "s"(soff) : "memory");
}
DI void st16(u32x4 d, i32x4 srd, unsigned voff, unsigned soff) {
  asm volatile("s_nop 4\n\tbuffer_store_dwordx4 %0, %1, %2, %3 offen\n\ts_nop 1" :: "v"(d), "v"(voff), "s"(srd), "s"(soff) : "memory");
}
template <int N> DI void bar_vm() { static_assert(N >= 0 && N < 64, "vmcnt range"); asm volatile("s_waitcnt vmcnt(%0) lgkmcnt(0)\n\ts_barrier" :: "n"(N) : "memory"); }
template <int N> DI void wait_vm() { static_assert(N >= 0 && N < 64, "vmcnt range"); asm volatile("s_waitcnt vmcnt(%0)" :: "n"(N) : "memory"); }
DI s16x4 ldtr(unsigned a) { return __builtin_amdgcn_ds_read_tr16_b64_v4i16(lp<s16x4>(a)); }
DI bf16x8 ldfrag_tr(unsigned a, unsigned second) {
  s16x4 lo = ldtr(a), hi = ldtr(a + second);
  return __builtin_shufflevector(lo, hi, 0, 1, 2, 3, 4, 5, 6, 7);
}
DI bf16x8 ld128(unsigned a) { return *lp<bf16x8>(a); }
DI unsigned cvtpk(float lo, float hi) { typedef __bf16 b2 __attribute__((ext_vector_type(2))); f32x2 v = {lo, hi}; return __builtin_bit_cast(unsigned, __builtin_convertvector(v, b2)); }
DI float bflo(unsigned v) { return __uint_as_float(v << 16); }
DI float bfhi(unsigned v) { return __uint_as_float(v & 0xffff0000u); }
DI float rbf(float f) { return bflo(cvtpk(f, f) & 0xffffu); }
DI int fresh(int v) { asm volatile("" : "+v"(v)); return v; }
template <class T> DI void pin(T& v) { asm volatile("" : "+v"(v)); }
DI unsigned pinv(unsigned v) { asm volatile("" : "+v"(v)); return v; }
#define MFMA32(a, b, c) __builtin_amdgcn_mfma_f32_32x32x16_bf16((a), (b), (c), 0, 0, 0)
#define MFMA16(a, b, c) __builtin_amdgcn_mfma_f32_16x16x32_bf16((a), (b), (c), 0, 0, 0)
#define MFMA4(a, b, c) __builtin_amdgcn_mfma_f32_4x4x4bf16_1k((a), (b), (c), 0, 0, 0)
#define SB() __builtin_amdgcn_sched_barrier(0)
DI float swap_add(float v) { auto r = __builtin_amdgcn_permlane32_swap(__float_as_uint(v), __float_as_uint(v), false, false); return __uint_as_float(r[0]) + __uint_as_float(r[1]); }
template <class T> DI float ror_add(float v, T) { return v + __int_as_float(__builtin_amdgcn_update_dpp(0, __float_as_int(v), T::value, 0xf, 0xf, true)); }
DI float swap16_add(float v) { auto r = __builtin_amdgcn_permlane16_swap(__float_as_uint(v), __float_as_uint(v), false, false); return __uint_as_float(r[0]) + __uint_as_float(r[1]); }
template <int C> using IC = std::integral_constant<int, C>;
DI float wave_sum(float v) { v = ror_add(v, IC<0x121>{}); v = ror_add(v, IC<0x122>{}); v = ror_add(v, IC<0x124>{}); v = ror_add(v, IC<0x128>{}); return swap_add(swap16_add(v)); }
DI float swap_max(float v) { auto r = __builtin_amdgcn_permlane32_swap(__float_as_uint(v), __float_as_uint(v), false, false); return __builtin_fmaxf(__uint_as_float(r[0]), __uint_as_float(r[1])); }

#define STAMP(i) do { } while (0)

struct Ctx {
  char* ws; u64 tag;
  i32x4 sX, sXall, sQKVw, sG, sU, sWO, sWD, sWS;
  int lane, wid, mg, ng, rb, cs, cg, xi, kq, rb2, bid; unsigned stampreg;
};

DI unsigned flag_off(int ev, int rb, int cs) { return (unsigned)(WS_FLAGS + (((ev * 16 + rb) * 16 + cs) << 7)); }
DI void publish(const Ctx& C, int ev) { if (C.wid == 0 && C.lane == 0) __hip_atomic_store((u64*)(C.ws + flag_off(ev, C.rb, C.cs)), C.tag, __ATOMIC_RELAXED, __HIP_MEMORY_SCOPE_AGENT); }
DI void give_up(char* ws) { __hip_atomic_store((unsigned*)(ws + WS_ERR), 1u, __ATOMIC_RELAXED, __HIP_MEMORY_SCOPE_AGENT); }
DI void spin_flags(char* ws, unsigned off, bool need, u64 tag) {
#pragma nounroll
  for (unsigned it = 0; it < SPIN_CAP; ++it) {
    u64 v = __hip_atomic_load((u64*)(ws + off), __ATOMIC_RELAXED, __HIP_MEMORY_SCOPE_AGENT);
    if (__all(!need || v == tag)) return;
    __builtin_amdgcn_s_sleep(2);
  }
  give_up(ws);
}
DI void check_flags(char* ws, unsigned off, bool need, u64 tag, u32x2& got) {
  asm volatile("" : "+v"(got));
  bool ok = !need || (got[0] == (unsigned)tag && got[1] == (unsigned)(tag >> 32));
  if (!__all(ok)) spin_flags(ws, off, need, tag);
}
template <int K0, int K1> DI void check_frags(const Ctx& C, u32x4 (&a2)[36], unsigned base) {
  auto dirty = [&]() { unsigned mx = 0;
    static_for<K0, K1>([&](auto kc) { constexpr int k = decltype(kc)::value;
      mx = __builtin_elementwise_max(mx, __builtin_elementwise_max(__builtin_elementwise_max(a2[k][0], a2[k][1]), __builtin_elementwise_max(a2[k][2], a2[k][3]))); });
    return __any(mx == POISON); };
  if (dirty()) {
#pragma nounroll
    for (unsigned it = 0; it < SPIN_CAP; ++it) {
      __builtin_amdgcn_s_sleep(2);
      static_for<K0, K1>([&](auto kc) { constexpr int k = decltype(kc)::value; ld16_sc1(a2[k], C.sWS, C.lane * 16, base + k * 1024); });
      wait_vm<0>();
      static_for<K0, K1>([&](auto kc) { constexpr int k = decltype(kc)::value; pin(a2[k]); });
      if (!dirty()) return;
    }
    give_up(C.ws);
  }
}

enum { T_H0 = 0, T_S4 = 8, T_E0 = 16, T_G0 = 19, T_POISON = 22, T_POLL, T_KL, T_POLL2, T_QL, T_QP, T_EPOCH, T_CSL, T_GR, T_WARM, T_NONE };
template <int TYPE> struct G1 {
  static constexpr int kind(int p) { return p == 0 ? 0 : (p == 1 && TYPE == 1) ? 1 : 2; }
  static constexpr int nt(int p) { return p == 0 ? 1 : 2; }
  static constexpr int nst(int p) { return kind(p) == 1 ? 3 : 2; }
  static constexpr int npoison() { return 3; }
  static constexpr unsigned slot(int s) {
    return s == 4 ? R0 : s == 5 ? R1 : s == 6 ? R2 : s == 7 ? R3 : s == 8 ? R0 : s == 9 ? R1 : s == 10 ? R2 : R0; }
  static constexpr int touchat(int pos) {
    return pos == 2 ? T_S4 : pos == 6 ? T_S4 + 1 : pos == 10 ? T_S4 + 2 : pos == 14 ? T_S4 + 3 : pos == 22 ? T_S4 + 4 : pos == 30 ? T_S4 + 5 : pos == 40 ? T_S4 + 6
         : pos == 52 ? T_S4 + 7 : pos == 62 ? T_G0 : pos == 70 ? T_G0 + 1 : pos == 78 ? T_G0 + 2 : T_NONE; }
  static constexpr int ntouch(int t) { return t == T_NONE ? 0 : (t >= T_S4 && t < T_E0 && kind((t - T_S4 + 4) >> 2) == 1) ? 2 : 1; }
  static constexpr int barpos(int bi) { return bi < 8 ? 4 * bi + 2 : 8 * (bi - 4) + 6; }
  static constexpr int barat(int pos) { return pos < 32 ? ((pos & 3) == 2 ? pos >> 2 : -1) : ((pos & 7) == 6 ? 4 + (pos >> 3) : -1); }
  static constexpr int grp(int bi) { return bi < 4 ? T_H0 + bi + 4 : bi < 11 ? T_S4 + (bi - 4) : bi == 11 ? T_NONE : bi == 12 ? T_S4 + 7 : T_G0 + (bi - 13); }
  static constexpr int gsize(int t) { return t == T_NONE ? 0 : t < T_S4 ? 3 : 4; }
  static constexpr int rel(int bi, int k) { return bi < 8 ? k : bi == 15 ? (k >> 1) : 2 * k; }
  static constexpr int after(int t, int bi) {
    int cnt = -1;
    auto see = [&](int tag, int n) { for (int i = 0; i < n; ++i) { if (tag == t) cnt = 0; else if (cnt >= 0) ++cnt; } };
    see(T_H0, gsize(T_H0)); see(T_EPOCH, 2); see(T_H0 + 1, gsize(T_H0 + 1)); see(T_WARM, NO_WARM ? 0 : 2); see(T_CSL, 4);
    if (bi == -1) return cnt;
    for (int pos = 0; pos < 96; ++pos) {
      if (pos < 2) see(T_H0 + 2 + pos, gsize(T_H0 + 2 + pos));
      const int b = barat(pos);
      if (b >= 0) {
        if (b == bi) return cnt;
        if (b == 12) see(T_KL, 6);
        if (b == 13) see(T_QL, 1);
        if (b == 14) see(T_POLL2, 1);
      }
      for (int bb = 0; bb < 16; ++bb) for (int k = 0; k < gsize(grp(bb)); ++k) if (barpos(bb) + rel(bb, k) == pos) see(grp(bb), 1);
      if (!NO_WARM) see(T_WARM, ntouch(touchat(pos)));
      if (pos == 32 && TYPE == 1) see(T_QP, 2);
      if (pos == 22) see(T_GR, 2);
      if (pos == 58) see(T_POLL, 1);
      if (pos == 50) see(T_POISON, npoison());
      if ((pos & 31) == 31) see(T_E0 + (pos >> 5), nst(pos >> 5));
    }
    return cnt;
  }
  static constexpr int mn(int a, int b) { return a < b ? a : b; }
};

template <int TYPE> DI unsigned tile_addr(const Ctx& C, int p, int t) {
  const int lane = fresh(C.lane), kind = G1<TYPE>::kind(p);
  const int g = (lane >> 4) & 1, h = lane >> 5, q = (lane & 15) >> 2, pp = lane & 3;
  const unsigned rdrow = (8 * h + q) * 128 + pp * 8;
  if (kind == 2) return g * 16384 + rdrow + (((2 * C.ng + t) ^ (2 * (q >> 1)) ^ g) << 5);
  if (kind == 0) return 16384 + rdrow + ((C.ng ^ (q >> 1)) << 6) + g * 32;
  if (t == 0) return rdrow + ((C.ng ^ (q >> 1)) << 6) + g * 32;
  return 16384 + g * 8192 + (8 * h + q) * 64 + pp * 8 + ((C.ng ^ g) << 5);
}
template <int TYPE> DI unsigned stage_voff(const Ctx& C, int p, int which) {
  const int kind = G1<TYPE>::kind(p), lane = fresh(C.lane);
  if (which == 1) return (16 * C.wid + (lane >> 2)) * 3584 + (8 + ((C.cs - 8) >> 1)) * 128 + ((C.cs - 8) & 1) * 64 + ((lane & 3) << 4);
  const unsigned row = 16 * C.wid + (lane >> 3), sw = ((lane & 7) ^ (((lane >> 4) & 1) << 2)) << 4;
  if (kind == 1) return row * 3072 + (C.cs - 8) * 128 + sw;
  return row * 3584 + (p == 1 ? C.cs : 12 + C.cs) * 128 + sw;
}
template <int TYPE> DI void issue_stage(const Ctx& C, int s, int k, unsigned v0) {
  const int kind = G1<TYPE>::kind(s >> 2);
  const unsigned slot = G1<TYPE>::slot(s), r0 = 128 * (s & 3);
  if (kind == 1) {
    if (k < 2) dma16(C.sQKVw, v0, (r0 + 8 * k) * 3072, slot + C.wid * 2048 + k * 1024);
    else dma16(k == 3 ? C.sU : C.sG, k == 3 ? v0 ^ 32 : v0, r0 * 3584, slot + 16384 + (k - 2) * 8192 + C.wid * 1024);
  } else dma16(k >> 1 ? C.sU : C.sG, k >> 1 ? v0 ^ 32 : v0, (r0 + 8 * (k & 1)) * 3584, slot + (k >> 1) * 16384 + C.wid * 2048 + (k & 1) * 1024);
}
DI unsigned hx_voff(const Ctx& C) { const int lane = fresh(C.lane); return (16 * C.wid + (lane >> 3)) * 1024 + (((lane & 7) ^ ((lane >> 4) & 3)) << 4); }
DI unsigned hw_voff(const Ctx& C) { const int lane = fresh(C.lane);
  return (8 * C.wid + (lane >> 3)) * 3072 + (8 + C.cs) * 128 + (((lane & 7) ^ (((lane >> 4) & 1) << 2)) << 4); }
DI void issue_half(const Ctx& C, int h, int k, unsigned vx, unsigned vw) {
  const unsigned slot = (h & 3) * 32768;
  if (k < 2) dma16(C.sX, k ? (vx ^ 64) + 8 * 1024 : vx, h * 128, slot + C.wid * 2048 + k * 1024);
  else dma16(C.sQKVw, vw + (k - 2) * 128, h * 64 * 3072, slot + 16384 + (k - 2) * 8192 + C.wid * 1024);
}
DI void warm(i32x4 srd, unsigned line0, unsigned lines_per_row_log2, unsigned pitch, unsigned colbyte, int lane) {
  const unsigned l = line0 + lane;
  dma4(srd, (l >> lines_per_row_log2) * pitch + colbyte + (l & ((1u << lines_per_row_log2) - 1)) * 128, L_JUNK);
}
DI unsigned g_voff(const Ctx& C) { const int lane = fresh(C.lane); return (32 * (C.wid & 1) + (lane >> 3)) * 1024 + C.cg * 128 + (((lane & 7) ^ (((lane >> 4) & 1) << 2)) << 4); }
constexpr unsigned gslot(int j) { return j == 0 ? R1 : j == 1 ? R2 : j == 2 ? R0 : j == 3 ? R3 : j == 4 ? R1 : j == 5 ? R2 : j == 6 ? R0 : j == 7 ? R3 : R1; }
DI void issue_g(const Ctx& C, int j, int i, unsigned v) {
  const int kq = C.wid >> 1;
  const int row0 = j < 3 ? (3 * kq + j) * 64 : j < 7 ? (12 + 4 * kq + (j - 3)) * 64 : 128 * kq + 64 * (j - 7);
  dma16(j < 7 ? C.sWD : C.sWO, v, (unsigned)(row0 + 8 * i) * 1024, gslot(j) + kq * 8192 + (C.wid & 1) * 4096 + i * 1024);
}

DI i32x4 sel_srd(bool c, const i32x4& a, const i32x4& b) { i32x4 r;
#pragma unroll
  for (int e = 0; e < 4; ++e) r[e] = c ? a[e] : b[e];
  return r; }
template <int TYPE> DI void touch_stage(const Ctx& C, int s) {
  const int p = s >> 2, kind = G1<TYPE>::kind(p), lf = fresh(C.lane);
  const unsigned r0 = 128 * (s & 3), gu = kind == 1 ? 8 + ((C.cs - 8) >> 1) : (p == 1 ? C.cs : 12 + C.cs);
  dma4(sel_srd(C.rb & 4, C.sU, C.sG), (r0 + (C.rb & 3) * 32 + C.wid * 4 + (lf & 3)) * 3584 + gu * 128, L_JUNK);
  if (kind == 1) dma4(C.sQKVw, (r0 + (C.rb & 7) * 16 + C.wid * 2 + (lf & 1)) * 3072 + (C.cs - 8) * 128, L_JUNK);
}
DI void touch_g(const Ctx& C, int j) {
  const int kq = C.xi & 3;
  const int row0 = j < 3 ? (3 * kq + j) * 64 : j < 7 ? (12 + 4 * kq + (j - 3)) * 64 : 128 * kq + 64 * (j - 7);
  dma4(j < 7 ? C.sWD : C.sWO, (row0 + C.wid * 8 + (fresh(C.lane) & 7)) * 1024 + C.cg * 128, L_JUNK);
}
template <int WHAT> DI void epi_qkv(const Ctx& C, const f32x16& acc, const f32x4 (&csr)[4], float m, float rinv, int qu, int h4) {
  const int h = C.lane >> 5;
  const float sc = WHAT == 0 ? rinv * QSCALE : rinv;
  float y[16];
#pragma unroll
  for (int i4 = 0; i4 < 4; ++i4)
#pragma unroll
    for (int e = 0; e < 4; ++e) y[4 * i4 + e] = (acc[4 * i4 + e] - csr[i4][e]) * sc;
  const unsigned base = (unsigned)WS_QKV + (((C.rb * 24 + qu) * 4 + C.mg) * 4 + h4) * 1024;
#pragma unroll
  for (int hh = 0; hh < 2; ++hh) {
    u32x4 f; f[0] = cvtpk(y[8 * hh], y[8 * hh + 1]); f[1] = cvtpk(y[8 * hh + 2], y[8 * hh + 3]); f[2] = cvtpk(y[8 * hh + 4], y[8 * hh + 5]); f[3] = cvtpk(y[8 * hh + 6], y[8 * hh + 7]);
    unsigned voff = C.lane * 16;
    if (WHAT == 2) {
      auto a = __builtin_amdgcn_permlane32_swap(f[0], f[2], false, false); auto b = __builtin_amdgcn_permlane32_swap(f[1], f[3], false, false);
      f[0] = a[0]; f[2] = a[1]; f[1] = b[0]; f[3] = b[1];
      voff = (C.lane & 31) * 32 + h * 16;
    }
    st16_sc1(f, C.sWS, voff, base + hh * 1024);
  }
}
DI void epi_gu(const Ctx& C, const f32x16& acc, const f32x4 (&csr)[4], float m, float r2, unsigned a2off) {
  float y[8];
#pragma unroll
  for (int i4 = 0; i4 < 2; ++i4)
#pragma unroll
    for (int e = 0; e < 4; ++e) y[4 * i4 + e] = ((acc[4 * i4 + e] - csr[i4][e]) * r2) * (acc[8 + 4 * i4 + e] - csr[2 + i4][e]);
  u32x4 f; f[0] = cvtpk(y[0], y[1]); f[1] = cvtpk(y[2], y[3]); f[2] = cvtpk(y[4], y[5]); f[3] = cvtpk(y[6], y[7]);
  st16_sc1(f, C.sWS, C.lane * 16, a2off);
}
template <int TYPE> DI unsigned a2_pass(const Ctx& C, int p) {
  if (p == 2) return (unsigned)WS_A2 + (((4 * C.rb + C.mg) * 4 + (C.cs >> 2)) * 36 + 4 * (3 + (C.cs & 3)) + 2 * C.ng) * 1024;
  if (TYPE == 0) return (unsigned)WS_A2 + (((4 * C.rb + C.mg) * 4 + C.cs / 3) * 36 + 4 * (C.cs % 3) + 2 * C.ng) * 1024;
  const int u = 8 + ((C.cs - 8) >> 1), half = (C.cs - 8) & 1;
  return (unsigned)WS_A2 + (((4 * C.rb + C.mg) * 4 + u / 3) * 36 + 4 * (u % 3) + 2 * half + C.ng) * 1024;
}
DI unsigned a2_o(const Ctx& C) { return (unsigned)WS_A2 + (((4 * C.rb + C.mg) * 4 + (C.cs >> 2)) * 36 + 28 + 2 * (C.cs & 3) + C.ng) * 1024; }

template <int TYPE> DI void gemm1(Ctx& C, unsigned vx, unsigned vw, u32x4& qfrag, u32x2& poll2, unsigned& poll2off) {
  using G = G1<TYPE>;
  const int lane = C.lane;
  char* ws = C.ws;
  bf16x8 xa[32];
  bf16x8 wf[3][2];
  f32x16 acc[2];
  unsigned ta[2], sv = 0;
  ta[0] = pinv(tile_addr<TYPE>(C, 0, 0)); ta[1] = pinv(tile_addr<TYPE>(C, 0, 1));
  unsigned th[2];
  th[0] = ta[0] + 65536; th[1] = ta[1] + 65536;
  const unsigned r31 = fresh(lane) & 31, hh = fresh(lane) >> 5;
  const unsigned xlane = (32 * C.mg + r31) * 128 + ((hh ^ ((r31 >> 1) & 7)) << 4);
  f32x4 csr[2][4];
  float m = 0.f, rinv = 0.f;
  u32x2 pollv, gs; u32x4 gk[6], gc;
  unsigned polloff = 0, gsoff = 0, gcoff = 0; bool pollneed = false;
  const unsigned qoff = (unsigned)WS_QKV + (((C.rb * 24 + (C.cs >> 1)) * 4 + C.mg) * 4 + 2 * (C.cs & 1) + C.ng) * 1024;

  u32x2 ev; ld8_sc1(ev, C.sWS, (unsigned)WS_EPOCH);
  u32x4 srow, cl[4];
  ld16(srow, C.sXall, (8 * C.bid + C.wid) * 1024 + fresh(lane) * 16, 0);
  issue_half(C, 1, 0, vx, vw); issue_half(C, 1, 1, vx, vw); issue_half(C, 1, 2, vx, vw);
  if (!NO_WARM) {
    const int lf = fresh(lane);
    const unsigned ix = (C.cs & 3) * 192 + C.wid * 24 + (lf < 24 ? lf : 23);
    dma4(C.sX, (ix / 6) * 1024 + (2 + ix % 6) * 128, L_JUNK);
    dma4(C.sQKVw, (128 + (C.rb & 7) * 48 + C.wid * 6 + (lf < 6 ? lf : 5)) * 3072 + (8 + C.cs) * 128, L_JUNK);
  }
  { const int pan = C.bid >> 1, lf = fresh(lane);
    const unsigned pitch = pan < 24 ? 3072 : 3584, col = (pan < 24 ? pan : pan < 52 ? pan - 24 : pan - 52) * 128;
    i32x4 sp;
#pragma unroll
    for (int e = 0; e < 4; ++e) sp[e] = pan < 24 ? C.sQKVw[e] : pan < 52 ? C.sG[e] : C.sU[e];
    const unsigned v = (256 * (C.bid & 1) + 32 * C.wid + (lf >> 3)) * pitch + col + (lf & 7) * 16 + (pan < 80 ? 0u : 0x40000000u);
#pragma unroll
    for (int k = 0; k < 4; ++k) ld16(cl[k], sp, v, 8 * k * pitch); }
  STAMP(1);
  bar_vm<G::after(T_EPOCH, -1)>();
  STAMP(2);
  {
  pin(ev); pin(srow);
  { const u64 t = (((u64)ev[1] << 32) | ev[0]) * 6364136223846793005ull + 1442695040888963407ull;
    C.tag = ((u64)__builtin_amdgcn_readfirstlane((unsigned)(t >> 32)) << 32) | __builtin_amdgcn_readfirstlane((unsigned)t); }
  float xv[8];
#pragma unroll
  for (int e = 0; e < 4; ++e) { xv[2 * e] = bflo(srow[e]); xv[2 * e + 1] = bfhi(srow[e]); }
  const float mean = wave_sum(((xv[0] + xv[1]) + (xv[2] + xv[3])) + ((xv[4] + xv[5]) + (xv[6] + xv[7]))) * (1.f / 512.f);
  float q2 = 0.f;
#pragma unroll
  for (int e = 0; e < 8; ++e) { const float d = xv[e] - mean; q2 += d * d; }
  const float var = wave_sum(q2) * (1.f / 512.f);
  const float sd = __builtin_amdgcn_sqrtf(rbf(rbf(var) + 1e-5f));
  if (lane == 0) __hip_atomic_store((u64*)(ws + WS_GS + (8 * C.bid + C.wid) * 8), ((u64)(unsigned)C.tag << 32) | cvtpk(mean, sd), __ATOMIC_RELAXED, __HIP_MEMORY_SCOPE_AGENT);
  }
  auto read_x = [&](int h) {
    const unsigned base = (h & 3) * 32768 + xlane;
#pragma unroll
    for (int i = 0; i < 4; ++i) xa[4 * h + i] = ld128(base ^ (i << 5));
  };
  auto read_w = [&](int pos) {
    const int p = pos >> 5, s = pos >> 3, i = pos & 7;
    if ((pos & 31) == 0 && pos > 0) { ta[0] = pinv(tile_addr<TYPE>(C, p, 0)); ta[1] = pinv(tile_addr<TYPE>(C, p, 1)); th[0] = pinv(ta[0] + 65536); th[1] = pinv(ta[1] + 65536); }
    const unsigned sb = p == 0 ? ((pos >> 2) & 3) * 32768 : G::slot(s), ks = p == 0 ? (i & 3) : i;
    const unsigned b0 = (sb & 65536) ? th[0] : ta[0], b1 = (sb & 65536) ? th[1] : ta[1], so = sb & 65535;
    wf[pos % 3][0] = ldfrag_tr(b0 + so + ks * 2048, 512);
    if (G::nt(p) == 2) { if (G::kind(p) == 1) wf[pos % 3][1] = ldfrag_tr(b1 + so + ks * 1024, 256); else wf[pos % 3][1] = ldfrag_tr(b1 + so + ks * 2048, 512); }
  };
  read_x(0); read_w(0); read_w(1);
  static_for<0, 96>([&](auto ic) {
    constexpr int pos = decltype(ic)::value;
    constexpr int p = pos >> 5, s = pos >> 3, st = s & 3, i = pos & 7, NT = G::nt(p), bi = G::barat(pos);
    if constexpr (pos < 2) static_for<0, G::gsize(T_H0)>([&](auto kc) { issue_half(C, 2 + pos, decltype(kc)::value, vx, vw); });
    if constexpr (bi >= 0) {
      STAMP(10 + 2 * bi);
      if constexpr (bi == 0) bar_vm<G::mn(G::after(T_H0 + 1, 0), G::after(T_CSL, 0))>();
      else if constexpr (bi < 7) bar_vm<G::after(T_H0 + bi + 1, bi)>();
      else if constexpr (bi == 7) bar_vm<G::mn(G::after(T_S4, 7), G::after(T_GR, 7))>();
      else if constexpr (bi == 9) { bar_vm<G::mn(G::mn(G::after(T_S4 + 2, 9), G::after(T_E0, 9)), TYPE == 1 ? G::after(T_QP, 9) : 63)>(); publish(C, 0); }
      else if constexpr (bi == 12) bar_vm<G::mn(G::after(T_S4 + 5, 12), G::after(T_POLL, 12))>();
      else if constexpr (bi == 13) { static_assert(G::after(T_E0 + 1, 13) <= G::after(T_POISON, 13), ""); bar_vm<G::mn(G::after(T_S4 + 6, 13), G::after(T_E0 + 1, 13))>(); }
      else if constexpr (bi == 14) bar_vm<G::mn(G::after(T_S4 + 7, 14), G::after(T_KL, 14))>();
      else if constexpr (bi == 15) bar_vm<63>();
      else bar_vm<G::after(T_S4 + bi - 7, bi)>();
      if constexpr (bi == 0) {
      pin(cl[0]); pin(cl[1]); pin(cl[2]); pin(cl[3]);
      float cz[8];
#pragma unroll
      for (int e = 0; e < 4; ++e) { cz[2 * e] = (bflo(cl[0][e]) + bflo(cl[1][e])) + (bflo(cl[2][e]) + bflo(cl[3][e])); cz[2 * e + 1] = (bfhi(cl[0][e]) + bfhi(cl[1][e])) + (bfhi(cl[2][e]) + bfhi(cl[3][e])); }
#pragma unroll
      for (int e = 0; e < 8; ++e) cz[e] = swap_add(swap16_add(ror_add(cz[e], IC<0x128>{})));
      if (lane < 8) { *lp<f32x4>(L_CSP + C.wid * 256 + lane * 32) = f32x4{cz[0], cz[1], cz[2], cz[3]}; *lp<f32x4>(L_CSP + C.wid * 256 + lane * 32 + 16) = f32x4{cz[4], cz[5], cz[6], cz[7]}; }
      }
      if constexpr (bi == 1) {
        if (C.wid == 0 && C.bid < 160) {
          float t = 0.f;
#pragma unroll
          for (int w = 0; w < 8; ++w) t += *lp<float>(L_CSP + w * 256 + lane * 4);
          __hip_atomic_store((u64*)(ws + WS_GC + ((C.bid >> 1) * 64 + lane) * 16 + (C.bid & 1) * 8), ((u64)(unsigned)C.tag << 32) | __float_as_uint(t), __ATOMIC_RELAXED, __HIP_MEMORY_SCOPE_AGENT);
        }
      }
      if constexpr (bi == 7) {
        pin(gs); pin(gc);
        if (!__all(gs[1] == (unsigned)C.tag && gc[1] == (unsigned)C.tag && gc[3] == (unsigned)C.tag)) {
#pragma nounroll
          for (unsigned it = 0; it < SPIN_CAP; ++it) {
            __builtin_amdgcn_s_sleep(2);
            ld8_sc1(gs, C.sWS, gsoff); ld16_sc1(gc, C.sWS, gcoff, 0); wait_vm<0>(); pin(gs); pin(gc);
            if (__all(gs[1] == (unsigned)C.tag && gc[1] == (unsigned)C.tag && gc[3] == (unsigned)C.tag)) break;
            if (it == SPIN_CAP - 1) give_up(ws);
          }
        }
        m = bflo(gs[0]); rinv = __builtin_amdgcn_rcpf(bfhi(gs[0]));
        *lp<float>(L_CS + (C.wid < 6 ? C.wid : 5) * 256 + lane * 4) = __uint_as_float(gc[0]) + __uint_as_float(gc[2]);
      }
      if constexpr (bi == 12) {
        check_flags(ws, polloff, pollneed, C.tag, pollv);
        const int u = C.cs >> 1, h4 = 2 * (C.cs & 1) + C.ng;
#pragma unroll
        for (int b = 0; b < 6; ++b) { const int rbp = C.rb - 2 + b % 3 < 0 ? 0 : C.rb - 2 + b % 3;
          ld16_sc1(gk[b], C.sWS, lane * 16, (unsigned)WS_QKV + (((rbp * 24 + (b < 3 ? 8 : 16) + u) * 4 + C.mg) * 4 + h4) * 1024); }
      }
      if constexpr (bi == 13) { publish(C, 1); ld16_sc1(qfrag, C.sWS, lane * 16, qoff); }
      if constexpr (bi == 14) {
#pragma unroll
        for (int b = 0; b < 6; ++b) pin(gk[b]);
#pragma unroll
        for (int b = 0; b < 3; ++b) { *lp<u32x4>(L_K + ((4 * b + C.mg) * 2 + C.ng) * 1024 + lane * 16) = gk[b];
                                      *lp<u32x4>(L_V + C.ng * 12288 + (4 * b + C.mg) * 1024 + lane * 16) = gk[3 + b]; }
        { const int ln = fresh(lane), l = ln < 10 ? ln : 0, uu = 3 * C.kq + (l >> 1);
          poll2off = flag_off(1, C.rb2, l < 6 ? (uu < 8 ? uu : 8 + 2 * (uu - 8) + (l & 1)) : 4 * C.kq + (l - 6)); }
        ld8_sc1(poll2, C.sWS, poll2off);
      }
      if constexpr (bi < 7) read_x(bi + 1);
      STAMP(11 + 2 * bi);
    }
    static_for<0, 16>([&](auto bc) {
      constexpr int bb = decltype(bc)::value, tg = G::grp(bb), k0 = pos - G::barpos(bb);
      static_for<0, 4>([&](auto kc) {
        constexpr int k = decltype(kc)::value;
        if constexpr (k < G::gsize(tg) && G::rel(bb, k) == k0) {
          if constexpr (tg < T_S4) issue_half(C, tg, k, vx, vw);
          else if constexpr (tg >= T_G0) { if constexpr (k == 0) sv = g_voff(C); issue_g(C, tg - T_G0, k, sv); }
          else { constexpr int pp_ = (tg - T_S4 + 4) >> 2;
            if constexpr (k == 0) sv = stage_voff<TYPE>(C, pp_, 0);
            if constexpr (k == 2 && G::kind(pp_) == 1) sv = stage_voff<TYPE>(C, pp_, 1);
            issue_stage<TYPE>(C, tg - T_S4 + 4, k, sv); }
        }
      });
    });
    if constexpr (G::touchat(pos) != T_NONE && !NO_WARM) {
      if constexpr (G::touchat(pos) >= T_G0) touch_g(C, G::touchat(pos) - T_G0); else touch_stage<TYPE>(C, G::touchat(pos) - T_S4 + 4);
    }
    if constexpr (pos == 32 && TYPE == 1) {
      const u32x4 pz = {POISON, POISON, POISON, POISON};
      const unsigned a = (unsigned)WS_QKV + (((C.rb * 24 + (C.cs - 8)) * 4 + C.mg) * 4 + 2 * C.ng) * 1024;
      st16_sc1(pz, C.sWS, lane * 16, a); st16_sc1(pz, C.sWS, lane * 16, a + 1024);
    }
    if constexpr (pos == 22) {
      const int lf = fresh(lane), w = C.wid < 6 ? C.wid : 5;
      const int guh = 8 + ((C.cs - 8) >> 1);
      const int pan = w == 0 ? 8 + C.cs : w >= 4 ? (w == 4 ? 24 : 52) + 12 + C.cs
                    : TYPE == 0 ? (w == 1 ? 24 + C.cs : 52 + C.cs) : (w == 1 ? C.cs - 8 : w == 2 ? 24 + guh : 52 + guh);
      gsoff = (unsigned)WS_GS + (C.rb * 128 + 32 * C.mg + (lf & 31)) * 8; gcoff = (unsigned)WS_GC + (pan * 64 + lf) * 16;
      ld8_sc1(gs, C.sWS, gsoff); ld16_sc1(gc, C.sWS, gcoff, 0);
    }
    if constexpr (pos == 58) {
      const int u = C.cs >> 1, l = fresh(lane) & 7;
      const int rbp = C.rb - 2 + l % 3;
      pollneed = l < 6 && rbp >= 0;
      polloff = flag_off(0, pollneed ? rbp : C.rb, l < 3 ? u : 8 + u);
      ld8_sc1(pollv, C.sWS, polloff);
    }
    if constexpr (pos == 50) {
      const u32x4 pz = {POISON, POISON, POISON, POISON};
      const unsigned a = a2_pass<TYPE>(C, 2);
      st16_sc1(pz, C.sWS, lane * 16, a);
      st16_sc1(pz, C.sWS, lane * 16, a + 1024);
      st16_sc1(pz, C.sWS, lane * 16, a2_o(C));
    }
    if constexpr (pos + 2 < 96) read_w(pos + 2);
    if constexpr ((pos & 31) == 0) { acc[0] = MFMA32(wf[pos % 3][0], xa[pos & 31], f32x16{}); if (NT == 2) acc[1] = MFMA32(wf[pos % 3][1], xa[pos & 31], f32x16{}); }
    else { acc[0] = MFMA32(wf[pos % 3][0], xa[pos & 31], acc[0]); if (NT == 2) acc[1] = MFMA32(wf[pos % 3][1], xa[pos & 31], acc[1]); }
    if constexpr (st == 3 && i == 7) {
      if constexpr (p == 0) asm volatile("s_waitcnt lgkmcnt(0)\n\ts_barrier" ::: "memory");
      static_for<0, NT>([&](auto tc) { constexpr int t = decltype(tc)::value;
        const bool gu = G::kind(p) == 2 || (G::kind(p) == 1 && t == 1);
        const int tl = G::kind(p) == 2 ? 2 * C.ng + t : 2 * ((C.cs - 8) & 1) + C.ng;
        const unsigned s0 = L_CS + (p == 0 ? 0 : p == 2 ? 4 : (G::kind(p) == 1 ? 1 + t : 1)) * 256;
#pragma unroll
        for (int i4 = 0; i4 < 4; ++i4) {
          const unsigned a = gu ? s0 + (i4 >> 1) * 256 + (16 * tl + 8 * (i4 & 1) + 4 * hh) * 4 : s0 + (32 * C.ng + 8 * i4 + 4 * hh) * 4;
          csr[t][i4] = *lp<f32x4>(a) * m; pin(csr[t][i4]); } });
    }
    SB();
    if constexpr ((pos & 31) == 31) {
      if constexpr (G::kind(p) == 2) {
        const unsigned a2 = a2_pass<TYPE>(C, p);
        epi_gu(C, acc[0], csr[0], m, rinv * rinv, a2); epi_gu(C, acc[1], csr[1], m, rinv * rinv, a2 + 1024);
      } else if constexpr (G::kind(p) == 1) {
        epi_qkv<0>(C, acc[0], csr[0], m, rinv, C.cs - 8, 2 * C.ng);
        epi_gu(C, acc[1], csr[1], m, rinv * rinv, a2_pass<TYPE>(C, p));
      } else {
        epi_qkv<1 + TYPE>(C, acc[0], csr[0], m, rinv, 8 + C.cs, 2 * C.ng);
      }
      SB();
    }
  });
  static_assert(G::after(T_POLL2, 16) == 10, "attention counts on this");
  wait_vm<G::after(T_QL, 16)>();
  pin(qfrag);
  { auto dirty = [&]() { return __any(__builtin_elementwise_max(__builtin_elementwise_max(qfrag[0], qfrag[1]), __builtin_elementwise_max(qfrag[2], qfrag[3])) == POISON); };
    if (dirty()) {
#pragma nounroll
      for (unsigned it = 0; it < SPIN_CAP; ++it) {
        __builtin_amdgcn_s_sleep(2);
        ld16_sc1(qfrag, C.sWS, lane * 16, qoff); wait_vm<0>(); pin(qfrag);
        if (!dirty()) break;
        if (it == SPIN_CAP - 1) give_up(ws);
      } } }
}

template <bool EDGE, class SIDE> DI u32x4 attention(Ctx& C, const u32x4& qraw, SIDE&& side) {
  const int lane = C.lane, h = lane >> 5, g = (lane >> 4) & 1, q = (lane & 15) >> 2, pp = lane & 3;
  const bf16x8 qf = __builtin_bit_cast(bf16x8, qraw);
  const unsigned kaddr = pinv(L_K + (C.mg * 2 + C.ng) * 1024 + lane * 16);
  const int t0 = EDGE ? (C.rb == 0 ? 8 : 4) - C.mg : 1;
  const int dq = (lane & 31) - 4 * h;
  const float NEG = -1e30f;
  auto older = [&](int r) { return ((r & 3) + 8 * (r >> 2)) > dq; };
  auto max16 = [&](float& a, float& b, const f32x16& s) {
#pragma unroll
    for (int r = 0; r < 16; r += 4) { a = __builtin_fmaxf(__builtin_fmaxf(a, s[r]), s[r + 1]); b = __builtin_fmaxf(__builtin_fmaxf(b, s[r + 2]), s[r + 3]); } };
  bf16x8 kf[9];
#pragma unroll
  for (int t = 0; t < 9; ++t) if (!EDGE || t > 0) kf[t] = ld128(kaddr + t * 2048);
  float mx;
  {
    f32x16 S[3];
    f32x16 sl = MFMA32(kf[8], qf, f32x16{});
    f32x16 sf;
    if (!EDGE) sf = MFMA32(kf[0], qf, f32x16{});
    S[1] = MFMA32(kf[1], qf, f32x16{});
    S[2] = MFMA32(kf[2], qf, f32x16{});
    float a = NEG, b = NEG;
    side(std::integral_constant<int, 0>{});
    SB();
#pragma unroll
    for (int r = 0; r < 16; ++r) sl[r] = older(r) ? (EDGE ? NEG : sf[r]) : sl[r];
    max16(a, b, sl);
    static_for<1, 8>([&](auto tc) { constexpr int t = decltype(tc)::value;
      if constexpr (t + 2 < 8) S[(t + 2) % 3] = MFMA32(kf[t + 2], qf, f32x16{});
      side(std::integral_constant<int, t>{});
      SB();
      if (!EDGE || t >= t0) max16(a, b, S[t % 3]);
      SB(); });
    mx = swap_max(__builtin_fmaxf(a, b));
  }
  f32x16 negm;
#pragma unroll
  for (int r = 0; r < 16; ++r) negm[r] = -mx;
  const unsigned vbase = pinv(g == 0 ? L_V + C.ng * 12288 + C.mg * 1024 + (4 * h + q) * 32 + pp * 8 : L_ONES + 128 * (1 - h) + q * 32 + pp * 8);
  const unsigned vstr = g == 0 ? 1024u : 0u;
  f32x16 o;
  auto expack = [&](const f32x16& s, u32x4& pa, u32x4& pb) {
#pragma unroll
    for (int i = 0; i < 4; ++i) pa[i] = cvtpk(__builtin_amdgcn_exp2f(s[2 * i]), __builtin_amdgcn_exp2f(s[2 * i + 1]));
#pragma unroll
    for (int i = 0; i < 4; ++i) pb[i] = cvtpk(__builtin_amdgcn_exp2f(s[8 + 2 * i]), __builtin_amdgcn_exp2f(s[9 + 2 * i])); };
  f32x16 S[2];
  bf16x8 vf[2][2];
  {
    f32x16 sl = MFMA32(kf[8], qf, negm);
    f32x16 sf;
    if (!EDGE) sf = MFMA32(kf[0], qf, negm);
    S[1] = MFMA32(kf[1], qf, negm);
    const unsigned v8 = vbase + 8 * vstr;
    const bf16x8 va = ldfrag_tr(v8, 256), vb = ldfrag_tr(v8 + 512, 256);
    bf16x8 vc, vd;
    if (!EDGE) { vc = ldfrag_tr(vbase, 256); vd = ldfrag_tr(vbase + 512, 256); }
    vf[1][0] = ldfrag_tr(vbase + vstr, 256); vf[1][1] = ldfrag_tr(vbase + vstr + 512, 256);
    side(std::integral_constant<int, 8>{});
    SB();
#pragma unroll
    for (int r = 0; r < 16; ++r) sl[r] = older(r) ? (EDGE ? NEG : sf[r]) : sl[r];
    u32x4 pa, pb, mk0, mk1;
    expack(sl, pa, pb);
#pragma unroll
    for (int i = 0; i < 4; ++i) {
      mk0[i] = (older(2 * i) ? 0xffffu : 0u) | (older(2 * i + 1) ? 0xffff0000u : 0u);
      mk1[i] = (older(8 + 2 * i) ? 0xffffu : 0u) | (older(9 + 2 * i) ? 0xffff0000u : 0u); }
    o = MFMA32(va, __builtin_bit_cast(bf16x8, pa & ~mk0), f32x16{});
    o = MFMA32(vb, __builtin_bit_cast(bf16x8, pb & ~mk1), o);
    if (!EDGE) {
      o = MFMA32(vc, __builtin_bit_cast(bf16x8, pa & mk0), o);
      o = MFMA32(vd, __builtin_bit_cast(bf16x8, pb & mk1), o);
    }
    SB();
  }
  static_for<1, 8>([&](auto tc) { constexpr int t = decltype(tc)::value;
    if constexpr (t + 1 < 8) {
      const unsigned vt = vbase + (t + 1) * vstr;
      vf[(t + 1) & 1][0] = ldfrag_tr(vt, 256); vf[(t + 1) & 1][1] = ldfrag_tr(vt + 512, 256);
      S[(t + 1) & 1] = MFMA32(kf[t + 1], qf, negm); }
    side(std::integral_constant<int, 8 + t>{});
    SB();
    if (!EDGE || t >= t0) {
      u32x4 pa, pb;
      expack(S[t & 1], pa, pb);
      o = MFMA32(vf[t & 1][0], __builtin_bit_cast(bf16x8, pa), o);
      o = MFMA32(vf[t & 1][1], __builtin_bit_cast(bf16x8, pb), o);
    }
    SB(); });
  const float li = __builtin_amdgcn_rcpf(o[8]);
  u32x4 f; f[0] = cvtpk(o[0] * li, o[1] * li); f[1] = cvtpk(o[2] * li, o[3] * li); f[2] = cvtpk(o[4] * li, o[5] * li); f[3] = cvtpk(o[6] * li, o[7] * li);
  return f;
}

__global__ void __launch_bounds__(512, 2)
fused_kernel(const u16* __restrict__ x, const u16* __restrict__ wqkv, const u16* __restrict__ wo, const u16* __restrict__ wg, const u16* __restrict__ wu,
             const u16* __restrict__ wd, u16* __restrict__ out, char* __restrict__ ws) {
  Ctx C;
  const int tid = threadIdx.x, lane = tid & 63, wid = __builtin_amdgcn_readfirstlane(tid >> 6);
  const int b = blockIdx.x, xj = b & 7, xi = b >> 3;
  C.stampreg = 0;
  STAMP(0);
  asm volatile("" :: "s"(x), "s"(wqkv), "s"(wo), "s"(wg), "s"(wu), "s"(wd), "s"(out), "s"(ws));
  C.ws = ws; C.lane = lane; C.wid = wid; C.mg = wid & 3; C.ng = wid >> 2;
  C.rb = 8 * (xj & 1) + (xi & 7); C.cs = 4 * (xj >> 1) + (xi >> 3); C.bid = b;
  C.sX = mk_srd(x + (size_t)C.rb * 128 * 512, 128 * 1024);
  C.sQKVw = mk_srd(wqkv, 512 * 3072);
  C.xi = xi;
  const unsigned vx = hx_voff(C), vw = hw_voff(C);
  STAMP(3);
  issue_half(C, 0, 0, vx, vw); issue_half(C, 0, 1, vx, vw); issue_half(C, 0, 2, vx, vw);
  STAMP(4);
  STAMP(5);
  C.sWS = mk_srd(ws, 32u << 20); C.sXall = mk_srd(x, 2048 * 1024);
  C.sG = mk_srd(wg, 512 * 3584); C.sU = mk_srd(wu, 512 * 3584); C.sWO = mk_srd(wo, 512 * 1024); C.sWD = mk_srd(wd, 1792 * 1024);
  if (wid == 7) { u64 pc; asm volatile("s_getpc_b64 %0" : "=s"(pc));
    dma4(mk_srd((const void*)(pc & ~255ull), CODE_BYTES), (xi * 9 + (lane < 9 ? lane : 0)) * 128, L_JUNK); }
  if (tid < 256) lp<unsigned>(L_ONES)[tid] = 0x3f803f80u;
  const int rt64 = 4 * xj + (xi & 3), cg = xi >> 2;
  C.cg = cg;
  const int rt = wid & 1, kq = wid >> 1, RT = 2 * rt64 + rt, rb2 = RT >> 2;
  C.kq = kq; C.rb2 = rb2;
  u32x4 qfrag; u32x2 poll2; unsigned poll2off;
  if (C.cs < 8) gemm1<0>(C, vx, vw, qfrag, poll2, poll2off); else gemm1<1>(C, vx, vw, qfrag, poll2, poll2off);
  STAMP(6);

  const unsigned a2base = (unsigned)WS_A2 + (RT * 4 + kq) * 36 * 1024;
  u32x4 a2[36];
  auto side = [&](auto kc) { constexpr int k = decltype(kc)::value;
    if constexpr (k == 1) { wait_vm<10 + (NO_WARM ? 0 : 1)>(); check_flags(ws, poll2off, lane < 10, C.tag, poll2); }
    if constexpr (k >= 1 && k < 7) { ld16_sc1(a2[2 * k - 2], C.sWS, lane * 16, a2base + (2 * k - 2) * 1024); ld16_sc1(a2[2 * k - 1], C.sWS, lane * 16, a2base + (2 * k - 1) * 1024); }
    if constexpr (k >= 12) static_for<0, 4>([&](auto jc) { constexpr int q_ = 12 + 4 * (k - 12) + decltype(jc)::value; ld16_sc1(a2[q_], C.sWS, lane * 16, a2base + q_ * 1024); });
    if constexpr ((k == 0 || (k >= 7 && k < 12)) && !NO_WARM) touch_g(C, k == 0 ? 3 : k - 3); };
  u32x4 of;
  if (C.rb >= 2) of = attention<false>(C, qfrag, side); else of = attention<true>(C, qfrag, side);
  st16_sc1(of, C.sWS, lane * 16, a2_o(C));
  STAMP(7);

  const int g = (lane >> 4) & 1, h = lane >> 5, q = (lane & 15) >> 2, pp = lane & 3;
  const unsigned w2l0 = kq * 8192 + (4 * h + q) * 128 + ((0 ^ (q >> 1)) << 6) + g * 32 + pp * 8;
  const unsigned w2l1 = kq * 8192 + (4 * h + q) * 128 + ((1 ^ (q >> 1)) << 6) + g * 32 + pp * 8;
  bf16x8 wf[3][2];
  f32x16 acc[2];
  u32x4 xres;
  unsigned wb[2], gv;
  auto read_w2 = [&](int pos) {
    if ((pos & 3) == 0) { wb[0] = pinv(w2l0 + gslot(pos >> 2)); wb[1] = pinv(w2l1 + gslot(pos >> 2)); }
    wf[pos % 3][0] = ldfrag_tr(wb[0] + (pos & 3) * 2048, 1024); wf[pos % 3][1] = ldfrag_tr(wb[1] + (pos & 3) * 2048, 1024); };
  bar_vm<17 + (NO_WARM ? 0 : 5)>();
  static_for<0, 12>([&](auto kc) { constexpr int k = decltype(kc)::value; pin(a2[k]); });
  read_w2(0); read_w2(1);
  gv = g_voff(C);
  STAMP(8);
  u32x4 ef[4];
  static_for<0, 36>([&](auto ic) {
    constexpr int pos = decltype(ic)::value;
    constexpr int j = pos >> 2, i = pos & 3;
    if constexpr (i == 2) {
      STAMP(44 + 2 * j);
      if constexpr (j <= 1) bar_vm<63>();
      if constexpr (j == 2) bar_vm<6>();
      if constexpr (j == 3) bar_vm<6>();
      if constexpr (j == 4) bar_vm<7>();
      if constexpr (j == 5) bar_vm<15>();
      if constexpr (j == 6) bar_vm<4>();
      if constexpr (j == 7) bar_vm<0>();
      STAMP(45 + 2 * j);
    }
    if constexpr (pos == 10) { static_for<12, 28>([&](auto kc) { pin(a2[decltype(kc)::value]); }); check_frags<12, 28>(C, a2, a2base); }
    if constexpr (pos == 26) { static_for<28, 36>([&](auto kc) { pin(a2[decltype(kc)::value]); }); pin(xres); check_frags<28, 36>(C, a2, a2base); }
    if constexpr (pos == 16) { ld16(xres, C.sXall, (RT * 32 + (lane & 31)) * 1024 + cg * 128 + kq * 32 + h * 16, 0); }
    if constexpr (pos == 18 || pos == 19) static_for<0, 4>([&](auto kc) { constexpr int k = 28 + 4 * (pos - 18) + decltype(kc)::value; ld16_sc1(a2[k], C.sWS, lane * 16, a2base + k * 1024); });
    if constexpr (pos < 24) issue_g(C, 3 + (pos >> 2), pos & 3, gv);
    if constexpr (pos == 30) { if (b == 0 && wid == 7) {
#pragma unroll
        for (int k = 0; k < 4; ++k) { u32x2 t; ld8_sc1(t, C.sWS, flag_off(0, 0, 0) + (lane * 4 + k) * 128); ef[k][0] = t[0]; ef[k][1] = t[1]; } } }
    if constexpr (pos + 2 < 36) read_w2(pos + 2);
    const bf16x8 af = __builtin_bit_cast(bf16x8, a2[pos]);
    if constexpr (pos == 0) { acc[0] = MFMA32(wf[0][0], af, f32x16{}); acc[1] = MFMA32(wf[0][1], af, f32x16{}); }
    else { acc[0] = MFMA32(wf[pos % 3][0], af, acc[0]); acc[1] = MFMA32(wf[pos % 3][1], af, acc[1]); }
    SB();
  });
  STAMP(9);
#pragma unroll
  for (int d = 0; d < 4; ++d) if (d != kq) {
    const int ct = d >> 1, r0 = (d & 1) * 8;
    const unsigned a = R2 + ((rt * 4 + d) * 4 + kq) * 2048 + lane * 16;
    *lp<f32x4>(a) = f32x4{acc[ct][r0], acc[ct][r0 + 1], acc[ct][r0 + 2], acc[ct][r0 + 3]};
    *lp<f32x4>(a + 1024) = f32x4{acc[ct][r0 + 4], acc[ct][r0 + 5], acc[ct][r0 + 6], acc[ct][r0 + 7]};
  }
  float s[8];
#pragma unroll
  for (int e = 0; e < 8; ++e) { const float lo = (kq & 1) ? acc[0][8 + e] : acc[0][e], hi = (kq & 1) ? acc[1][8 + e] : acc[1][e]; s[e] = (kq & 2) ? hi : lo; }
  asm volatile("s_waitcnt lgkmcnt(0)\n\ts_barrier" ::: "memory");
#pragma unroll
  for (int d = 0; d < 4; ++d) if (d != kq) {
    const unsigned a = R2 + ((rt * 4 + kq) * 4 + d) * 2048 + lane * 16;
    const f32x4 u = *lp<f32x4>(a), v = *lp<f32x4>(a + 1024);
#pragma unroll
    for (int e = 0; e < 4; ++e) { s[e] += u[e]; s[4 + e] += v[e]; }
  }
  {
    unsigned u0 = __float_as_uint(s[0]), u1 = __float_as_uint(s[1]), u2 = __float_as_uint(s[2]), u3 = __float_as_uint(s[3]);
    unsigned v0 = __float_as_uint(s[4]), v1 = __float_as_uint(s[5]), v2 = __float_as_uint(s[6]), v3 = __float_as_uint(s[7]);
    auto r0 = __builtin_amdgcn_permlane32_swap(u0, v0, false, false); auto r1 = __builtin_amdgcn_permlane32_swap(u1, v1, false, false);
    auto r2 = __builtin_amdgcn_permlane32_swap(u2, v2, false, false); auto r3 = __builtin_amdgcn_permlane32_swap(u3, v3, false, false);
    const float c0 = __uint_as_float(r0[0]), c1 = __uint_as_float(r1[0]), c2 = __uint_as_float(r2[0]), c3 = __uint_as_float(r3[0]);
    const float c4 = __uint_as_float(r0[1]), c5 = __uint_as_float(r1[1]), c6 = __uint_as_float(r2[1]), c7 = __uint_as_float(r3[1]);
    u32x4 f;
    f[0] = cvtpk(c0 + bflo(xres[0]), c1 + bfhi(xres[0])); f[1] = cvtpk(c2 + bflo(xres[1]), c3 + bfhi(xres[1]));
    f[2] = cvtpk(c4 + bflo(xres[2]), c5 + bfhi(xres[2])); f[3] = cvtpk(c6 + bflo(xres[3]), c7 + bfhi(xres[3]));
    const i32x4 sO = mk_srd(out, 2048 * 1024);
    st16(f, sO, (RT * 32 + (lane & 31)) * 1024 + cg * 128 + kq * 32 + h * 16, 0);
  }
  if (b == 0 && wid == 7) {
    wait_vm<0>();
    asm volatile("" : "+v"(ef[0]), "+v"(ef[1]), "+v"(ef[2]), "+v"(ef[3]));
    bool ok = true;
#pragma unroll
    for (int k = 0; k < 4; ++k) ok = ok && ef[k][0] == (unsigned)C.tag && ef[k][1] == (unsigned)(C.tag >> 32);
    if (!__all(ok)) {
#pragma unroll 1
      for (int k = 0; k < 4; ++k) spin_flags(ws, flag_off(0, 0, 0) + (lane * 4 + k) * 128, true, C.tag);
    }
    if (lane == 0) __hip_atomic_store((u64*)(ws + WS_EPOCH), C.tag, __ATOMIC_RELAXED, __HIP_MEMORY_SCOPE_AGENT);
  }
  STAMP(60);
}

extern "C" void kernel_launch(void* const* d_in, const int* in_sizes, int n_in, void* d_out, int out_size, void* d_ws, size_t ws_size, hipStream_t stream) {
  (void)in_sizes; (void)n_in; (void)out_size; (void)ws_size;
  (void)hipFuncSetAttribute((const void*)fused_kernel, hipFuncAttributeMaxDynamicSharedMemorySize, LDS_BYTES);
  fused_kernel<<<256, 512, LDS_BYTES, stream>>>((const u16*)d_in[0], (const u16*)d_in[1], (const u16*)d_in[2], (const u16*)d_in[4], (const u16*)d_in[5], (const u16*)d_in[6],
                                                 (u16*)d_out, (char*)d_ws);
}
```

```cpp
#include <hip/hip_runtime.h>
#include <stdint.h>
#include <type_traits>

typedef unsigned short u16;
typedef unsigned long long u64;
typedef short bf16x8 __attribute__((ext_vector_type(8)));
typedef short s16x4 __attribute__((ext_vector_type(4)));
typedef float f32x2 __attribute__((ext_vector_type(2)));
typedef float f32x4 __attribute__((ext_vector_type(4)));
typedef float f32x16 __attribute__((ext_vector_type(16)));
typedef unsigned u32x2 __attribute__((ext_vector_type(2)));
typedef unsigned u32x4 __attribute__((ext_vector_type(4)));
typedef int i32x4 __attribute__((ext_vector_type(4)));
#define LDSP __attribute__((address_space(3)))
#define DI __device__ __forceinline__
template <class T> DI LDSP T* lp(unsigned a) { return (LDSP T*)(size_t)a; }
template <int I, int N, class F> DI void static_for(F&& f) { if constexpr (I < N) { f(std::integral_constant<int, I>{}); static_for<I + 1, N>(f); } }

constexpr size_t WS_EPOCH = 0;
constexpr size_t WS_ERR   = 64;
constexpr size_t WS_FLAGS = 4096;
constexpr size_t WS_GS    = 512u << 10;
constexpr size_t WS_GC    = 576u << 10;
constexpr size_t WS_QKV   = 1u << 20;
constexpr size_t WS_A2    = 8u << 20;
constexpr size_t WS_DBG   = 24u << 20;

constexpr unsigned R0 = 0, R1 = 32768, R2 = 65536, R3 = 98304;
constexpr unsigned L_K = 98304, L_V = 122880;
constexpr unsigned L_ONES = 147456;
constexpr unsigned L_CSP = 148480;
constexpr unsigned L_CS = 150528;
constexpr unsigned L_JUNK = 154624;
constexpr unsigned LDS_BYTES = 163840;
constexpr unsigned CODE_BYTES = 36864;
constexpr unsigned POISON = 0xffffffffu;
constexpr unsigned SPIN_CAP = 1u << 13;

#define NO_WARM 1
#define NO_CS 0
#define NO_DMA 0
constexpr float QSCALE = 0.25f * 1.4426950408889634f;

DI i32x4 mk_srd(const void* p, unsigned bytes) { u64 a = (u64)p; i32x4 r; r[0] = (int)(unsigned)a; r[1] = (int)((unsigned)(a >> 32) & 0xffffu); r[2] = (int)bytes; r[3] = 0x00020000; return r; }
DI void dma16(i32x4 srd, unsigned voff, unsigned soff, unsigned lds) {
  if (NO_DMA) return;
  asm volatile("s_mov_b32 m0, %3\n\ts_nop 3\n\tbuffer_load_dwordx4 %0, %1, %2 offen lds" :: "v"(voff), "s"(srd), "s"(soff), "s"(lds) : "memory");
}
DI void dma4(i32x4 srd, unsigned voff, unsigned lds) {
  asm volatile("s_mov_b32 m0, %2\n\ts_nop 3\n\tbuffer_load_dword %0, %1, 0 offen lds" :: "v"(voff), "s"(srd), "s"(lds) : "memory");
}
DI void ld16_sc1(u32x4& d, i32x4 srd, unsigned voff, unsigned soff) {
  asm volatile("s_nop 4\n\tbuffer_load_dwordx4 %0, %1, %2, %3 offen sc1" : "=v"(d) : "v"(voff), "s"(srd), "s"(soff) : "memory");
}
DI void ld16(u32x4& d, i32x4 srd, unsigned voff, unsigned soff) {
  asm volatile("s_nop 4\n\tbuffer_load_dwordx4 %0, %1, %2, %3 offen" : "=v"(d) : "v"(voff), "s"(srd), "s"(soff) : "memory");
}
DI void ld8_sc1(u32x2& d, i32x4 srd, unsigned voff) {
  asm volatile("s_nop 4\n\tbuffer_load_dwordx2 %0, %1, %2, 0 offen sc1" : "=v"(d) : "v"(voff), "s"(srd) : "memory");
}
DI void st16_sc1(u32x4 d, i32x4 srd, unsigned voff, unsigned soff) {
  asm volatile("s_nop 4\n\tbuffer_store_dwordx4 %0, %1, %2, %3 offen sc1\n\ts_nop 1" :: "v"(d), "v"(voff), "s"(srd), "s"(soff) : "memory");
}
DI void st16(u32x4 d, i32x4 srd, unsigned voff, unsigned soff) {
  asm volatile("s_nop 4\n\tbuffer_store_dwordx4 %0, %1, %2, %3 offen\n\ts_nop 1" :: "v"(d), "v"(voff), "s"(srd), "s"(soff) : "memory");
}
template <int N> DI void bar_vm() { static_assert(N >= 0 && N < 64, "vmcnt range"); asm volatile("s_waitcnt vmcnt(%0) lgkmcnt(0)\n\ts_barrier" :: "n"(N) : "memory"); }
template <int N> DI void wait_vm() { static_assert(N >= 0 && N < 64, "vmcnt range"); asm volatile("s_waitcnt vmcnt(%0)" :: "n"(N) : "memory"); }
DI s16x4 ldtr(unsigned a) { return __builtin_amdgcn_ds_read_tr16_b64_v4i16(lp<s16x4>(a)); }
DI bf16x8 ldfrag_tr(unsigned a, unsigned second) {
  s16x4 lo = ldtr(a), hi = ldtr(a + second);
  return __builtin_shufflevector(lo, hi, 0, 1, 2, 3, 4, 5, 6, 7);
}
DI bf16x8 ld128(unsigned a) { return *lp<bf16x8>(a); }
DI unsigned cvtpk(float lo, float hi) { typedef __bf16 b2 __attribute__((ext_vector_type(2))); f32x2 v = {lo, hi}; return __builtin_bit_cast(unsigned, __builtin_convertvector(v, b2)); }
DI float bflo(unsigned v) { return __uint_as_float(v << 16); }
DI float bfhi(unsigned v) { return __uint_as_float(v & 0xffff0000u); }
DI float rbf(float f) { return bflo(cvtpk(f, f) & 0xffffu); }
DI int fresh(int v) { asm volatile("" : "+v"(v)); return v; }
template <class T> DI void pin(T& v) { asm volatile("" : "+v"(v)); }
DI unsigned pinv(unsigned v) { asm volatile("" : "+v"(v)); return v; }
#define MFMA32(a, b, c) __builtin_amdgcn_mfma_f32_32x32x16_bf16((a), (b), (c), 0, 0, 0)
#define MFMA16(a, b, c) __builtin_amdgcn_mfma_f32_16x16x32_bf16((a), (b), (c), 0, 0, 0)
#define MFMA4(a, b, c) __builtin_amdgcn_mfma_f32_4x4x4bf16_1k((a), (b), (c), 0, 0, 0)
#define SB() __builtin_amdgcn_sched_barrier(0)
DI float swap_add(float v) { auto r = __builtin_amdgcn_permlane32_swap(__float_as_uint(v), __float_as_uint(v), false, false); return __uint_as_float(r[0]) + __uint_as_float(r[1]); }
template <class T> DI float ror_add(float v, T) { return v + __int_as_float(__builtin_amdgcn_update_dpp(0, __float_as_int(v), T::value, 0xf, 0xf, true)); }
DI float swap16_add(float v) { auto r = __builtin_amdgcn_permlane16_swap(__float_as_uint(v), __float_as_uint(v), false, false); return __uint_as_float(r[0]) + __uint_as_float(r[1]); }
template <int C> using IC = std::integral_constant<int, C>;
DI float wave_sum(float v) { v = ror_add(v, IC<0x121>{}); v = ror_add(v, IC<0x122>{}); v = ror_add(v, IC<0x124>{}); v = ror_add(v, IC<0x128>{}); return swap_add(swap16_add(v)); }
DI float swap_max(float v) { auto r = __builtin_amdgcn_permlane32_swap(__float_as_uint(v), __float_as_uint(v), false, false); return __builtin_fmaxf(__uint_as_float(r[0]), __uint_as_float(r[1])); }

#define STAMP(i) do { } while (0)

struct Ctx {
  char* ws; u64 tag;
  i32x4 sX, sXall, sQKVw, sG, sU, sWO, sWD, sWS;
  int lane, wid, mg, ng, rb, cs, cg, xi, kq, rb2, bid; unsigned stampreg;
};

DI unsigned flag_off(int ev, int rb, int cs) { return (unsigned)(WS_FLAGS + (((ev * 16 + rb) * 16 + cs) << 7)); }
DI void publish(const Ctx& C, int ev) { if (C.wid == 0 && C.lane == 0) __hip_atomic_store((u64*)(C.ws + flag_off(ev, C.rb, C.cs)), C.tag, __ATOMIC_RELAXED, __HIP_MEMORY_SCOPE_AGENT); }
DI void give_up(char* ws) { __hip_atomic_store((unsigned*)(ws + WS_ERR), 1u, __ATOMIC_RELAXED, __HIP_MEMORY_SCOPE_AGENT); }
DI void spin_flags(char* ws, unsigned off, bool need, u64 tag) {
#pragma nounroll
  for (unsigned it = 0; it < SPIN_CAP; ++it) {
    u64 v = __hip_atomic_load((u64*)(ws + off), __ATOMIC_RELAXED, __HIP_MEMORY_SCOPE_AGENT);
    if (__all(!need || v == tag)) return;
    __builtin_amdgcn_s_sleep(2);
  }
  give_up(ws);
}
DI void check_flags(char* ws, unsigned off, bool need, u64 tag, u32x2& got) {
  asm volatile("" : "+v"(got));
  bool ok = !need || (got[0] == (unsigned)tag && got[1] == (unsigned)(tag >> 32));
  if (!__all(ok)) spin_flags(ws, off, need, tag);
}
template <int K0, int K1> DI void check_frags(const Ctx& C, u32x4 (&a2)[36], unsigned base) {
  auto dirty = [&]() { unsigned mx = 0;
    static_for<K0, K1>([&](auto kc) { constexpr int k = decltype(kc)::value;
      mx = __builtin_elementwise_max(mx, __builtin_elementwise_max(__builtin_elementwise_max(a2[k][0], a2[k][1]), __builtin_elementwise_max(a2[k][2], a2[k][3]))); });
    return __any(mx == POISON); };
  if (dirty()) {
#pragma nounroll
    for (unsigned it = 0; it < SPIN_CAP; ++it) {
      __builtin_amdgcn_s_sleep(2);
      static_for<K0, K1>([&](auto kc) { constexpr int k = decltype(kc)::value; ld16_sc1(a2[k], C.sWS, C.lane * 16, base + k * 1024); });
      wait_vm<0>();
      static_for<K0, K1>([&](auto kc) { constexpr int k = decltype(kc)::value; pin(a2[k]); });
      if (!dirty()) return;
    }
    give_up(C.ws);
  }
}

enum { T_H0 = 0, T_S4 = 8, T_E0 = 16, T_G0 = 19, T_POISON = 22, T_POLL, T_KL, T_POLL2, T_QL, T_QP, T_EPOCH, T_CSL, T_GR, T_WARM, T_NONE };
template <int TYPE> struct G1 {
  static constexpr int kind(int p) { return p == 0 ? 0 : (p == 1 && TYPE == 1) ? 1 : 2; }
  static constexpr int nt(int p) { return p == 0 ? 1 : 2; }
  static constexpr int nst(int p) { return kind(p) == 1 ? 3 : 2; }
  static constexpr int npoison() { return 3; }
  static constexpr unsigned hslot(int h) { return (h % 6) * 24576; }
  static constexpr unsigned slot(int s) { return (s & 3) == 0 ? R2 : (s & 3) == 1 ? R3 : (s & 3) == 2 ? R0 : R1; }
  static constexpr int barpos(int bi) { return bi < 8 ? 4 * bi + 2 : 8 * (bi - 4) + 6; }
  static constexpr int barat(int pos) { return pos < 32 ? ((pos & 3) == 2 ? pos >> 2 : -1) : ((pos & 7) == 6 ? 4 + (pos >> 3) : -1); }
  static constexpr int grp(int bi) {
    return bi == 0 ? T_H0 + 6 : bi == 1 ? T_H0 + 7 : bi == 3 ? T_S4 : bi == 5 ? T_S4 + 1 : bi == 7 ? T_S4 + 2 : (bi >= 8 && bi <= 11) ? T_S4 + (bi - 4)
         : bi == 12 ? T_G0 : bi == 14 ? T_G0 + 1 : bi == 15 ? T_G0 + 2 : T_NONE; }
  static constexpr int freegrp(int pos) { return pos == 0 ? T_H0 + 2 : pos == 1 ? T_H0 + 3 : pos == 3 ? T_H0 + 4 : pos == 4 ? T_H0 + 5 : (pos >= 34 && pos < 38) ? T_S4 + 3 : T_NONE; }
  static constexpr int freen(int pos) { return freegrp(pos) == T_NONE ? 0 : pos < 8 ? 3 : 1; }
  static constexpr int gsize(int t) { return t == T_NONE ? 0 : t < T_S4 ? 3 : 4; }
  static constexpr int rel(int bi, int k) { return bi < 8 ? k : bi == 15 ? (k >> 1) : 2 * k; }
  static constexpr int after(int t, int bi) {
    int cnt = -1;
    auto see = [&](int tag, int n) { for (int i = 0; i < n; ++i) { if (tag == t) cnt = 0; else if (cnt >= 0) ++cnt; } };
    see(T_H0, gsize(T_H0)); see(T_EPOCH, 2); see(T_H0 + 1, gsize(T_H0 + 1)); see(T_CSL, 4);
    if (bi == -1) return cnt;
    for (int pos = 0; pos < 96; ++pos) {
      if (pos < 2) see(freegrp(pos), freen(pos));
      const int b = barat(pos);
      if (b >= 0) {
        if (b == bi) return cnt;
        if (b == 12) see(T_KL, 6);
        if (b == 13) see(T_QL, 1);
        if (b == 14) see(T_POLL2, 1);
      }
      for (int bb = 0; bb < 16; ++bb) for (int k = 0; k < gsize(grp(bb)); ++k) if (barpos(bb) + rel(bb, k) == pos) see(grp(bb), 1);
      if (pos >= 2) see(freegrp(pos), freen(pos));
      if (pos == 32 && TYPE == 1) see(T_QP, 2);
      if (pos == 22) see(T_GR, 2);
      if (pos == 58) see(T_POLL, 1);
      if (pos == 50) see(T_POISON, npoison());
      if ((pos & 31) == 31) see(T_E0 + (pos >> 5), nst(pos >> 5));
    }
    return cnt;
  }
  static constexpr int mn(int a, int b) { return a < b ? a : b; }
};

template <int TYPE> DI unsigned tile_addr(const Ctx& C, int p, int t) {
  const int lane = fresh(C.lane), kind = G1<TYPE>::kind(p);
  const int g = (lane >> 4) & 1, h = lane >> 5, q = (lane & 15) >> 2, pp = lane & 3;
  const unsigned rdrow = (8 * h + q) * 128 + pp * 8;
  if (kind == 2) return g * 16384 + rdrow + (((2 * C.ng + t) ^ (2 * (q >> 1)) ^ g) << 5);
  if (kind == 0) return 16384 + rdrow + ((C.ng ^ (q >> 1)) << 6) + g * 32;
  if (t == 0) return rdrow + ((C.ng ^ (q >> 1)) << 6) + g * 32;
  return 16384 + g * 8192 + (8 * h + q) * 64 + pp * 8 + ((C.ng ^ g) << 5);
}
template <int TYPE> DI unsigned stage_voff(const Ctx& C, int p, int which) {
  const int kind = G1<TYPE>::kind(p), lane = fresh(C.lane);
  if (which == 1) return (16 * C.wid + (lane >> 2)) * 3584 + (8 + ((C.cs - 8) >> 1)) * 128 + ((C.cs - 8) & 1) * 64 + ((lane & 3) << 4);
  const unsigned row = 16 * C.wid + (lane >> 3), sw = ((lane & 7) ^ (((lane >> 4) & 1) << 2)) << 4;
  if (kind == 1) return row * 3072 + (C.cs - 8) * 128 + sw;
  return row * 3584 + (p == 1 ? C.cs : 12 + C.cs) * 128 + sw;
}
template <int TYPE> DI void issue_stage(const Ctx& C, int s, int k, unsigned v0) {
  const int kind = G1<TYPE>::kind(s >> 2);
  const unsigned slot = G1<TYPE>::slot(s), r0 = 128 * (s & 3);
  if (kind == 1) {
    if (k < 2) dma16(C.sQKVw, v0, (r0 + 8 * k) * 3072, slot + C.wid * 2048 + k * 1024);
    else dma16(k == 3 ? C.sU : C.sG, k == 3 ? v0 ^ 32 : v0, r0 * 3584, slot + 16384 + (k - 2) * 8192 + C.wid * 1024);
  } else dma16(k >> 1 ? C.sU : C.sG, k >> 1 ? v0 ^ 32 : v0, (r0 + 8 * (k & 1)) * 3584, slot + (k >> 1) * 16384 + C.wid * 2048 + (k & 1) * 1024);
}
DI unsigned hx_voff(const Ctx& C) { const int lane = fresh(C.lane); return (16 * C.wid + (lane >> 3)) * 1024 + (((lane & 7) ^ ((lane >> 4) & 3)) << 4); }
DI unsigned hw_voff(const Ctx& C) { const int lane = fresh(C.lane);
  return (8 * C.wid + (lane >> 3)) * 3072 + (8 + C.cs) * 128 + (((lane & 7) ^ (((lane >> 4) & 1) << 2)) << 4); }
DI void issue_half(const Ctx& C, int h, int k, unsigned vx, unsigned vw) {
  const unsigned slot = (h % 6) * 24576;
  if (k < 2) dma16(C.sX, k ? (vx ^ 64) + 8 * 1024 : vx, h * 128, slot + C.wid * 2048 + k * 1024);
  else dma16(C.sQKVw, vw + (k - 2) * 128, h * 64 * 3072, slot + 16384 + (k - 2) * 8192 + C.wid * 1024);
}
DI void warm(i32x4 srd, unsigned line0, unsigned lines_per_row_log2, unsigned pitch, unsigned colbyte, int lane) {
  const unsigned l = line0 + lane;
  dma4(srd, (l >> lines_per_row_log2) * pitch + colbyte + (l & ((1u << lines_per_row_log2) - 1)) * 128, L_JUNK);
}
DI unsigned g_voff(const Ctx& C) { const int lane = fresh(C.lane); return (32 * (C.wid & 1) + (lane >> 3)) * 1024 + C.cg * 128 + (((lane & 7) ^ (((lane >> 4) & 1) << 2)) << 4); }
constexpr unsigned gslot(int j) { return j == 0 ? R2 : j == 1 ? R0 : j == 2 ? R1 : j == 3 ? R3 : j == 4 ? R2 : j == 5 ? R0 : j == 6 ? R1 : j == 7 ? R3 : R2; }
DI void issue_g(const Ctx& C, int j, int i, unsigned v) {
  const int kq = C.wid >> 1;
  const int row0 = j < 3 ? (3 * kq + j) * 64 : j < 7 ? (12 + 4 * kq + (j - 3)) * 64 : 128 * kq + 64 * (j - 7);
  dma16(j < 7 ? C.sWD : C.sWO, v, (unsigned)(row0 + 8 * i) * 1024, gslot(j) + kq * 8192 + (C.wid & 1) * 4096 + i * 1024);
}

template <int WHAT> DI void epi_qkv(const Ctx& C, const f32x16& acc, const f32x4 (&csr)[4], float m, float rinv, int qu, int h4) {
  const int h = C.lane >> 5;
  const float sc = WHAT == 0 ? rinv * QSCALE : rinv;
  float y[16];
#pragma unroll
  for (int i4 = 0; i4 < 4; ++i4)
#pragma unroll
    for (int e = 0; e < 4; ++e) y[4 * i4 + e] = (acc[4 * i4 + e] - csr[i4][e]) * sc;
  const unsigned base = (unsigned)WS_QKV + (((C.rb * 24 + qu) * 4 + C.mg) * 4 + h4) * 1024;
#pragma unroll
  for (int hh = 0; hh < 2; ++hh) {
    u32x4 f; f[0] = cvtpk(y[8 * hh], y[8 * hh + 1]); f[1] = cvtpk(y[8 * hh + 2], y[8 * hh + 3]); f[2] = cvtpk(y[8 * hh + 4], y[8 * hh + 5]); f[3] = cvtpk(y[8 * hh + 6], y[8 * hh + 7]);
    unsigned voff = C.lane * 16;
    if (WHAT == 2) {
      auto a = __builtin_amdgcn_permlane32_swap(f[0], f[2], false, false); auto b = __builtin_amdgcn_permlane32_swap(f[1], f[3], false, false);
      f[0] = a[0]; f[2] = a[1]; f[1] = b[0]; f[3] = b[1];
      voff = (C.lane & 31) * 32 + h * 16;
    }
    st16_sc1(f, C.sWS, voff, base + hh * 1024);
  }
}
DI void epi_gu(const Ctx& C, const f32x16& acc, const f32x4 (&csr)[4], float m, float r2, unsigned a2off) {
  float y[8];
#pragma unroll
  for (int i4 = 0; i4 < 2; ++i4)
#pragma unroll
    for (int e = 0; e < 4; ++e) y[4 * i4 + e] = ((acc[4 * i4 + e] - csr[i4][e]) * r2) * (acc[8 + 4 * i4 + e] - csr[2 + i4][e]);
  u32x4 f; f[0] = cvtpk(y[0], y[1]); f[1] = cvtpk(y[2], y[3]); f[2] = cvtpk(y[4], y[5]); f[3] = cvtpk(y[6], y[7]);
  st16_sc1(f, C.sWS, C.lane * 16, a2off);
}
template <int TYPE> DI unsigned a2_pass(const Ctx& C, int p) {
  if (p == 2) return (unsigned)WS_A2 + (((4 * C.rb + C.mg) * 4 + (C.cs >> 2)) * 36 + 4 * (3 + (C.cs & 3)) + 2 * C.ng) * 1024;
  if (TYPE == 0) return (unsigned)WS_A2 + (((4 * C.rb + C.mg) * 4 + C.cs / 3) * 36 + 4 * (C.cs % 3) + 2 * C.ng) * 1024;
  const int u = 8 + ((C.cs - 8) >> 1), half = (C.cs - 8) & 1;
  return (unsigned)WS_A2 + (((4 * C.rb + C.mg) * 4 + u / 3) * 36 + 4 * (u % 3) + 2 * half + C.ng) * 1024;
}
DI unsigned a2_o(const Ctx& C) { return (unsigned)WS_A2 + (((4 * C.rb + C.mg) * 4 + (C.cs >> 2)) * 36 + 28 + 2 * (C.cs & 3) + C.ng) * 1024; }

template <int TYPE> DI void gemm1(Ctx& C, unsigned vx, unsigned vw, u32x4& qfrag, u32x2& poll2, unsigned& poll2off) {
  using G = G1<TYPE>;
  const int lane = C.lane;
  char* ws = C.ws;
  bf16x8 xa[32];
  bf16x8 wf[3][2];
  f32x16 acc[2];
  unsigned ta[2], sv = 0, sv2 = 0;
  ta[0] = pinv(tile_addr<TYPE>(C, 0, 0)); ta[1] = pinv(tile_addr<TYPE>(C, 0, 1));
  unsigned th[2];
  th[0] = ta[0] + 65536; th[1] = ta[1] + 65536;
  const unsigned r31 = fresh(lane) & 31, hh = fresh(lane) >> 5;
  const unsigned xlane = (32 * C.mg + r31) * 128 + ((hh ^ ((r31 >> 1) & 7)) << 4);
  f32x4 csr[2][4];
  float m = 0.f, rinv = 0.f;
  u32x2 pollv, gs; u32x4 gk[6], gc;
  unsigned polloff = 0, gsoff = 0, gcoff = 0; bool pollneed = false;
  const unsigned qoff = (unsigned)WS_QKV + (((C.rb * 24 + (C.cs >> 1)) * 4 + C.mg) * 4 + 2 * (C.cs & 1) + C.ng) * 1024;

  u32x2 ev; ld8_sc1(ev, C.sWS, (unsigned)WS_EPOCH);
  u32x4 srow, cl[4];
  ld16(srow, C.sXall, (8 * C.bid + C.wid) * 1024 + fresh(lane) * 16, 0);
  issue_half(C, 1, 0, vx, vw); issue_half(C, 1, 1, vx, vw); issue_half(C, 1, 2, vx, vw);
  { const int pan = C.bid >> 1, lf = fresh(lane);
    const int t = C.bid - 160, row = 24 * t + 3 * C.wid;
    const unsigned pitch = pan < 24 ? 3072 : pan < 80 ? 3584 : 128, col = (pan < 24 ? pan : pan < 52 ? pan - 24 : pan - 52) * 128;
    i32x4 sp;
#pragma unroll
    for (int e = 0; e < 4; ++e) sp[e] = pan < 24 ? C.sQKVw[e] : pan < 52 ? C.sG[e] : pan < 80 ? C.sU[e] : row < 512 ? C.sWO[e] : C.sWD[e];
    const unsigned v = pan < 80 ? (256 * (C.bid & 1) + 32 * C.wid + (lf >> 3)) * pitch + col + (lf & 7) * 16 : (row < 512 ? row : row - 512) * 1024 + lf * 16;
#pragma unroll
    for (int k = 0; k < 4; ++k) ld16(cl[k], sp, v, 8 * k * pitch); }
  STAMP(1);
  bar_vm<G::after(T_EPOCH, -1)>();
  STAMP(2);
  {
  pin(ev); pin(srow);
  { const u64 t = (((u64)ev[1] << 32) | ev[0]) * 6364136223846793005ull + 1442695040888963407ull;
    C.tag = ((u64)__builtin_amdgcn_readfirstlane((unsigned)(t >> 32)) << 32) | __builtin_amdgcn_readfirstlane((unsigned)t); }
  float xv[8];
#pragma unroll
  for (int e = 0; e < 4; ++e) { xv[2 * e] = bflo(srow[e]); xv[2 * e + 1] = bfhi(srow[e]); }
  const float mean = wave_sum(((xv[0] + xv[1]) + (xv[2] + xv[3])) + ((xv[4] + xv[5]) + (xv[6] + xv[7]))) * (1.f / 512.f);
  float q2 = 0.f;
#pragma unroll
  for (int e = 0; e < 8; ++e) { const float d = xv[e] - mean; q2 += d * d; }
  const float var = wave_sum(q2) * (1.f / 512.f);
  const float sd = __builtin_amdgcn_sqrtf(rbf(rbf(var) + 1e-5f));
  if (lane == 0) __hip_atomic_store((u64*)(ws + WS_GS + (8 * C.bid + C.wid) * 8), ((u64)(unsigned)C.tag << 32) | cvtpk(mean, sd), __ATOMIC_RELAXED, __HIP_MEMORY_SCOPE_AGENT);
  }
  auto read_x = [&](int h) {
    const unsigned base = G::hslot(h) + xlane;
#pragma unroll
    for (int i = 0; i < 4; ++i) xa[4 * h + i] = ld128(base ^ (i << 5));
  };
  auto read_w = [&](int pos) {
    const int p = pos >> 5, s = pos >> 3, i = pos & 7;
    if ((pos & 31) == 0 && pos > 0) { ta[0] = pinv(tile_addr<TYPE>(C, p, 0)); ta[1] = pinv(tile_addr<TYPE>(C, p, 1)); th[0] = pinv(ta[0] + 65536); th[1] = pinv(ta[1] + 65536); }
    const unsigned sb = p == 0 ? G::hslot(pos >> 2) : G::slot(s), ks = p == 0 ? (i & 3) : i;
    const bool hi = sb >= 65536;
    const unsigned b0 = hi ? th[0] : ta[0], b1 = hi ? th[1] : ta[1], so = hi ? sb - 65536 : sb;
    wf[pos % 3][0] = ldfrag_tr(b0 + so + ks * 2048, 512);
    if (G::nt(p) == 2) { if (G::kind(p) == 1) wf[pos % 3][1] = ldfrag_tr(b1 + so + ks * 1024, 256); else wf[pos % 3][1] = ldfrag_tr(b1 + so + ks * 2048, 512); }
  };
  read_x(0); read_w(0); read_w(1);
  static_for<0, 96>([&](auto ic) {
    constexpr int pos = decltype(ic)::value;
    constexpr int p = pos >> 5, s = pos >> 3, st = s & 3, i = pos & 7, NT = G::nt(p), bi = G::barat(pos);
    if constexpr (pos < 2) static_for<0, 3>([&](auto kc) { issue_half(C, 2 + pos, decltype(kc)::value, vx, vw); });
    if constexpr (bi >= 0) {
      STAMP(10 + 2 * bi);
      if constexpr (bi == 0) bar_vm<G::mn(G::after(T_H0 + 1, 0), G::after(T_CSL, 0))>();
      else if constexpr (bi < 7) bar_vm<G::after(T_H0 + bi + 1, bi)>();
      else if constexpr (bi == 7) bar_vm<G::mn(G::after(T_S4, 7), G::after(T_GR, 7))>();
      else if constexpr (bi == 9) { bar_vm<G::mn(G::mn(G::after(T_S4 + 2, 9), G::after(T_E0, 9)), TYPE == 1 ? G::after(T_QP, 9) : 63)>(); publish(C, 0); }
      else if constexpr (bi == 12) bar_vm<G::mn(G::after(T_S4 + 5, 12), G::after(T_POLL, 12))>();
      else if constexpr (bi == 13) { static_assert(G::after(T_E0 + 1, 13) <= G::after(T_POISON, 13), ""); bar_vm<G::mn(G::after(T_S4 + 6, 13), G::after(T_E0 + 1, 13))>(); }
      else if constexpr (bi == 14) bar_vm<G::mn(G::after(T_S4 + 7, 14), G::after(T_KL, 14))>();
      else if constexpr (bi == 15) bar_vm<63>();
      else bar_vm<G::after(T_S4 + bi - 7, bi)>();
      if constexpr (bi == 0) {
      pin(cl[0]); pin(cl[1]); pin(cl[2]); pin(cl[3]);
      float cz[8];
#pragma unroll
      for (int e = 0; e < 4; ++e) { cz[2 * e] = (bflo(cl[0][e]) + bflo(cl[1][e])) + (bflo(cl[2][e]) + bflo(cl[3][e])); cz[2 * e + 1] = (bfhi(cl[0][e]) + bfhi(cl[1][e])) + (bfhi(cl[2][e]) + bfhi(cl[3][e])); }
#pragma unroll
      for (int e = 0; e < 8; ++e) cz[e] = swap_add(swap16_add(ror_add(cz[e], IC<0x128>{})));
      if (lane < 8) { *lp<f32x4>(L_CSP + C.wid * 256 + lane * 32) = f32x4{cz[0], cz[1], cz[2], cz[3]}; *lp<f32x4>(L_CSP + C.wid * 256 + lane * 32 + 16) = f32x4{cz[4], cz[5], cz[6], cz[7]}; }
      }
      if constexpr (bi == 1) {
        if (C.wid == 0 && C.bid < 160) {
          float t = 0.f;
#pragma unroll
          for (int w = 0; w < 8; ++w) t += *lp<float>(L_CSP + w * 256 + lane * 4);
          __hip_atomic_store((u64*)(ws + WS_GC + ((C.bid >> 1) * 64 + lane) * 16 + (C.bid & 1) * 8), ((u64)(unsigned)C.tag << 32) | __float_as_uint(t), __ATOMIC_RELAXED, __HIP_MEMORY_SCOPE_AGENT);
        }
      }
      if constexpr (bi == 7) {
        pin(gs); pin(gc);
        if (!__all(gs[1] == (unsigned)C.tag && gc[1] == (unsigned)C.tag && gc[3] == (unsigned)C.tag)) {
#pragma nounroll
          for (unsigned it = 0; it < SPIN_CAP; ++it) {
            __builtin_amdgcn_s_sleep(2);
            ld8_sc1(gs, C.sWS, gsoff); ld16_sc1(gc, C.sWS, gcoff, 0); wait_vm<0>(); pin(gs); pin(gc);
            if (__all(gs[1] == (unsigned)C.tag && gc[1] == (unsigned)C.tag && gc[3] == (unsigned)C.tag)) break;
            if (it == SPIN_CAP - 1) give_up(ws);
          }
        }
        m = bflo(gs[0]); rinv = __builtin_amdgcn_rcpf(bfhi(gs[0]));
        *lp<float>(L_CS + (C.wid < 6 ? C.wid : 5) * 256 + lane * 4) = __uint_as_float(gc[0]) + __uint_as_float(gc[2]);
      }
      if constexpr (bi == 12) {
        check_flags(ws, polloff, pollneed, C.tag, pollv);
        const int u = C.cs >> 1, h4 = 2 * (C.cs & 1) + C.ng;
#pragma unroll
        for (int b = 0; b < 6; ++b) { const int rbp = C.rb - 2 + b % 3 < 0 ? 0 : C.rb - 2 + b % 3;
          ld16_sc1(gk[b], C.sWS, lane * 16, (unsigned)WS_QKV + (((rbp * 24 + (b < 3 ? 8 : 16) + u) * 4 + C.mg) * 4 + h4) * 1024); }
      }
      if constexpr (bi == 13) { publish(C, 1); ld16_sc1(qfrag, C.sWS, lane * 16, qoff); }
      if constexpr (bi == 14) {
#pragma unroll
        for (int b = 0; b < 6; ++b) pin(gk[b]);
#pragma unroll
        for (int b = 0; b < 3; ++b) { *lp<u32x4>(L_K + ((4 * b + C.mg) * 2 + C.ng) * 1024 + lane * 16) = gk[b];
                                      *lp<u32x4>(L_V + C.ng * 12288 + (4 * b + C.mg) * 1024 + lane * 16) = gk[3 + b]; }
        { const int ln = fresh(lane), l = ln < 10 ? ln : 0, uu = 3 * C.kq + (l >> 1);
          poll2off = flag_off(1, C.rb2, l < 6 ? (uu < 8 ? uu : 8 + 2 * (uu - 8) + (l & 1)) : 4 * C.kq + (l - 6)); }
        ld8_sc1(poll2, C.sWS, poll2off);
      }
      if constexpr (bi < 7) read_x(bi + 1);
      STAMP(11 + 2 * bi);
    }
    static_for<0, 16>([&](auto bc) {
      constexpr int bb = decltype(bc)::value, tg = G::grp(bb), k0 = pos - G::barpos(bb);
      static_for<0, 4>([&](auto kc) {
        constexpr int k = decltype(kc)::value;
        if constexpr (k < G::gsize(tg) && G::rel(bb, k) == k0) {
          if constexpr (tg < T_S4) issue_half(C, tg, k, vx, vw);
          else if constexpr (tg >= T_G0) { if constexpr (k == 0) sv = g_voff(C); issue_g(C, tg - T_G0, k, sv); }
          else { constexpr int pp_ = (tg - T_S4 + 4) >> 2;
            if constexpr (k == 0) sv = stage_voff<TYPE>(C, pp_, 0);
            if constexpr (k == 2 && G::kind(pp_) == 1) sv = stage_voff<TYPE>(C, pp_, 1);
            issue_stage<TYPE>(C, tg - T_S4 + 4, k, sv); }
        }
      });
    });
    if constexpr (pos == 3 || pos == 4) static_for<0, 3>([&](auto kc) { issue_half(C, G::freegrp(pos) - T_H0, decltype(kc)::value, vx, vw); });
    if constexpr (pos >= 34 && pos < 38) {
      if constexpr (pos == 34) sv2 = stage_voff<TYPE>(C, 1, 0);
      if constexpr (pos == 36 && G::kind(1) == 1) sv2 = stage_voff<TYPE>(C, 1, 1);
      issue_stage<TYPE>(C, 7, pos - 34, sv2);
    }
    if constexpr (pos == 32 && TYPE == 1) {
      const u32x4 pz = {POISON, POISON, POISON, POISON};
      const unsigned a = (unsigned)WS_QKV + (((C.rb * 24 + (C.cs - 8)) * 4 + C.mg) * 4 + 2 * C.ng) * 1024;
      st16_sc1(pz, C.sWS, lane * 16, a); st16_sc1(pz, C.sWS, lane * 16, a + 1024);
    }
    if constexpr (pos == 22) {
      const int lf = fresh(lane), w = C.wid < 6 ? C.wid : 5;
      const int guh = 8 + ((C.cs - 8) >> 1);
      const int pan = w == 0 ? 8 + C.cs : w >= 4 ? (w == 4 ? 24 : 52) + 12 + C.cs
                    : TYPE == 0 ? (w == 1 ? 24 + C.cs : 52 + C.cs) : (w == 1 ? C.cs - 8 : w == 2 ? 24 + guh : 52 + guh);
      gsoff = (unsigned)WS_GS + (C.rb * 128 + 32 * C.mg + (lf & 31)) * 8; gcoff = (unsigned)WS_GC + (pan * 64 + lf) * 16;
      ld8_sc1(gs, C.sWS, gsoff); ld16_sc1(gc, C.sWS, gcoff, 0);
    }
    if constexpr (pos == 58) {
      const int u = C.cs >> 1, l = fresh(lane) & 7;
      const int rbp = C.rb - 2 + l % 3;
      pollneed = l < 6 && rbp >= 0;
      polloff = flag_off(0, pollneed ? rbp : C.rb, l < 3 ? u : 8 + u);
      ld8_sc1(pollv, C.sWS, polloff);
    }
    if constexpr (pos == 50) {
      const u32x4 pz = {POISON, POISON, POISON, POISON};
      const unsigned a = a2_pass<TYPE>(C, 2);
      st16_sc1(pz, C.sWS, lane * 16, a);
      st16_sc1(pz, C.sWS, lane * 16, a + 1024);
      st16_sc1(pz, C.sWS, lane * 16, a2_o(C));
    }
    if constexpr (pos + 2 < 96) read_w(pos + 2);
    if constexpr ((pos & 31) == 0) { acc[0] = MFMA32(wf[pos % 3][0], xa[pos & 31], f32x16{}); if (NT == 2) acc[1] = MFMA32(wf[pos % 3][1], xa[pos & 31], f32x16{}); }
    else { acc[0] = MFMA32(wf[pos % 3][0], xa[pos & 31], acc[0]); if (NT == 2) acc[1] = MFMA32(wf[pos % 3][1], xa[pos & 31], acc[1]); }
    if constexpr (st == 3 && i == 7) {
      if constexpr (p == 0) asm volatile("s_waitcnt lgkmcnt(0)\n\ts_barrier" ::: "memory");
      static_for<0, NT>([&](auto tc) { constexpr int t = decltype(tc)::value;
        const bool gu = G::kind(p) == 2 || (G::kind(p) == 1 && t == 1);
        const int tl = G::kind(p) == 2 ? 2 * C.ng + t : 2 * ((C.cs - 8) & 1) + C.ng;
        const unsigned s0 = L_CS + (p == 0 ? 0 : p == 2 ? 4 : (G::kind(p) == 1 ? 1 + t : 1)) * 256;
#pragma unroll
        for (int i4 = 0; i4 < 4; ++i4) {
          const unsigned a = gu ? s0 + (i4 >> 1) * 256 + (16 * tl + 8 * (i4 & 1) + 4 * hh) * 4 : s0 + (32 * C.ng + 8 * i4 + 4 * hh) * 4;
          csr[t][i4] = *lp<f32x4>(a) * m; pin(csr[t][i4]); } });
    }
    SB();
    if constexpr ((pos & 31) == 31) {
      if constexpr (G::kind(p) == 2) {
        const unsigned a2 = a2_pass<TYPE>(C, p);
        epi_gu(C, acc[0], csr[0], m, rinv * rinv, a2); epi_gu(C, acc[1], csr[1], m, rinv * rinv, a2 + 1024);
      } else if constexpr (G::kind(p) == 1) {
        epi_qkv<0>(C, acc[0], csr[0], m, rinv, C.cs - 8, 2 * C.ng);
        epi_gu(C, acc[1], csr[1], m, rinv * rinv, a2_pass<TYPE>(C, p));
      } else {
        epi_qkv<1 + TYPE>(C, acc[0], csr[0], m, rinv, 8 + C.cs, 2 * C.ng);
      }
      SB();
    }
  });
  static_assert(G::after(T_POLL2, 16) == 10, "attention counts on this");
  wait_vm<G::after(T_QL, 16)>();
  pin(qfrag);
  { auto dirty = [&]() { return __any(__builtin_elementwise_max(__builtin_elementwise_max(qfrag[0], qfrag[1]), __builtin_elementwise_max(qfrag[2], qfrag[3])) == POISON); };
    if (dirty()) {
#pragma nounroll
      for (unsigned it = 0; it < SPIN_CAP; ++it) {
        __builtin_amdgcn_s_sleep(2);
        ld16_sc1(qfrag, C.sWS, lane * 16, qoff); wait_vm<0>(); pin(qfrag);
        if (!dirty()) break;
        if (it == SPIN_CAP - 1) give_up(ws);
      } } }
}

template <bool EDGE, class SIDE> DI u32x4 attention(Ctx& C, const u32x4& qraw, SIDE&& side) {
  const int lane = C.lane, h = lane >> 5, g = (lane >> 4) & 1, q = (lane & 15) >> 2, pp = lane & 3;
  const bf16x8 qf = __builtin_bit_cast(bf16x8, qraw);
  const unsigned kaddr = pinv(L_K + (C.mg * 2 + C.ng) * 1024 + lane * 16);
  const int t0 = EDGE ? (C.rb == 0 ? 8 : 4) - C.mg : 1;
  const int dq = (lane & 31) - 4 * h;
  const float NEG = -1e30f;
  auto older = [&](int r) { return ((r & 3) + 8 * (r >> 2)) > dq; };
  auto max16 = [&](float& a, float& b, const f32x16& s) {
#pragma unroll
    for (int r = 0; r < 16; r += 4) { a = __builtin_fmaxf(__builtin_fmaxf(a, s[r]), s[r + 1]); b = __builtin_fmaxf(__builtin_fmaxf(b, s[r + 2]), s[r + 3]); } };
  bf16x8 kf[9];
#pragma unroll
  for (int t = 0; t < 9; ++t) if (!EDGE || t > 0) kf[t] = ld128(kaddr + t * 2048);
  float mx;
  {
    f32x16 S[3];
    f32x16 sl = MFMA32(kf[8], qf, f32x16{});
    f32x16 sf;
    if (!EDGE) sf = MFMA32(kf[0], qf, f32x16{});
    S[1] = MFMA32(kf[1], qf, f32x16{});
    S[2] = MFMA32(kf[2], qf, f32x16{});
    float a = NEG, b = NEG;
    side(std::integral_constant<int, 0>{});
    SB();
#pragma unroll
    for (int r = 0; r < 16; ++r) sl[r] = older(r) ? (EDGE ? NEG : sf[r]) : sl[r];
    max16(a, b, sl);
    static_for<1, 8>([&](auto tc) { constexpr int t = decltype(tc)::value;
      if constexpr (t + 2 < 8) S[(t + 2) % 3] = MFMA32(kf[t + 2], qf, f32x16{});
      side(std::integral_constant<int, t>{});
      SB();
      if (!EDGE || t >= t0) max16(a, b, S[t % 3]);
      SB(); });
    mx = swap_max(__builtin_fmaxf(a, b));
  }
  f32x16 negm;
#pragma unroll
  for (int r = 0; r < 16; ++r) negm[r] = -mx;
  const unsigned vbase = pinv(g == 0 ? L_V + C.ng * 12288 + C.mg * 1024 + (4 * h + q) * 32 + pp * 8 : L_ONES + 128 * (1 - h) + q * 32 + pp * 8);
  const unsigned vstr = g == 0 ? 1024u : 0u;
  f32x16 o;
  auto expack = [&](const f32x16& s, u32x4& pa, u32x4& pb) {
#pragma unroll
    for (int i = 0; i < 4; ++i) pa[i] = cvtpk(__builtin_amdgcn_exp2f(s[2 * i]), __builtin_amdgcn_exp2f(s[2 * i + 1]));
#pragma unroll
    for (int i = 0; i < 4; ++i) pb[i] = cvtpk(__builtin_amdgcn_exp2f(s[8 + 2 * i]), __builtin_amdgcn_exp2f(s[9 + 2 * i])); };
  f32x16 S[2];
  bf16x8 vf[2][2];
  {
    f32x16 sl = MFMA32(kf[8], qf, negm);
    f32x16 sf;
    if (!EDGE) sf = MFMA32(kf[0], qf, negm);
    S[1] = MFMA32(kf[1], qf, negm);
    const unsigned v8 = vbase + 8 * vstr;
    const bf16x8 va = ldfrag_tr(v8, 256), vb = ldfrag_tr(v8 + 512, 256);
    bf16x8 vc, vd;
    if (!EDGE) { vc = ldfrag_tr(vbase, 256); vd = ldfrag_tr(vbase + 512, 256); }
    vf[1][0] = ldfrag_tr(vbase + vstr, 256); vf[1][1] = ldfrag_tr(vbase + vstr + 512, 256);
    side(std::integral_constant<int, 8>{});
    SB();
#pragma unroll
    for (int r = 0; r < 16; ++r) sl[r] = older(r) ? (EDGE ? NEG : sf[r]) : sl[r];
    u32x4 pa, pb, mk0, mk1;
    expack(sl, pa, pb);
#pragma unroll
    for (int i = 0; i < 4; ++i) {
      mk0[i] = (older(2 * i) ? 0xffffu : 0u) | (older(2 * i + 1) ? 0xffff0000u : 0u);
      mk1[i] = (older(8 + 2 * i) ? 0xffffu : 0u) | (older(9 + 2 * i) ? 0xffff0000u : 0u); }
    o = MFMA32(va, __builtin_bit_cast(bf16x8, pa & ~mk0), f32x16{});
    o = MFMA32(vb, __builtin_bit_cast(bf16x8, pb & ~mk1), o);
    if (!EDGE) {
      o = MFMA32(vc, __builtin_bit_cast(bf16x8, pa & mk0), o);
      o = MFMA32(vd, __builtin_bit_cast(bf16x8, pb & mk1), o);
    }
    SB();
  }
  static_for<1, 8>([&](auto tc) { constexpr int t = decltype(tc)::value;
    if constexpr (t + 1 < 8) {
      const unsigned vt = vbase + (t + 1) * vstr;
      vf[(t + 1) & 1][0] = ldfrag_tr(vt, 256); vf[(t + 1) & 1][1] = ldfrag_tr(vt + 512, 256);
      S[(t + 1) & 1] = MFMA32(kf[t + 1], qf, negm); }
    side(std::integral_constant<int, 8 + t>{});
    SB();
    if (!EDGE || t >= t0) {
      u32x4 pa, pb;
      expack(S[t & 1], pa, pb);
      o = MFMA32(vf[t & 1][0], __builtin_bit_cast(bf16x8, pa), o);
      o = MFMA32(vf[t & 1][1], __builtin_bit_cast(bf16x8, pb), o);
    }
    SB(); });
  const float li = __builtin_amdgcn_rcpf(o[8]);
  u32x4 f; f[0] = cvtpk(o[0] * li, o[1] * li); f[1] = cvtpk(o[2] * li, o[3] * li); f[2] = cvtpk(o[4] * li, o[5] * li); f[3] = cvtpk(o[6] * li, o[7] * li);
  return f;
}

__global__ void __launch_bounds__(512, 2)
fused_kernel(const u16* __restrict__ x, const u16* __restrict__ wqkv, const u16* __restrict__ wo, const u16* __restrict__ wg, const u16* __restrict__ wu,
             const u16* __restrict__ wd, u16* __restrict__ out, char* __restrict__ ws) {
  Ctx C;
  const int tid = threadIdx.x, lane = tid & 63, wid = __builtin_amdgcn_readfirstlane(tid >> 6);
  const int b = blockIdx.x, xj = b & 7, xi = b >> 3;
  C.stampreg = 0;
  STAMP(0);
  asm volatile("" :: "s"(x), "s"(wqkv), "s"(wo), "s"(wg), "s"(wu), "s"(wd), "s"(out), "s"(ws));
  C.ws = ws; C.lane = lane; C.wid = wid; C.mg = wid & 3; C.ng = wid >> 2;
  C.rb = 8 * (xj & 1) + (xi & 7); C.cs = 4 * (xj >> 1) + (xi >> 3); C.bid = b;
  C.sX = mk_srd(x + (size_t)C.rb * 128 * 512, 128 * 1024);
  C.sQKVw = mk_srd(wqkv, 512 * 3072);
  C.xi = xi;
  const unsigned vx = hx_voff(C), vw = hw_voff(C);
  STAMP(3);
  issue_half(C, 0, 0, vx, vw); issue_half(C, 0, 1, vx, vw); issue_half(C, 0, 2, vx, vw);
  STAMP(4);
  STAMP(5);
  C.sWS = mk_srd(ws, 32u << 20); C.sXall = mk_srd(x, 2048 * 1024);
  C.sG = mk_srd(wg, 512 * 3584); C.sU = mk_srd(wu, 512 * 3584); C.sWO = mk_srd(wo, 512 * 1024); C.sWD = mk_srd(wd, 1792 * 1024);
  if (wid == 7) { u64 pc; asm volatile("s_getpc_b64 %0" : "=s"(pc));
    dma4(mk_srd((const void*)(pc & ~255ull), CODE_BYTES), (xi * 9 + (lane < 9 ? lane : 0)) * 128, L_JUNK); }
  if (tid < 256) lp<unsigned>(L_ONES)[tid] = 0x3f803f80u;
  const int rt64 = 4 * xj + (xi & 3), cg = xi >> 2;
  C.cg = cg;
  const int rt = wid & 1, kq = wid >> 1, RT = 2 * rt64 + rt, rb2 = RT >> 2;
  C.kq = kq; C.rb2 = rb2;
  u32x4 qfrag; u32x2 poll2; unsigned poll2off;
  if (C.cs < 8) gemm1<0>(C, vx, vw, qfrag, poll2, poll2off); else gemm1<1>(C, vx, vw, qfrag, poll2, poll2off);
  STAMP(6);

  const unsigned a2base = (unsigned)WS_A2 + (RT * 4 + kq) * 36 * 1024;
  u32x4 a2[36];
  auto side = [&](auto kc) { constexpr int k = decltype(kc)::value;
    if constexpr (k == 1) { wait_vm<10>(); check_flags(ws, poll2off, lane < 10, C.tag, poll2); }
    if constexpr (k >= 1 && k < 7) { ld16_sc1(a2[2 * k - 2], C.sWS, lane * 16, a2base + (2 * k - 2) * 1024); ld16_sc1(a2[2 * k - 1], C.sWS, lane * 16, a2base + (2 * k - 1) * 1024); }
    if constexpr (k >= 12) static_for<0, 4>([&](auto jc) { constexpr int q_ = 12 + 4 * (k - 12) + decltype(jc)::value; ld16_sc1(a2[q_], C.sWS, lane * 16, a2base + q_ * 1024); });
    if constexpr (k >= 7 && k < 13 && !NO_WARM) { const unsigned l = (xi * 6 + (k - 7)) * 64 + wid * 8 + (fresh(lane) & 7);
      dma4(l < 8192 ? C.sWD : C.sWO, (l < 8192 ? l + 768 * 8 : l - 8192) * 128, L_JUNK); } };
  u32x4 of;
  if (C.rb >= 2) of = attention<false>(C, qfrag, side); else of = attention<true>(C, qfrag, side);
  st16_sc1(of, C.sWS, lane * 16, a2_o(C));
  STAMP(7);

  const int g = (lane >> 4) & 1, h = lane >> 5, q = (lane & 15) >> 2, pp = lane & 3;
  const unsigned w2l0 = kq * 8192 + (4 * h + q) * 128 + ((0 ^ (q >> 1)) << 6) + g * 32 + pp * 8;
  const unsigned w2l1 = kq * 8192 + (4 * h + q) * 128 + ((1 ^ (q >> 1)) << 6) + g * 32 + pp * 8;
  bf16x8 wf[3][2];
  f32x16 acc[2];
  u32x4 xres;
  unsigned wb[2], gv;
  auto read_w2 = [&](int pos) {
    if ((pos & 3) == 0) { wb[0] = pinv(w2l0 + gslot(pos >> 2)); wb[1] = pinv(w2l1 + gslot(pos >> 2)); }
    wf[pos % 3][0] = ldfrag_tr(wb[0] + (pos & 3) * 2048, 1024); wf[pos % 3][1] = ldfrag_tr(wb[1] + (pos & 3) * 2048, 1024); };
  bar_vm<17 + (NO_WARM ? 0 : 6)>();
  static_for<0, 12>([&](auto kc) { constexpr int k = decltype(kc)::value; pin(a2[k]); });
  read_w2(0); read_w2(1);
  gv = g_voff(C);
  STAMP(8);
  u32x4 ef[4];
  static_for<0, 36>([&](auto ic) {
    constexpr int pos = decltype(ic)::value;
    constexpr int j = pos >> 2, i = pos & 3;
    if constexpr (i == 2) {
      STAMP(44 + 2 * j);
      if constexpr (j <= 1) bar_vm<63>();
      if constexpr (j == 2) bar_vm<6>();
      if constexpr (j == 3) bar_vm<6>();
      if constexpr (j == 4) bar_vm<7>();
      if constexpr (j == 5) bar_vm<15>();
      if constexpr (j == 6) bar_vm<4>();
      if constexpr (j == 7) bar_vm<0>();
      STAMP(45 + 2 * j);
    }
    if constexpr (pos == 10) { static_for<12, 28>([&](auto kc) { pin(a2[decltype(kc)::value]); }); check_frags<12, 28>(C, a2, a2base); }
    if constexpr (pos == 26) { static_for<28, 36>([&](auto kc) { pin(a2[decltype(kc)::value]); }); pin(xres); check_frags<28, 36>(C, a2, a2base); }
    if constexpr (pos == 16) { ld16(xres, C.sXall, (RT * 32 + (lane & 31)) * 1024 + cg * 128 + kq * 32 + h * 16, 0); }
    if constexpr (pos == 18 || pos == 19) static_for<0, 4>([&](auto kc) { constexpr int k = 28 + 4 * (pos - 18) + decltype(kc)::value; ld16_sc1(a2[k], C.sWS, lane * 16, a2base + k * 1024); });
    if constexpr (pos < 24) issue_g(C, 3 + (pos >> 2), pos & 3, gv);
    if constexpr (pos == 30) { if (b == 0 && wid == 7) {
#pragma unroll
        for (int k = 0; k < 4; ++k) { u32x2 t; ld8_sc1(t, C.sWS, flag_off(0, 0, 0) + (lane * 4 + k) * 128); ef[k][0] = t[0]; ef[k][1] = t[1]; } } }
    if constexpr (pos + 2 < 36) read_w2(pos + 2);
    const bf16x8 af = __builtin_bit_cast(bf16x8, a2[pos]);
    if constexpr (pos == 0) { acc[0] = MFMA32(wf[0][0], af, f32x16{}); acc[1] = MFMA32(wf[0][1], af, f32x16{}); }
    else { acc[0] = MFMA32(wf[pos % 3][0], af, acc[0]); acc[1] = MFMA32(wf[pos % 3][1], af, acc[1]); }
    SB();
  });
  STAMP(9);
#pragma unroll
  for (int d = 0; d < 4; ++d) if (d != kq) {
    const int ct = d >> 1, r0 = (d & 1) * 8;
    const unsigned a = R0 + ((rt * 4 + d) * 4 + kq) * 2048 + lane * 16;
    *lp<f32x4>(a) = f32x4{acc[ct][r0], acc[ct][r0 + 1], acc[ct][r0 + 2], acc[ct][r0 + 3]};
    *lp<f32x4>(a + 1024) = f32x4{acc[ct][r0 + 4], acc[ct][r0 + 5], acc[ct][r0 + 6], acc[ct][r0 + 7]};
  }
  float s[8];
#pragma unroll
  for (int e = 0; e < 8; ++e) { const float lo = (kq & 1) ? acc[0][8 + e] : acc[0][e], hi = (kq & 1) ? acc[1][8 + e] : acc[1][e]; s[e] = (kq & 2) ? hi : lo; }
  asm volatile("s_waitcnt lgkmcnt(0)\n\ts_barrier" ::: "memory");
#pragma unroll
  for (int d = 0; d < 4; ++d) if (d != kq) {
    const unsigned a = R0 + ((rt * 4 + kq) * 4 + d) * 2048 + lane * 16;
    const f32x4 u = *lp<f32x4>(a), v = *lp<f32x4>(a + 1024);
#pragma unroll
    for (int e = 0; e < 4; ++e) { s[e] += u[e]; s[4 + e] += v[e]; }
  }
  {
    unsigned u0 = __float_as_uint(s[0]), u1 = __float_as_uint(s[1]), u2 = __float_as_uint(s[2]), u3 = __float_as_uint(s[3]);
    unsigned v0 = __float_as_uint(s[4]), v1 = __float_as_uint(s[5]), v2 = __float_as_uint(s[6]), v3 = __float_as_uint(s[7]);
    auto r0 = __builtin_amdgcn_permlane32_swap(u0, v0, false, false); auto r1 = __builtin_amdgcn_permlane32_swap(u1, v1, false, false);
    auto r2 = __builtin_amdgcn_permlane32_swap(u2, v2, false, false); auto r3 = __builtin_amdgcn_permlane32_swap(u3, v3, false, false);
    const float c0 = __uint_as_float(r0[0]), c1 = __uint_as_float(r1[0]), c2 = __uint_as_float(r2[0]), c3 = __uint_as_float(r3[0]);
    const float c4 = __uint_as_float(r0[1]), c5 = __uint_as_float(r1[1]), c6 = __uint_as_float(r2[1]), c7 = __uint_as_float(r3[1]);
    u32x4 f;
    f[0] = cvtpk(c0 + bflo(xres[0]), c1 + bfhi(xres[0])); f[1] = cvtpk(c2 + bflo(xres[1]), c3 + bfhi(xres[1]));
    f[2] = cvtpk(c4 + bflo(xres[2]), c5 + bfhi(xres[2])); f[3] = cvtpk(c6 + bflo(xres[3]), c7 + bfhi(xres[3]));
    const i32x4 sO = mk_srd(out, 2048 * 1024);
    st16(f, sO, (RT * 32 + (lane & 31)) * 1024 + cg * 128 + kq * 32 + h * 16, 0);
  }
  if (b == 0 && wid == 7) {
    wait_vm<0>();
    asm volatile("" : "+v"(ef[0]), "+v"(ef[1]), "+v"(ef[2]), "+v"(ef[3]));
    bool ok = true;
#pragma unroll
    for (int k = 0; k < 4; ++k) ok = ok && ef[k][0] == (unsigned)C.tag && ef[k][1] == (unsigned)(C.tag >> 32);
    if (!__all(ok)) {
#pragma unroll 1
      for (int k = 0; k < 4; ++k) spin_flags(ws, flag_off(0, 0, 0) + (lane * 4 + k) * 128, true, C.tag);
    }
    if (lane == 0) __hip_atomic_store((u64*)(ws + WS_EPOCH), C.tag, __ATOMIC_RELAXED, __HIP_MEMORY_SCOPE_AGENT);
  }
  STAMP(60);
}

extern "C" void kernel_launch(void* const* d_in, const int* in_sizes, int n_in, void* d_out, int out_size, void* d_ws, size_t ws_size, hipStream_t stream) {
  (void)in_sizes; (void)n_in; (void)out_size; (void)ws_size;
  (void)hipFuncSetAttribute((const void*)fused_kernel, hipFuncAttributeMaxDynamicSharedMemorySize, LDS_BYTES);
  fused_kernel<<<256, 512, LDS_BYTES, stream>>>((const u16*)d_in[0], (const u16*)d_in[1], (const u16*)d_in[2], (const u16*)d_in[4], (const u16*)d_in[5], (const u16*)d_in[6],
                                                 (u16*)d_out, (char*)d_ws);
}
```

```cpp
#include <hip/hip_runtime.h>
#include <stdint.h>
#include <type_traits>

typedef unsigned short u16;
typedef unsigned long long u64;
typedef short bf16x8 __attribute__((ext_vector_type(8)));
typedef short s16x4 __attribute__((ext_vector_type(4)));
typedef float f32x2 __attribute__((ext_vector_type(2)));
typedef float f32x4 __attribute__((ext_vector_type(4)));
typedef float f32x16 __attribute__((ext_vector_type(16)));
typedef unsigned u32x2 __attribute__((ext_vector_type(2)));
typedef unsigned u32x4 __attribute__((ext_vector_type(4)));
typedef int i32x4 __attribute__((ext_vector_type(4)));
#define LDSP __attribute__((address_space(3)))
#define DI __device__ __forceinline__
template <class T> DI LDSP T* lp(unsigned a) { return (LDSP T*)(size_t)a; }
template <int I, int N, class F> DI void static_for(F&& f) { if constexpr (I < N) { f(std::integral_constant<int, I>{}); static_for<I + 1, N>(f); } }

constexpr size_t WS_EPOCH = 0;
constexpr size_t WS_ERR   = 64;
constexpr size_t WS_FLAGS = 4096;
constexpr size_t WS_GS    = 512u << 10;
constexpr size_t WS_GC    = 576u << 10;
constexpr size_t WS_QKV   = 1u << 20;
constexpr size_t WS_A2    = 8u << 20;
constexpr size_t WS_DBG   = 24u << 20;

constexpr unsigned R0 = 0, R1 = 32768, R2 = 65536, R3 = 98304;
constexpr unsigned L_K = 98304, L_V = 122880;
constexpr unsigned L_ONES = 147456;
constexpr unsigned L_CSP = 148480;
constexpr unsigned L_CS = 150528;
constexpr unsigned L_JUNK = 154624;
constexpr unsigned LDS_BYTES = 163840;
constexpr unsigned CODE_BYTES = 36864;
constexpr unsigned POISON = 0xffffffffu;
constexpr unsigned SPIN_CAP = 1u << 13;

#define NO_WARM 1
#define NO_CS 0
#define NO_DMA 0
constexpr float QSCALE = 0.25f * 1.4426950408889634f;

DI i32x4 mk_srd(const void* p, unsigned bytes) { u64 a = (u64)p; i32x4 r; r[0] = (int)(unsigned)a; r[1] = (int)((unsigned)(a >> 32) & 0xffffu); r[2] = (int)bytes; r[3] = 0x00020000; return r; }
DI void dma16(i32x4 srd, unsigned voff, unsigned soff, unsigned lds) {
  if (NO_DMA) return;
  asm volatile("s_mov_b32 m0, %3\n\ts_nop 3\n\tbuffer_load_dwordx4 %0, %1, %2 offen lds" :: "v"(voff), "s"(srd), "s"(soff), "s"(lds) : "memory");
}
DI void dma4(i32x4 srd, unsigned voff, unsigned lds) {
  asm volatile("s_mov_b32 m0, %2\n\ts_nop 3\n\tbuffer_load_dword %0, %1, 0 offen lds" :: "v"(voff), "s"(srd), "s"(lds) : "memory");
}
DI void ld16_sc1(u32x4& d, i32x4 srd, unsigned voff, unsigned soff) {
  asm volatile("s_nop 4\n\tbuffer_load_dwordx4 %0, %1, %2, %3 offen sc1" : "=v"(d) : "v"(voff), "s"(srd), "s"(soff) : "memory");
}
DI void ld16(u32x4& d, i32x4 srd, unsigned voff, unsigned soff) {
  asm volatile("s_nop 4\n\tbuffer_load_dwordx4 %0, %1, %2, %3 offen" : "=v"(d) : "v"(voff), "s"(srd), "s"(soff) : "memory");
}
DI void ld8_sc1(u32x2& d, i32x4 srd, unsigned voff) {
  asm volatile("s_nop 4\n\tbuffer_load_dwordx2 %0, %1, %2, 0 offen sc1" : "=v"(d) : "v"(voff), "s"(srd) : "memory");
}
DI void st16_sc1(u32x4 d, i32x4 srd, unsigned voff, unsigned soff) {
  asm volatile("s_nop 4\n\tbuffer_store_dwordx4 %0, %1, %2, %3 offen sc1\n\ts_nop 1" :: "v"(d), "v"(voff), "s"(srd), "s"(soff) : "memory");
}
DI void st16(u32x4 d, i32x4 srd, unsigned voff, unsigned soff) {
  asm volatile("s_nop 4\n\tbuffer_store_dwordx4 %0, %1, %2, %3 offen\n\ts_nop 1" :: "v"(d), "v"(voff), "s"(srd), "s"(soff) : "memory");
}
template <int N> DI void bar_vm() { static_assert(N >= 0 && N < 64, "vmcnt range"); asm volatile("s_waitcnt vmcnt(%0) lgkmcnt(0)\n\ts_barrier" :: "n"(N) : "memory"); }
template <int N> DI void wait_vm() { static_assert(N >= 0 && N < 64, "vmcnt range"); asm volatile("s_waitcnt vmcnt(%0)" :: "n"(N) : "memory"); }
DI s16x4 ldtr(unsigned a) { return __builtin_amdgcn_ds_read_tr16_b64_v4i16(lp<s16x4>(a)); }
DI bf16x8 ldfrag_tr(unsigned a, unsigned second) {
  s16x4 lo = ldtr(a), hi = ldtr(a + second);
  return __builtin_shufflevector(lo, hi, 0, 1, 2, 3, 4, 5, 6, 7);
}
DI bf16x8 ld128(unsigned a) { return *lp<bf16x8>(a); }
DI unsigned cvtpk(float lo, float hi) { typedef __bf16 b2 __attribute__((ext_vector_type(2))); f32x2 v = {lo, hi}; return __builtin_bit_cast(unsigned, __builtin_convertvector(v, b2)); }
DI float bflo(unsigned v) { return __uint_as_float(v << 16); }
DI float bfhi(unsigned v) { return __uint_as_float(v & 0xffff0000u); }
DI float rbf(float f) { return bflo(cvtpk(f, f) & 0xffffu); }
DI int fresh(int v) { asm volatile("" : "+v"(v)); return v; }
template <class T> DI void pin(T& v) { asm volatile("" : "+v"(v)); }
DI unsigned pinv(unsigned v) { asm volatile("" : "+v"(v)); return v; }
#define MFMA32(a, b, c) __builtin_amdgcn_mfma_f32_32x32x16_bf16((a), (b), (c), 0, 0, 0)
#define MFMA16(a, b, c) __builtin_amdgcn_mfma_f32_16x16x32_bf16((a), (b), (c), 0, 0, 0)
#define MFMA4(a, b, c) __builtin_amdgcn_mfma_f32_4x4x4bf16_1k((a), (b), (c), 0, 0, 0)
#define SB() __builtin_amdgcn_sched_barrier(0)
DI float swap_add(float v) { auto r = __builtin_amdgcn_permlane32_swap(__float_as_uint(v), __float_as_uint(v), false, false); return __uint_as_float(r[0]) + __uint_as_float(r[1]); }
template <class T> DI float ror_add(float v, T) { return v + __int_as_float(__builtin_amdgcn_update_dpp(0, __float_as_int(v), T::value, 0xf, 0xf, true)); }
DI float swap16_add(float v) { auto r = __builtin_amdgcn_permlane16_swap(__float_as_uint(v), __float_as_uint(v), false, false); return __uint_as_float(r[0]) + __uint_as_float(r[1]); }
template <int C> using IC = std::integral_constant<int, C>;
DI float wave_sum(float v) { v = ror_add(v, IC<0x121>{}); v = ror_add(v, IC<0x122>{}); v = ror_add(v, IC<0x124>{}); v = ror_add(v, IC<0x128>{}); return swap_add(swap16_add(v)); }
DI float swap_max(float v) { auto r = __builtin_amdgcn_permlane32_swap(__float_as_uint(v), __float_as_uint(v), false, false); return __builtin_fmaxf(__uint_as_float(r[0]), __uint_as_float(r[1])); }

#define STAMP(i) do { } while (0)

struct Ctx {
  char* ws; u64 tag;
  i32x4 sX, sXall, sQKVw, sG, sU, sWO, sWD, sWS;
  int lane, wid, mg, ng, rb, cs, cg, xi, kq, rb2, bid; unsigned stampreg;
};

DI unsigned flag_off(int ev, int rb, int cs) { return (unsigned)(WS_FLAGS + (((ev * 16 + rb) * 16 + cs) << 7)); }
DI void publish(const Ctx& C, int ev) { if (C.wid == 0 && C.lane == 0) __hip_atomic_store((u64*)(C.ws + flag_off(ev, C.rb, C.cs)), C.tag, __ATOMIC_RELAXED, __HIP_MEMORY_SCOPE_AGENT); }
DI void give_up(char* ws) { __hip_atomic_store((unsigned*)(ws + WS_ERR), 1u, __ATOMIC_RELAXED, __HIP_MEMORY_SCOPE_AGENT); }
DI void spin_flags(char* ws, unsigned off, bool need, u64 tag) {
#pragma nounroll
  for (unsigned it = 0; it < SPIN_CAP; ++it) {
    u64 v = __hip_atomic_load((u64*)(ws + off), __ATOMIC_RELAXED, __HIP_MEMORY_SCOPE_AGENT);
    if (__all(!need || v == tag)) return;
    __builtin_amdgcn_s_sleep(2);
  }
  give_up(ws);
}
DI void check_flags(char* ws, unsigned off, bool need, u64 tag, u32x2& got) {
  asm volatile("" : "+v"(got));
  bool ok = !need || (got[0] == (unsigned)tag && got[1] == (unsigned)(tag >> 32));
  if (!__all(ok)) spin_flags(ws, off, need, tag);
}
template <int K0, int K1> DI void check_frags(const Ctx& C, u32x4 (&a2)[36], unsigned base) {
  auto dirty = [&]() { unsigned mx = 0;
    static_for<K0, K1>([&](auto kc) { constexpr int k = decltype(kc)::value;
      mx = __builtin_elementwise_max(mx, __builtin_elementwise_max(__builtin_elementwise_max(a2[k][0], a2[k][1]), __builtin_elementwise_max(a2[k][2], a2[k][3]))); });
    return __any(mx == POISON); };
  if (dirty()) {
#pragma nounroll
    for (unsigned it = 0; it < SPIN_CAP; ++it) {
      __builtin_amdgcn_s_sleep(2);
      static_for<K0, K1>([&](auto kc) { constexpr int k = decltype(kc)::value; ld16_sc1(a2[k], C.sWS, C.lane * 16, base + k * 1024); });
      wait_vm<0>();
      static_for<K0, K1>([&](auto kc) { constexpr int k = decltype(kc)::value; pin(a2[k]); });
      if (!dirty()) return;
    }
    give_up(C.ws);
  }
}

enum { T_H0 = 0, T_S4 = 8, T_E0 = 16, T_G0 = 19, T_POISON = 22, T_POLL, T_KL, T_POLL2, T_QL, T_QP, T_EPOCH, T_CSL, T_GR, T_WARM, T_NONE };
template <int TYPE> struct G1 {
  static constexpr int kind(int p) { return p == 0 ? 0 : (p == 1 && TYPE == 1) ? 1 : 2; }
  static constexpr int nt(int p) { return p == 0 ? 1 : 2; }
  static constexpr int nst(int p) { return kind(p) == 1 ? 3 : 2; }
  static constexpr int npoison() { return 3; }
  static constexpr unsigned hslot(int h) { return (h % 6) * 24576; }
  static constexpr unsigned slot(int s) { return (s & 3) == 0 ? R2 : (s & 3) == 1 ? R3 : (s & 3) == 2 ? R0 : R1; }
  static constexpr int barpos(int bi) { return bi < 8 ? 4 * bi + 2 : 8 * (bi - 4) + 6; }
  static constexpr int barat(int pos) { return pos < 32 ? ((pos & 3) == 2 ? pos >> 2 : -1) : ((pos & 7) == 6 ? 4 + (pos >> 3) : -1); }
  static constexpr int grp(int bi) {
    return bi == 0 ? T_H0 + 6 : bi == 1 ? T_H0 + 7 : bi == 3 ? T_S4 : bi == 5 ? T_S4 + 1 : bi == 7 ? T_S4 + 2 : (bi >= 8 && bi <= 11) ? T_S4 + (bi - 4)
         : bi == 12 ? T_G0 : bi == 14 ? T_G0 + 1 : bi == 15 ? T_G0 + 2 : T_NONE; }
  static constexpr int freegrp(int pos) { return pos == 0 ? T_H0 + 2 : pos == 1 ? T_H0 + 3 : pos == 3 ? T_H0 + 4 : pos == 4 ? T_H0 + 5 : (pos >= 34 && pos < 38) ? T_S4 + 3 : T_NONE; }
  static constexpr int freen(int pos) { return freegrp(pos) == T_NONE ? 0 : pos < 8 ? 3 : 1; }
  static constexpr int gsize(int t) { return t == T_NONE ? 0 : t < T_S4 ? 3 : 4; }
  static constexpr int rel(int bi, int k) { return bi < 8 ? k : bi == 15 ? (k >> 1) : 2 * k; }
  static constexpr int after(int t, int bi) {
    int cnt = -1;
    auto see = [&](int tag, int n) { for (int i = 0; i < n; ++i) { if (tag == t) cnt = 0; else if (cnt >= 0) ++cnt; } };
    see(T_H0, gsize(T_H0)); see(T_EPOCH, 2); see(T_H0 + 1, gsize(T_H0 + 1)); see(T_CSL, 4);
    if (bi == -1) return cnt;
    for (int pos = 0; pos < 96; ++pos) {
      if (pos < 2) see(freegrp(pos), freen(pos));
      const int b = barat(pos);
      if (b >= 0) {
        if (b == bi) return cnt;
        if (b == 13) { see(T_KL, 6); see(T_QL, 1); }
        if (b == 14) see(T_POLL2, 1);
      }
      for (int bb = 0; bb < 16; ++bb) for (int k = 0; k < gsize(grp(bb)); ++k) if (barpos(bb) + rel(bb, k) == pos) see(grp(bb), 1);
      if (pos >= 2) see(freegrp(pos), freen(pos));
      if (pos == 32 && TYPE == 1) see(T_QP, 2);
      if (pos == 30) see(T_GR, 2);
      if (pos == 74) see(T_POLL, 1);
      if (pos == 50) see(T_POISON, npoison());
      if (pos == 46) see(T_E0, nst(0));
      if (pos == 63 || pos == 95) see(T_E0 + (pos >> 5), nst(pos >> 5));
    }
    return cnt;
  }
  static constexpr int mn(int a, int b) { return a < b ? a : b; }
};

template <int TYPE> DI unsigned tile_addr(const Ctx& C, int p, int t) {
  const int lane = fresh(C.lane), kind = G1<TYPE>::kind(p);
  const int g = (lane >> 4) & 1, h = lane >> 5, q = (lane & 15) >> 2, pp = lane & 3;
  const unsigned rdrow = (8 * h + q) * 128 + pp * 8;
  if (kind == 2) return g * 16384 + rdrow + (((2 * C.ng + t) ^ (2 * (q >> 1)) ^ g) << 5);
  if (kind == 0) return 16384 + rdrow + ((C.ng ^ (q >> 1)) << 6) + g * 32;
  if (t == 0) return rdrow + ((C.ng ^ (q >> 1)) << 6) + g * 32;
  return 16384 + g * 8192 + (8 * h + q) * 64 + pp * 8 + ((C.ng ^ g) << 5);
}
template <int TYPE> DI unsigned stage_voff(const Ctx& C, int p, int which) {
  const int kind = G1<TYPE>::kind(p), lane = fresh(C.lane);
  if (which == 1) return (16 * C.wid + (lane >> 2)) * 3584 + (8 + ((C.cs - 8) >> 1)) * 128 + ((C.cs - 8) & 1) * 64 + ((lane & 3) << 4);
  const unsigned row = 16 * C.wid + (lane >> 3), sw = ((lane & 7) ^ (((lane >> 4) & 1) << 2)) << 4;
  if (kind == 1) return row * 3072 + (C.cs - 8) * 128 + sw;
  return row * 3584 + (p == 1 ? C.cs : 12 + C.cs) * 128 + sw;
}
template <int TYPE> DI void issue_stage(const Ctx& C, int s, int k, unsigned v0) {
  const int kind = G1<TYPE>::kind(s >> 2);
  const unsigned slot = G1<TYPE>::slot(s), r0 = 128 * (s & 3);
  if (kind == 1) {
    if (k < 2) dma16(C.sQKVw, v0, (r0 + 8 * k) * 3072, slot + C.wid * 2048 + k * 1024);
    else dma16(k == 3 ? C.sU : C.sG, k == 3 ? v0 ^ 32 : v0, r0 * 3584, slot + 16384 + (k - 2) * 8192 + C.wid * 1024);
  } else dma16(k >> 1 ? C.sU : C.sG, k >> 1 ? v0 ^ 32 : v0, (r0 + 8 * (k & 1)) * 3584, slot + (k >> 1) * 16384 + C.wid * 2048 + (k & 1) * 1024);
}
DI unsigned hx_voff(const Ctx& C) { const int lane = fresh(C.lane); return (16 * C.wid + (lane >> 3)) * 1024 + (((lane & 7) ^ ((lane >> 4) & 3)) << 4); }
DI unsigned hw_voff(const Ctx& C) { const int lane = fresh(C.lane);
  return (8 * C.wid + (lane >> 3)) * 3072 + (8 + C.cs) * 128 + (((lane & 7) ^ (((lane >> 4) & 1) << 2)) << 4); }
DI void issue_half(const Ctx& C, int h, int k, unsigned vx, unsigned vw) {
  const unsigned slot = (h % 6) * 24576;
  if (k < 2) dma16(C.sX, k ? (vx ^ 64) + 8 * 1024 : vx, h * 128, slot + C.wid * 2048 + k * 1024);
  else dma16(C.sQKVw, vw + (k - 2) * 128, h * 64 * 3072, slot + 16384 + (k - 2) * 8192 + C.wid * 1024);
}
DI void warm(i32x4 srd, unsigned line0, unsigned lines_per_row_log2, unsigned pitch, unsigned colbyte, int lane) {
  const unsigned l = line0 + lane;
  dma4(srd, (l >> lines_per_row_log2) * pitch + colbyte + (l & ((1u << lines_per_row_log2) - 1)) * 128, L_JUNK);
}
DI unsigned g_voff(const Ctx& C) { const int lane = fresh(C.lane); return (32 * (C.wid & 1) + (lane >> 3)) * 1024 + C.cg * 128 + (((lane & 7) ^ (((lane >> 4) & 1) << 2)) << 4); }
constexpr unsigned gslot(int j) { return j == 0 ? R2 : j == 1 ? R0 : j == 2 ? R1 : j == 3 ? R3 : j == 4 ? R2 : j == 5 ? R0 : j == 6 ? R1 : j == 7 ? R3 : R2; }
DI void issue_g(const Ctx& C, int j, int i, unsigned v) {
  const int kq = C.wid >> 1;
  const int row0 = j < 3 ? (3 * kq + j) * 64 : j < 7 ? (12 + 4 * kq + (j - 3)) * 64 : 128 * kq + 64 * (j - 7);
  dma16(j < 7 ? C.sWD : C.sWO, v, (unsigned)(row0 + 8 * i) * 1024, gslot(j) + kq * 8192 + (C.wid & 1) * 4096 + i * 1024);
}

template <int WHAT> DI void epi_qkv(const Ctx& C, const f32x16& acc, const f32x4 (&csr)[4], float m, float rinv, int qu, int h4) {
  const int h = C.lane >> 5;
  const float sc = WHAT == 0 ? rinv * QSCALE : rinv;
  float y[16];
#pragma unroll
  for (int i4 = 0; i4 < 4; ++i4)
#pragma unroll
    for (int e = 0; e < 4; ++e) y[4 * i4 + e] = (acc[4 * i4 + e] - csr[i4][e]) * sc;
  const unsigned base = (unsigned)WS_QKV + (((C.rb * 24 + qu) * 4 + C.mg) * 4 + h4) * 1024;
#pragma unroll
  for (int hh = 0; hh < 2; ++hh) {
    u32x4 f; f[0] = cvtpk(y[8 * hh], y[8 * hh + 1]); f[1] = cvtpk(y[8 * hh + 2], y[8 * hh + 3]); f[2] = cvtpk(y[8 * hh + 4], y[8 * hh + 5]); f[3] = cvtpk(y[8 * hh + 6], y[8 * hh + 7]);
    unsigned voff = C.lane * 16;
    if (WHAT == 2) {
      auto a = __builtin_amdgcn_permlane32_swap(f[0], f[2], false, false); auto b = __builtin_amdgcn_permlane32_swap(f[1], f[3], false, false);
      f[0] = a[0]; f[2] = a[1]; f[1] = b[0]; f[3] = b[1];
      voff = (C.lane & 31) * 32 + h * 16;
    }
    st16_sc1(f, C.sWS, voff, base + hh * 1024);
  }
}
DI void epi_gu(const Ctx& C, const f32x16& acc, const f32x4 (&csr)[4], float m, float r2, unsigned a2off) {
  float y[8];
#pragma unroll
  for (int i4 = 0; i4 < 2; ++i4)
#pragma unroll
    for (int e = 0; e < 4; ++e) y[4 * i4 + e] = ((acc[4 * i4 + e] - csr[i4][e]) * r2) * (acc[8 + 4 * i4 + e] - csr[2 + i4][e]);
  u32x4 f; f[0] = cvtpk(y[0], y[1]); f[1] = cvtpk(y[2], y[3]); f[2] = cvtpk(y[4], y[5]); f[3] = cvtpk(y[6], y[7]);
  st16_sc1(f, C.sWS, C.lane * 16, a2off);
}
template <int TYPE> DI unsigned a2_pass(const Ctx& C, int p) {
  if (p == 2) return (unsigned)WS_A2 + (((4 * C.rb + C.mg) * 4 + (C.cs >> 2)) * 36 + 4 * (3 + (C.cs & 3)) + 2 * C.ng) * 1024;
  if (TYPE == 0) return (unsigned)WS_A2 + (((4 * C.rb + C.mg) * 4 + C.cs / 3) * 36 + 4 * (C.cs % 3) + 2 * C.ng) * 1024;
  const int u = 8 + ((C.cs - 8) >> 1), half = (C.cs - 8) & 1;
  return (unsigned)WS_A2 + (((4 * C.rb + C.mg) * 4 + u / 3) * 36 + 4 * (u % 3) + 2 * half + C.ng) * 1024;
}
DI unsigned a2_o(const Ctx& C) { return (unsigned)WS_A2 + (((4 * C.rb + C.mg) * 4 + (C.cs >> 2)) * 36 + 28 + 2 * (C.cs & 3) + C.ng) * 1024; }

template <int TYPE> DI void gemm1(Ctx& C, unsigned vx, unsigned vw, u32x4& qfrag, u32x2& poll2, unsigned& poll2off) {
  using G = G1<TYPE>;
  const int lane = C.lane;
  char* ws = C.ws;
  bf16x8 xa[32];
  bf16x8 wf[3][2];
  f32x16 acc[2], acc0;
  unsigned ta[2], sv = 0, sv2 = 0;
  ta[0] = pinv(tile_addr<TYPE>(C, 0, 0)); ta[1] = pinv(tile_addr<TYPE>(C, 0, 1));
  unsigned th[2];
  th[0] = ta[0] + 65536; th[1] = ta[1] + 65536;
  const unsigned r31 = fresh(lane) & 31, hh = fresh(lane) >> 5;
  const unsigned xlane = (32 * C.mg + r31) * 128 + ((hh ^ ((r31 >> 1) & 7)) << 4);
  f32x4 csr[2][4];
  float m = 0.f, rinv = 0.f;
  u32x2 pollv, gs; u32x4 gk[6], gc;
  unsigned polloff = 0, gsoff = 0, gcoff = 0; bool pollneed = false;
  const unsigned qoff = (unsigned)WS_QKV + (((C.rb * 24 + (C.cs >> 1)) * 4 + C.mg) * 4 + 2 * (C.cs & 1) + C.ng) * 1024;

  u32x2 ev; ld8_sc1(ev, C.sWS, (unsigned)WS_EPOCH);
  u32x4 srow, cl[4];
  ld16(srow, C.sXall, (8 * C.bid + C.wid) * 1024 + fresh(lane) * 16, 0);
  issue_half(C, 1, 0, vx, vw); issue_half(C, 1, 1, vx, vw); issue_half(C, 1, 2, vx, vw);
  { const int pan = C.bid >> 1, lf = fresh(lane);
    const int t = C.bid - 160, row = 24 * t + 3 * C.wid;
    const unsigned pitch = pan < 24 ? 3072 : pan < 80 ? 3584 : 128, col = (pan < 24 ? pan : pan < 52 ? pan - 24 : pan - 52) * 128;
    i32x4 sp;
#pragma unroll
    for (int e = 0; e < 4; ++e) sp[e] = pan < 24 ? C.sQKVw[e] : pan < 52 ? C.sG[e] : pan < 80 ? C.sU[e] : row < 512 ? C.sWO[e] : C.sWD[e];
    const unsigned v = pan < 80 ? (256 * (C.bid & 1) + 32 * C.wid + (lf >> 3)) * pitch + col + (lf & 7) * 16 : (row < 512 ? row : row - 512) * 1024 + lf * 16;
#pragma unroll
    for (int k = 0; k < 4; ++k) ld16(cl[k], sp, v, 8 * k * pitch); }
  STAMP(1);
  bar_vm<G::after(T_EPOCH, -1)>();
  STAMP(2);
  {
  pin(ev); pin(srow);
  { const u64 t = (((u64)ev[1] << 32) | ev[0]) * 6364136223846793005ull + 1442695040888963407ull;
    C.tag = ((u64)__builtin_amdgcn_readfirstlane((unsigned)(t >> 32)) << 32) | __builtin_amdgcn_readfirstlane((unsigned)t); }
  float xv[8];
#pragma unroll
  for (int e = 0; e < 4; ++e) { xv[2 * e] = bflo(srow[e]); xv[2 * e + 1] = bfhi(srow[e]); }
  const float mean = wave_sum(((xv[0] + xv[1]) + (xv[2] + xv[3])) + ((xv[4] + xv[5]) + (xv[6] + xv[7]))) * (1.f / 512.f);
  float q2 = 0.f;
#pragma unroll
  for (int e = 0; e < 8; ++e) { const float d = xv[e] - mean; q2 += d * d; }
  const float var = wave_sum(q2) * (1.f / 512.f);
  const float sd = __builtin_amdgcn_sqrtf(rbf(rbf(var) + 1e-5f));
  if (lane == 0) __hip_atomic_store((u64*)(ws + WS_GS + (8 * C.bid + C.wid) * 8), ((u64)(unsigned)C.tag << 32) | cvtpk(mean, sd), __ATOMIC_RELAXED, __HIP_MEMORY_SCOPE_AGENT);
  }
  auto read_x = [&](int h) {
    const unsigned base = G::hslot(h) + xlane;
#pragma unroll
    for (int i = 0; i < 4; ++i) xa[4 * h + i] = ld128(base ^ (i << 5));
  };
  auto read_w = [&](int pos) {
    const int p = pos >> 5, s = pos >> 3, i = pos & 7;
    if ((pos & 31) == 0 && pos > 0) { ta[0] = pinv(tile_addr<TYPE>(C, p, 0)); ta[1] = pinv(tile_addr<TYPE>(C, p, 1)); th[0] = pinv(ta[0] + 65536); th[1] = pinv(ta[1] + 65536); }
    const unsigned sb = p == 0 ? G::hslot(pos >> 2) : G::slot(s), ks = p == 0 ? (i & 3) : i;
    const bool hi = sb >= 65536;
    const unsigned b0 = hi ? th[0] : ta[0], b1 = hi ? th[1] : ta[1], so = hi ? sb - 65536 : sb;
    wf[pos % 3][0] = ldfrag_tr(b0 + so + ks * 2048, 512);
    if (G::nt(p) == 2) { if (G::kind(p) == 1) wf[pos % 3][1] = ldfrag_tr(b1 + so + ks * 1024, 256); else wf[pos % 3][1] = ldfrag_tr(b1 + so + ks * 2048, 512); }
  };
  read_x(0); read_w(0); read_w(1);
  static_for<0, 96>([&](auto ic) {
    constexpr int pos = decltype(ic)::value;
    constexpr int p = pos >> 5, s = pos >> 3, st = s & 3, i = pos & 7, NT = G::nt(p), bi = G::barat(pos);
    if constexpr (pos < 2) static_for<0, 3>([&](auto kc) { issue_half(C, 2 + pos, decltype(kc)::value, vx, vw); });
    if constexpr (bi >= 0) {
      STAMP(10 + 2 * bi);
      if constexpr (bi == 0) bar_vm<G::mn(G::after(T_H0 + 1, 0), G::after(T_CSL, 0))>();
      else if constexpr (bi < 7) bar_vm<G::after(T_H0 + bi + 1, bi)>();
      else if constexpr (bi == 8) bar_vm<G::mn(G::after(T_S4 + 1, 8), G::after(T_GR, 8))>();
      else if constexpr (bi == 11) { bar_vm<G::mn(G::mn(G::after(T_S4 + 4, 11), G::after(T_E0, 11)), TYPE == 1 ? G::after(T_QP, 11) : 63)>(); publish(C, 0); }
      else if constexpr (bi == 13) { static_assert(G::after(T_E0 + 1, 13) <= G::after(T_POISON, 13), ""); bar_vm<G::mn(G::mn(G::after(T_S4 + 6, 13), G::after(T_E0 + 1, 13)), G::after(T_POLL, 13))>(); }
      else if constexpr (bi == 15) bar_vm<G::after(T_KL, 15)>();
      else bar_vm<G::after(T_S4 + bi - 7, bi)>();
      if constexpr (bi == 0) {
      pin(cl[0]); pin(cl[1]); pin(cl[2]); pin(cl[3]);
      float cz[8];
#pragma unroll
      for (int e = 0; e < 4; ++e) { cz[2 * e] = (bflo(cl[0][e]) + bflo(cl[1][e])) + (bflo(cl[2][e]) + bflo(cl[3][e])); cz[2 * e + 1] = (bfhi(cl[0][e]) + bfhi(cl[1][e])) + (bfhi(cl[2][e]) + bfhi(cl[3][e])); }
#pragma unroll
      for (int e = 0; e < 8; ++e) cz[e] = swap_add(swap16_add(ror_add(cz[e], IC<0x128>{})));
      if (lane < 8) { *lp<f32x4>(L_CSP + C.wid * 256 + lane * 32) = f32x4{cz[0], cz[1], cz[2], cz[3]}; *lp<f32x4>(L_CSP + C.wid * 256 + lane * 32 + 16) = f32x4{cz[4], cz[5], cz[6], cz[7]}; }
      }
      if constexpr (bi == 1) {
        if (C.wid == 0 && C.bid < 160) {
          float t = 0.f;
#pragma unroll
          for (int w = 0; w < 8; ++w) t += *lp<float>(L_CSP + w * 256 + lane * 4);
          __hip_atomic_store((u64*)(ws + WS_GC + ((C.bid >> 1) * 64 + lane) * 16 + (C.bid & 1) * 8), ((u64)(unsigned)C.tag << 32) | __float_as_uint(t), __ATOMIC_RELAXED, __HIP_MEMORY_SCOPE_AGENT);
        }
      }
      if constexpr (bi == 8) {
        pin(gs); pin(gc);
        if (!__all(gs[1] == (unsigned)C.tag && gc[1] == (unsigned)C.tag && gc[3] == (unsigned)C.tag)) {
#pragma nounroll
          for (unsigned it = 0; it < SPIN_CAP; ++it) {
            __builtin_amdgcn_s_sleep(2);
            ld8_sc1(gs, C.sWS, gsoff); ld16_sc1(gc, C.sWS, gcoff, 0); wait_vm<0>(); pin(gs); pin(gc);
            if (__all(gs[1] == (unsigned)C.tag && gc[1] == (unsigned)C.tag && gc[3] == (unsigned)C.tag)) break;
            if (it == SPIN_CAP - 1) give_up(ws);
          }
        }
        m = bflo(gs[0]); rinv = __builtin_amdgcn_rcpf(bfhi(gs[0]));
        *lp<float>(L_CS + (C.wid < 6 ? C.wid : 5) * 256 + lane * 4) = __uint_as_float(gc[0]) + __uint_as_float(gc[2]);
      }
      if constexpr (bi == 9) {
        f32x4 c0[4];
#pragma unroll
        for (int i4 = 0; i4 < 4; ++i4) c0[i4] = *lp<f32x4>(L_CS + (32 * C.ng + 8 * i4 + 4 * hh) * 4) * m;
        epi_qkv<1 + TYPE>(C, acc0, c0, m, rinv, 8 + C.cs, 2 * C.ng);
      }
      if constexpr (bi == 13) {
        check_flags(ws, polloff, pollneed, C.tag, pollv);
        const int u = C.cs >> 1, h4 = 2 * (C.cs & 1) + C.ng;
#pragma unroll
        for (int b = 0; b < 6; ++b) { const int rbp = C.rb - 2 + b % 3 < 0 ? 0 : C.rb - 2 + b % 3;
          ld16_sc1(gk[b], C.sWS, lane * 16, (unsigned)WS_QKV + (((rbp * 24 + (b < 3 ? 8 : 16) + u) * 4 + C.mg) * 4 + h4) * 1024); }
        publish(C, 1); ld16_sc1(qfrag, C.sWS, lane * 16, qoff);
      }
      if constexpr (bi == 15) {
#pragma unroll
        for (int b = 0; b < 6; ++b) pin(gk[b]);
#pragma unroll
        for (int b = 0; b < 3; ++b) { *lp<u32x4>(L_K + ((4 * b + C.mg) * 2 + C.ng) * 1024 + lane * 16) = gk[b];
                                      *lp<u32x4>(L_V + C.ng * 12288 + (4 * b + C.mg) * 1024 + lane * 16) = gk[3 + b]; }
      }
      if constexpr (bi == 14) {
        { const int ln = fresh(lane), l = ln < 10 ? ln : 0, uu = 3 * C.kq + (l >> 1);
          poll2off = flag_off(1, C.rb2, l < 6 ? (uu < 8 ? uu : 8 + 2 * (uu - 8) + (l & 1)) : 4 * C.kq + (l - 6)); }
        ld8_sc1(poll2, C.sWS, poll2off);
      }
      if constexpr (bi < 7) read_x(bi + 1);
      STAMP(11 + 2 * bi);
    }
    static_for<0, 16>([&](auto bc) {
      constexpr int bb = decltype(bc)::value, tg = G::grp(bb), k0 = pos - G::barpos(bb);
      static_for<0, 4>([&](auto kc) {
        constexpr int k = decltype(kc)::value;
        if constexpr (k < G::gsize(tg) && G::rel(bb, k) == k0) {
          if constexpr (tg < T_S4) issue_half(C, tg, k, vx, vw);
          else if constexpr (tg >= T_G0) { if constexpr (k == 0) sv = g_voff(C); issue_g(C, tg - T_G0, k, sv); }
          else { constexpr int pp_ = (tg - T_S4 + 4) >> 2;
            if constexpr (k == 0) sv = stage_voff<TYPE>(C, pp_, 0);
            if constexpr (k == 2 && G::kind(pp_) == 1) sv = stage_voff<TYPE>(C, pp_, 1);
            issue_stage<TYPE>(C, tg - T_S4 + 4, k, sv); }
        }
      });
    });
    if constexpr (pos == 3 || pos == 4) static_for<0, 3>([&](auto kc) { issue_half(C, G::freegrp(pos) - T_H0, decltype(kc)::value, vx, vw); });
    if constexpr (pos >= 34 && pos < 38) {
      if constexpr (pos == 34) sv2 = stage_voff<TYPE>(C, 1, 0);
      if constexpr (pos == 36 && G::kind(1) == 1) sv2 = stage_voff<TYPE>(C, 1, 1);
      issue_stage<TYPE>(C, 7, pos - 34, sv2);
    }
    if constexpr (pos == 32 && TYPE == 1) {
      const u32x4 pz = {POISON, POISON, POISON, POISON};
      const unsigned a = (unsigned)WS_QKV + (((C.rb * 24 + (C.cs - 8)) * 4 + C.mg) * 4 + 2 * C.ng) * 1024;
      st16_sc1(pz, C.sWS, lane * 16, a); st16_sc1(pz, C.sWS, lane * 16, a + 1024);
    }
    if constexpr (pos == 30) {
      const int lf = fresh(lane), w = C.wid < 6 ? C.wid : 5;
      const int guh = 8 + ((C.cs - 8) >> 1);
      const int pan = w == 0 ? 8 + C.cs : w >= 4 ? (w == 4 ? 24 : 52) + 12 + C.cs
                    : TYPE == 0 ? (w == 1 ? 24 + C.cs : 52 + C.cs) : (w == 1 ? C.cs - 8 : w == 2 ? 24 + guh : 52 + guh);
      gsoff = (unsigned)WS_GS + (C.rb * 128 + 32 * C.mg + (lf & 31)) * 8; gcoff = (unsigned)WS_GC + (pan * 64 + lf) * 16;
      ld8_sc1(gs, C.sWS, gsoff); ld16_sc1(gc, C.sWS, gcoff, 0);
    }
    if constexpr (pos == 74) {
      const int u = C.cs >> 1, l = fresh(lane) & 7;
      const int rbp = C.rb - 2 + l % 3;
      pollneed = l < 6 && rbp >= 0;
      polloff = flag_off(0, pollneed ? rbp : C.rb, l < 3 ? u : 8 + u);
      ld8_sc1(pollv, C.sWS, polloff);
    }
    if constexpr (pos == 50) {
      const u32x4 pz = {POISON, POISON, POISON, POISON};
      const unsigned a = a2_pass<TYPE>(C, 2);
      st16_sc1(pz, C.sWS, lane * 16, a);
      st16_sc1(pz, C.sWS, lane * 16, a + 1024);
      st16_sc1(pz, C.sWS, lane * 16, a2_o(C));
    }
    if constexpr (pos + 2 < 96) read_w(pos + 2);
    if constexpr ((pos & 31) == 0) { acc[0] = MFMA32(wf[pos % 3][0], xa[pos & 31], f32x16{}); if (NT == 2) acc[1] = MFMA32(wf[pos % 3][1], xa[pos & 31], f32x16{}); }
    else { acc[0] = MFMA32(wf[pos % 3][0], xa[pos & 31], acc[0]); if (NT == 2) acc[1] = MFMA32(wf[pos % 3][1], xa[pos & 31], acc[1]); }
    if constexpr (st == 3 && i == 7 && p > 0) {
      static_for<0, NT>([&](auto tc) { constexpr int t = decltype(tc)::value;
        const bool gu = G::kind(p) == 2 || (G::kind(p) == 1 && t == 1);
        const int tl = G::kind(p) == 2 ? 2 * C.ng + t : 2 * ((C.cs - 8) & 1) + C.ng;
        const unsigned s0 = L_CS + (p == 0 ? 0 : p == 2 ? 4 : (G::kind(p) == 1 ? 1 + t : 1)) * 256;
#pragma unroll
        for (int i4 = 0; i4 < 4; ++i4) {
          const unsigned a = gu ? s0 + (i4 >> 1) * 256 + (16 * tl + 8 * (i4 & 1) + 4 * hh) * 4 : s0 + (32 * C.ng + 8 * i4 + 4 * hh) * 4;
          csr[t][i4] = *lp<f32x4>(a) * m; pin(csr[t][i4]); } });
    }
    SB();
    if constexpr ((pos & 31) == 31) {
      if constexpr (G::kind(p) == 2) {
        const unsigned a2 = a2_pass<TYPE>(C, p);
        epi_gu(C, acc[0], csr[0], m, rinv * rinv, a2); epi_gu(C, acc[1], csr[1], m, rinv * rinv, a2 + 1024);
      } else if constexpr (G::kind(p) == 1) {
        epi_qkv<0>(C, acc[0], csr[0], m, rinv, C.cs - 8, 2 * C.ng);
        epi_gu(C, acc[1], csr[1], m, rinv * rinv, a2_pass<TYPE>(C, p));
      } else {
        acc0 = acc[0];
      }
      SB();
    }
  });
  static_assert(G::after(T_POLL2, 16) == 10, "attention counts on this");
  wait_vm<G::after(T_QL, 16)>();
  pin(qfrag);
  { auto dirty = [&]() { return __any(__builtin_elementwise_max(__builtin_elementwise_max(qfrag[0], qfrag[1]), __builtin_elementwise_max(qfrag[2], qfrag[3])) == POISON); };
    if (dirty()) {
#pragma nounroll
      for (unsigned it = 0; it < SPIN_CAP; ++it) {
        __builtin_amdgcn_s_sleep(2);
        ld16_sc1(qfrag, C.sWS, lane * 16, qoff); wait_vm<0>(); pin(qfrag);
        if (!dirty()) break;
        if (it == SPIN_CAP - 1) give_up(ws);
      } } }
}

template <bool EDGE, class SIDE> DI u32x4 attention(Ctx& C, const u32x4& qraw, SIDE&& side) {
  const int lane = C.lane, h = lane >> 5, g = (lane >> 4) & 1, q = (lane & 15) >> 2, pp = lane & 3;
  const bf16x8 qf = __builtin_bit_cast(bf16x8, qraw);
  const unsigned kaddr = pinv(L_K + (C.mg * 2 + C.ng) * 1024 + lane * 16);
  const int t0 = EDGE ? (C.rb == 0 ? 8 : 4) - C.mg : 1;
  const int dq = (lane & 31) - 4 * h;
  const float NEG = -1e30f;
  auto older = [&](int r) { return ((r & 3) + 8 * (r >> 2)) > dq; };
  auto max16 = [&](float& a, float& b, const f32x16& s) {
#pragma unroll
    for (int r = 0; r < 16; r += 4) { a = __builtin_fmaxf(__builtin_fmaxf(a, s[r]), s[r + 1]); b = __builtin_fmaxf(__builtin_fmaxf(b, s[r + 2]), s[r + 3]); } };
  bf16x8 kf[9];
#pragma unroll
  for (int t = 0; t < 9; ++t) if (!EDGE || t > 0) kf[t] = ld128(kaddr + t * 2048);
  float mx;
  {
    f32x16 S[3];
    f32x16 sl = MFMA32(kf[8], qf, f32x16{});
    f32x16 sf;
    if (!EDGE) sf = MFMA32(kf[0], qf, f32x16{});
    S[1] = MFMA32(kf[1], qf, f32x16{});
    S[2] = MFMA32(kf[2], qf, f32x16{});
    float a = NEG, b = NEG;
    side(std::integral_constant<int, 0>{});
    SB();
#pragma unroll
    for (int r = 0; r < 16; ++r) sl[r] = older(r) ? (EDGE ? NEG : sf[r]) : sl[r];
    max16(a, b, sl);
    static_for<1, 8>([&](auto tc) { constexpr int t = decltype(tc)::value;
      if constexpr (t + 2 < 8) S[(t + 2) % 3] = MFMA32(kf[t + 2], qf, f32x16{});
      side(std::integral_constant<int, t>{});
      SB();
      if (!EDGE || t >= t0) max16(a, b, S[t % 3]);
      SB(); });
    mx = swap_max(__builtin_fmaxf(a, b));
  }
  f32x16 negm;
#pragma unroll
  for (int r = 0; r < 16; ++r) negm[r] = -mx;
  const unsigned vbase = pinv(g == 0 ? L_V + C.ng * 12288 + C.mg * 1024 + (4 * h + q) * 32 + pp * 8 : L_ONES + 128 * (1 - h) + q * 32 + pp * 8);
  const unsigned vstr = g == 0 ? 1024u : 0u;
  f32x16 o;
  auto expack = [&](const f32x16& s, u32x4& pa, u32x4& pb) {
#pragma unroll
    for (int i = 0; i < 4; ++i) pa[i] = cvtpk(__builtin_amdgcn_exp2f(s[2 * i]), __builtin_amdgcn_exp2f(s[2 * i + 1]));
#pragma unroll
    for (int i = 0; i < 4; ++i) pb[i] = cvtpk(__builtin_amdgcn_exp2f(s[8 + 2 * i]), __builtin_amdgcn_exp2f(s[9 + 2 * i])); };
  f32x16 S[2];
  bf16x8 vf[2][2];
  {
    f32x16 sl = MFMA32(kf[8], qf, negm);
    f32x16 sf;
    if (!EDGE) sf = MFMA32(kf[0], qf, negm);
    S[1] = MFMA32(kf[1], qf, negm);
    const unsigned v8 = vbase + 8 * vstr;
    const bf16x8 va = ldfrag_tr(v8, 256), vb = ldfrag_tr(v8 + 512, 256);
    bf16x8 vc, vd;
    if (!EDGE) { vc = ldfrag_tr(vbase, 256); vd = ldfrag_tr(vbase + 512, 256); }
    vf[1][0] = ldfrag_tr(vbase + vstr, 256); vf[1][1] = ldfrag_tr(vbase + vstr + 512, 256);
    side(std::integral_constant<int, 8>{});
    SB();
#pragma unroll
    for (int r = 0; r < 16; ++r) sl[r] = older(r) ? (EDGE ? NEG : sf[r]) : sl[r];
    u32x4 pa, pb, mk0, mk1;
    expack(sl, pa, pb);
#pragma unroll
    for (int i = 0; i < 4; ++i) {
      mk0[i] = (older(2 * i) ? 0xffffu : 0u) | (older(2 * i + 1) ? 0xffff0000u : 0u);
      mk1[i] = (older(8 + 2 * i) ? 0xffffu : 0u) | (older(9 + 2 * i) ? 0xffff0000u : 0u); }
    o = MFMA32(va, __builtin_bit_cast(bf16x8, pa & ~mk0), f32x16{});
    o = MFMA32(vb, __builtin_bit_cast(bf16x8, pb & ~mk1), o);
    if (!EDGE) {
      o = MFMA32(vc, __builtin_bit_cast(bf16x8, pa & mk0), o);
      o = MFMA32(vd, __builtin_bit_cast(bf16x8, pb & mk1), o);
    }
    SB();
  }
  static_for<1, 8>([&](auto tc) { constexpr int t = decltype(tc)::value;
    if constexpr (t + 1 < 8) {
      const unsigned vt = vbase + (t + 1) * vstr;
      vf[(t + 1) & 1][0] = ldfrag_tr(vt, 256); vf[(t + 1) & 1][1] = ldfrag_tr(vt + 512, 256);
      S[(t + 1) & 1] = MFMA32(kf[t + 1], qf, negm); }
    side(std::integral_constant<int, 8 + t>{});
    SB();
    if (!EDGE || t >= t0) {
      u32x4 pa, pb;
      expack(S[t & 1], pa, pb);
      o = MFMA32(vf[t & 1][0], __builtin_bit_cast(bf16x8, pa), o);
      o = MFMA32(vf[t & 1][1], __builtin_bit_cast(bf16x8, pb), o);
    }
    SB(); });
  const float li = __builtin_amdgcn_rcpf(o[8]);
  u32x4 f; f[0] = cvtpk(o[0] * li, o[1] * li); f[1] = cvtpk(o[2] * li, o[3] * li); f[2] = cvtpk(o[4] * li, o[5] * li); f[3] = cvtpk(o[6] * li, o[7] * li);
  return f;
}

__global__ void __launch_bounds__(512, 2)
fused_kernel(const u16* __restrict__ x, const u16* __restrict__ wqkv, const u16* __restrict__ wo, const u16* __restrict__ wg, const u16* __restrict__ wu,
             const u16* __restrict__ wd, u16* __restrict__ out, char* __restrict__ ws) {
  Ctx C;
  const int tid = threadIdx.x, lane = tid & 63, wid = __builtin_amdgcn_readfirstlane(tid >> 6);
  const int b = blockIdx.x, xj = b & 7, xi = b >> 3;
  C.stampreg = 0;
  STAMP(0);
  asm volatile("" :: "s"(x), "s"(wqkv), "s"(wo), "s"(wg), "s"(wu), "s"(wd), "s"(out), "s"(ws));
  C.ws = ws; C.lane = lane; C.wid = wid; C.mg = wid & 3; C.ng = wid >> 2;
  C.rb = 8 * (xj & 1) + (xi & 7); C.cs = 4 * (xj >> 1) + (xi >> 3); C.bid = b;
  C.sX = mk_srd(x + (size_t)C.rb * 128 * 512, 128 * 1024);
  C.sQKVw = mk_srd(wqkv, 512 * 3072);
  C.xi = xi;
  const unsigned vx = hx_voff(C), vw = hw_voff(C);
  STAMP(3);
  issue_half(C, 0, 0, vx, vw); issue_half(C, 0, 1, vx, vw); issue_half(C, 0, 2, vx, vw);
  STAMP(4);
  STAMP(5);
  C.sWS = mk_srd(ws, 32u << 20); C.sXall = mk_srd(x, 2048 * 1024);
  C.sG = mk_srd(wg, 512 * 3584); C.sU = mk_srd(wu, 512 * 3584); C.sWO = mk_srd(wo, 512 * 1024); C.sWD = mk_srd(wd, 1792 * 1024);
  if (wid == 7) { u64 pc; asm volatile("s_getpc_b64 %0" : "=s"(pc));
    dma4(mk_srd((const void*)(pc & ~255ull), CODE_BYTES), (xi * 9 + (lane < 9 ? lane : 0)) * 128, L_JUNK); }
  if (tid < 256) lp<unsigned>(L_ONES)[tid] = 0x3f803f80u;
  const int rt64 = 4 * xj + (xi & 3), cg = xi >> 2;
  C.cg = cg;
  const int rt = wid & 1, kq = wid >> 1, RT = 2 * rt64 + rt, rb2 = RT >> 2;
  C.kq = kq; C.rb2 = rb2;
  u32x4 qfrag; u32x2 poll2; unsigned poll2off;
  if (C.cs < 8) gemm1<0>(C, vx, vw, qfrag, poll2, poll2off); else gemm1<1>(C, vx, vw, qfrag, poll2, poll2off);
  STAMP(6);

  asm volatile("s_waitcnt lgkmcnt(0)\n\ts_barrier" ::: "memory");
  const unsigned a2base = (unsigned)WS_A2 + (RT * 4 + kq) * 36 * 1024;
  u32x4 a2[36];
  auto side = [&](auto kc) { constexpr int k = decltype(kc)::value;
    if constexpr (k == 1) { wait_vm<10>(); check_flags(ws, poll2off, lane < 10, C.tag, poll2); }
    if constexpr (k >= 1 && k < 7) { ld16_sc1(a2[2 * k - 2], C.sWS, lane * 16, a2base + (2 * k - 2) * 1024); ld16_sc1(a2[2 * k - 1], C.sWS, lane * 16, a2base + (2 * k - 1) * 1024); }
    if constexpr (k >= 12) static_for<0, 4>([&](auto jc) { constexpr int q_ = 12 + 4 * (k - 12) + decltype(jc)::value; ld16_sc1(a2[q_], C.sWS, lane * 16, a2base + q_ * 1024); });
    if constexpr (k >= 7 && k < 13 && !NO_WARM) { const unsigned l = (xi * 6 + (k - 7)) * 64 + wid * 8 + (fresh(lane) & 7);
      dma4(l < 8192 ? C.sWD : C.sWO, (l < 8192 ? l + 768 * 8 : l - 8192) * 128, L_JUNK); } };
  u32x4 of;
  if (C.rb >= 2) of = attention<false>(C, qfrag, side); else of = attention<true>(C, qfrag, side);
  st16_sc1(of, C.sWS, lane * 16, a2_o(C));
  STAMP(7);

  const int g = (lane >> 4) & 1, h = lane >> 5, q = (lane & 15) >> 2, pp = lane & 3;
  const unsigned w2l0 = kq * 8192 + (4 * h + q) * 128 + ((0 ^ (q >> 1)) << 6) + g * 32 + pp * 8;
  const unsigned w2l1 = kq * 8192 + (4 * h + q) * 128 + ((1 ^ (q >> 1)) << 6) + g * 32 + pp * 8;
  bf16x8 wf[3][2];
  f32x16 acc[2];
  u32x4 xres;
  unsigned wb[2], gv;
  auto read_w2 = [&](int pos) {
    if ((pos & 3) == 0) { wb[0] = pinv(w2l0 + gslot(pos >> 2)); wb[1] = pinv(w2l1 + gslot(pos >> 2)); }
    wf[pos % 3][0] = ldfrag_tr(wb[0] + (pos & 3) * 2048, 1024); wf[pos % 3][1] = ldfrag_tr(wb[1] + (pos & 3) * 2048, 1024); };
  bar_vm<17 + (NO_WARM ? 0 : 6)>();
  static_for<0, 12>([&](auto kc) { constexpr int k = decltype(kc)::value; pin(a2[k]); });
  read_w2(0); read_w2(1);
  gv = g_voff(C);
  STAMP(8);
  u32x4 ef[4];
  static_for<0, 36>([&](auto ic) {
    constexpr int pos = decltype(ic)::value;
    constexpr int j = pos >> 2, i = pos & 3;
    if constexpr (i == 2) {
      STAMP(44 + 2 * j);
      if constexpr (j <= 1) bar_vm<63>();
      if constexpr (j == 2) bar_vm<6>();
      if constexpr (j == 3) bar_vm<6>();
      if constexpr (j == 4) bar_vm<7>();
      if constexpr (j == 5) bar_vm<15>();
      if constexpr (j == 6) bar_vm<4>();
      if constexpr (j == 7) bar_vm<0>();
      STAMP(45 + 2 * j);
    }
    if constexpr (pos == 10) { static_for<12, 28>([&](auto kc) { pin(a2[decltype(kc)::value]); }); check_frags<12, 28>(C, a2, a2base); }
    if constexpr (pos == 26) { static_for<28, 36>([&](auto kc) { pin(a2[decltype(kc)::value]); }); pin(xres); check_frags<28, 36>(C, a2, a2base); }
    if constexpr (pos == 16) { ld16(xres, C.sXall, (RT * 32 + (lane & 31)) * 1024 + cg * 128 + kq * 32 + h * 16, 0); }
    if constexpr (pos == 18 || pos == 19) static_for<0, 4>([&](auto kc) { constexpr int k = 28 + 4 * (pos - 18) + decltype(kc)::value; ld16_sc1(a2[k], C.sWS, lane * 16, a2base + k * 1024); });
    if constexpr (pos < 24) issue_g(C, 3 + (pos >> 2), pos & 3, gv);
    if constexpr (pos == 30) { if (b == 0 && wid == 7) {
#pragma unroll
        for (int k = 0; k < 4; ++k) { u32x2 t; ld8_sc1(t, C.sWS, flag_off(0, 0, 0) + (lane * 4 + k) * 128); ef[k][0] = t[0]; ef[k][1] = t[1]; } } }
    if constexpr (pos + 2 < 36) read_w2(pos + 2);
    const bf16x8 af = __builtin_bit_cast(bf16x8, a2[pos]);
    if constexpr (pos == 0) { acc[0] = MFMA32(wf[0][0], af, f32x16{}); acc[1] = MFMA32(wf[0][1], af, f32x16{}); }
    else { acc[0] = MFMA32(wf[pos % 3][0], af, acc[0]); acc[1] = MFMA32(wf[pos % 3][1], af, acc[1]); }
    SB();
  });
  STAMP(9);
#pragma unroll
  for (int d = 0; d < 4; ++d) if (d != kq) {
    const int ct = d >> 1, r0 = (d & 1) * 8;
    const unsigned a = R0 + ((rt * 4 + d) * 4 + kq) * 2048 + lane * 16;
    *lp<f32x4>(a) = f32x4{acc[ct][r0], acc[ct][r0 + 1], acc[ct][r0 + 2], acc[ct][r0 + 3]};
    *lp<f32x4>(a + 1024) = f32x4{acc[ct][r0 + 4], acc[ct][r0 + 5], acc[ct][r0 + 6], acc[ct][r0 + 7]};
  }
  float s[8];
#pragma unroll
  for (int e = 0; e < 8; ++e) { const float lo = (kq & 1) ? acc[0][8 + e] : acc[0][e], hi = (kq & 1) ? acc[1][8 + e] : acc[1][e]; s[e] = (kq & 2) ? hi : lo; }
  asm volatile("s_waitcnt lgkmcnt(0)\n\ts_barrier" ::: "memory");
#pragma unroll
  for (int d = 0; d < 4; ++d) if (d != kq) {
    const unsigned a = R0 + ((rt * 4 + kq) * 4 + d) * 2048 + lane * 16;
    const f32x4 u = *lp<f32x4>(a), v = *lp<f32x4>(a + 1024);
#pragma unroll
    for (int e = 0; e < 4; ++e) { s[e] += u[e]; s[4 + e] += v[e]; }
  }
  {
    unsigned u0 = __float_as_uint(s[0]), u1 = __float_as_uint(s[1]), u2 = __float_as_uint(s[2]), u3 = __float_as_uint(s[3]);
    unsigned v0 = __float_as_uint(s[4]), v1 = __float_as_uint(s[5]), v2 = __float_as_uint(s[6]), v3 = __float_as_uint(s[7]);
    auto r0 = __builtin_amdgcn_permlane32_swap(u0, v0, false, false); auto r1 = __builtin_amdgcn_permlane32_swap(u1, v1, false, false);
    auto r2 = __builtin_amdgcn_permlane32_swap(u2, v2, false, false); auto r3 = __builtin_amdgcn_permlane32_swap(u3, v3, false, false);
    const float c0 = __uint_as_float(r0[0]), c1 = __uint_as_float(r1[0]), c2 = __uint_as_float(r2[0]), c3 = __uint_as_float(r3[0]);
    const float c4 = __uint_as_float(r0[1]), c5 = __uint_as_float(r1[1]), c6 = __uint_as_float(r2[1]), c7 = __uint_as_float(r3[1]);
    u32x4 f;
    f[0] = cvtpk(c0 + bflo(xres[0]), c1 + bfhi(xres[0])); f[1] = cvtpk(c2 + bflo(xres[1]), c3 + bfhi(xres[1]));
    f[2] = cvtpk(c4 + bflo(xres[2]), c5 + bfhi(xres[2])); f[3] = cvtpk(c6 + bflo(xres[3]), c7 + bfhi(xres[3]));
    const i32x4 sO = mk_srd(out, 2048 * 1024);
    st16(f, sO, (RT * 32 + (lane & 31)) * 1024 + cg * 128 + kq * 32 + h * 16, 0);
  }
  if (b == 0 && wid == 7) {
    wait_vm<0>();
    asm volatile("" : "+v"(ef[0]), "+v"(ef[1]), "+v"(ef[2]), "+v"(ef[3]));
    bool ok = true;
#pragma unroll
    for (int k = 0; k < 4; ++k) ok = ok && ef[k][0] == (unsigned)C.tag && ef[k][1] == (unsigned)(C.tag >> 32);
    if (!__all(ok)) {
#pragma unroll 1
      for (int k = 0; k < 4; ++k) spin_flags(ws, flag_off(0, 0, 0) + (lane * 4 + k) * 128, true, C.tag);
    }
    if (lane == 0) __hip_atomic_store((u64*)(ws + WS_EPOCH), C.tag, __ATOMIC_RELAXED, __HIP_MEMORY_SCOPE_AGENT);
  }
  STAMP(60);
}

extern "C" void kernel_launch(void* const* d_in, const int* in_sizes, int n_in, void* d_out, int out_size, void* d_ws, size_t ws_size, hipStream_t stream) {
  (void)in_sizes; (void)n_in; (void)out_size; (void)ws_size;
  (void)hipFuncSetAttribute((const void*)fused_kernel, hipFuncAttributeMaxDynamicSharedMemorySize, LDS_BYTES);
  fused_kernel<<<256, 512, LDS_BYTES, stream>>>((const u16*)d_in[0], (const u16*)d_in[1], (const u16*)d_in[2], (const u16*)d_in[4], (const u16*)d_in[5], (const u16*)d_in[6],
                                                 (u16*)d_out, (char*)d_ws);
}
```

```cpp
#include <hip/hip_runtime.h>
#include <stdint.h>
#include <type_traits>

typedef unsigned short u16;
typedef unsigned long long u64;
typedef short bf16x8 __attribute__((ext_vector_type(8)));
typedef short s16x4 __attribute__((ext_vector_type(4)));
typedef float f32x2 __attribute__((ext_vector_type(2)));
typedef float f32x4 __attribute__((ext_vector_type(4)));
typedef float f32x16 __attribute__((ext_vector_type(16)));
typedef unsigned u32x2 __attribute__((ext_vector_type(2)));
typedef unsigned u32x4 __attribute__((ext_vector_type(4)));
typedef int i32x4 __attribute__((ext_vector_type(4)));
#define LDSP __attribute__((address_space(3)))
#define DI __device__ __forceinline__
template <class T> DI LDSP T* lp(unsigned a) { return (LDSP T*)(size_t)a; }
template <int I, int N, class F> DI void static_for(F&& f) { if constexpr (I < N) { f(std::integral_constant<int, I>{}); static_for<I + 1, N>(f); } }

constexpr size_t WS_EPOCH = 0;
constexpr size_t WS_ERR   = 64;
constexpr size_t WS_FLAGS = 4096;
constexpr size_t WS_GS    = 512u << 10;
constexpr size_t WS_GC    = 576u << 10;
constexpr size_t WS_QKV   = 1u << 20;
constexpr size_t WS_A2    = 8u << 20;
constexpr size_t WS_DBG   = 24u << 20;

constexpr unsigned R0 = 0, R1 = 32768, R2 = 65536, R3 = 98304;
constexpr unsigned L_K = 98304, L_V = 122880;
constexpr unsigned L_ONES = 147456;
constexpr unsigned L_CSP = 148480;
constexpr unsigned L_CS = 150528;
constexpr unsigned L_JUNK = 154624;
constexpr unsigned LDS_BYTES = 163840;
constexpr unsigned CODE_BYTES = 36864;
constexpr unsigned POISON = 0xffffffffu;
constexpr unsigned SPIN_CAP = 1u << 13;

#define NO_WARM 1
#define NO_CS 0
#define NO_DMA 0
constexpr float QSCALE = 0.25f * 1.4426950408889634f;

DI i32x4 mk_srd(const void* p, unsigned bytes) { u64 a = (u64)p; i32x4 r; r[0] = (int)(unsigned)a; r[1] = (int)((unsigned)(a >> 32) & 0xffffu); r[2] = (int)bytes; r[3] = 0x00020000; return r; }
DI void dma16(i32x4 srd, unsigned voff, unsigned soff, unsigned lds) {
  if (NO_DMA) return;
  asm volatile("s_mov_b32 m0, %3\n\ts_nop 3\n\tbuffer_load_dwordx4 %0, %1, %2 offen lds" :: "v"(voff), "s"(srd), "s"(soff), "s"(lds) : "memory");
}
DI void dma4(i32x4 srd, unsigned voff, unsigned lds) {
  asm volatile("s_mov_b32 m0, %2\n\ts_nop 3\n\tbuffer_load_dword %0, %1, 0 offen lds" :: "v"(voff), "s"(srd), "s"(lds) : "memory");
}
DI void ld16_sc1(u32x4& d, i32x4 srd, unsigned voff, unsigned soff) {
  asm volatile("s_nop 4\n\tbuffer_load_dwordx4 %0, %1, %2, %3 offen sc1" : "=v"(d) : "v"(voff), "s"(srd), "s"(soff) : "memory");
}
DI void ld16(u32x4& d, i32x4 srd, unsigned voff, unsigned soff) {
  asm volatile("s_nop 4\n\tbuffer_load_dwordx4 %0, %1, %2, %3 offen" : "=v"(d) : "v"(voff), "s"(srd), "s"(soff) : "memory");
}
DI void ld8_sc1(u32x2& d, i32x4 srd, unsigned voff) {
  asm volatile("s_nop 4\n\tbuffer_load_dwordx2 %0, %1, %2, 0 offen sc1" : "=v"(d) : "v"(voff), "s"(srd) : "memory");
}
DI void st16_sc1(u32x4 d, i32x4 srd, unsigned voff, unsigned soff) {
  asm volatile("s_nop 4\n\tbuffer_store_dwordx4 %0, %1, %2, %3 offen sc1\n\ts_nop 1" :: "v"(d), "v"(voff), "s"(srd), "s"(soff) : "memory");
}
DI void st16(u32x4 d, i32x4 srd, unsigned voff, unsigned soff) {
  asm volatile("s_nop 4\n\tbuffer_store_dwordx4 %0, %1, %2, %3 offen\n\ts_nop 1" :: "v"(d), "v"(voff), "s"(srd), "s"(soff) : "memory");
}
template <int N> DI void bar_vm() { static_assert(N >= 0 && N < 64, "vmcnt range"); asm volatile("s_waitcnt vmcnt(%0) lgkmcnt(0)\n\ts_barrier" :: "n"(N) : "memory"); }
template <int N> DI void wait_vm() { static_assert(N >= 0 && N < 64, "vmcnt range"); asm volatile("s_waitcnt vmcnt(%0)" :: "n"(N) : "memory"); }
DI s16x4 ldtr(unsigned a) { return __builtin_amdgcn_ds_read_tr16_b64_v4i16(lp<s16x4>(a)); }
DI bf16x8 ldfrag_tr(unsigned a, unsigned second) {
  s16x4 lo = ldtr(a), hi = ldtr(a + second);
  return __builtin_shufflevector(lo, hi, 0, 1, 2, 3, 4, 5, 6, 7);
}
DI bf16x8 ld128(unsigned a) { return *lp<bf16x8>(a); }
DI unsigned cvtpk(float lo, float hi) { typedef __bf16 b2 __attribute__((ext_vector_type(2))); f32x2 v = {lo, hi}; return __builtin_bit_cast(unsigned, __builtin_convertvector(v, b2)); }
DI float bflo(unsigned v) { return __uint_as_float(v << 16); }
DI float bfhi(unsigned v) { return __uint_as_float(v & 0xffff0000u); }
DI float rbf(float f) { return bflo(cvtpk(f, f) & 0xffffu); }
DI int fresh(int v) { asm volatile("" : "+v"(v)); return v; }
template <class T> DI void pin(T& v) { asm volatile("" : "+v"(v)); }
DI unsigned pinv(unsigned v) { asm volatile("" : "+v"(v)); return v; }
#define MFMA32(a, b, c) __builtin_amdgcn_mfma_f32_32x32x16_bf16((a), (b), (c), 0, 0, 0)
#define MFMA16(a, b, c) __builtin_amdgcn_mfma_f32_16x16x32_bf16((a), (b), (c), 0, 0, 0)
#define MFMA4(a, b, c) __builtin_amdgcn_mfma_f32_4x4x4bf16_1k((a), (b), (c), 0, 0, 0)
#define SB() __builtin_amdgcn_sched_barrier(0)
DI float swap_add(float v) { auto r = __builtin_amdgcn_permlane32_swap(__float_as_uint(v), __float_as_uint(v), false, false); return __uint_as_float(r[0]) + __uint_as_float(r[1]); }
template <class T> DI float ror_add(float v, T) { return v + __int_as_float(__builtin_amdgcn_update_dpp(0, __float_as_int(v), T::value, 0xf, 0xf, true)); }
DI float swap16_add(float v) { auto r = __builtin_amdgcn_permlane16_swap(__float_as_uint(v), __float_as_uint(v), false, false); return __uint_as_float(r[0]) + __uint_as_float(r[1]); }
template <int C> using IC = std::integral_constant<int, C>;
DI float wave_sum(float v) { v = ror_add(v, IC<0x121>{}); v = ror_add(v, IC<0x122>{}); v = ror_add(v, IC<0x124>{}); v = ror_add(v, IC<0x128>{}); return swap_add(swap16_add(v)); }
DI float swap_max(float v) { auto r = __builtin_amdgcn_permlane32_swap(__float_as_uint(v), __float_as_uint(v), false, false); return __builtin_fmaxf(__uint_as_float(r[0]), __uint_as_float(r[1])); }

#define STAMP(i) do { } while (0)

struct Ctx {
  char* ws; u64 tag;
  i32x4 sX, sXall, sQKVw, sG, sU, sWO, sWD, sWS;
  int lane, wid, mg, ng, rb, cs, cg, xi, kq, rb2, bid; unsigned stampreg; u64 prt0, prtx;
};

DI unsigned flag_off(int ev, int rb, int cs) { return (unsigned)(WS_FLAGS + (((ev * 16 + rb) * 16 + cs) << 7)); }
DI void publish(const Ctx& C, int ev) { if (C.wid == 0 && C.lane == 0) __hip_atomic_store((u64*)(C.ws + flag_off(ev, C.rb, C.cs)), C.tag, __ATOMIC_RELAXED, __HIP_MEMORY_SCOPE_AGENT); }
DI void give_up(char* ws) { __hip_atomic_store((unsigned*)(ws + WS_ERR), 1u, __ATOMIC_RELAXED, __HIP_MEMORY_SCOPE_AGENT); }
DI void spin_flags(char* ws, unsigned off, bool need, u64 tag) {
#pragma nounroll
  for (unsigned it = 0; it < SPIN_CAP; ++it) {
    u64 v = __hip_atomic_load((u64*)(ws + off), __ATOMIC_RELAXED, __HIP_MEMORY_SCOPE_AGENT);
    if (__all(!need || v == tag)) return;
    __builtin_amdgcn_s_sleep(2);
  }
  give_up(ws);
}
DI void check_flags(char* ws, unsigned off, bool need, u64 tag, u32x2& got) {
  asm volatile("" : "+v"(got));
  bool ok = !need || (got[0] == (unsigned)tag && got[1] == (unsigned)(tag >> 32));
  if (!__all(ok)) spin_flags(ws, off, need, tag);
}
template <int K0, int K1> DI void check_frags(const Ctx& C, u32x4 (&a2)[36], unsigned base) {
  auto dirty = [&]() { unsigned mx = 0;
    static_for<K0, K1>([&](auto kc) { constexpr int k = decltype(kc)::value;
      mx = __builtin_elementwise_max(mx, __builtin_elementwise_max(__builtin_elementwise_max(a2[k][0], a2[k][1]), __builtin_elementwise_max(a2[k][2], a2[k][3]))); });
    return __any(mx == POISON); };
  if (dirty()) {
#pragma nounroll
    for (unsigned it = 0; it < SPIN_CAP; ++it) {
      __builtin_amdgcn_s_sleep(2);
      static_for<K0, K1>([&](auto kc) { constexpr int k = decltype(kc)::value; ld16_sc1(a2[k], C.sWS, C.lane * 16, base + k * 1024); });
      wait_vm<0>();
      static_for<K0, K1>([&](auto kc) { constexpr int k = decltype(kc)::value; pin(a2[k]); });
      if (!dirty()) return;
    }
    give_up(C.ws);
  }
}

enum { T_H0 = 0, T_S4 = 8, T_E0 = 16, T_G0 = 19, T_POISON = 22, T_POLL, T_KL, T_POLL2, T_QL, T_QP, T_EPOCH, T_CSL, T_GR, T_WARM, T_NONE };
template <int TYPE> struct G1 {
  static constexpr int kind(int p) { return p == 0 ? 0 : (p == 1 && TYPE == 1) ? 1 : 2; }
  static constexpr int nt(int p) { return p == 0 ? 1 : 2; }
  static constexpr int nst(int p) { return kind(p) == 1 ? 3 : 2; }
  static constexpr int npoison() { return 3; }
  static constexpr unsigned hslot(int h) { return (h % 6) * 24576; }
  static constexpr unsigned slot(int s) { return (s & 3) == 0 ? R2 : (s & 3) == 1 ? R3 : (s & 3) == 2 ? R0 : R1; }
  static constexpr int barpos(int bi) { return bi < 8 ? 4 * bi + 2 : 8 * (bi - 4) + 6; }
  static constexpr int barat(int pos) { return pos < 32 ? ((pos & 3) == 2 ? pos >> 2 : -1) : ((pos & 7) == 6 ? 4 + (pos >> 3) : -1); }
  static constexpr int grp(int bi) {
    return bi == 0 ? T_H0 + 6 : bi == 1 ? T_H0 + 7 : bi == 3 ? T_S4 : bi == 5 ? T_S4 + 1 : bi == 7 ? T_S4 + 2 : (bi >= 8 && bi <= 11) ? T_S4 + (bi - 4)
         : bi == 12 ? T_G0 : bi == 14 ? T_G0 + 1 : bi == 15 ? T_G0 + 2 : T_NONE; }
  static constexpr int freegrp(int pos) { return pos == 0 ? T_H0 + 2 : pos == 1 ? T_H0 + 3 : pos == 3 ? T_H0 + 4 : pos == 4 ? T_H0 + 5 : (pos >= 34 && pos < 38) ? T_S4 + 3 : T_NONE; }
  static constexpr int freen(int pos) { return freegrp(pos) == T_NONE ? 0 : pos < 8 ? 3 : 1; }
  static constexpr int gsize(int t) { return t == T_NONE ? 0 : t < T_S4 ? 3 : 4; }
  static constexpr int rel(int bi, int k) { return bi < 8 ? k : bi == 15 ? (k >> 1) : 2 * k; }
  static constexpr int after(int t, int bi) {
    int cnt = -1;
    auto see = [&](int tag, int n) { for (int i = 0; i < n; ++i) { if (tag == t) cnt = 0; else if (cnt >= 0) ++cnt; } };
    see(T_H0, gsize(T_H0)); see(T_EPOCH, 2); see(T_H0 + 1, gsize(T_H0 + 1)); see(T_CSL, 4);
    if (bi == -1) return cnt;
    for (int pos = 0; pos < 96; ++pos) {
      if (pos < 2) see(freegrp(pos), freen(pos));
      const int b = barat(pos);
      if (b >= 0) {
        if (b == bi) return cnt;
        if (b == 13) { see(T_KL, 6); see(T_QL, 1); }
        if (b == 14) see(T_POLL2, 1);
      }
      for (int bb = 0; bb < 16; ++bb) for (int k = 0; k < gsize(grp(bb)); ++k) if (barpos(bb) + rel(bb, k) == pos) see(grp(bb), 1);
      if (pos >= 2) see(freegrp(pos), freen(pos));
      if (pos == 32 && TYPE == 1) see(T_QP, 2);
      if (pos == 30) see(T_GR, 2);
      if (pos == 74) see(T_POLL, 1);
      if (pos == 50) see(T_POISON, npoison());
      if (pos == 46) see(T_E0, nst(0));
      if (pos == 63 || pos == 95) see(T_E0 + (pos >> 5), nst(pos >> 5));
    }
    return cnt;
  }
  static constexpr int mn(int a, int b) { return a < b ? a : b; }
};

template <int TYPE> DI unsigned tile_addr(const Ctx& C, int p, int t) {
  const int lane = fresh(C.lane), kind = G1<TYPE>::kind(p);
  const int g = (lane >> 4) & 1, h = lane >> 5, q = (lane & 15) >> 2, pp = lane & 3;
  const unsigned rdrow = (8 * h + q) * 128 + pp * 8;
  if (kind == 2) return g * 16384 + rdrow + (((2 * C.ng + t) ^ (2 * (q >> 1)) ^ g) << 5);
  if (kind == 0) return 16384 + rdrow + ((C.ng ^ (q >> 1)) << 6) + g * 32;
  if (t == 0) return rdrow + ((C.ng ^ (q >> 1)) << 6) + g * 32;
  return 16384 + g * 8192 + (8 * h + q) * 64 + pp * 8 + ((C.ng ^ g) << 5);
}
template <int TYPE> DI unsigned stage_voff(const Ctx& C, int p, int which) {
  const int kind = G1<TYPE>::kind(p), lane = fresh(C.lane);
  if (which == 1) return (16 * C.wid + (lane >> 2)) * 3584 + (8 + ((C.cs - 8) >> 1)) * 128 + ((C.cs - 8) & 1) * 64 + ((lane & 3) << 4);
  const unsigned row = 16 * C.wid + (lane >> 3), sw = ((lane & 7) ^ (((lane >> 4) & 1) << 2)) << 4;
  if (kind == 1) return row * 3072 + (C.cs - 8) * 128 + sw;
  return row * 3584 + (p == 1 ? C.cs : 12 + C.cs) * 128 + sw;
}
template <int TYPE> DI void issue_stage(const Ctx& C, int s, int k, unsigned v0) {
  const int kind = G1<TYPE>::kind(s >> 2);
  const unsigned slot = G1<TYPE>::slot(s), r0 = 128 * (s & 3);
  if (kind == 1) {
    if (k < 2) dma16(C.sQKVw, v0, (r0 + 8 * k) * 3072, slot + C.wid * 2048 + k * 1024);
    else dma16(k == 3 ? C.sU : C.sG, k == 3 ? v0 ^ 32 : v0, r0 * 3584, slot + 16384 + (k - 2) * 8192 + C.wid * 1024);
  } else dma16(k >> 1 ? C.sU : C.sG, k >> 1 ? v0 ^ 32 : v0, (r0 + 8 * (k & 1)) * 3584, slot + (k >> 1) * 16384 + C.wid * 2048 + (k & 1) * 1024);
}
DI unsigned hx_voff(const Ctx& C) { const int lane = fresh(C.lane); return (16 * C.wid + (lane >> 3)) * 1024 + (((lane & 7) ^ ((lane >> 4) & 3)) << 4); }
DI unsigned hw_voff(const Ctx& C) { const int lane = fresh(C.lane);
  return (8 * C.wid + (lane >> 3)) * 3072 + (8 + C.cs) * 128 + (((lane & 7) ^ (((lane >> 4) & 1) << 2)) << 4); }
DI void issue_half(const Ctx& C, int h, int k, unsigned vx, unsigned vw) {
  const unsigned slot = (h % 6) * 24576;
  if (k < 2) dma16(C.sX, k ? (vx ^ 64) + 8 * 1024 : vx, h * 128, slot + C.wid * 2048 + k * 1024);
  else dma16(C.sQKVw, vw + (k - 2) * 128, h * 64 * 3072, slot + 16384 + (k - 2) * 8192 + C.wid * 1024);
}
DI void warm(i32x4 srd, unsigned line0, unsigned lines_per_row_log2, unsigned pitch, unsigned colbyte, int lane) {
  const unsigned l = line0 + lane;
  dma4(srd, (l >> lines_per_row_log2) * pitch + colbyte + (l & ((1u << lines_per_row_log2) - 1)) * 128, L_JUNK);
}
DI unsigned g_voff(const Ctx& C) { const int lane = fresh(C.lane); return (32 * (C.wid & 1) + (lane >> 3)) * 1024 + C.cg * 128 + (((lane & 7) ^ (((lane >> 4) & 1) << 2)) << 4); }
constexpr unsigned gslot(int j) { return j == 0 ? R2 : j == 1 ? R0 : j == 2 ? R1 : j == 3 ? R3 : j == 4 ? R2 : j == 5 ? R0 : j == 6 ? R1 : j == 7 ? R3 : R2; }
DI void issue_g(const Ctx& C, int j, int i, unsigned v) {
  const int kq = C.wid >> 1;
  const int row0 = j < 3 ? (3 * kq + j) * 64 : j < 7 ? (12 + 4 * kq + (j - 3)) * 64 : 128 * kq + 64 * (j - 7);
  dma16(j < 7 ? C.sWD : C.sWO, v, (unsigned)(row0 + 8 * i) * 1024, gslot(j) + kq * 8192 + (C.wid & 1) * 4096 + i * 1024);
}

template <int WHAT> DI void epi_qkv(const Ctx& C, const f32x16& acc, const f32x4 (&csr)[4], float m, float rinv, int qu, int h4) {
  const int h = C.lane >> 5;
  const float sc = WHAT == 0 ? rinv * QSCALE : rinv;
  float y[16];
#pragma unroll
  for (int i4 = 0; i4 < 4; ++i4)
#pragma unroll
    for (int e = 0; e < 4; ++e) y[4 * i4 + e] = (acc[4 * i4 + e] - csr[i4][e]) * sc;
  const unsigned base = (unsigned)WS_QKV + (((C.rb * 24 + qu) * 4 + C.mg) * 4 + h4) * 1024;
#pragma unroll
  for (int hh = 0; hh < 2; ++hh) {
    u32x4 f; f[0] = cvtpk(y[8 * hh], y[8 * hh + 1]); f[1] = cvtpk(y[8 * hh + 2], y[8 * hh + 3]); f[2] = cvtpk(y[8 * hh + 4], y[8 * hh + 5]); f[3] = cvtpk(y[8 * hh + 6], y[8 * hh + 7]);
    unsigned voff = C.lane * 16;
    if (WHAT == 2) {
      auto a = __builtin_amdgcn_permlane32_swap(f[0], f[2], false, false); auto b = __builtin_amdgcn_permlane32_swap(f[1], f[3], false, false);
      f[0] = a[0]; f[2] = a[1]; f[1] = b[0]; f[3] = b[1];
      voff = (C.lane & 31) * 32 + h * 16;
    }
    st16_sc1(f, C.sWS, voff, base + hh * 1024);
  }
}
DI void epi_gu(const Ctx& C, const f32x16& acc, const f32x4 (&csr)[4], float m, float r2, unsigned a2off) {
  float y[8];
#pragma unroll
  for (int i4 = 0; i4 < 2; ++i4)
#pragma unroll
    for (int e = 0; e < 4; ++e) y[4 * i4 + e] = ((acc[4 * i4 + e] - csr[i4][e]) * r2) * (acc[8 + 4 * i4 + e] - csr[2 + i4][e]);
  u32x4 f; f[0] = cvtpk(y[0], y[1]); f[1] = cvtpk(y[2], y[3]); f[2] = cvtpk(y[4], y[5]); f[3] = cvtpk(y[6], y[7]);
  st16_sc1(f, C.sWS, C.lane * 16, a2off);
}
template <int TYPE> DI unsigned a2_pass(const Ctx& C, int p) {
  if (p == 2) return (unsigned)WS_A2 + (((4 * C.rb + C.mg) * 4 + (C.cs >> 2)) * 36 + 4 * (3 + (C.cs & 3)) + 2 * C.ng) * 1024;
  if (TYPE == 0) return (unsigned)WS_A2 + (((4 * C.rb + C.mg) * 4 + C.cs / 3) * 36 + 4 * (C.cs % 3) + 2 * C.ng) * 1024;
  const int u = 8 + ((C.cs - 8) >> 1), half = (C.cs - 8) & 1;
  return (unsigned)WS_A2 + (((4 * C.rb + C.mg) * 4 + u / 3) * 36 + 4 * (u % 3) + 2 * half + C.ng) * 1024;
}
DI unsigned a2_o(const Ctx& C) { return (unsigned)WS_A2 + (((4 * C.rb + C.mg) * 4 + (C.cs >> 2)) * 36 + 28 + 2 * (C.cs & 3) + C.ng) * 1024; }

template <int TYPE> DI void gemm1(Ctx& C, unsigned vx, unsigned vw, u32x4& qfrag, u32x2& poll2, unsigned& poll2off) {
  using G = G1<TYPE>;
  const int lane = C.lane;
  char* ws = C.ws;
  bf16x8 xa[32];
  bf16x8 wf[3][2];
  f32x16 acc[2], acc0;
  unsigned ta[2], sv = 0, sv2 = 0;
  ta[0] = pinv(tile_addr<TYPE>(C, 0, 0)); ta[1] = pinv(tile_addr<TYPE>(C, 0, 1));
  unsigned th[2];
  th[0] = ta[0] + 65536; th[1] = ta[1] + 65536;
  const unsigned r31 = fresh(lane) & 31, hh = fresh(lane) >> 5;
  const unsigned xlane = (32 * C.mg + r31) * 128 + ((hh ^ ((r31 >> 1) & 7)) << 4);
  f32x4 csr[2][4];
  float m = 0.f, rinv = 0.f;
  u32x2 pollv, gs; u32x4 gk[6], gc;
  unsigned polloff = 0, gsoff = 0, gcoff = 0; bool pollneed = false;
  const unsigned qoff = (unsigned)WS_QKV + (((C.rb * 24 + (C.cs >> 1)) * 4 + C.mg) * 4 + 2 * (C.cs & 1) + C.ng) * 1024;

  u32x2 ev; ld8_sc1(ev, C.sWS, (unsigned)WS_EPOCH);
  u32x4 srow, cl[4];
  ld16(srow, C.sXall, (8 * C.bid + C.wid) * 1024 + fresh(lane) * 16, 0);
  issue_half(C, 1, 0, vx, vw); issue_half(C, 1, 1, vx, vw); issue_half(C, 1, 2, vx, vw);
  { const int pan = C.bid >> 1, lf = fresh(lane);
    const int t = C.bid - 160, row = 24 * t + 3 * C.wid;
    const unsigned pitch = pan < 24 ? 3072 : pan < 80 ? 3584 : 128, col = (pan < 24 ? pan : pan < 52 ? pan - 24 : pan - 52) * 128;
    i32x4 sp;
#pragma unroll
    for (int e = 0; e < 4; ++e) sp[e] = pan < 24 ? C.sQKVw[e] : pan < 52 ? C.sG[e] : pan < 80 ? C.sU[e] : row < 512 ? C.sWO[e] : C.sWD[e];
    const unsigned v = pan < 80 ? (256 * (C.bid & 1) + 32 * C.wid + (lf >> 3)) * pitch + col + (lf & 7) * 16 : (row < 512 ? row : row - 512) * 1024 + lf * 16;
#pragma unroll
    for (int k = 0; k < 4; ++k) ld16(cl[k], sp, v, 8 * k * pitch); }
  STAMP(1);
  bar_vm<G::after(T_EPOCH, -1)>();
  STAMP(2);
  {
  pin(ev); pin(srow);
  { const u64 t = (((u64)ev[1] << 32) | ev[0]) * 6364136223846793005ull + 1442695040888963407ull;
    C.tag = ((u64)__builtin_amdgcn_readfirstlane((unsigned)(t >> 32)) << 32) | __builtin_amdgcn_readfirstlane((unsigned)t); }
  float xv[8];
#pragma unroll
  for (int e = 0; e < 4; ++e) { xv[2 * e] = bflo(srow[e]); xv[2 * e + 1] = bfhi(srow[e]); }
  const float mean = wave_sum(((xv[0] + xv[1]) + (xv[2] + xv[3])) + ((xv[4] + xv[5]) + (xv[6] + xv[7]))) * (1.f / 512.f);
  float q2 = 0.f;
#pragma unroll
  for (int e = 0; e < 8; ++e) { const float d = xv[e] - mean; q2 += d * d; }
  const float var = wave_sum(q2) * (1.f / 512.f);
  const float sd = __builtin_amdgcn_sqrtf(rbf(rbf(var) + 1e-5f));
  if (lane == 0) __hip_atomic_store((u64*)(ws + WS_GS + (8 * C.bid + C.wid) * 8), ((u64)(unsigned)C.tag << 32) | cvtpk(mean, sd), __ATOMIC_RELAXED, __HIP_MEMORY_SCOPE_AGENT);
  }
  auto read_x = [&](int h) {
    const unsigned base = G::hslot(h) + xlane;
#pragma unroll
    for (int i = 0; i < 4; ++i) xa[4 * h + i] = ld128(base ^ (i << 5));
  };
  auto read_w = [&](int pos) {
    const int p = pos >> 5, s = pos >> 3, i = pos & 7;
    if ((pos & 31) == 0 && pos > 0) { ta[0] = pinv(tile_addr<TYPE>(C, p, 0)); ta[1] = pinv(tile_addr<TYPE>(C, p, 1)); th[0] = pinv(ta[0] + 65536); th[1] = pinv(ta[1] + 65536); }
    const unsigned sb = p == 0 ? G::hslot(pos >> 2) : G::slot(s), ks = p == 0 ? (i & 3) : i;
    const bool hi = sb >= 65536;
    const unsigned b0 = hi ? th[0] : ta[0], b1 = hi ? th[1] : ta[1], so = hi ? sb - 65536 : sb;
    wf[pos % 3][0] = ldfrag_tr(b0 + so + ks * 2048, 512);
    if (G::nt(p) == 2) { if (G::kind(p) == 1) wf[pos % 3][1] = ldfrag_tr(b1 + so + ks * 1024, 256); else wf[pos % 3][1] = ldfrag_tr(b1 + so + ks * 2048, 512); }
  };
  read_x(0); read_w(0); read_w(1);
  static_for<0, 96>([&](auto ic) {
    constexpr int pos = decltype(ic)::value;
    constexpr int p = pos >> 5, s = pos >> 3, st = s & 3, i = pos & 7, NT = G::nt(p), bi = G::barat(pos);
    if constexpr (pos < 2) static_for<0, 3>([&](auto kc) { issue_half(C, 2 + pos, decltype(kc)::value, vx, vw); });
    if constexpr (bi >= 0) {
      STAMP(10 + 2 * bi);
      if constexpr (bi == 0) bar_vm<G::mn(G::after(T_H0 + 1, 0), G::after(T_CSL, 0))>();
      else if constexpr (bi < 7) bar_vm<G::after(T_H0 + bi + 1, bi)>();
      else if constexpr (bi == 8) bar_vm<G::mn(G::after(T_S4 + 1, 8), G::after(T_GR, 8))>();
      else if constexpr (bi == 11) { bar_vm<G::mn(G::mn(G::after(T_S4 + 4, 11), G::after(T_E0, 11)), TYPE == 1 ? G::after(T_QP, 11) : 63)>(); publish(C, 0); }
      else if constexpr (bi == 13) { static_assert(G::after(T_E0 + 1, 13) <= G::after(T_POISON, 13), ""); bar_vm<G::mn(G::mn(G::after(T_S4 + 6, 13), G::after(T_E0 + 1, 13)), G::after(T_POLL, 13))>(); }
      else if constexpr (bi == 15) bar_vm<G::after(T_KL, 15)>();
      else bar_vm<G::after(T_S4 + bi - 7, bi)>();
      if constexpr (bi == 0) {
      pin(cl[0]); pin(cl[1]); pin(cl[2]); pin(cl[3]);
      float cz[8];
#pragma unroll
      for (int e = 0; e < 4; ++e) { cz[2 * e] = (bflo(cl[0][e]) + bflo(cl[1][e])) + (bflo(cl[2][e]) + bflo(cl[3][e])); cz[2 * e + 1] = (bfhi(cl[0][e]) + bfhi(cl[1][e])) + (bfhi(cl[2][e]) + bfhi(cl[3][e])); }
#pragma unroll
      for (int e = 0; e < 8; ++e) cz[e] = swap_add(swap16_add(ror_add(cz[e], IC<0x128>{})));
      if (lane < 8) { *lp<f32x4>(L_CSP + C.wid * 256 + lane * 32) = f32x4{cz[0], cz[1], cz[2], cz[3]}; *lp<f32x4>(L_CSP + C.wid * 256 + lane * 32 + 16) = f32x4{cz[4], cz[5], cz[6], cz[7]}; }
      }
      if constexpr (bi == 1) {
        if (C.wid == 0 && C.bid < 160) {
          float t = 0.f;
#pragma unroll
          for (int w = 0; w < 8; ++w) t += *lp<float>(L_CSP + w * 256 + lane * 4);
          __hip_atomic_store((u64*)(ws + WS_GC + ((C.bid >> 1) * 64 + lane) * 16 + (C.bid & 1) * 8), ((u64)(unsigned)C.tag << 32) | __float_as_uint(t), __ATOMIC_RELAXED, __HIP_MEMORY_SCOPE_AGENT);
        }
      }
      if constexpr (bi == 8) {
        pin(gs); pin(gc);
        if (!__all(gs[1] == (unsigned)C.tag && gc[1] == (unsigned)C.tag && gc[3] == (unsigned)C.tag)) {
#pragma nounroll
          for (unsigned it = 0; it < SPIN_CAP; ++it) {
            __builtin_amdgcn_s_sleep(2);
            ld8_sc1(gs, C.sWS, gsoff); ld16_sc1(gc, C.sWS, gcoff, 0); wait_vm<0>(); pin(gs); pin(gc);
            if (__all(gs[1] == (unsigned)C.tag && gc[1] == (unsigned)C.tag && gc[3] == (unsigned)C.tag)) break;
            if (it == SPIN_CAP - 1) give_up(ws);
          }
        }
        m = bflo(gs[0]); rinv = __builtin_amdgcn_rcpf(bfhi(gs[0]));
        *lp<float>(L_CS + (C.wid < 6 ? C.wid : 5) * 256 + lane * 4) = __uint_as_float(gc[0]) + __uint_as_float(gc[2]);
      }
      if constexpr (bi == 9) {
        f32x4 c0[4];
#pragma unroll
        for (int i4 = 0; i4 < 4; ++i4) c0[i4] = *lp<f32x4>(L_CS + (32 * C.ng + 8 * i4 + 4 * hh) * 4) * m;
        epi_qkv<1 + TYPE>(C, acc0, c0, m, rinv, 8 + C.cs, 2 * C.ng);
      }
      if constexpr (bi == 13) {
        check_flags(ws, polloff, pollneed, C.tag, pollv);
        const int u = C.cs >> 1, h4 = 2 * (C.cs & 1) + C.ng;
#pragma unroll
        for (int b = 0; b < 6; ++b) { const int rbp = C.rb - 2 + b % 3 < 0 ? 0 : C.rb - 2 + b % 3;
          ld16_sc1(gk[b], C.sWS, lane * 16, (unsigned)WS_QKV + (((rbp * 24 + (b < 3 ? 8 : 16) + u) * 4 + C.mg) * 4 + h4) * 1024); }
        publish(C, 1); ld16_sc1(qfrag, C.sWS, lane * 16, qoff);
      }
      if constexpr (bi == 15) {
#pragma unroll
        for (int b = 0; b < 6; ++b) pin(gk[b]);
#pragma unroll
        for (int b = 0; b < 3; ++b) { *lp<u32x4>(L_K + ((4 * b + C.mg) * 2 + C.ng) * 1024 + lane * 16) = gk[b];
                                      *lp<u32x4>(L_V + C.ng * 12288 + (4 * b + C.mg) * 1024 + lane * 16) = gk[3 + b]; }
      }
      if constexpr (bi == 14) {
        { const int ln = fresh(lane), l = ln < 10 ? ln : 0, uu = 3 * C.kq + (l >> 1);
          poll2off = flag_off(1, C.rb2, l < 6 ? (uu < 8 ? uu : 8 + 2 * (uu - 8) + (l & 1)) : 4 * C.kq + (l - 6)); }
        ld8_sc1(poll2, C.sWS, poll2off);
      }
      if constexpr (bi < 7) read_x(bi + 1);
      STAMP(11 + 2 * bi);
      if constexpr (bi == 7) C.prtx = __builtin_amdgcn_s_memrealtime();
    }
    static_for<0, 16>([&](auto bc) {
      constexpr int bb = decltype(bc)::value, tg = G::grp(bb), k0 = pos - G::barpos(bb);
      static_for<0, 4>([&](auto kc) {
        constexpr int k = decltype(kc)::value;
        if constexpr (k < G::gsize(tg) && G::rel(bb, k) == k0) {
          if constexpr (tg < T_S4) issue_half(C, tg, k, vx, vw);
          else if constexpr (tg >= T_G0) { if constexpr (k == 0) sv = g_voff(C); issue_g(C, tg - T_G0, k, sv); }
          else { constexpr int pp_ = (tg - T_S4 + 4) >> 2;
            if constexpr (k == 0) sv = stage_voff<TYPE>(C, pp_, 0);
            if constexpr (k == 2 && G::kind(pp_) == 1) sv = stage_voff<TYPE>(C, pp_, 1);
            issue_stage<TYPE>(C, tg - T_S4 + 4, k, sv); }
        }
      });
    });
    if constexpr (pos == 3 || pos == 4) static_for<0, 3>([&](auto kc) { issue_half(C, G::freegrp(pos) - T_H0, decltype(kc)::value, vx, vw); });
    if constexpr (pos >= 34 && pos < 38) {
      if constexpr (pos == 34) sv2 = stage_voff<TYPE>(C, 1, 0);
      if constexpr (pos == 36 && G::kind(1) == 1) sv2 = stage_voff<TYPE>(C, 1, 1);
      issue_stage<TYPE>(C, 7, pos - 34, sv2);
    }
    if constexpr (pos == 32 && TYPE == 1) {
      const u32x4 pz = {POISON, POISON, POISON, POISON};
      const unsigned a = (unsigned)WS_QKV + (((C.rb * 24 + (C.cs - 8)) * 4 + C.mg) * 4 + 2 * C.ng) * 1024;
      st16_sc1(pz, C.sWS, lane * 16, a); st16_sc1(pz, C.sWS, lane * 16, a + 1024);
    }
    if constexpr (pos == 30) {
      const int lf = fresh(lane), w = C.wid < 6 ? C.wid : 5;
      const int guh = 8 + ((C.cs - 8) >> 1);
      const int pan = w == 0 ? 8 + C.cs : w >= 4 ? (w == 4 ? 24 : 52) + 12 + C.cs
                    : TYPE == 0 ? (w == 1 ? 24 + C.cs : 52 + C.cs) : (w == 1 ? C.cs - 8 : w == 2 ? 24 + guh : 52 + guh);
      gsoff = (unsigned)WS_GS + (C.rb * 128 + 32 * C.mg + (lf & 31)) * 8; gcoff = (unsigned)WS_GC + (pan * 64 + lf) * 16;
      ld8_sc1(gs, C.sWS, gsoff); ld16_sc1(gc, C.sWS, gcoff, 0);
    }
    if constexpr (pos == 74) {
      const int u = C.cs >> 1, l = fresh(lane) & 7;
      const int rbp = C.rb - 2 + l % 3;
      pollneed = l < 6 && rbp >= 0;
      polloff = flag_off(0, pollneed ? rbp : C.rb, l < 3 ? u : 8 + u);
      ld8_sc1(pollv, C.sWS, polloff);
    }
    if constexpr (pos == 50) {
      const u32x4 pz = {POISON, POISON, POISON, POISON};
      const unsigned a = a2_pass<TYPE>(C, 2);
      st16_sc1(pz, C.sWS, lane * 16, a);
      st16_sc1(pz, C.sWS, lane * 16, a + 1024);
      st16_sc1(pz, C.sWS, lane * 16, a2_o(C));
    }
    if constexpr (pos + 2 < 96) read_w(pos + 2);
    if constexpr ((pos & 31) == 0) { acc[0] = MFMA32(wf[pos % 3][0], xa[pos & 31], f32x16{}); if (NT == 2) acc[1] = MFMA32(wf[pos % 3][1], xa[pos & 31], f32x16{}); }
    else { acc[0] = MFMA32(wf[pos % 3][0], xa[pos & 31], acc[0]); if (NT == 2) acc[1] = MFMA32(wf[pos % 3][1], xa[pos & 31], acc[1]); }
    if constexpr (st == 3 && i == 7 && p > 0) {
      static_for<0, NT>([&](auto tc) { constexpr int t = decltype(tc)::value;
        const bool gu = G::kind(p) == 2 || (G::kind(p) == 1 && t == 1);
        const int tl = G::kind(p) == 2 ? 2 * C.ng + t : 2 * ((C.cs - 8) & 1) + C.ng;
        const unsigned s0 = L_CS + (p == 0 ? 0 : p == 2 ? 4 : (G::kind(p) == 1 ? 1 + t : 1)) * 256;
#pragma unroll
        for (int i4 = 0; i4 < 4; ++i4) {
          const unsigned a = gu ? s0 + (i4 >> 1) * 256 + (16 * tl + 8 * (i4 & 1) + 4 * hh) * 4 : s0 + (32 * C.ng + 8 * i4 + 4 * hh) * 4;
          csr[t][i4] = *lp<f32x4>(a) * m; pin(csr[t][i4]); } });
    }
    SB();
    if constexpr ((pos & 31) == 31) {
      if constexpr (G::kind(p) == 2) {
        const unsigned a2 = a2_pass<TYPE>(C, p);
        epi_gu(C, acc[0], csr[0], m, rinv * rinv, a2); epi_gu(C, acc[1], csr[1], m, rinv * rinv, a2 + 1024);
      } else if constexpr (G::kind(p) == 1) {
        epi_qkv<0>(C, acc[0], csr[0], m, rinv, C.cs - 8, 2 * C.ng);
        epi_gu(C, acc[1], csr[1], m, rinv * rinv, a2_pass<TYPE>(C, p));
      } else {
        acc0 = acc[0];
      }
      SB();
    }
  });
  static_assert(G::after(T_POLL2, 16) == 10, "attention counts on this");
  wait_vm<G::after(T_QL, 16)>();
  pin(qfrag);
  { auto dirty = [&]() { return __any(__builtin_elementwise_max(__builtin_elementwise_max(qfrag[0], qfrag[1]), __builtin_elementwise_max(qfrag[2], qfrag[3])) == POISON); };
    if (dirty()) {
#pragma nounroll
      for (unsigned it = 0; it < SPIN_CAP; ++it) {
        __builtin_amdgcn_s_sleep(2);
        ld16_sc1(qfrag, C.sWS, lane * 16, qoff); wait_vm<0>(); pin(qfrag);
        if (!dirty()) break;
        if (it == SPIN_CAP - 1) give_up(ws);
      } } }
}

template <bool EDGE, class SIDE> DI u32x4 attention(Ctx& C, const u32x4& qraw, SIDE&& side) {
  const int lane = C.lane, h = lane >> 5, g = (lane >> 4) & 1, q = (lane & 15) >> 2, pp = lane & 3;
  const bf16x8 qf = __builtin_bit_cast(bf16x8, qraw);
  const unsigned kaddr = pinv(L_K + (C.mg * 2 + C.ng) * 1024 + lane * 16);
  const int t0 = EDGE ? (C.rb == 0 ? 8 : 4) - C.mg : 1;
  const int dq = (lane & 31) - 4 * h;
  const float NEG = -1e30f;
  auto older = [&](int r) { return ((r & 3) + 8 * (r >> 2)) > dq; };
  auto max16 = [&](float& a, float& b, const f32x16& s) {
#pragma unroll
    for (int r = 0; r < 16; r += 4) { a = __builtin_fmaxf(__builtin_fmaxf(a, s[r]), s[r + 1]); b = __builtin_fmaxf(__builtin_fmaxf(b, s[r + 2]), s[r + 3]); } };
  bf16x8 kf[9];
#pragma unroll
  for (int t = 0; t < 9; ++t) if (!EDGE || t > 0) kf[t] = ld128(kaddr + t * 2048);
  float mx;
  {
    f32x16 S[3];
    f32x16 sl = MFMA32(kf[8], qf, f32x16{});
    f32x16 sf;
    if (!EDGE) sf = MFMA32(kf[0], qf, f32x16{});
    S[1] = MFMA32(kf[1], qf, f32x16{});
    S[2] = MFMA32(kf[2], qf, f32x16{});
    float a = NEG, b = NEG;
    side(std::integral_constant<int, 0>{});
    SB();
#pragma unroll
    for (int r = 0; r < 16; ++r) sl[r] = older(r) ? (EDGE ? NEG : sf[r]) : sl[r];
    max16(a, b, sl);
    static_for<1, 8>([&](auto tc) { constexpr int t = decltype(tc)::value;
      if constexpr (t + 2 < 8) S[(t + 2) % 3] = MFMA32(kf[t + 2], qf, f32x16{});
      side(std::integral_constant<int, t>{});
      SB();
      if (!EDGE || t >= t0) max16(a, b, S[t % 3]);
      SB(); });
    mx = swap_max(__builtin_fmaxf(a, b));
  }
  f32x16 negm;
#pragma unroll
  for (int r = 0; r < 16; ++r) negm[r] = -mx;
  const unsigned vbase = pinv(g == 0 ? L_V + C.ng * 12288 + C.mg * 1024 + (4 * h + q) * 32 + pp * 8 : L_ONES + 128 * (1 - h) + q * 32 + pp * 8);
  const unsigned vstr = g == 0 ? 1024u : 0u;
  f32x16 o;
  auto expack = [&](const f32x16& s, u32x4& pa, u32x4& pb) {
#pragma unroll
    for (int i = 0; i < 4; ++i) pa[i] = cvtpk(__builtin_amdgcn_exp2f(s[2 * i]), __builtin_amdgcn_exp2f(s[2 * i + 1]));
#pragma unroll
    for (int i = 0; i < 4; ++i) pb[i] = cvtpk(__builtin_amdgcn_exp2f(s[8 + 2 * i]), __builtin_amdgcn_exp2f(s[9 + 2 * i])); };
  f32x16 S[2];
  bf16x8 vf[2][2];
  {
    f32x16 sl = MFMA32(kf[8], qf, negm);
    f32x16 sf;
    if (!EDGE) sf = MFMA32(kf[0], qf, negm);
    S[1] = MFMA32(kf[1], qf, negm);
    const unsigned v8 = vbase + 8 * vstr;
    const bf16x8 va = ldfrag_tr(v8, 256), vb = ldfrag_tr(v8 + 512, 256);
    bf16x8 vc, vd;
    if (!EDGE) { vc = ldfrag_tr(vbase, 256); vd = ldfrag_tr(vbase + 512, 256); }
    vf[1][0] = ldfrag_tr(vbase + vstr, 256); vf[1][1] = ldfrag_tr(vbase + vstr + 512, 256);
    side(std::integral_constant<int, 8>{});
    SB();
#pragma unroll
    for (int r = 0; r < 16; ++r) sl[r] = older(r) ? (EDGE ? NEG : sf[r]) : sl[r];
    u32x4 pa, pb, mk0, mk1;
    expack(sl, pa, pb);
#pragma unroll
    for (int i = 0; i < 4; ++i) {
      mk0[i] = (older(2 * i) ? 0xffffu : 0u) | (older(2 * i + 1) ? 0xffff0000u : 0u);
      mk1[i] = (older(8 + 2 * i) ? 0xffffu : 0u) | (older(9 + 2 * i) ? 0xffff0000u : 0u); }
    o = MFMA32(va, __builtin_bit_cast(bf16x8, pa & ~mk0), f32x16{});
    o = MFMA32(vb, __builtin_bit_cast(bf16x8, pb & ~mk1), o);
    if (!EDGE) {
      o = MFMA32(vc, __builtin_bit_cast(bf16x8, pa & mk0), o);
      o = MFMA32(vd, __builtin_bit_cast(bf16x8, pb & mk1), o);
    }
    SB();
  }
  static_for<1, 8>([&](auto tc) { constexpr int t = decltype(tc)::value;
    if constexpr (t + 1 < 8) {
      const unsigned vt = vbase + (t + 1) * vstr;
      vf[(t + 1) & 1][0] = ldfrag_tr(vt, 256); vf[(t + 1) & 1][1] = ldfrag_tr(vt + 512, 256);
      S[(t + 1) & 1] = MFMA32(kf[t + 1], qf, negm); }
    side(std::integral_constant<int, 8 + t>{});
    SB();
    if (!EDGE || t >= t0) {
      u32x4 pa, pb;
      expack(S[t & 1], pa, pb);
      o = MFMA32(vf[t & 1][0], __builtin_bit_cast(bf16x8, pa), o);
      o = MFMA32(vf[t & 1][1], __builtin_bit_cast(bf16x8, pb), o);
    }
    SB(); });
  const float li = __builtin_amdgcn_rcpf(o[8]);
  u32x4 f; f[0] = cvtpk(o[0] * li, o[1] * li); f[1] = cvtpk(o[2] * li, o[3] * li); f[2] = cvtpk(o[4] * li, o[5] * li); f[3] = cvtpk(o[6] * li, o[7] * li);
  return f;
}

__global__ void __launch_bounds__(512, 2)
fused_kernel(const u16* __restrict__ x, const u16* __restrict__ wqkv, const u16* __restrict__ wo, const u16* __restrict__ wg, const u16* __restrict__ wu,
             const u16* __restrict__ wd, u16* __restrict__ out, char* __restrict__ ws) {
  Ctx C;
  const int tid = threadIdx.x, lane = tid & 63, wid = __builtin_amdgcn_readfirstlane(tid >> 6);
  const int b = blockIdx.x, xj = b & 7, xi = b >> 3;
  C.stampreg = 0;
  STAMP(0);
  C.prt0 = __builtin_amdgcn_s_memrealtime(); C.prtx = C.prt0;
  asm volatile("" :: "s"(x), "s"(wqkv), "s"(wo), "s"(wg), "s"(wu), "s"(wd), "s"(out), "s"(ws));
  C.ws = ws; C.lane = lane; C.wid = wid; C.mg = wid & 3; C.ng = wid >> 2;
  C.rb = 8 * (xj & 1) + (xi & 7); C.cs = 4 * (xj >> 1) + (xi >> 3); C.bid = b;
  C.sX = mk_srd(x + (size_t)C.rb * 128 * 512, 128 * 1024);
  C.sQKVw = mk_srd(wqkv, 512 * 3072);
  C.xi = xi;
  const unsigned vx = hx_voff(C), vw = hw_voff(C);
  STAMP(3);
  issue_half(C, 0, 0, vx, vw); issue_half(C, 0, 1, vx, vw); issue_half(C, 0, 2, vx, vw);
  STAMP(4);
  STAMP(5);
  C.sWS = mk_srd(ws, 32u << 20); C.sXall = mk_srd(x, 2048 * 1024);
  C.sG = mk_srd(wg, 512 * 3584); C.sU = mk_srd(wu, 512 * 3584); C.sWO = mk_srd(wo, 512 * 1024); C.sWD = mk_srd(wd, 1792 * 1024);
  if (wid == 7) { u64 pc; asm volatile("s_getpc_b64 %0" : "=s"(pc));
    dma4(mk_srd((const void*)(pc & ~255ull), CODE_BYTES), (xi * 9 + (lane < 9 ? lane : 0)) * 128, L_JUNK); }
  if (tid < 256) lp<unsigned>(L_ONES)[tid] = 0x3f803f80u;
  const int rt64 = 4 * xj + (xi & 3), cg = xi >> 2;
  C.cg = cg;
  const int rt = wid & 1, kq = wid >> 1, RT = 2 * rt64 + rt, rb2 = RT >> 2;
  C.kq = kq; C.rb2 = rb2;
  u32x4 qfrag; u32x2 poll2; unsigned poll2off;
  if (C.cs < 8) gemm1<0>(C, vx, vw, qfrag, poll2, poll2off); else gemm1<1>(C, vx, vw, qfrag, poll2, poll2off);
  STAMP(6);

  asm volatile("s_waitcnt lgkmcnt(0)\n\ts_barrier" ::: "memory");
  const unsigned a2base = (unsigned)WS_A2 + (RT * 4 + kq) * 36 * 1024;
  u32x4 a2[36];
  auto side = [&](auto kc) { constexpr int k = decltype(kc)::value;
    if constexpr (k == 1) { wait_vm<10>(); check_flags(ws, poll2off, lane < 10, C.tag, poll2); }
    if constexpr (k >= 1 && k < 7) { ld16_sc1(a2[2 * k - 2], C.sWS, lane * 16, a2base + (2 * k - 2) * 1024); ld16_sc1(a2[2 * k - 1], C.sWS, lane * 16, a2base + (2 * k - 1) * 1024); }
    if constexpr (k >= 12) static_for<0, 4>([&](auto jc) { constexpr int q_ = 12 + 4 * (k - 12) + decltype(jc)::value; ld16_sc1(a2[q_], C.sWS, lane * 16, a2base + q_ * 1024); });
    if constexpr (k >= 7 && k < 13 && !NO_WARM) { const unsigned l = (xi * 6 + (k - 7)) * 64 + wid * 8 + (fresh(lane) & 7);
      dma4(l < 8192 ? C.sWD : C.sWO, (l < 8192 ? l + 768 * 8 : l - 8192) * 128, L_JUNK); } };
  u32x4 of;
  if (C.rb >= 2) of = attention<false>(C, qfrag, side); else of = attention<true>(C, qfrag, side);
  st16_sc1(of, C.sWS, lane * 16, a2_o(C));
  STAMP(7);

  const int g = (lane >> 4) & 1, h = lane >> 5, q = (lane & 15) >> 2, pp = lane & 3;
  const unsigned w2l0 = kq * 8192 + (4 * h + q) * 128 + ((0 ^ (q >> 1)) << 6) + g * 32 + pp * 8;
  const unsigned w2l1 = kq * 8192 + (4 * h + q) * 128 + ((1 ^ (q >> 1)) << 6) + g * 32 + pp * 8;
  bf16x8 wf[3][2];
  f32x16 acc[2];
  u32x4 xres;
  unsigned wb[2], gv;
  auto read_w2 = [&](int pos) {
    if ((pos & 3) == 0) { wb[0] = pinv(w2l0 + gslot(pos >> 2)); wb[1] = pinv(w2l1 + gslot(pos >> 2)); }
    wf[pos % 3][0] = ldfrag_tr(wb[0] + (pos & 3) * 2048, 1024); wf[pos % 3][1] = ldfrag_tr(wb[1] + (pos & 3) * 2048, 1024); };
  bar_vm<17 + (NO_WARM ? 0 : 6)>();
  static_for<0, 12>([&](auto kc) { constexpr int k = decltype(kc)::value; pin(a2[k]); });
  read_w2(0); read_w2(1);
  gv = g_voff(C);
  STAMP(8);
  u32x4 ef[4];
  static_for<0, 36>([&](auto ic) {
    constexpr int pos = decltype(ic)::value;
    constexpr int j = pos >> 2, i = pos & 3;
    if constexpr (i == 2) {
      STAMP(44 + 2 * j);
      if constexpr (j <= 1) bar_vm<63>();
      if constexpr (j == 2) bar_vm<6>();
      if constexpr (j == 3) bar_vm<6>();
      if constexpr (j == 4) bar_vm<7>();
      if constexpr (j == 5) bar_vm<15>();
      if constexpr (j == 6) bar_vm<4>();
      if constexpr (j == 7) bar_vm<0>();
      STAMP(45 + 2 * j);
    }
    if constexpr (pos == 10) { static_for<12, 28>([&](auto kc) { pin(a2[decltype(kc)::value]); }); check_frags<12, 28>(C, a2, a2base); }
    if constexpr (pos == 26) { static_for<28, 36>([&](auto kc) { pin(a2[decltype(kc)::value]); }); pin(xres); check_frags<28, 36>(C, a2, a2base); }
    if constexpr (pos == 16) { ld16(xres, C.sXall, (RT * 32 + (lane & 31)) * 1024 + cg * 128 + kq * 32 + h * 16, 0); }
    if constexpr (pos == 18 || pos == 19) static_for<0, 4>([&](auto kc) { constexpr int k = 28 + 4 * (pos - 18) + decltype(kc)::value; ld16_sc1(a2[k], C.sWS, lane * 16, a2base + k * 1024); });
    if constexpr (pos < 24) issue_g(C, 3 + (pos >> 2), pos & 3, gv);
    if constexpr (pos == 30) { if (b == 0 && wid == 7) {
#pragma unroll
        for (int k = 0; k < 4; ++k) { u32x2 t; ld8_sc1(t, C.sWS, flag_off(0, 0, 0) + (lane * 4 + k) * 128); ef[k][0] = t[0]; ef[k][1] = t[1]; } } }
    if constexpr (pos + 2 < 36) read_w2(pos + 2);
    const bf16x8 af = __builtin_bit_cast(bf16x8, a2[pos]);
    if constexpr (pos == 0) { acc[0] = MFMA32(wf[0][0], af, f32x16{}); acc[1] = MFMA32(wf[0][1], af, f32x16{}); }
    else { acc[0] = MFMA32(wf[pos % 3][0], af, acc[0]); acc[1] = MFMA32(wf[pos % 3][1], af, acc[1]); }
    SB();
  });
  STAMP(9);
#pragma unroll
  for (int d = 0; d < 4; ++d) if (d != kq) {
    const int ct = d >> 1, r0 = (d & 1) * 8;
    const unsigned a = R0 + ((rt * 4 + d) * 4 + kq) * 2048 + lane * 16;
    *lp<f32x4>(a) = f32x4{acc[ct][r0], acc[ct][r0 + 1], acc[ct][r0 + 2], acc[ct][r0 + 3]};
    *lp<f32x4>(a + 1024) = f32x4{acc[ct][r0 + 4], acc[ct][r0 + 5], acc[ct][r0 + 6], acc[ct][r0 + 7]};
  }
  float s[8];
#pragma unroll
  for (int e = 0; e < 8; ++e) { const float lo = (kq & 1) ? acc[0][8 + e] : acc[0][e], hi = (kq & 1) ? acc[1][8 + e] : acc[1][e]; s[e] = (kq & 2) ? hi : lo; }
  asm volatile("s_waitcnt lgkmcnt(0)\n\ts_barrier" ::: "memory");
#pragma unroll
  for (int d = 0; d < 4; ++d) if (d != kq) {
    const unsigned a = R0 + ((rt * 4 + kq) * 4 + d) * 2048 + lane * 16;
    const f32x4 u = *lp<f32x4>(a), v = *lp<f32x4>(a + 1024);
#pragma unroll
    for (int e = 0; e < 4; ++e) { s[e] += u[e]; s[4 + e] += v[e]; }
  }
  {
    unsigned u0 = __float_as_uint(s[0]), u1 = __float_as_uint(s[1]), u2 = __float_as_uint(s[2]), u3 = __float_as_uint(s[3]);
    unsigned v0 = __float_as_uint(s[4]), v1 = __float_as_uint(s[5]), v2 = __float_as_uint(s[6]), v3 = __float_as_uint(s[7]);
    auto r0 = __builtin_amdgcn_permlane32_swap(u0, v0, false, false); auto r1 = __builtin_amdgcn_permlane32_swap(u1, v1, false, false);
    auto r2 = __builtin_amdgcn_permlane32_swap(u2, v2, false, false); auto r3 = __builtin_amdgcn_permlane32_swap(u3, v3, false, false);
    const float c0 = __uint_as_float(r0[0]), c1 = __uint_as_float(r1[0]), c2 = __uint_as_float(r2[0]), c3 = __uint_as_float(r3[0]);
    const float c4 = __uint_as_float(r0[1]), c5 = __uint_as_float(r1[1]), c6 = __uint_as_float(r2[1]), c7 = __uint_as_float(r3[1]);
    u32x4 f;
    f[0] = cvtpk(c0 + bflo(xres[0]), c1 + bfhi(xres[0])); f[1] = cvtpk(c2 + bflo(xres[1]), c3 + bfhi(xres[1]));
    f[2] = cvtpk(c4 + bflo(xres[2]), c5 + bfhi(xres[2])); f[3] = cvtpk(c6 + bflo(xres[3]), c7 + bfhi(xres[3]));
    const i32x4 sO = mk_srd(out, 2048 * 1024);
    st16(f, sO, (RT * 32 + (lane & 31)) * 1024 + cg * 128 + kq * 32 + h * 16, 0);
  }
  if (b == 0 && wid == 7) {
    wait_vm<0>();
    asm volatile("" : "+v"(ef[0]), "+v"(ef[1]), "+v"(ef[2]), "+v"(ef[3]));
    bool ok = true;
#pragma unroll
    for (int k = 0; k < 4; ++k) ok = ok && ef[k][0] == (unsigned)C.tag && ef[k][1] == (unsigned)(C.tag >> 32);
    if (!__all(ok)) {
#pragma unroll 1
      for (int k = 0; k < 4; ++k) spin_flags(ws, flag_off(0, 0, 0) + (lane * 4 + k) * 128, true, C.tag);
    }
    if (lane == 0) __hip_atomic_store((u64*)(ws + WS_EPOCH), C.tag, __ATOMIC_RELAXED, __HIP_MEMORY_SCOPE_AGENT);
  }
  STAMP(60);
  if (b == 100 && wid == 0) { const u64 e = __builtin_amdgcn_s_memrealtime(); const u64 until = e + 2 * (C.prtx - C.prt0); while (__builtin_amdgcn_s_memrealtime() < until) __builtin_amdgcn_s_sleep(1); }
}

extern "C" void kernel_launch(void* const* d_in, const int* in_sizes, int n_in, void* d_out, int out_size, void* d_ws, size_t ws_size, hipStream_t stream) {
  (void)in_sizes; (void)n_in; (void)out_size; (void)ws_size;
  (void)hipFuncSetAttribute((const void*)fused_kernel, hipFuncAttributeMaxDynamicSharedMemorySize, LDS_BYTES);
  fused_kernel<<<256, 512, LDS_BYTES, stream>>>((const u16*)d_in[0], (const u16*)d_in[1], (const u16*)d_in[2], (const u16*)d_in[4], (const u16*)d_in[5], (const u16*)d_in[6],
                                                 (u16*)d_out, (char*)d_ws);
}
```

```cpp
#include <hip/hip_runtime.h>
#include <stdint.h>
#include <type_traits>

typedef unsigned short u16;
typedef unsigned long long u64;
typedef short bf16x8 __attribute__((ext_vector_type(8)));
typedef short s16x4 __attribute__((ext_vector_type(4)));
typedef float f32x2 __attribute__((ext_vector_type(2)));
typedef float f32x4 __attribute__((ext_vector_type(4)));
typedef float f32x16 __attribute__((ext_vector_type(16)));
typedef unsigned u32x2 __attribute__((ext_vector_type(2)));
typedef unsigned u32x4 __attribute__((ext_vector_type(4)));
typedef int i32x4 __attribute__((ext_vector_type(4)));
#define LDSP __attribute__((address_space(3)))
#define DI __device__ __forceinline__
template <class T> DI LDSP T* lp(unsigned a) { return (LDSP T*)(size_t)a; }
template <int I, int N, class F> DI void static_for(F&& f) { if constexpr (I < N) { f(std::integral_constant<int, I>{}); static_for<I + 1, N>(f); } }

constexpr size_t WS_EPOCH = 0;
constexpr size_t WS_ERR   = 64;
constexpr size_t WS_FLAGS = 4096;
constexpr size_t WS_GS    = 512u << 10;
constexpr size_t WS_GC    = 576u << 10;
constexpr size_t WS_QKV   = 1u << 20;
constexpr size_t WS_A2    = 8u << 20;
constexpr size_t WS_DBG   = 24u << 20;

constexpr unsigned R0 = 0, R1 = 32768, R2 = 65536, R3 = 98304;
constexpr unsigned L_K = 98304, L_V = 122880;
constexpr unsigned L_ONES = 147456;
constexpr unsigned L_CSP = 148480;
constexpr unsigned L_CS = 150528;
constexpr unsigned L_JUNK = 154624;
constexpr unsigned LDS_BYTES = 163840;
constexpr unsigned CODE_BYTES = 36864;
constexpr unsigned POISON = 0xffffffffu;
constexpr unsigned SPIN_CAP = 1u << 13;

#define NO_WARM 0
#define NO_CS 0
#define NO_DMA 0
constexpr float QSCALE = 0.25f * 1.4426950408889634f;

DI i32x4 mk_srd(const void* p, unsigned bytes) { u64 a = (u64)p; i32x4 r; r[0] = (int)(unsigned)a; r[1] = (int)((unsigned)(a >> 32) & 0xffffu); r[2] = (int)bytes; r[3] = 0x00020000; return r; }
DI void dma16(i32x4 srd, unsigned voff, unsigned soff, unsigned lds) {
  if (NO_DMA) return;
  asm volatile("s_mov_b32 m0, %3\n\ts_nop 3\n\tbuffer_load_dwordx4 %0, %1, %2 offen lds" :: "v"(voff), "s"(srd), "s"(soff), "s"(lds) : "memory");
}
DI void dma4(i32x4 srd, unsigned voff, unsigned lds) {
  asm volatile("s_mov_b32 m0, %2\n\ts_nop 3\n\tbuffer_load_dword %0, %1, 0 offen lds" :: "v"(voff), "s"(srd), "s"(lds) : "memory");
}
DI void ld16_sc1(u32x4& d, i32x4 srd, unsigned voff, unsigned soff) {
  asm volatile("s_nop 4\n\tbuffer_load_dwordx4 %0, %1, %2, %3 offen sc1" : "=v"(d) : "v"(voff), "s"(srd), "s"(soff) : "memory");
}
DI void ld16(u32x4& d, i32x4 srd, unsigned voff, unsigned soff) {
  asm volatile("s_nop 4\n\tbuffer_load_dwordx4 %0, %1, %2, %3 offen" : "=v"(d) : "v"(voff), "s"(srd), "s"(soff) : "memory");
}
DI void ld8_sc1(u32x2& d, i32x4 srd, unsigned voff) {
  asm volatile("s_nop 4\n\tbuffer_load_dwordx2 %0, %1, %2, 0 offen sc1" : "=v"(d) : "v"(voff), "s"(srd) : "memory");
}
DI void st16_sc1(u32x4 d, i32x4 srd, unsigned voff, unsigned soff) {
  asm volatile("s_nop 4\n\tbuffer_store_dwordx4 %0, %1, %2, %3 offen sc1\n\ts_nop 1" :: "v"(d), "v"(voff), "s"(srd), "s"(soff) : "memory");
}
DI void st16(u32x4 d, i32x4 srd, unsigned voff, unsigned soff) {
  asm volatile("s_nop 4\n\tbuffer_store_dwordx4 %0, %1, %2, %3 offen\n\ts_nop 1" :: "v"(d), "v"(voff), "s"(srd), "s"(soff) : "memory");
}
template <int N> DI void bar_vm() { static_assert(N >= 0 && N < 64, "vmcnt range"); asm volatile("s_waitcnt vmcnt(%0) lgkmcnt(0)\n\ts_barrier" :: "n"(N) : "memory"); }
template <int N> DI void wait_vm() { static_assert(N >= 0 && N < 64, "vmcnt range"); asm volatile("s_waitcnt vmcnt(%0)" :: "n"(N) : "memory"); }
DI s16x4 ldtr(unsigned a) { return __builtin_amdgcn_ds_read_tr16_b64_v4i16(lp<s16x4>(a)); }
DI bf16x8 ldfrag_tr(unsigned a, unsigned second) {
  s16x4 lo = ldtr(a), hi = ldtr(a + second);
  return __builtin_shufflevector(lo, hi, 0, 1, 2, 3, 4, 5, 6, 7);
}
DI bf16x8 ld128(unsigned a) { return *lp<bf16x8>(a); }
DI unsigned cvtpk(float lo, float hi) { typedef __bf16 b2 __attribute__((ext_vector_type(2))); f32x2 v = {lo, hi}; return __builtin_bit_cast(unsigned, __builtin_convertvector(v, b2)); }
DI float bflo(unsigned v) { return __uint_as_float(v << 16); }
DI float bfhi(unsigned v) { return __uint_as_float(v & 0xffff0000u); }
DI float rbf(float f) { return bflo(cvtpk(f, f) & 0xffffu); }
DI int fresh(int v) { asm volatile("" : "+v"(v)); return v; }
template <class T> DI void pin(T& v) { asm volatile("" : "+v"(v)); }
DI unsigned pinv(unsigned v) { asm volatile("" : "+v"(v)); return v; }
#define MFMA32(a, b, c) __builtin_amdgcn_mfma_f32_32x32x16_bf16((a), (b), (c), 0, 0, 0)
#define MFMA16(a, b, c) __builtin_amdgcn_mfma_f32_16x16x32_bf16((a), (b), (c), 0, 0, 0)
#define MFMA4(a, b, c) __builtin_amdgcn_mfma_f32_4x4x4bf16_1k((a), (b), (c), 0, 0, 0)
#define SB() __builtin_amdgcn_sched_barrier(0)
DI float swap_add(float v) { auto r = __builtin_amdgcn_permlane32_swap(__float_as_uint(v), __float_as_uint(v), false, false); return __uint_as_float(r[0]) + __uint_as_float(r[1]); }
template <class T> DI float ror_add(float v, T) { return v + __int_as_float(__builtin_amdgcn_update_dpp(0, __float_as_int(v), T::value, 0xf, 0xf, true)); }
DI float swap16_add(float v) { auto r = __builtin_amdgcn_permlane16_swap(__float_as_uint(v), __float_as_uint(v), false, false); return __uint_as_float(r[0]) + __uint_as_float(r[1]); }
template <int C> using IC = std::integral_constant<int, C>;
DI float wave_sum(float v) { v = ror_add(v, IC<0x121>{}); v = ror_add(v, IC<0x122>{}); v = ror_add(v, IC<0x124>{}); v = ror_add(v, IC<0x128>{}); return swap_add(swap16_add(v)); }
DI float swap_max(float v) { auto r = __builtin_amdgcn_permlane32_swap(__float_as_uint(v), __float_as_uint(v), false, false); return __builtin_fmaxf(__uint_as_float(r[0]), __uint_as_float(r[1])); }

#define STAMP(i) do { } while (0)

struct Ctx {
  char* ws; u64 tag;
  i32x4 sX, sXall, sQKVw, sG, sU, sWO, sWD, sWS;
  int lane, wid, mg, ng, rb, cs, cg, xi, kq, rb2, bid; unsigned stampreg;
};

DI unsigned flag_off(int ev, int rb, int cs) { return (unsigned)(WS_FLAGS + (((ev * 16 + rb) * 16 + cs) << 7)); }
DI void publish(const Ctx& C, int ev) { if (C.wid == 0 && C.lane == 0) __hip_atomic_store((u64*)(C.ws + flag_off(ev, C.rb, C.cs)), C.tag, __ATOMIC_RELAXED, __HIP_MEMORY_SCOPE_AGENT); }
DI void give_up(char* ws) { __hip_atomic_store((unsigned*)(ws + WS_ERR), 1u, __ATOMIC_RELAXED, __HIP_MEMORY_SCOPE_AGENT); }
DI void spin_flags(char* ws, unsigned off, bool need, u64 tag) {
#pragma nounroll
  for (unsigned it = 0; it < SPIN_CAP; ++it) {
    u64 v = __hip_atomic_load((u64*)(ws + off), __ATOMIC_RELAXED, __HIP_MEMORY_SCOPE_AGENT);
    if (__all(!need || v == tag)) return;
    __builtin_amdgcn_s_sleep(2);
  }
  give_up(ws);
}
DI void check_flags(char* ws, unsigned off, bool need, u64 tag, u32x2& got) {
  asm volatile("" : "+v"(got));
  bool ok = !need || (got[0] == (unsigned)tag && got[1] == (unsigned)(tag >> 32));
  if (!__all(ok)) spin_flags(ws, off, need, tag);
}
template <int K0, int K1> DI void check_frags(const Ctx& C, u32x4 (&a2)[36], unsigned base) {
  auto dirty = [&]() { unsigned mx = 0;
    static_for<K0, K1>([&](auto kc) { constexpr int k = decltype(kc)::value;
      mx = __builtin_elementwise_max(mx, __builtin_elementwise_max(__builtin_elementwise_max(a2[k][0], a2[k][1]), __builtin_elementwise_max(a2[k][2], a2[k][3]))); });
    return __any(mx == POISON); };
  if (dirty()) {
#pragma nounroll
    for (unsigned it = 0; it < SPIN_CAP; ++it) {
      __builtin_amdgcn_s_sleep(2);
      static_for<K0, K1>([&](auto kc) { constexpr int k = decltype(kc)::value; ld16_sc1(a2[k], C.sWS, C.lane * 16, base + k * 1024); });
      wait_vm<0>();
      static_for<K0, K1>([&](auto kc) { constexpr int k = decltype(kc)::value; pin(a2[k]); });
      if (!dirty()) return;
    }
    give_up(C.ws);
  }
}

enum { T_H0 = 0, T_S4 = 8, T_E0 = 16, T_G0 = 19, T_POISON = 22, T_POLL, T_KL, T_POLL2, T_QL, T_QP, T_EPOCH, T_CSL, T_GR, T_WARM, T_NONE };
template <int TYPE> struct G1 {
  static constexpr int kind(int p) { return p == 0 ? 0 : (p == 1 && TYPE == 1) ? 1 : 2; }
  static constexpr int nt(int p) { return p == 0 ? 1 : 2; }
  static constexpr int nst(int p) { return kind(p) == 1 ? 3 : 2; }
  static constexpr int npoison() { return 3; }
  static constexpr unsigned slot(int s) {
    return s == 4 ? R0 : s == 5 ? R1 : s == 6 ? R2 : s == 7 ? R3 : s == 8 ? R0 : s == 9 ? R1 : s == 10 ? R2 : R0; }
  static constexpr int warmat(int pos) { return (pos >= 39 && pos < 39 + 4 * 9 && ((pos - 39) & 3) == 0) ? (pos - 39) >> 2 : -1; }
  static constexpr int barpos(int bi) { return bi < 8 ? 4 * bi + 2 : 8 * (bi - 4) + 6; }
  static constexpr int barat(int pos) { return pos < 32 ? ((pos & 3) == 2 ? pos >> 2 : -1) : ((pos & 7) == 6 ? 4 + (pos >> 3) : -1); }
  static constexpr int grp(int bi) { return bi < 4 ? T_H0 + bi + 4 : bi < 11 ? T_S4 + (bi - 4) : bi == 11 ? T_NONE : bi == 12 ? T_S4 + 7 : T_G0 + (bi - 13); }
  static constexpr int gsize(int t) { return t == T_NONE ? 0 : t < T_S4 ? 3 : 4; }
  static constexpr int rel(int bi, int k) { return bi < 8 ? k : bi == 15 ? (k >> 1) : 2 * k; }
  static constexpr int after(int t, int bi) {
    int cnt = -1;
    auto see = [&](int tag, int n) { for (int i = 0; i < n; ++i) { if (tag == t) cnt = 0; else if (cnt >= 0) ++cnt; } };
    see(T_H0, gsize(T_H0)); see(T_EPOCH, 2); see(T_H0 + 1, gsize(T_H0 + 1)); see(T_CSL, 4);
    if (bi == -1) return cnt;
    for (int pos = 0; pos < 96; ++pos) {
      if (pos < 2) see(T_H0 + 2 + pos, gsize(T_H0 + 2 + pos));
      const int b = barat(pos);
      if (b >= 0) {
        if (b == bi) return cnt;
        if (b == 12) see(T_KL, 6);
        if (b == 13) see(T_QL, 1);
        if (b == 14) see(T_POLL2, 1);
      }
      for (int bb = 0; bb < 16; ++bb) for (int k = 0; k < gsize(grp(bb)); ++k) if (barpos(bb) + rel(bb, k) == pos) see(grp(bb), 1);
      if (pos == 32 && TYPE == 1) see(T_QP, 2);
      if (pos == 22) see(T_GR, 2);
      if (pos == 58) see(T_POLL, 1);
      if (pos == 50) see(T_POISON, npoison());
      if ((pos & 31) == 31) see(T_E0 + (pos >> 5), nst(pos >> 5));
    }
    return cnt;
  }
  static constexpr int mn(int a, int b) { return a < b ? a : b; }
};

template <int TYPE> DI unsigned tile_addr(const Ctx& C, int p, int t) {
  const int lane = fresh(C.lane), kind = G1<TYPE>::kind(p);
  const int g = (lane >> 4) & 1, h = lane >> 5, q = (lane & 15) >> 2, pp = lane & 3;
  const unsigned rdrow = (8 * h + q) * 128 + pp * 8;
  if (kind == 2) return g * 16384 + rdrow + (((2 * C.ng + t) ^ (2 * (q >> 1)) ^ g) << 5);
  if (kind == 0) return 16384 + rdrow + ((C.ng ^ (q >> 1)) << 6) + g * 32;
  if (t == 0) return rdrow + ((C.ng ^ (q >> 1)) << 6) + g * 32;
  return 16384 + g * 8192 + (8 * h + q) * 64 + pp * 8 + ((C.ng ^ g) << 5);
}
template <int TYPE> DI unsigned stage_voff(const Ctx& C, int p, int which) {
  const int kind = G1<TYPE>::kind(p), lane = fresh(C.lane);
  if (which == 1) return (16 * C.wid + (lane >> 2)) * 3584 + (8 + ((C.cs - 8) >> 1)) * 128 + ((C.cs - 8) & 1) * 64 + ((lane & 3) << 4);
  const unsigned row = 16 * C.wid + (lane >> 3), sw = ((lane & 7) ^ (((lane >> 4) & 1) << 2)) << 4;
  if (kind == 1) return row * 3072 + (C.cs - 8) * 128 + sw;
  return row * 3584 + (p == 1 ? C.cs : 12 + C.cs) * 128 + sw;
}
template <int TYPE> DI void issue_stage(const Ctx& C, int s, int k, unsigned v0) {
  const int kind = G1<TYPE>::kind(s >> 2);
  const unsigned slot = G1<TYPE>::slot(s), r0 = 128 * (s & 3);
  if (kind == 1) {
    if (k < 2) dma16(C.sQKVw, v0, (r0 + 8 * k) * 3072, slot + C.wid * 2048 + k * 1024);
    else dma16(k == 3 ? C.sU : C.sG, k == 3 ? v0 ^ 32 : v0, r0 * 3584, slot + 16384 + (k - 2) * 8192 + C.wid * 1024);
  } else dma16(k >> 1 ? C.sU : C.sG, k >> 1 ? v0 ^ 32 : v0, (r0 + 8 * (k & 1)) * 3584, slot + (k >> 1) * 16384 + C.wid * 2048 + (k & 1) * 1024);
}
DI unsigned hx_voff(const Ctx& C) { const int lane = fresh(C.lane); return (16 * C.wid + (lane >> 3)) * 1024 + (((lane & 7) ^ ((lane >> 4) & 3)) << 4); }
DI unsigned hw_voff(const Ctx& C) { const int lane = fresh(C.lane);
  return (8 * C.wid + (lane >> 3)) * 3072 + (8 + C.cs) * 128 + (((lane & 7) ^ (((lane >> 4) & 1) << 2)) << 4); }
DI void issue_half(const Ctx& C, int h, int k, unsigned vx, unsigned vw) {
  const unsigned slot = (h & 3) * 32768;
  if (k < 2) dma16(C.sX, k ? (vx ^ 64) + 8 * 1024 : vx, h * 128, slot + C.wid * 2048 + k * 1024);
  else dma16(C.sQKVw, vw + (k - 2) * 128, h * 64 * 3072, slot + 16384 + (k - 2) * 8192 + C.wid * 1024);
}
DI void warm(i32x4 srd, unsigned line0, unsigned lines_per_row_log2, unsigned pitch, unsigned colbyte, int lane) {
  const unsigned l = line0 + lane;
  dma4(srd, (l >> lines_per_row_log2) * pitch + colbyte + (l & ((1u << lines_per_row_log2) - 1)) * 128, L_JUNK);
}
DI unsigned g_voff(const Ctx& C) { const int lane = fresh(C.lane); return (32 * (C.wid & 1) + (lane >> 3)) * 1024 + C.cg * 128 + (((lane & 7) ^ (((lane >> 4) & 1) << 2)) << 4); }
constexpr unsigned gslot(int j) { return j == 0 ? R1 : j == 1 ? R2 : j == 2 ? R0 : j == 3 ? R3 : j == 4 ? R1 : j == 5 ? R2 : j == 6 ? R0 : j == 7 ? R3 : R1; }
DI void issue_g(const Ctx& C, int j, int i, unsigned v) {
  const int kq = C.wid >> 1;
  const int row0 = j < 3 ? (3 * kq + j) * 64 : j < 7 ? (12 + 4 * kq + (j - 3)) * 64 : 128 * kq + 64 * (j - 7);
  dma16(j < 7 ? C.sWD : C.sWO, v, (unsigned)(row0 + 8 * i) * 1024, gslot(j) + kq * 8192 + (C.wid & 1) * 4096 + i * 1024);
}

template <int WHAT> DI void epi_qkv(const Ctx& C, const f32x16& acc, const f32x4 (&csr)[4], float m, float rinv, int qu, int h4) {
  const int h = C.lane >> 5;
  const float sc = WHAT == 0 ? rinv * QSCALE : rinv;
  float y[16];
#pragma unroll
  for (int i4 = 0; i4 < 4; ++i4)
#pragma unroll
    for (int e = 0; e < 4; ++e) y[4 * i4 + e] = (acc[4 * i4 + e] - csr[i4][e]) * sc;
  const unsigned base = (unsigned)WS_QKV + (((C.rb * 24 + qu) * 4 + C.mg) * 4 + h4) * 1024;
#pragma unroll
  for (int hh = 0; hh < 2; ++hh) {
    u32x4 f; f[0] = cvtpk(y[8 * hh], y[8 * hh + 1]); f[1] = cvtpk(y[8 * hh + 2], y[8 * hh + 3]); f[2] = cvtpk(y[8 * hh + 4], y[8 * hh + 5]); f[3] = cvtpk(y[8 * hh + 6], y[8 * hh + 7]);
    unsigned voff = C.lane * 16;
    if (WHAT == 2) {
      auto a = __builtin_amdgcn_permlane32_swap(f[0], f[2], false, false); auto b = __builtin_amdgcn_permlane32_swap(f[1], f[3], false, false);
      f[0] = a[0]; f[2] = a[1]; f[1] = b[0]; f[3] = b[1];
      voff = (C.lane & 31) * 32 + h * 16;
    }
    st16_sc1(f, C.sWS, voff, base + hh * 1024);
  }
}
DI void epi_gu(const Ctx& C, const f32x16& acc, const f32x4 (&csr)[4], float m, float r2, unsigned a2off) {
  float y[8];
#pragma unroll
  for (int i4 = 0; i4 < 2; ++i4)
#pragma unroll
    for (int e = 0; e < 4; ++e) y[4 * i4 + e] = ((acc[4 * i4 + e] - csr[i4][e]) * r2) * (acc[8 + 4 * i4 + e] - csr[2 + i4][e]);
  u32x4 f; f[0] = cvtpk(y[0], y[1]); f[1] = cvtpk(y[2], y[3]); f[2] = cvtpk(y[4], y[5]); f[3] = cvtpk(y[6], y[7]);
  st16_sc1(f, C.sWS, C.lane * 16, a2off);
}
template <int TYPE> DI unsigned a2_pass(const Ctx& C, int p) {
  if (p == 2) return (unsigned)WS_A2 + (((4 * C.rb + C.mg) * 4 + (C.cs >> 2)) * 36 + 4 * (3 + (C.cs & 3)) + 2 * C.ng) * 1024;
  if (TYPE == 0) return (unsigned)WS_A2 + (((4 * C.rb + C.mg) * 4 + C.cs / 3) * 36 + 4 * (C.cs % 3) + 2 * C.ng) * 1024;
  const int u = 8 + ((C.cs - 8) >> 1), half = (C.cs - 8) & 1;
  return (unsigned)WS_A2 + (((4 * C.rb + C.mg) * 4 + u / 3) * 36 + 4 * (u % 3) + 2 * half + C.ng) * 1024;
}
DI unsigned a2_o(const Ctx& C) { return (unsigned)WS_A2 + (((4 * C.rb + C.mg) * 4 + (C.cs >> 2)) * 36 + 28 + 2 * (C.cs & 3) + C.ng) * 1024; }

template <int TYPE> DI void gemm1(Ctx& C, unsigned vx, unsigned vw, u32x4& qfrag, u32x2& poll2, unsigned& poll2off) {
  using G = G1<TYPE>;
  const int lane = C.lane;
  char* ws = C.ws;
  bf16x8 xa[32];
  bf16x8 wf[3][2];
  f32x16 acc[2];
  unsigned ta[2], sv = 0;
  ta[0] = pinv(tile_addr<TYPE>(C, 0, 0)); ta[1] = pinv(tile_addr<TYPE>(C, 0, 1));
  unsigned th[2];
  th[0] = ta[0] + 65536; th[1] = ta[1] + 65536;
  const unsigned r31 = fresh(lane) & 31, hh = fresh(lane) >> 5;
  const unsigned xlane = (32 * C.mg + r31) * 128 + ((hh ^ ((r31 >> 1) & 7)) << 4);
  f32x4 csr[2][4];
  float m = 0.f, rinv = 0.f;
  u32x2 pollv, gs; u32x4 gk[6], gc;
  unsigned polloff = 0, gsoff = 0, gcoff = 0; bool pollneed = false;
  const unsigned qoff = (unsigned)WS_QKV + (((C.rb * 24 + (C.cs >> 1)) * 4 + C.mg) * 4 + 2 * (C.cs & 1) + C.ng) * 1024;

  u32x2 ev; ld8_sc1(ev, C.sWS, (unsigned)WS_EPOCH);
  u32x4 srow, cl[4];
  ld16(srow, C.sXall, (8 * C.bid + C.wid) * 1024 + fresh(lane) * 16, 0);
  issue_half(C, 1, 0, vx, vw); issue_half(C, 1, 1, vx, vw); issue_half(C, 1, 2, vx, vw);
  { const int pan = C.bid >> 1, lf = fresh(lane);
    const unsigned pitch = pan < 24 ? 3072 : 3584, col = (pan < 24 ? pan : pan < 52 ? pan - 24 : pan - 52) * 128;
    i32x4 sp;
#pragma unroll
    for (int e = 0; e < 4; ++e) sp[e] = pan < 24 ? C.sQKVw[e] : pan < 52 ? C.sG[e] : C.sU[e];
    const unsigned v = (256 * (C.bid & 1) + 32 * C.wid + (lf >> 3)) * pitch + col + (lf & 7) * 16 + (pan < 80 ? 0u : 0x40000000u);
#pragma unroll
    for (int k = 0; k < 4; ++k) ld16(cl[k], sp, v, 8 * k * pitch); }
  STAMP(1);
  bar_vm<G::after(T_EPOCH, -1)>();
  STAMP(2);
  {
  pin(ev); pin(srow);
  { const u64 t = (((u64)ev[1] << 32) | ev[0]) * 6364136223846793005ull + 1442695040888963407ull;
    C.tag = ((u64)__builtin_amdgcn_readfirstlane((unsigned)(t >> 32)) << 32) | __builtin_amdgcn_readfirstlane((unsigned)t); }
  float xv[8];
#pragma unroll
  for (int e = 0; e < 4; ++e) { xv[2 * e] = bflo(srow[e]); xv[2 * e + 1] = bfhi(srow[e]); }
  const float mean = wave_sum(((xv[0] + xv[1]) + (xv[2] + xv[3])) + ((xv[4] + xv[5]) + (xv[6] + xv[7]))) * (1.f / 512.f);
  float q2 = 0.f;
#pragma unroll
  for (int e = 0; e < 8; ++e) { const float d = xv[e] - mean; q2 += d * d; }
  const float var = wave_sum(q2) * (1.f / 512.f);
  const float sd = __builtin_amdgcn_sqrtf(rbf(rbf(var) + 1e-5f));
  if (lane == 0) __hip_atomic_store((u64*)(ws + WS_GS + (8 * C.bid + C.wid) * 8), ((u64)(unsigned)C.tag << 32) | cvtpk(mean, sd), __ATOMIC_RELAXED, __HIP_MEMORY_SCOPE_AGENT);
  }
  auto read_x = [&](int h) {
    const unsigned base = (h & 3) * 32768 + xlane;
#pragma unroll
    for (int i = 0; i < 4; ++i) xa[4 * h + i] = ld128(base ^ (i << 5));
  };
  auto read_w = [&](int pos) {
    const int p = pos >> 5, s = pos >> 3, i = pos & 7;
    if ((pos & 31) == 0 && pos > 0) { ta[0] = pinv(tile_addr<TYPE>(C, p, 0)); ta[1] = pinv(tile_addr<TYPE>(C, p, 1)); th[0] = pinv(ta[0] + 65536); th[1] = pinv(ta[1] + 65536); }
    const unsigned sb = p == 0 ? ((pos >> 2) & 3) * 32768 : G::slot(s), ks = p == 0 ? (i & 3) : i;
    const unsigned b0 = (sb & 65536) ? th[0] : ta[0], b1 = (sb & 65536) ? th[1] : ta[1], so = sb & 65535;
    wf[pos % 3][0] = ldfrag_tr(b0 + so + ks * 2048, 512);
    if (G::nt(p) == 2) { if (G::kind(p) == 1) wf[pos % 3][1] = ldfrag_tr(b1 + so + ks * 1024, 256); else wf[pos % 3][1] = ldfrag_tr(b1 + so + ks * 2048, 512); }
  };
  read_x(0); read_w(0); read_w(1);
  static_for<0, 96>([&](auto ic) {
    constexpr int pos = decltype(ic)::value;
    constexpr int p = pos >> 5, s = pos >> 3, st = s & 3, i = pos & 7, NT = G::nt(p), bi = G::barat(pos);
    if constexpr (pos < 2) static_for<0, G::gsize(T_H0)>([&](auto kc) { issue_half(C, 2 + pos, decltype(kc)::value, vx, vw); });
    if constexpr (bi >= 0) {
      STAMP(10 + 2 * bi);
      if constexpr (bi == 0) bar_vm<G::mn(G::after(T_H0 + 1, 0), G::after(T_CSL, 0))>();
      else if constexpr (bi < 7) bar_vm<G::after(T_H0 + bi + 1, bi)>();
      else if constexpr (bi == 7) bar_vm<G::mn(G::after(T_S4, 7), G::after(T_GR, 7))>();
      else if constexpr (bi == 9) { bar_vm<G::mn(G::mn(G::after(T_S4 + 2, 9), G::after(T_E0, 9)), TYPE == 1 ? G::after(T_QP, 9) : 63)>(); publish(C, 0); }
      else if constexpr (bi == 12) bar_vm<G::mn(G::after(T_S4 + 5, 12), G::after(T_POLL, 12))>();
      else if constexpr (bi == 13) { static_assert(G::after(T_E0 + 1, 13) <= G::after(T_POISON, 13), ""); bar_vm<G::mn(G::after(T_S4 + 6, 13), G::after(T_E0 + 1, 13))>(); }
      else if constexpr (bi == 14) bar_vm<G::mn(G::after(T_S4 + 7, 14), G::after(T_KL, 14))>();
      else if constexpr (bi == 15) bar_vm<63>();
      else bar_vm<G::after(T_S4 + bi - 7, bi)>();
      if constexpr (bi == 0) {
      pin(cl[0]); pin(cl[1]); pin(cl[2]); pin(cl[3]);
      float cz[8];
#pragma unroll
      for (int e = 0; e < 4; ++e) { cz[2 * e] = (bflo(cl[0][e]) + bflo(cl[1][e])) + (bflo(cl[2][e]) + bflo(cl[3][e])); cz[2 * e + 1] = (bfhi(cl[0][e]) + bfhi(cl[1][e])) + (bfhi(cl[2][e]) + bfhi(cl[3][e])); }
#pragma unroll
      for (int e = 0; e < 8; ++e) cz[e] = swap_add(swap16_add(ror_add(cz[e], IC<0x128>{})));
      if (lane < 8) { *lp<f32x4>(L_CSP + C.wid * 256 + lane * 32) = f32x4{cz[0], cz[1], cz[2], cz[3]}; *lp<f32x4>(L_CSP + C.wid * 256 + lane * 32 + 16) = f32x4{cz[4], cz[5], cz[6], cz[7]}; }
      }
      if constexpr (bi == 1) {
        if (C.wid == 0 && C.bid < 160) {
          float t = 0.f;
#pragma unroll
          for (int w = 0; w < 8; ++w) t += *lp<float>(L_CSP + w * 256 + lane * 4);
          __hip_atomic_store((u64*)(ws + WS_GC + ((C.bid >> 1) * 64 + lane) * 16 + (C.bid & 1) * 8), ((u64)(unsigned)C.tag << 32) | __float_as_uint(t), __ATOMIC_RELAXED, __HIP_MEMORY_SCOPE_AGENT);
        }
      }
      if constexpr (bi == 7) {
        pin(gs); pin(gc);
        if (!__all(gs[1] == (unsigned)C.tag && gc[1] == (unsigned)C.tag && gc[3] == (unsigned)C.tag)) {
#pragma nounroll
          for (unsigned it = 0; it < SPIN_CAP; ++it) {
            __builtin_amdgcn_s_sleep(2);
            ld8_sc1(gs, C.sWS, gsoff); ld16_sc1(gc, C.sWS, gcoff, 0); wait_vm<0>(); pin(gs); pin(gc);
            if (__all(gs[1] == (unsigned)C.tag && gc[1] == (unsigned)C.tag && gc[3] == (unsigned)C.tag)) break;
            if (it == SPIN_CAP - 1) give_up(ws);
          }
        }
        m = bflo(gs[0]); rinv = __builtin_amdgcn_rcpf(bfhi(gs[0]));
        *lp<float>(L_CS + (C.wid < 6 ? C.wid : 5) * 256 + lane * 4) = __uint_as_float(gc[0]) + __uint_as_float(gc[2]);
      }
      if constexpr (bi == 12) {
        check_flags(ws, polloff, pollneed, C.tag, pollv);
        const int u = C.cs >> 1, h4 = 2 * (C.cs & 1) + C.ng;
#pragma unroll
        for (int b = 0; b < 6; ++b) { const int rbp = C.rb - 2 + b % 3 < 0 ? 0 : C.rb - 2 + b % 3;
          ld16_sc1(gk[b], C.sWS, lane * 16, (unsigned)WS_QKV + (((rbp * 24 + (b < 3 ? 8 : 16) + u) * 4 + C.mg) * 4 + h4) * 1024); }
      }
      if constexpr (bi == 13) { publish(C, 1); ld16_sc1(qfrag, C.sWS, lane * 16, qoff); }
      if constexpr (bi == 14) {
#pragma unroll
        for (int b = 0; b < 6; ++b) pin(gk[b]);
#pragma unroll
        for (int b = 0; b < 3; ++b) { *lp<u32x4>(L_K + ((4 * b + C.mg) * 2 + C.ng) * 1024 + lane * 16) = gk[b];
                                      *lp<u32x4>(L_V + C.ng * 12288 + (4 * b + C.mg) * 1024 + lane * 16) = gk[3 + b]; }
        { const int ln = fresh(lane), l = ln < 10 ? ln : 0, uu = 3 * C.kq + (l >> 1);
          poll2off = flag_off(1, C.rb2, l < 6 ? (uu < 8 ? uu : 8 + 2 * (uu - 8) + (l & 1)) : 4 * C.kq + (l - 6)); }
        ld8_sc1(poll2, C.sWS, poll2off);
      }
      if constexpr (bi < 7) read_x(bi + 1);
      STAMP(11 + 2 * bi);
    }
    static_for<0, 16>([&](auto bc) {
      constexpr int bb = decltype(bc)::value, tg = G::grp(bb), k0 = pos - G::barpos(bb);
      static_for<0, 4>([&](auto kc) {
        constexpr int k = decltype(kc)::value;
        if constexpr (k < G::gsize(tg) && G::rel(bb, k) == k0) {
          if constexpr (tg < T_S4) issue_half(C, tg, k, vx, vw);
          else if constexpr (tg >= T_G0) { if constexpr (k == 0) sv = g_voff(C); issue_g(C, tg - T_G0, k, sv); }
          else { constexpr int pp_ = (tg - T_S4 + 4) >> 2;
            if constexpr (k == 0) sv = stage_voff<TYPE>(C, pp_, 0);
            if constexpr (k == 2 && G::kind(pp_) == 1) sv = stage_voff<TYPE>(C, pp_, 1);
            issue_stage<TYPE>(C, tg - T_S4 + 4, k, sv); }
        }
      });
    });
    if constexpr (pos == 32 && TYPE == 1) {
      const u32x4 pz = {POISON, POISON, POISON, POISON};
      const unsigned a = (unsigned)WS_QKV + (((C.rb * 24 + (C.cs - 8)) * 4 + C.mg) * 4 + 2 * C.ng) * 1024;
      st16_sc1(pz, C.sWS, lane * 16, a); st16_sc1(pz, C.sWS, lane * 16, a + 1024);
    }
    if constexpr (pos == 22) {
      const int lf = fresh(lane), w = C.wid < 6 ? C.wid : 5;
      const int guh = 8 + ((C.cs - 8) >> 1);
      const int pan = w == 0 ? 8 + C.cs : w >= 4 ? (w == 4 ? 24 : 52) + 12 + C.cs
                    : TYPE == 0 ? (w == 1 ? 24 + C.cs : 52 + C.cs) : (w == 1 ? C.cs - 8 : w == 2 ? 24 + guh : 52 + guh);
      gsoff = (unsigned)WS_GS + (C.rb * 128 + 32 * C.mg + (lf & 31)) * 8; gcoff = (unsigned)WS_GC + (pan * 64 + lf) * 16;
      ld8_sc1(gs, C.sWS, gsoff); ld16_sc1(gc, C.sWS, gcoff, 0);
    }
    if constexpr (pos == 58) {
      const int u = C.cs >> 1, l = fresh(lane) & 7;
      const int rbp = C.rb - 2 + l % 3;
      pollneed = l < 6 && rbp >= 0;
      polloff = flag_off(0, pollneed ? rbp : C.rb, l < 3 ? u : 8 + u);
      ld8_sc1(pollv, C.sWS, polloff);
    }
    if constexpr (pos == 50) {
      const u32x4 pz = {POISON, POISON, POISON, POISON};
      const unsigned a = a2_pass<TYPE>(C, 2);
      st16_sc1(pz, C.sWS, lane * 16, a);
      st16_sc1(pz, C.sWS, lane * 16, a + 1024);
      st16_sc1(pz, C.sWS, lane * 16, a2_o(C));
    }
    if constexpr (pos + 2 < 96) read_w(pos + 2);
    if constexpr ((pos & 31) == 0) { acc[0] = MFMA32(wf[pos % 3][0], xa[pos & 31], f32x16{}); if (NT == 2) acc[1] = MFMA32(wf[pos % 3][1], xa[pos & 31], f32x16{}); }
    else { acc[0] = MFMA32(wf[pos % 3][0], xa[pos & 31], acc[0]); if (NT == 2) acc[1] = MFMA32(wf[pos % 3][1], xa[pos & 31], acc[1]); }
    if constexpr (st == 3 && i == 7) {
      if constexpr (p == 0) asm volatile("s_waitcnt lgkmcnt(0)\n\ts_barrier" ::: "memory");
      static_for<0, NT>([&](auto tc) { constexpr int t = decltype(tc)::value;
        const bool gu = G::kind(p) == 2 || (G::kind(p) == 1 && t == 1);
        const int tl = G::kind(p) == 2 ? 2 * C.ng + t : 2 * ((C.cs - 8) & 1) + C.ng;
        const unsigned s0 = L_CS + (p == 0 ? 0 : p == 2 ? 4 : (G::kind(p) == 1 ? 1 + t : 1)) * 256;
#pragma unroll
        for (int i4 = 0; i4 < 4; ++i4) {
          const unsigned a = gu ? s0 + (i4 >> 1) * 256 + (16 * tl + 8 * (i4 & 1) + 4 * hh) * 4 : s0 + (32 * C.ng + 8 * i4 + 4 * hh) * 4;
          csr[t][i4] = *lp<f32x4>(a) * m; pin(csr[t][i4]); } });
    }
    SB();
    if constexpr ((pos & 31) == 31) {
      if constexpr (G::kind(p) == 2) {
        const unsigned a2 = a2_pass<TYPE>(C, p);
        epi_gu(C, acc[0], csr[0], m, rinv * rinv, a2); epi_gu(C, acc[1], csr[1], m, rinv * rinv, a2 + 1024);
      } else if constexpr (G::kind(p) == 1) {
        epi_qkv<0>(C, acc[0], csr[0], m, rinv, C.cs - 8, 2 * C.ng);
        epi_gu(C, acc[1], csr[1], m, rinv * rinv, a2_pass<TYPE>(C, p));
      } else {
        epi_qkv<1 + TYPE>(C, acc[0], csr[0], m, rinv, 8 + C.cs, 2 * C.ng);
      }
      SB();
    }
  });
  static_assert(G::after(T_POLL2, 16) == 10, "attention counts on this");
  wait_vm<G::after(T_QL, 16)>();
  pin(qfrag);
  { auto dirty = [&]() { return __any(__builtin_elementwise_max(__builtin_elementwise_max(qfrag[0], qfrag[1]), __builtin_elementwise_max(qfrag[2], qfrag[3])) == POISON); };
    if (dirty()) {
#pragma nounroll
      for (unsigned it = 0; it < SPIN_CAP; ++it) {
        __builtin_amdgcn_s_sleep(2);
        ld16_sc1(qfrag, C.sWS, lane * 16, qoff); wait_vm<0>(); pin(qfrag);
        if (!dirty()) break;
        if (it == SPIN_CAP - 1) give_up(ws);
      } } }
}

template <bool EDGE, class SIDE> DI u32x4 attention(Ctx& C, const u32x4& qraw, SIDE&& side) {
  const int lane = C.lane, h = lane >> 5, g = (lane >> 4) & 1, q = (lane & 15) >> 2, pp = lane & 3;
  const bf16x8 qf = __builtin_bit_cast(bf16x8, qraw);
  const unsigned kaddr = pinv(L_K + (C.mg * 2 + C.ng) * 1024 + lane * 16);
  const int t0 = EDGE ? (C.rb == 0 ? 8 : 4) - C.mg : 1;
  const int dq = (lane & 31) - 4 * h;
  const float NEG = -1e30f;
  auto older = [&](int r) { return ((r & 3) + 8 * (r >> 2)) > dq; };
  auto max16 = [&](float& a, float& b, const f32x16& s) {
#pragma unroll
    for (int r = 0; r < 16; r += 4) { a = __builtin_fmaxf(__builtin_fmaxf(a, s[r]), s[r + 1]); b = __builtin_fmaxf(__builtin_fmaxf(b, s[r + 2]), s[r + 3]); } };
  bf16x8 kf[9];
#pragma unroll
  for (int t = 0; t < 9; ++t) if (!EDGE || t > 0) kf[t] = ld128(kaddr + t * 2048);
  float mx;
  {
    f32x16 S[3];
    f32x16 sl = MFMA32(kf[8], qf, f32x16{});
    f32x16 sf;
    if (!EDGE) sf = MFMA32(kf[0], qf, f32x16{});
    S[1] = MFMA32(kf[1], qf, f32x16{});
    S[2] = MFMA32(kf[2], qf, f32x16{});
    float a = NEG, b = NEG;
    side(std::integral_constant<int, 0>{});
    SB();
#pragma unroll
    for (int r = 0; r < 16; ++r) sl[r] = older(r) ? (EDGE ? NEG : sf[r]) : sl[r];
    max16(a, b, sl);
    static_for<1, 8>([&](auto tc) { constexpr int t = decltype(tc)::value;
      if constexpr (t + 2 < 8) S[(t + 2) % 3] = MFMA32(kf[t + 2], qf, f32x16{});
      side(std::integral_constant<int, t>{});
      SB();
      if (!EDGE || t >= t0) max16(a, b, S[t % 3]);
      SB(); });
    mx = swap_max(__builtin_fmaxf(a, b));
  }
  f32x16 negm;
#pragma unroll
  for (int r = 0; r < 16; ++r) negm[r] = -mx;
  const unsigned vbase = pinv(g == 0 ? L_V + C.ng * 12288 + C.mg * 1024 + (4 * h + q) * 32 + pp * 8 : L_ONES + 128 * (1 - h) + q * 32 + pp * 8);
  const unsigned vstr = g == 0 ? 1024u : 0u;
  f32x16 o;
  auto expack = [&](const f32x16& s, u32x4& pa, u32x4& pb) {
#pragma unroll
    for (int i = 0; i < 4; ++i) pa[i] = cvtpk(__builtin_amdgcn_exp2f(s[2 * i]), __builtin_amdgcn_exp2f(s[2 * i + 1]));
#pragma unroll
    for (int i = 0; i < 4; ++i) pb[i] = cvtpk(__builtin_amdgcn_exp2f(s[8 + 2 * i]), __builtin_amdgcn_exp2f(s[9 + 2 * i])); };
  f32x16 S[2];
  bf16x8 vf[2][2];
  {
    f32x16 sl = MFMA32(kf[8], qf, negm);
    f32x16 sf;
    if (!EDGE) sf = MFMA32(kf[0], qf, negm);
    S[1] = MFMA32(kf[1], qf, negm);
    const unsigned v8 = vbase + 8 * vstr;
    const bf16x8 va = ldfrag_tr(v8, 256), vb = ldfrag_tr(v8 + 512, 256);
    bf16x8 vc, vd;
    if (!EDGE) { vc = ldfrag_tr(vbase, 256); vd = ldfrag_tr(vbase + 512, 256); }
    vf[1][0] = ldfrag_tr(vbase + vstr, 256); vf[1][1] = ldfrag_tr(vbase + vstr + 512, 256);
    side(std::integral_constant<int, 8>{});
    SB();
#pragma unroll
    for (int r = 0; r < 16; ++r) sl[r] = older(r) ? (EDGE ? NEG : sf[r]) : sl[r];
    u32x4 pa, pb, mk0, mk1;
    expack(sl, pa, pb);
#pragma unroll
    for (int i = 0; i < 4; ++i) {
      mk0[i] = (older(2 * i) ? 0xffffu : 0u) | (older(2 * i + 1) ? 0xffff0000u : 0u);
      mk1[i] = (older(8 + 2 * i) ? 0xffffu : 0u) | (older(9 + 2 * i) ? 0xffff0000u : 0u); }
    o = MFMA32(va, __builtin_bit_cast(bf16x8, pa & ~mk0), f32x16{});
    o = MFMA32(vb, __builtin_bit_cast(bf16x8, pb & ~mk1), o);
    if (!EDGE) {
      o = MFMA32(vc, __builtin_bit_cast(bf16x8, pa & mk0), o);
      o = MFMA32(vd, __builtin_bit_cast(bf16x8, pb & mk1), o);
    }
    SB();
  }
  static_for<1, 8>([&](auto tc) { constexpr int t = decltype(tc)::value;
    if constexpr (t + 1 < 8) {
      const unsigned vt = vbase + (t + 1) * vstr;
      vf[(t + 1) & 1][0] = ldfrag_tr(vt, 256); vf[(t + 1) & 1][1] = ldfrag_tr(vt + 512, 256);
      S[(t + 1) & 1] = MFMA32(kf[t + 1], qf, negm); }
    side(std::integral_constant<int, 8 + t>{});
    SB();
    if (!EDGE || t >= t0) {
      u32x4 pa, pb;
      expack(S[t & 1], pa, pb);
      o = MFMA32(vf[t & 1][0], __builtin_bit_cast(bf16x8, pa), o);
      o = MFMA32(vf[t & 1][1], __builtin_bit_cast(bf16x8, pb), o);
    }
    SB(); });
  const float li = __builtin_amdgcn_rcpf(o[8]);
  u32x4 f; f[0] = cvtpk(o[0] * li, o[1] * li); f[1] = cvtpk(o[2] * li, o[3] * li); f[2] = cvtpk(o[4] * li, o[5] * li); f[3] = cvtpk(o[6] * li, o[7] * li);
  return f;
}

__global__ void __launch_bounds__(512, 2)
fused_kernel(const u16* __restrict__ x, const u16* __restrict__ wqkv, const u16* __restrict__ wo, const u16* __restrict__ wg, const u16* __restrict__ wu,
             const u16* __restrict__ wd, u16* __restrict__ out, char* __restrict__ ws) {
  Ctx C;
  const int tid = threadIdx.x, lane = tid & 63, wid = __builtin_amdgcn_readfirstlane(tid >> 6);
  const int b = blockIdx.x, xj = b & 7, xi = b >> 3;
  C.stampreg = 0;
  STAMP(0);
  asm volatile("" :: "s"(x), "s"(wqkv), "s"(wo), "s"(wg), "s"(wu), "s"(wd), "s"(out), "s"(ws));
  C.ws = ws; C.lane = lane; C.wid = wid; C.mg = wid & 3; C.ng = wid >> 2;
  C.rb = 8 * (xj & 1) + (xi & 7); C.cs = 4 * (xj >> 1) + (xi >> 3); C.bid = b;
  C.sX = mk_srd(x + (size_t)C.rb * 128 * 512, 128 * 1024);
  C.sQKVw = mk_srd(wqkv, 512 * 3072);
  C.xi = xi;
  const unsigned vx = hx_voff(C), vw = hw_voff(C);
  STAMP(3);
  issue_half(C, 0, 0, vx, vw); issue_half(C, 0, 1, vx, vw); issue_half(C, 0, 2, vx, vw);
  STAMP(4);
  STAMP(5);
  C.sWS = mk_srd(ws, 32u << 20); C.sXall = mk_srd(x, 2048 * 1024);
  C.sG = mk_srd(wg, 512 * 3584); C.sU = mk_srd(wu, 512 * 3584); C.sWO = mk_srd(wo, 512 * 1024); C.sWD = mk_srd(wd, 1792 * 1024);
  if (wid == 7) { u64 pc; asm volatile("s_getpc_b64 %0" : "=s"(pc));
    dma4(mk_srd((const void*)(pc & ~255ull), CODE_BYTES), (xi * 9 + (lane < 9 ? lane : 0)) * 128, L_JUNK); }
  if (tid < 256) lp<unsigned>(L_ONES)[tid] = 0x3f803f80u;
  const int rt64 = 4 * xj + (xi & 3), cg = xi >> 2;
  C.cg = cg;
  const int rt = wid & 1, kq = wid >> 1, RT = 2 * rt64 + rt, rb2 = RT >> 2;
  C.kq = kq; C.rb2 = rb2;
  u32x4 qfrag; u32x2 poll2; unsigned poll2off;
  if (C.cs < 8) gemm1<0>(C, vx, vw, qfrag, poll2, poll2off); else gemm1<1>(C, vx, vw, qfrag, poll2, poll2off);
  STAMP(6);

  const unsigned a2base = (unsigned)WS_A2 + (RT * 4 + kq) * 36 * 1024;
  u32x4 a2[36];
  auto side = [&](auto kc) { constexpr int k = decltype(kc)::value;
    if constexpr (k == 1) { wait_vm<10>(); check_flags(ws, poll2off, lane < 10, C.tag, poll2); }
    if constexpr (k >= 1 && k < 7) { ld16_sc1(a2[2 * k - 2], C.sWS, lane * 16, a2base + (2 * k - 2) * 1024); ld16_sc1(a2[2 * k - 1], C.sWS, lane * 16, a2base + (2 * k - 1) * 1024); }
    if constexpr (k >= 12) static_for<0, 4>([&](auto jc) { constexpr int q_ = 12 + 4 * (k - 12) + decltype(jc)::value; ld16_sc1(a2[q_], C.sWS, lane * 16, a2base + q_ * 1024); });
    if constexpr (k >= 7 && k < 13 && !NO_WARM) { const unsigned l = (xi * 6 + (k - 7)) * 64 + wid * 8 + (fresh(lane) & 7);
      dma4(l < 8192 ? C.sWD : C.sWO, (l < 8192 ? l + 768 * 8 : l - 8192) * 128, L_JUNK); } };
  u32x4 of;
  if (C.rb >= 2) of = attention<false>(C, qfrag, side); else of = attention<true>(C, qfrag, side);
  st16_sc1(of, C.sWS, lane * 16, a2_o(C));
  STAMP(7);

  const int g = (lane >> 4) & 1, h = lane >> 5, q = (lane & 15) >> 2, pp = lane & 3;
  const unsigned w2l0 = kq * 8192 + (4 * h + q) * 128 + ((0 ^ (q >> 1)) << 6) + g * 32 + pp * 8;
  const unsigned w2l1 = kq * 8192 + (4 * h + q) * 128 + ((1 ^ (q >> 1)) << 6) + g * 32 + pp * 8;
  bf16x8 wf[3][2];
  f32x16 acc[2];
  u32x4 xres;
  unsigned wb[2], gv;
  auto read_w2 = [&](int pos) {
    if ((pos & 3) == 0) { wb[0] = pinv(w2l0 + gslot(pos >> 2)); wb[1] = pinv(w2l1 + gslot(pos >> 2)); }
    wf[pos % 3][0] = ldfrag_tr(wb[0] + (pos & 3) * 2048, 1024); wf[pos % 3][1] = ldfrag_tr(wb[1] + (pos & 3) * 2048, 1024); };
  bar_vm<17 + (NO_WARM ? 0 : 6)>();
  static_for<0, 12>([&](auto kc) { constexpr int k = decltype(kc)::value; pin(a2[k]); });
  read_w2(0); read_w2(1);
  gv = g_voff(C);
  STAMP(8);
  u32x4 ef[4];
  static_for<0, 36>([&](auto ic) {
    constexpr int pos = decltype(ic)::value;
    constexpr int j = pos >> 2, i = pos & 3;
    if constexpr (i == 2) {
      STAMP(44 + 2 * j);
      if constexpr (j <= 1) bar_vm<63>();
      if constexpr (j == 2) bar_vm<6>();
      if constexpr (j == 3) bar_vm<6>();
      if constexpr (j == 4) bar_vm<7>();
      if constexpr (j == 5) bar_vm<15>();
      if constexpr (j == 6) bar_vm<4>();
      if constexpr (j == 7) bar_vm<0>();
      STAMP(45 + 2 * j);
    }
    if constexpr (pos == 10) { static_for<12, 28>([&](auto kc) { pin(a2[decltype(kc)::value]); }); check_frags<12, 28>(C, a2, a2base); }
    if constexpr (pos == 26) { static_for<28, 36>([&](auto kc) { pin(a2[decltype(kc)::value]); }); pin(xres); check_frags<28, 36>(C, a2, a2base); }
    if constexpr (pos == 16) { ld16(xres, C.sXall, (RT * 32 + (lane & 31)) * 1024 + cg * 128 + kq * 32 + h * 16, 0); }
    if constexpr (pos == 18 || pos == 19) static_for<0, 4>([&](auto kc) { constexpr int k = 28 + 4 * (pos - 18) + decltype(kc)::value; ld16_sc1(a2[k], C.sWS, lane * 16, a2base + k * 1024); });
    if constexpr (pos < 24) issue_g(C, 3 + (pos >> 2), pos & 3, gv);
    if constexpr (pos == 30) { if (b == 0 && wid == 7) {
#pragma unroll
        for (int k = 0; k < 4; ++k) { u32x2 t; ld8_sc1(t, C.sWS, flag_off(0, 0, 0) + (lane * 4 + k) * 128); ef[k][0] = t[0]; ef[k][1] = t[1]; } } }
    if constexpr (pos + 2 < 36) read_w2(pos + 2);
    const bf16x8 af = __builtin_bit_cast(bf16x8, a2[pos]);
    if constexpr (pos == 0) { acc[0] = MFMA32(wf[0][0], af, f32x16{}); acc[1] = MFMA32(wf[0][1], af, f32x16{}); }
    else { acc[0] = MFMA32(wf[pos % 3][0], af, acc[0]); acc[1] = MFMA32(wf[pos % 3][1], af, acc[1]); }
    SB();
  });
  STAMP(9);
#pragma unroll
  for (int d = 0; d < 4; ++d) if (d != kq) {
    const int ct = d >> 1, r0 = (d & 1) * 8;
    const unsigned a = R2 + ((rt * 4 + d) * 4 + kq) * 2048 + lane * 16;
    *lp<f32x4>(a) = f32x4{acc[ct][r0], acc[ct][r0 + 1], acc[ct][r0 + 2], acc[ct][r0 + 3]};
    *lp<f32x4>(a + 1024) = f32x4{acc[ct][r0 + 4], acc[ct][r0 + 5], acc[ct][r0 + 6], acc[ct][r0 + 7]};
  }
  float s[8];
#pragma unroll
  for (int e = 0; e < 8; ++e) { const float lo = (kq & 1) ? acc[0][8 + e] : acc[0][e], hi = (kq & 1) ? acc[1][8 + e] : acc[1][e]; s[e] = (kq & 2) ? hi : lo; }
  asm volatile("s_waitcnt lgkmcnt(0)\n\ts_barrier" ::: "memory");
#pragma unroll
  for (int d = 0; d < 4; ++d) if (d != kq) {
    const unsigned a = R2 + ((rt * 4 + kq) * 4 + d) * 2048 + lane * 16;
    const f32x4 u = *lp<f32x4>(a), v = *lp<f32x4>(a + 1024);
#pragma unroll
    for (int e = 0; e < 4; ++e) { s[e] += u[e]; s[4 + e] += v[e]; }
  }
  {
    unsigned u0 = __float_as_uint(s[0]), u1 = __float_as_uint(s[1]), u2 = __float_as_uint(s[2]), u3 = __float_as_uint(s[3]);
    unsigned v0 = __float_as_uint(s[4]), v1 = __float_as_uint(s[5]), v2 = __float_as_uint(s[6]), v3 = __float_as_uint(s[7]);
    auto r0 = __builtin_amdgcn_permlane32_swap(u0, v0, false, false); auto r1 = __builtin_amdgcn_permlane32_swap(u1, v1, false, false);
    auto r2 = __builtin_amdgcn_permlane32_swap(u2, v2, false, false); auto r3 = __builtin_amdgcn_permlane32_swap(u3, v3, false, false);
    const float c0 = __uint_as_float(r0[0]), c1 = __uint_as_float(r1[0]), c2 = __uint_as_float(r2[0]), c3 = __uint_as_float(r3[0]);
    const float c4 = __uint_as_float(r0[1]), c5 = __uint_as_float(r1[1]), c6 = __uint_as_float(r2[1]), c7 = __uint_as_float(r3[1]);
    u32x4 f;
    f[0] = cvtpk(c0 + bflo(xres[0]), c1 + bfhi(xres[0])); f[1] = cvtpk(c2 + bflo(xres[1]), c3 + bfhi(xres[1]));
    f[2] = cvtpk(c4 + bflo(xres[2]), c5 + bfhi(xres[2])); f[3] = cvtpk(c6 + bflo(xres[3]), c7 + bfhi(xres[3]));
    const i32x4 sO = mk_srd(out, 2048 * 1024);
    st16(f, sO, (RT * 32 + (lane & 31)) * 1024 + cg * 128 + kq * 32 + h * 16, 0);
  }
  if (b == 0 && wid == 7) {
    wait_vm<0>();
    asm volatile("" : "+v"(ef[0]), "+v"(ef[1]), "+v"(ef[2]), "+v"(ef[3]));
    bool ok = true;
#pragma unroll
    for (int k = 0; k < 4; ++k) ok = ok && ef[k][0] == (unsigned)C.tag && ef[k][1] == (unsigned)(C.tag >> 32);
    if (!__all(ok)) {
#pragma unroll 1
      for (int k = 0; k < 4; ++k) spin_flags(ws, flag_off(0, 0, 0) + (lane * 4 + k) * 128, true, C.tag);
    }
    if (lane == 0) __hip_atomic_store((u64*)(ws + WS_EPOCH), C.tag, __ATOMIC_RELAXED, __HIP_MEMORY_SCOPE_AGENT);
  }
  STAMP(60);
}

extern "C" void kernel_launch(void* const* d_in, const int* in_sizes, int n_in, void* d_out, int out_size, void* d_ws, size_t ws_size, hipStream_t stream) {
  (void)in_sizes; (void)n_in; (void)out_size; (void)ws_size;
  (void)hipFuncSetAttribute((const void*)fused_kernel, hipFuncAttributeMaxDynamicSharedMemorySize, LDS_BYTES);
  fused_kernel<<<256, 512, LDS_BYTES, stream>>>((const u16*)d_in[0], (const u16*)d_in[1], (const u16*)d_in[2], (const u16*)d_in[4], (const u16*)d_in[5], (const u16*)d_in[6],
                                                 (u16*)d_out, (char*)d_ws);
}
```
